```python
import torch

def _softmax(x, axis=-1):
    return torch.softmax(x, dim=axis)
def _gelu(x):
    return torch.nn.functional.gelu(x, approximate="tanh")
def _silu(x):
    return torch.nn.functional.silu(x)
def _sigmoid(x):
    return torch.sigmoid(x)
def _layernorm(x, axis=-1, eps=1e-5):
    m = x.mean(dim=axis, keepdim=True)
    v = x.var(dim=axis, keepdim=True, unbiased=False)
    return (x - m) / torch.sqrt(v + eps)
def _scatter_add(tgt, idx, src, axis=0):
    return tgt.clone().index_add_(axis, idx, src)
def _segment_sum(src, seg, n):
    out = torch.zeros((n,) + src.shape[1:], dtype=src.dtype)
    return out.index_add_(0, seg, src)
def _pad(x, pw):
    flat = tuple(v for pair in reversed(pw) for v in pair)
    return torch.nn.functional.pad(x, flat)

kernel_name = 'parallel_sum__ssm_conv_s5__dense_activation_gelu__prompt'

def setup_inputs():

    _w = (lambda a: (lambda t: t.to(torch.bfloat16) if t.is_floating_point() else t)(torch.as_tensor(a)))
    def _A_setup():
        B, S, D = 1, 2048, 512
        P = D
        Lambda_re = -0.5 * (lambda _x: torch.exp(torch.as_tensor(_x)))((lambda *s: torch.randn(*s, dtype=torch.bfloat16) * 0.2)(P))
        Lambda_im = (lambda *s: torch.randn(*s, dtype=torch.bfloat16) * 0.8)(P) * 1.0
        B_re = (lambda *s: torch.randn(*s, dtype=torch.bfloat16) * 0.4)(P, D) * (D ** -0.5)
        B_im = (lambda *s: torch.randn(*s, dtype=torch.bfloat16) * 0.4)(P, D) * (D ** -0.5)
        C_re = (lambda *s: torch.randn(*s, dtype=torch.bfloat16) * 0.4)(D, P) * (P ** -0.5)
        C_im = (lambda *s: torch.randn(*s, dtype=torch.bfloat16) * 0.4)(D, P) * (P ** -0.5)
        log_dt = (lambda *s: torch.randn(*s, dtype=torch.bfloat16) * 0.2)(P) * 0.3 - 2.0
        Dskip = (lambda *s: torch.randn(*s, dtype=torch.bfloat16) * 0.05)(D)
        return {
            "x": _w((lambda *s: torch.randn(*s, dtype=torch.bfloat16) * 0.05)(B, S, D)),
            "Lambda_re": _w(Lambda_re),
            "Lambda_im": _w(Lambda_im),
            "B_re": _w(B_re),
            "B_im": _w(B_im),
            "C_re": _w(C_re),
            "C_im": _w(C_im),
            "log_dt": _w(log_dt),
            "Dskip": _w(Dskip),
        }
    _A_in = _A_setup()

    def _B_setup():
        B, S, D, F = 1, 2048, 512, 1792
        return {
            "x": _w((lambda *s: torch.randn(*s, dtype=torch.bfloat16) * 0.05)(B, S, D)),
            "w1": _w((lambda *s: torch.randn(*s, dtype=torch.bfloat16) * 0.8)(D, F) * (D ** -0.5)),
            "b1": _w((lambda *s: torch.randn(*s, dtype=torch.bfloat16) * 12.8)(F) * 0.01),
            "w2": _w((lambda *s: torch.randn(*s, dtype=torch.bfloat16) * 0.8)(F, D) * (F ** -0.5)),
            "b2": _w((lambda *s: torch.randn(*s, dtype=torch.bfloat16) * 12.8)(D) * 0.01),
        }
    _B_in = _B_setup()

    return {
        "A_x": _A_in["x"],
        "A_Lambda_re": _A_in["Lambda_re"],
        "A_Lambda_im": _A_in["Lambda_im"],
        "A_B_re": _A_in["B_re"],
        "A_B_im": _A_in["B_im"],
        "A_C_re": _A_in["C_re"],
        "A_C_im": _A_in["C_im"],
        "A_log_dt": _A_in["log_dt"],
        "A_Dskip": _A_in["Dskip"],
        "B_x": _B_in["x"],
        "B_w1": _B_in["w1"],
        "B_b1": _B_in["b1"],
        "B_w2": _B_in["w2"],
        "B_b2": _B_in["b2"],
    }

def reference(A_x, A_Lambda_re, A_Lambda_im, A_B_re, A_B_im, A_C_re, A_C_im, A_log_dt, A_Dskip, B_x, B_w1, B_b1, B_w2, B_b2):
    def _A_ref(x, Lambda_re, Lambda_im, B_re, B_im, C_re, C_im, log_dt, Dskip):
        Bsz, L, H = x.shape
        Lam = Lambda_re + 1j * Lambda_im
        Bmat = B_re + 1j * B_im
        Cmat = C_re + 1j * C_im
        dt = torch.exp(log_dt)
        Lam_bar = torch.exp(dt * Lam)
        B_bar = ((Lam_bar - 1.0) / Lam)[:, None] * Bmat
        Bu = torch.einsum("bld,pd->blp", x + 0j, B_bar)
        h = torch.zeros_like(Bu[:, 0, :])
        hs = []
        for t in range(L):
            h = Lam_bar[None, :] * h + Bu[:, t, :]
            hs.append(h)
        hseq = torch.stack(hs, 1)
        y = 2.0 * torch.einsum("blp,dp->bld", hseq, Cmat).real + x * Dskip[None, None, :]
        return y

    def _B_ref(x, w1, b1, w2, b2):
        h = torch.einsum("bsd,df->bsf", x, w1) + b1
        h = _gelu(h)
        return torch.einsum("bsf,fd->bsd", h, w2) + b2

    x = A_x
    xn = _layernorm(x, axis=-1)
    return x + _A_ref(xn, A_Lambda_re, A_Lambda_im, A_B_re, A_B_im, A_C_re, A_C_im, A_log_dt, A_Dskip) + _B_ref(xn, B_w1, B_b1, B_w2, B_b2)
```

```cpp
#include <hip/hip_runtime.h>
#include <hip/hip_bf16.h>
typedef __hip_bfloat16 bf16;
static __device__ __forceinline__ float b2f(bf16 x){return __bfloat162float(x);}
static __device__ __forceinline__ float rb(float x){return __bfloat162float(__float2bfloat16(x));}
constexpr int L=2048,D=512,P=512,F=1792;
__global__ void k_ln(const bf16* x, float* xn){
  int row=blockIdx.x; int t=threadIdx.x;
  float v[8]; float s=0;
  for(int i=0;i<8;i++){v[i]=b2f(x[row*D+t*8+i]); s+=v[i];}
  for(int m=32;m>=1;m>>=1)s+=__shfl_xor(s,m);
  float mean=s/D; float q=0;
  for(int i=0;i<8;i++){float d=v[i]-mean;q+=d*d;}
  for(int m=32;m>=1;m>>=1)q+=__shfl_xor(q,m);
  float var=q/D; float r=rsqrtf(var+1e-5f);
  for(int i=0;i<8;i++) xn[row*D+t*8+i]=rb((v[i]-mean)*r);
}
__global__ void k_g1(const float* xn,const bf16* Bre,const bf16* Bim,const bf16* w1,const bf16* b1,float* U,float* G){
  int row=blockIdx.x; int c=blockIdx.y*256+threadIdx.x;
  __shared__ float xs[D];
  for(int i=threadIdx.x;i<D;i+=256)xs[i]=xn[row*D+i];
  __syncthreads();
  float acc=0;
  if(c<512){for(int k=0;k<D;k++)acc+=xs[k]*b2f(Bre[c*D+k]); U[row*1024+c]=acc;}
  else if(c<1024){for(int k=0;k<D;k++)acc+=xs[k]*b2f(Bim[(c-512)*D+k]); U[row*1024+c]=acc;}
  else {int f=c-1024; for(int k=0;k<D;k++)acc+=xs[k]*b2f(w1[k*F+f]); acc+=b2f(b1[f]);
    float u=0.7978845608f*(acc+0.044715f*acc*acc*acc); float g=0.5f*acc*(1.f+tanhf(u)); G[row*F+f]=rb(g);}
}
__global__ void k_scan(float* U,const bf16* Lre,const bf16* Lim,const bf16* logdt){
  int p=blockIdx.x*64+threadIdx.x;
  float lr=b2f(Lre[p]),li=b2f(Lim[p]); float dt=rb(expf(b2f(logdt[p])));
  float e=expf(dt*lr); float ar=e*cosf(dt*li), ai=e*sinf(dt*li);
  float nr=ar-1.f, ni=ai; float den=lr*lr+li*li;
  float cr=(nr*lr+ni*li)/den, ci=(ni*lr-nr*li)/den;
  float hr=0,hi=0;
  for(int t=0;t<L;t++){
    float ur=U[t*1024+p], ui=U[t*1024+512+p];
    float br=cr*ur-ci*ui, bi=cr*ui+ci*ur;
    float nhr=ar*hr-ai*hi+br, nhi=ar*hi+ai*hr+bi; hr=nhr;hi=nhi;
    U[t*1024+p]=rb(hr); U[t*1024+512+p]=rb(hi);
  }
}
__global__ void k_g2(const bf16* x,const float* xn,const float* U,const float* G,const bf16* Cre,const bf16* Cim,const bf16* Dk,const bf16* w2,const bf16* b2,float* out){
  int row=blockIdx.x; int d=blockIdx.y*256+threadIdx.x;
  __shared__ float hs[1024+F];
  for(int i=threadIdx.x;i<1024;i+=256)hs[i]=U[row*1024+i];
  for(int i=threadIdx.x;i<F;i+=256)hs[1024+i]=G[row*F+i];
  __syncthreads();
  float a=0; for(int p=0;p<P;p++) a+=hs[p]*b2f(Cre[d*P+p])-hs[512+p]*b2f(Cim[d*P+p]);
  float m=0; for(int f=0;f<F;f++) m+=hs[1024+f]*b2f(w2[f*D+d]);
  out[row*D+d]=b2f(x[row*D+d])+2.f*a+xn[row*D+d]*b2f(Dk[d])+m+b2f(b2[d]);
}
extern "C" void kernel_launch(void* const* d_in, const int* in_sizes, int n_in, void* d_out, int out_size, void* d_ws, size_t ws_size, hipStream_t stream){
  const bf16* x=(const bf16*)d_in[0]; const bf16* Lre=(const bf16*)d_in[1]; const bf16* Lim=(const bf16*)d_in[2];
  const bf16* Bre=(const bf16*)d_in[3]; const bf16* Bim=(const bf16*)d_in[4]; const bf16* Cre=(const bf16*)d_in[5]; const bf16* Cim=(const bf16*)d_in[6];
  const bf16* logdt=(const bf16*)d_in[7]; const bf16* Dk=(const bf16*)d_in[8];
  const bf16* w1=(const bf16*)d_in[10]; const bf16* b1=(const bf16*)d_in[11]; const bf16* w2=(const bf16*)d_in[12]; const bf16* b2=(const bf16*)d_in[13];
  float* xn=(float*)d_ws; float* U=xn+L*D; float* G=U+L*1024;
  k_ln<<<L,64,0,stream>>>(x,xn);
  k_g1<<<dim3(L,11),256,0,stream>>>(xn,Bre,Bim,w1,b1,U,G);
  k_scan<<<8,64,0,stream>>>(U,Lre,Lim,logdt);
  k_g2<<<dim3(L,2),256,0,stream>>>(x,xn,U,G,Cre,Cim,Dk,w2,b2,(float*)d_out);
}
```

```cpp
#include <hip/hip_runtime.h>
#include <hip/hip_bf16.h>
typedef __hip_bfloat16 bf16;
static __device__ __forceinline__ float b2f(bf16 x){return __bfloat162float(x);}
static __device__ __forceinline__ float rb(float x){return __bfloat162float(__float2bfloat16(x));}
constexpr int L=2048,D=512,P=512,F=1792;
__global__ void k_ln(const bf16* x, float* xn){
  int row=blockIdx.x; int t=threadIdx.x;
  float v[8]; float s=0;
  for(int i=0;i<8;i++){v[i]=b2f(x[row*D+t*8+i]); s+=v[i];}
  for(int m=32;m>=1;m>>=1)s+=__shfl_xor(s,m);
  float mean=s/D; float q=0;
  for(int i=0;i<8;i++){float d=v[i]-mean;q+=d*d;}
  for(int m=32;m>=1;m>>=1)q+=__shfl_xor(q,m);
  float var=q/D; float r=rsqrtf(var+1e-5f);
  for(int i=0;i<8;i++) xn[row*D+t*8+i]=rb((v[i]-mean)*r);
}
__global__ void k_g1(const float* xn,const bf16* Bre,const bf16* Bim,const bf16* w1,const bf16* b1,float* U,float* G){
  int row=blockIdx.x; int c=blockIdx.y*256+threadIdx.x;
  __shared__ float xs[D];
  for(int i=threadIdx.x;i<D;i+=256)xs[i]=xn[row*D+i];
  __syncthreads();
  float acc=0;
  if(c<512){for(int k=0;k<D;k++)acc+=xs[k]*b2f(Bre[c*D+k]); U[row*1024+c]=acc;}
  else if(c<1024){for(int k=0;k<D;k++)acc+=xs[k]*b2f(Bim[(c-512)*D+k]); U[row*1024+c]=acc;}
  else {int f=c-1024; for(int k=0;k<D;k++)acc+=xs[k]*b2f(w1[k*F+f]); acc+=b2f(b1[f]);
    float u=0.7978845608f*(acc+0.044715f*acc*acc*acc); float g=0.5f*acc*(1.f+tanhf(u)); G[row*F+f]=rb(g);}
}
__global__ void k_scan(float* U,const bf16* Lre,const bf16* Lim,const bf16* logdt){
  int p=blockIdx.x*64+threadIdx.x;
  float lr=b2f(Lre[p]),li=b2f(Lim[p]); float dt=rb(expf(b2f(logdt[p])));
  float e=expf(dt*lr); float ar=e*cosf(dt*li), ai=e*sinf(dt*li);
  float nr=ar-1.f, ni=ai; float den=lr*lr+li*li;
  float cr=(nr*lr+ni*li)/den, ci=(ni*lr-nr*li)/den;
  float hr=0,hi=0;
  for(int t=0;t<L;t++){
    float ur=U[t*1024+p], ui=U[t*1024+512+p];
    float br=cr*ur-ci*ui, bi=cr*ui+ci*ur;
    float nhr=ar*hr-ai*hi+br, nhi=ar*hi+ai*hr+bi; hr=nhr;hi=nhi;
    U[t*1024+p]=rb(hr); U[t*1024+512+p]=rb(hi);
  }
}
__global__ void k_g2(const bf16* x,const float* xn,const float* U,const float* G,const bf16* Cre,const bf16* Cim,const bf16* Dk,const bf16* w2,const bf16* b2,float* out){
  int row=blockIdx.x; int d=blockIdx.y*256+threadIdx.x;
  __shared__ float hs[1024+F];
  for(int i=threadIdx.x;i<1024;i+=256)hs[i]=U[row*1024+i];
  for(int i=threadIdx.x;i<F;i+=256)hs[1024+i]=G[row*F+i];
  __syncthreads();
  float a=0; for(int p=0;p<P;p++) a+=hs[p]*b2f(Cre[d*P+p])-hs[512+p]*b2f(Cim[d*P+p]);
  float m=0; for(int f=0;f<F;f++) m+=hs[1024+f]*b2f(w2[f*D+d]);
  out[row*D+d]=b2f(x[row*D+d])+2.f*a+xn[row*D+d]*b2f(Dk[d])+m+b2f(b2[d]);
}

__global__ void k_probe(const char* p, int n, int stride, int reps, unsigned* sink){
  unsigned acc=0;
  for(int r=0;r<reps;r++)
  for(int i=0;i<n;i++){ unsigned v=__builtin_nontemporal_load((const unsigned*)(p+(size_t)i*stride+(acc&0x40000000u & (acc>>1) & (acc<<1) & 0x4))); acc=(acc^v)&0x3fffffffu; asm volatile(""::"v"(acc)); }
  if(acc==0x7fffffffu) sink[0]=acc;
}
extern "C" void kernel_launch(void* const* d_in, const int* in_sizes, int n_in, void* d_out, int out_size, void* d_ws, size_t ws_size, hipStream_t stream){
  const bf16* x=(const bf16*)d_in[0]; const bf16* Lre=(const bf16*)d_in[1]; const bf16* Lim=(const bf16*)d_in[2];
  const bf16* Bre=(const bf16*)d_in[3]; const bf16* Bim=(const bf16*)d_in[4]; const bf16* Cre=(const bf16*)d_in[5]; const bf16* Cim=(const bf16*)d_in[6];
  const bf16* logdt=(const bf16*)d_in[7]; const bf16* Dk=(const bf16*)d_in[8];
  const bf16* w1=(const bf16*)d_in[10]; const bf16* b1=(const bf16*)d_in[11]; const bf16* w2=(const bf16*)d_in[12]; const bf16* b2=(const bf16*)d_in[13];
  float* xn=(float*)d_ws; float* U=xn+L*D; float* G=U+L*1024;

  k_ln<<<L,64,0,stream>>>(x,xn);
  k_g1<<<dim3(L,11),256,0,stream>>>(xn,Bre,Bim,w1,b1,U,G);
  k_scan<<<8,64,0,stream>>>(U,Lre,Lim,logdt);
  k_g2<<<dim3(L,2),256,0,stream>>>(x,xn,U,G,Cre,Cim,Dk,w2,b2,(float*)d_out);
}
```

```cpp
#include <hip/hip_runtime.h>
#include <stdint.h>

typedef short s16x4 __attribute__((ext_vector_type(4)));
typedef short s16x8 __attribute__((ext_vector_type(8)));
typedef __bf16 bf16x8 __attribute__((ext_vector_type(8)));
typedef __bf16 bf16x2 __attribute__((ext_vector_type(2)));
typedef float f32x4 __attribute__((ext_vector_type(4)));
typedef float f32x2 __attribute__((ext_vector_type(2)));
typedef unsigned u32x4 __attribute__((ext_vector_type(4)));
typedef unsigned u32x2 __attribute__((ext_vector_type(2)));
#define LAS __attribute__((address_space(3)))
#define GAS __attribute__((address_space(1)))
#define DI __device__ __forceinline__

#define STAMPS 0

struct Params {
  const char *x, *lre, *lim, *bre, *bim, *cre, *cim, *logdt, *dsk, *w1, *b1, *w2, *b2;
  char *out, *ws;
};

constexpr unsigned WS_EPOCH = 0;
constexpr unsigned WS_STAT = 4096;
constexpr unsigned WS_TOT = WS_STAT + 2048 * 16;
constexpr unsigned WS_FLAG = WS_TOT + 16 * 2 * 16 * 16 * 16;
constexpr unsigned WS_H = WS_FLAG + 32 * 8 * 128;
constexpr unsigned H_MT = 88 * 4096;
constexpr unsigned WS_END = WS_H + 32 * H_MT;
constexpr unsigned WS_STAMP = (WS_END + 4095) & ~4095u;
constexpr unsigned SLOT = 40960, SL_W = 16384, SL_B = 32768;
constexpr unsigned L_TRANSIT = 30 * 4096;
constexpr unsigned L_MISC = 38 * 4096;
constexpr unsigned L_STAT = L_MISC, L_CS = L_MISC + 1024, L_SEG = L_MISC + 2048, L_SEGF = L_MISC + 4096, L_DUMMY = L_MISC + 4352;
constexpr unsigned LDS_BYTES = 163840;
constexpr int SPIN_CAP = 40000;
constexpr unsigned CODE_BYTES = 20480;

DI void dma16(unsigned voff, const char* sbase, unsigned ldsaddr) {
  asm volatile("s_mov_b32 m0, %2\n\ts_nop 0\n\tglobal_load_lds_dwordx4 %0, %1" ::"v"(voff), "s"(sbase), "s"(ldsaddr));
}
DI void dma4(unsigned voff, const char* sbase, unsigned ldsaddr) {
  asm volatile("s_mov_b32 m0, %2\n\ts_nop 0\n\tglobal_load_lds_dword %0, %1" ::"v"(voff), "s"(sbase), "s"(ldsaddr));
}
template <int N> DI void wait_vm() { asm volatile("s_waitcnt vmcnt(%0)" ::"i"(N) : "memory"); }
DI void wait_lgkm0() { asm volatile("" ::: "memory"); __builtin_amdgcn_s_waitcnt(0xC07F); asm volatile("" ::: "memory"); }
DI void barrier() {
  asm volatile("" ::: "memory");
  __builtin_amdgcn_s_waitcnt(0xC07F);
  __builtin_amdgcn_s_barrier();
  asm volatile("" ::: "memory");
}
#define SCHED() __builtin_amdgcn_sched_barrier(0)
DI s16x8 lds128(unsigned a) { return *(LAS s16x8*)(size_t)a; }
DI f32x4 ldsf4(unsigned a) { return *(LAS f32x4*)(size_t)a; }
DI f32x2 ldsf2(unsigned a) { return *(LAS f32x2*)(size_t)a; }
DI s16x8 ldstr(unsigned a0, unsigned a1) {
  s16x4 lo = __builtin_amdgcn_ds_read_tr16_b64_v4i16((LAS s16x4*)(size_t)a0);
  s16x4 hi = __builtin_amdgcn_ds_read_tr16_b64_v4i16((LAS s16x4*)(size_t)a1);
  return __builtin_shufflevector(lo, hi, 0, 1, 2, 3, 4, 5, 6, 7);
}
DI f32x4 mfma(s16x8 a, s16x8 b, f32x4 c) {
  return __builtin_amdgcn_mfma_f32_16x16x32_bf16(__builtin_bit_cast(bf16x8, a), __builtin_bit_cast(bf16x8, b), c, 0, 0, 0);
}
DI unsigned pk2(float a, float b) { f32x2 v = {a, b}; return __builtin_bit_cast(unsigned, __builtin_convertvector(v, bf16x2)); }
DI float u2f(unsigned u) { return __builtin_bit_cast(float, u); }
DI float bflo(unsigned u) { return __builtin_bit_cast(float, u << 16); }
DI float bfhi(unsigned u) { return __builtin_bit_cast(float, u & 0xffff0000u); }
template <int CTRL> DI float dpp_add(float v) {
  return v + __builtin_bit_cast(float, __builtin_amdgcn_update_dpp(0, __builtin_bit_cast(int, v), CTRL, 0xf, 0xf, true));
}
DI float wave_sum(float v) {
  v = dpp_add<0xB1>(v);
  v = dpp_add<0x4E>(v);
  v = dpp_add<0x141>(v);
  v = dpp_add<0x140>(v);
  const int iv = __builtin_bit_cast(int, v);
  const float a = __builtin_bit_cast(float, __builtin_amdgcn_readlane(iv, 0)), b = __builtin_bit_cast(float, __builtin_amdgcn_readlane(iv, 16));
  const float c = __builtin_bit_cast(float, __builtin_amdgcn_readlane(iv, 32)), d = __builtin_bit_cast(float, __builtin_amdgcn_readlane(iv, 48));
  return (a + b) + (c + d);
}
struct cf { float r, i; };
DI cf cmul(cf a, cf b) { return {a.r * b.r - a.i * b.i, a.r * b.i + a.i * b.r}; }
DI cf cfma(cf a, cf b, cf c) { return {fmaf(a.r, b.r, fmaf(-a.i, b.i, c.r)), fmaf(a.r, b.i, fmaf(a.i, b.r, c.i))}; }
DI cf csel(bool p, cf a, cf b) { return {p ? a.r : b.r, p ? a.i : b.i}; }

#define STAMP(k) do {} while (0)

__global__ void __launch_bounds__(512, 2) fused_kernel(Params p) {
  __shared__ __attribute__((aligned(16))) char lds_mem[LDS_BYTES];
  unsigned long long pc;
  asm volatile("s_getpc_b64 %0" : "=s"(pc));
  const unsigned LB = (unsigned)(size_t)(LAS char*)lds_mem;
  const int tid = threadIdx.x, lane = tid & 63;
  const int w = __builtin_amdgcn_readfirstlane(tid >> 6);
  const int b = blockIdx.x, idx = b >> 3;
  const int r = (b & 7) * 2 + (idx >> 4), j = idx & 15;
  const int mtile = 2 * r + (j >> 3), ntile = j & 7;
  const int i16 = lane & 15, g = lane >> 4;
  const int rh = (w >> 1) & 1, sub = w & 1, q = w & 3;
  const int rot = (j + 4 * (r & 1) + (r >> 1)) & 7;
  const __amdgpu_buffer_rsrc_t wsr = __builtin_amdgcn_make_buffer_rsrc(p.ws, 0, WS_END, 0x00020000);
  const s16x8 ones = {0x3f80, 0x3f80, 0x3f80, 0x3f80, 0x3f80, 0x3f80, 0x3f80, 0x3f80};
  f32x4 acc2[4][4];
  unsigned tagA, tagB;
  STAMP(0);

  if (w >= 4) {
    unsigned v_lre, v_lim, v_ldt;
    u32x2 v_ep;
    {
      const unsigned o = (unsigned)(32 * j + 16 * sub + i16) * 2u, z = 0;
      asm volatile("global_load_ushort %0, %4, %5\n\tglobal_load_ushort %1, %4, %6\n\tglobal_load_ushort %2, %4, %7\n\tglobal_load_dwordx2 %3, %8, %9 sc1"
                   : "=&v"(v_lre), "=&v"(v_lim), "=&v"(v_ldt), "=&v"(v_ep)
                   : "v"(o), "s"(p.lre), "s"(p.lim), "s"(p.logdt), "v"(z), "s"(p.ws));
    }
    unsigned vx[4], vw[4], vb[2];
#pragma unroll
    for (int i = 0; i < 4; ++i) {
      const int mt = (2 * q + (i >> 1)) & 3, ii = 8 * (i & 1) + (lane >> 3);
      const int t = 64 * (q >> 1) + 16 * (ii >> 2) + 4 * mt + (ii & 3);
      vx[i] = t * 1024 + (((lane & 7) ^ ((ii >> 1) & 7)) * 16);
      const int row = 16 * q + 4 * i + g, sg = g | (((i >> 1) & 1) << 2);
      vw[i] = row * 3584 + ((((i16 >> 1) ^ sg) << 1) | (i16 & 1)) * 16;
    }
#pragma unroll
    for (int i = 0; i < 2; ++i) vb[i] = ((16 * q + 8 * i + (lane >> 3)) & 31) * 1024 + (((lane & 7) ^ ((4 * i + g) & 7)) * 16);
    const char* xb = p.x + (size_t)r * 131072;
    const char* wb = p.w1 + (j < 14 ? j : 13) * 256;
    const char* bb = (q < 2 ? p.bre : p.bim) + j * 32768;
    auto issue = [&](int c, unsigned slot) {
      const int kc = (c + rot) & 7;
      const char* xs = xb + kc * 128;
      const char* ws_ = wb + kc * (64 * 3584);
      const char* bs = bb + kc * 128;
      const unsigned d = LB + slot + q * 4096;
#pragma unroll
      for (int i = 0; i < 4; ++i) dma16(vx[i], xs, d + i * 1024);
#pragma unroll
      for (int i = 0; i < 4; ++i) dma16(vw[i], ws_, d + SL_W + i * 1024);
#pragma unroll
      for (int i = 0; i < 2; ++i) dma16(vb[i], bs, LB + slot + SL_B + q * 2048 + i * 1024);
    };
    issue(0, 0); issue(1, SLOT); issue(2, 2 * SLOT);
    const unsigned T = LB + L_TRANSIT + q * 8192;
    const unsigned vc0 = (lane >> 3) * 1024 + (((lane & 7) ^ g) * 16), vc1 = (lane >> 3) * 1024 + (((lane & 7) ^ (4 + g)) * 16);
    auto issueC = [&](int hh, int nh) {
      const size_t o = (size_t)(64 * ntile + 32 * nh) * 1024 + q * 256 + hh * 128;
#pragma unroll
      for (int ii = 0; ii < 4; ++ii) dma16((ii & 1) ? vc1 : vc0, p.cre + o + ii * 8192, T + ii * 1024);
#pragma unroll
      for (int ii = 0; ii < 4; ++ii) dma16((ii & 1) ? vc1 : vc0, p.cim + o + ii * 8192, T + 4096 + ii * 1024);
    };
    const unsigned crd = T + (g >> 1) * 4096 + i16 * 128;
    auto readC = [&](int ksl, int ntl) { return lds128(crd + ntl * 2048 + (((2 * ksl + (g & 1)) ^ (i16 >> 1)) * 16)); };

    asm volatile("s_waitcnt vmcnt(30)" : "+v"(v_lre), "+v"(v_lim), "+v"(v_ldt), "+v"(v_ep));
    tagA = __builtin_amdgcn_readfirstlane(v_ep.x) + 1u;
    tagB = __builtin_amdgcn_readfirstlane(v_ep.y) + 1u;
    cf a1, kk, a16, a512, aseg, acar;
    {
      const float lr = bflo(v_lre), li = bflo(v_lim);
      const float dt = bflo(pk2(expf(bflo(v_ldt)), 0.f));
      float sn, cs;
      sincosf(dt * li, &sn, &cs);
      const float e = expf(dt * lr);
      a1 = {e * cs, e * sn};
      const float nr = a1.r - 1.f, ni = a1.i, den = lr * lr + li * li;
      kk = {2.f * (nr * lr + ni * li) / den, 2.f * (ni * lr - nr * li) / den};
      cf t = cmul(a1, a1); t = cmul(t, t); t = cmul(t, t); a16 = cmul(t, t);
      const cf a32 = cmul(a16, a16), a64 = cmul(a32, a32), a128 = cmul(a64, a64), a256 = cmul(a128, a128);
      a512 = cmul(a256, a256);
      const cf one = {1.f, 0.f};
      aseg = cmul(cmul(csel(g & 1, a16, one), csel(g & 2, a32, one)), csel(rh, a64, one));
      const int e4 = (r - 1 - g) & 3;
      acar = cmul(csel(e4 & 1, a128, one), csel(e4 & 2, a256, one));
    }

    f32x4 acc[4][2], csb[2];
#pragma unroll
    for (int m = 0; m < 4; ++m) { acc[m][0] = (f32x4){0, 0, 0, 0}; acc[m][1] = (f32x4){0, 0, 0, 0}; }
    csb[0] = (f32x4){0, 0, 0, 0}; csb[1] = (f32x4){0, 0, 0, 0};
    s16x8 xa[4], ba[2], xbf[4], bbf[2];
    const unsigned rx = LB + (64 * rh + i16) * 128, rbb = LB + SL_B + (16 * sub + i16) * 128;
    const unsigned k0 = (g ^ (i16 >> 1)) * 16, k1 = ((4 + g) ^ (i16 >> 1)) * 16;
    auto mma = [&](const s16x8* xf, const s16x8* bf) {
#pragma unroll
      for (int m = 0; m < 4; ++m) { acc[m][0] = mfma(xf[m], bf[0], acc[m][0]); acc[m][1] = mfma(xf[m], bf[1], acc[m][1]); }
      csb[0] = mfma(ones, bf[0], csb[0]); csb[1] = mfma(ones, bf[1], csb[1]);
    };
    unsigned slot = 0, pslot = 2 * SLOT;
    STAMP(1);
#pragma unroll 1
    for (int c = 0; c < 8; ++c) {
      if (c == 0) wait_vm<20>(); else if (c == 7) wait_vm<8>(); else wait_vm<10>();
      barrier();
#pragma unroll
      for (int m = 0; m < 4; ++m) xa[m] = lds128(rx + slot + k0 + m * 2048);
      ba[0] = lds128(rbb + slot + k0); ba[1] = lds128(rbb + slot + k0 + 4096);
      if (c) {
        if (c < 6) issue(c + 2, pslot); else if (c == 6) issueC(0, 0);
        mma(xbf, bbf);
      }
      SCHED();
#pragma unroll
      for (int m = 0; m < 4; ++m) xbf[m] = lds128(rx + slot + k1 + m * 2048);
      bbf[0] = lds128(rbb + slot + k1); bbf[1] = lds128(rbb + slot + k1 + 4096);
      SCHED();
      mma(xa, ba);
      pslot = slot;
      slot = (slot == 2 * SLOT) ? 0 : slot + SLOT;
    }
    mma(xbf, bbf);
    STAMP(2);
    s16x8 cf0[4][2], cf1[4][2], cf2[4][2];
    wait_vm<0>();
#pragma unroll
    for (int k = 0; k < 4; ++k) { cf0[k][0] = readC(k, 0); cf0[k][1] = readC(k, 1); }
    wait_lgkm0();
    issueC(0, 1);
    barrier();
    float ur[16], ui[16];
#pragma unroll
    for (int s2 = 0; s2 < 8; ++s2) {
      const f32x4 st = ldsf4(LB + L_STAT + (64 * rh + 16 * g + 2 * s2) * 8);
#pragma unroll
      for (int h = 0; h < 2; ++h) {
        const int s = 2 * s2 + h;
        ur[s] = fmaf(acc[s >> 2][0][s & 3], st[2 * h], -st[2 * h + 1] * csb[0][0]);
        ui[s] = fmaf(acc[s >> 2][1][s & 3], st[2 * h], -st[2 * h + 1] * csb[1][0]);
      }
    }
    cf E = {ur[0], ui[0]};
#pragma unroll
    for (int s = 1; s < 16; ++s) E = cfma(a1, E, cf{ur[s], ui[s]});
    *(LAS f32x2*)(size_t)(LB + L_SEG + sub * 1024 + (4 * rh + g) * 128 + i16 * 8) = (f32x2){E.r, E.i};
    wait_lgkm0();
    if (rh == 0) {
      if (lane == 0) *(volatile LAS unsigned*)(size_t)(LB + L_SEGF + sub * 4) = 1u;
    } else {
      _Pragma("unroll 1") for (int spin = 0; spin < SPIN_CAP * 16; ++spin)
        if (__builtin_amdgcn_readfirstlane(*(volatile LAS unsigned*)(size_t)(LB + L_SEGF + sub * 4)) != 0u) break;
    }
    asm volatile("" ::: "memory");
    cf P = {0.f, 0.f}, Tt = {0.f, 0.f};
#pragma unroll
    for (int s = 0; s < 8; ++s) {
      if (s < 4 || rh) {
        const f32x2 e = ldsf2(LB + L_SEG + sub * 1024 + s * 128 + i16 * 8);
        P = csel(s == 4 * rh + g, Tt, P);
        Tt = cfma(a16, Tt, cf{e[0], e[1]});
      }
    }
    wait_vm<0>();
    const unsigned totb = WS_TOT + (unsigned)(j * 2 + sub) * 4096;
    if (rh && g == 0) {
      const u32x4 v = {tagA, __builtin_bit_cast(unsigned, Tt.r), tagB, __builtin_bit_cast(unsigned, Tt.i)};
      __builtin_amdgcn_raw_buffer_store_b128(v, wsr, totb + r * 256 + i16 * 16, 0, 16);
    }
    STAMP(3);
#pragma unroll
    for (int k = 0; k < 4; ++k) { cf1[k][0] = readC(k, 0); cf1[k][1] = readC(k, 1); }
    wait_lgkm0();
    issueC(1, 0);
    cf car = {0.f, 0.f};
    if (r > 0) {
      u32x4 tv[4];
      _Pragma("unroll 1") for (int spin = 0; spin < SPIN_CAP; ++spin) {
        bool ok = true;
#pragma unroll
        for (int i = 0; i < 4; ++i) tv[i] = __builtin_amdgcn_raw_buffer_load_b128(wsr, totb + i * 1024 + lane * 16, 0, 16);
#pragma unroll
        for (int i = 0; i < 4; ++i) ok &= (4 * i + g >= r) | ((tv[i].x == tagA) & (tv[i].z == tagB));
        if (__all(ok)) break;
        __builtin_amdgcn_s_sleep(1);
      }
#pragma unroll
      for (int i = 0; i < 4; ++i) {
        const cf n = cfma(a512, car, cf{u2f(tv[i].y), u2f(tv[i].w)});
        car = csel(4 * i + g < r, n, car);
      }
      car = cmul(car, acar);
      car.r += __shfl_xor(car.r, 16); car.i += __shfl_xor(car.i, 16);
      car.r += __shfl_xor(car.r, 32); car.i += __shfl_xor(car.i, 32);
    }
    STAMP(4);
    wait_vm<0>();
#pragma unroll
    for (int k = 0; k < 4; ++k) { cf2[k][0] = readC(k, 0); cf2[k][1] = readC(k, 1); }
    wait_lgkm0();
    issueC(1, 1);
    {
      cf h = cfma(aseg, car, P);
      const unsigned tp = LB + (q < 2 ? 28 + q : 6 + q) * 4096;
      const unsigned wa = tp + i16 * 32 + ((g ^ (i16 >> 2)) * 8);
#pragma unroll
      for (int m = 0; m < 4; ++m) {
        float o_r[4], o_i[4];
#pragma unroll
        for (int e = 0; e < 4; ++e) {
          h = cfma(a1, h, cf{ur[4 * m + e], ui[4 * m + e]});
          o_r[e] = kk.r * h.r - kk.i * h.i;
          o_i[e] = -(kk.r * h.i + kk.i * h.r);
        }
        *(LAS u32x2*)(size_t)(wa + m * 1024) = (u32x2){pk2(o_r[0], o_r[1]), pk2(o_r[2], o_r[3])};
        *(LAS u32x2*)(size_t)(wa + m * 1024 + 512) = (u32x2){pk2(o_i[0], o_i[1]), pk2(o_i[2], o_i[3])};
      }
      wait_lgkm0();
      const int qq = i16 >> 2, pp = i16 & 3;
      const unsigned ra0 = tp + (8 * g + qq) * 32 + ((pp ^ ((2 * g) & 3)) * 8), ra1 = tp + (8 * g + 4 + qq) * 32 + ((pp ^ ((2 * g + 1) & 3)) * 8);
      const unsigned ho = WS_H + (unsigned)(2 * r + rh) * H_MT + (unsigned)(56 + 2 * j + sub) * 4096 + lane * 16;
#pragma unroll
      for (int m = 0; m < 4; ++m) {
        const s16x8 f = ldstr(ra0 + m * 1024, ra1 + m * 1024);
        __builtin_amdgcn_raw_buffer_store_b128(__builtin_bit_cast(u32x4, f), wsr, ho + m * 1024, 0, 16);
      }
      wait_vm<0>();
      const int ks = 2 * j + sub;
      if (lane == 0) __builtin_amdgcn_raw_buffer_store_b64((u32x2){tagA, tagB}, wsr, WS_FLAG + ((2 * r + rh) * 8 + 4 + (ks >> 3)) * 128 + (ks & 7) * 8, 0, 16);
    }
    STAMP(5);
    _Pragma("unroll 1") for (int spin = 0; spin < SPIN_CAP; ++spin) {
      const u32x2 f = __builtin_amdgcn_raw_buffer_load_b64(wsr, WS_FLAG + (mtile * 8 + 4 + q) * 128 + (lane & 7) * 8, 0, 16);
      if (__all((f.x == tagA) & (f.y == tagB))) break;
      __builtin_amdgcn_s_sleep(1);
    }
    asm volatile("" ::: "memory");
    STAMP(6);
    const unsigned hb = WS_H + (unsigned)mtile * H_MT + (unsigned)(56 + 8 * q) * 4096 + lane * 16;
    u32x4 hf[3][4];
#pragma unroll
    for (int i = 0; i < 3; ++i)
#pragma unroll
      for (int m = 0; m < 4; ++m) hf[i][m] = __builtin_amdgcn_raw_buffer_load_b128(wsr, hb + m * 1024, i * 4096, 16);
#pragma unroll
    for (int n = 0; n < 4; ++n)
#pragma unroll
      for (int m = 0; m < 4; ++m) acc2[n][m] = (f32x4){0, 0, 0, 0};
    SCHED();
    s16x8 cf3[4][2];
#pragma unroll
    for (int i = 0; i < 8; ++i) {
      if (i == 3) {
#pragma unroll
        for (int k = 0; k < 4; ++k) { cf3[k][0] = readC(k, 0); cf3[k][1] = readC(k, 1); }
      }
#pragma unroll
      for (int n = 0; n < 4; ++n) {
        const s16x8 cfr = i < 4 ? (n < 2 ? cf0[i & 3][n & 1] : cf1[i & 3][n & 1]) : (n < 2 ? cf2[i & 3][n & 1] : cf3[i & 3][n & 1]);
#pragma unroll
        for (int m = 0; m < 4; ++m) acc2[n][m] = mfma(cfr, __builtin_bit_cast(s16x8, hf[i % 3][m]), acc2[n][m]);
      }
      SCHED();
      if (i + 3 < 8) {
#pragma unroll
        for (int m = 0; m < 4; ++m) hf[i % 3][m] = __builtin_amdgcn_raw_buffer_load_b128(wsr, hb + m * 1024, (i + 3) * 4096, 16);
      }
      SCHED();
    }
    STAMP(7);
  } else {
    const u32x4 xr0 = *(const GAS u32x4*)(p.x + (size_t)(128 * r + 8 * j + 2 * w) * 1024 + lane * 16);
    const u32x4 xr1 = *(const GAS u32x4*)(p.x + (size_t)(128 * r + 8 * j + 2 * w + 1) * 1024 + lane * 16);
    const u32x2 ep = __builtin_amdgcn_raw_buffer_load_b64(wsr, WS_EPOCH, 0, 16);
    u32x2 b1v[4];
#pragma unroll
    for (int s = 0; s < 4; ++s) b1v[s] = *(const GAS u32x2*)(p.b1 + ((j < 14 ? j : 13) * 128 + 64 * sub + 16 * (s ^ (2 * rh)) + 4 * g) * 2);
    {
      const unsigned co = (unsigned)(w * 64 + lane) * 128u;
      dma4(co < CODE_BYTES ? co : 0u, (const char*)(pc & ~255ull), LB + L_DUMMY + w * 256);
      if (w == 0) { if (lane < 56) dma4(lane * 128, p.w2 + b * 7168, LB + L_DUMMY); }
      else if (w == 1) { if (lane < 16) dma4(lane * 128, p.cre + b * 2048, LB + L_DUMMY + 256); }
      else if (w == 2) { if (lane < 16) dma4(lane * 128, p.cim + b * 2048, LB + L_DUMMY + 512); }
      else { if (lane < 1) { dma4(0, p.dsk + ntile * 128, LB + L_DUMMY + 768); dma4(0, p.b2 + ntile * 128, LB + L_DUMMY + 768); } }
    }
    if (tid < 2) *(LAS unsigned*)(size_t)(LB + L_SEGF + tid * 4) = 0u;
    tagA = __builtin_amdgcn_readfirstlane(ep.x) + 1u;
    tagB = __builtin_amdgcn_readfirstlane(ep.y) + 1u;
    {
      float v0[8], v1[8], s0 = 0.f, s1 = 0.f;
#pragma unroll
      for (int e = 0; e < 4; ++e) {
        v0[2 * e] = bflo(xr0[e]); v0[2 * e + 1] = bfhi(xr0[e]); v1[2 * e] = bflo(xr1[e]); v1[2 * e + 1] = bfhi(xr1[e]);
        s0 += v0[2 * e] + v0[2 * e + 1]; s1 += v1[2 * e] + v1[2 * e + 1];
      }
      const float m0 = wave_sum(s0) * (1.f / 512.f), m1 = wave_sum(s1) * (1.f / 512.f);
      float q0 = 0.f, q1 = 0.f;
#pragma unroll
      for (int e = 0; e < 8; ++e) { const float d0 = v0[e] - m0, d1 = v1[e] - m1; q0 = fmaf(d0, d0, q0); q1 = fmaf(d1, d1, q1); }
      const float r0 = __builtin_amdgcn_rsqf(wave_sum(q0) * (1.f / 512.f) + 1e-5f), r1 = __builtin_amdgcn_rsqf(wave_sum(q1) * (1.f / 512.f) + 1e-5f);
      if (lane < 2) {
        const float rs = lane ? r1 : r0, mm = lane ? m1 : m0;
        const u32x4 v = {tagA, __builtin_bit_cast(unsigned, rs), tagB, __builtin_bit_cast(unsigned, mm * rs)};
        __builtin_amdgcn_raw_buffer_store_b128(v, wsr, WS_STAT + (128 * r + 8 * j + 2 * w + lane) * 16, 0, 16);
      }
    }
    asm volatile("" : "+v"(b1v[0]), "+v"(b1v[1]), "+v"(b1v[2]), "+v"(b1v[3]));
    f32x4 acc[4][4], cs[2];
#pragma unroll
    for (int s = 0; s < 4; ++s)
#pragma unroll
      for (int m = 0; m < 4; ++m) acc[s][m] = (f32x4){0, 0, 0, 0};
    cs[0] = (f32x4){0, 0, 0, 0}; cs[1] = (f32x4){0, 0, 0, 0};
    s16x8 xa[4], wa[4], xbf[4], wbf[4];
    const int qq = i16 >> 2, pp = i16 & 3;
    const unsigned rx = LB + (64 * rh + i16) * 128;
    const unsigned k0 = (g ^ (i16 >> 1)) * 16, k1 = ((4 + g) ^ (i16 >> 1)) * 16;
    unsigned rw[4];
#pragma unroll
    for (int s = 0; s < 4; ++s) rw[s] = LB + SL_W + (8 * g + qq) * 256 + (((4 * sub + (s ^ (2 * rh))) ^ (qq | ((g & 1) << 2))) * 32) + pp * 8;
    auto mma = [&](const s16x8* xf, const s16x8* wf) {
#pragma unroll
      for (int s = 0; s < 4; ++s)
#pragma unroll
        for (int m = 0; m < 4; ++m) acc[s][m] = mfma(wf[s], xf[m], acc[s][m]);
      cs[0] = mfma(wf[0], ones, cs[0]); cs[1] = mfma(wf[1], ones, cs[1]);
    };
    unsigned slot = 0;
    STAMP(1);
#pragma unroll 1
    for (int c = 0; c < 8; ++c) {
      barrier();
#pragma unroll
      for (int m = 0; m < 4; ++m) xa[m] = lds128(rx + slot + k0 + m * 2048);
#pragma unroll
      for (int s = 0; s < 4; ++s) wa[s] = ldstr(rw[s] + slot, rw[s] + slot + 1024);
      if (c) mma(xbf, wbf);
      SCHED();
#pragma unroll
      for (int m = 0; m < 4; ++m) xbf[m] = lds128(rx + slot + k1 + m * 2048);
#pragma unroll
      for (int s = 0; s < 4; ++s) wbf[s] = ldstr(rw[s] + slot + 8192, rw[s] + slot + 8192 + 1024);
      SCHED();
      mma(xa, wa);
      slot = (slot == 2 * SLOT) ? 0 : slot + SLOT;
    }
    u32x4 sv = __builtin_amdgcn_raw_buffer_load_b128(wsr, WS_STAT + (128 * r + 64 * rh + lane) * 16, 0, 16);
    SCHED();
    mma(xbf, wbf);
    SCHED();
    STAMP(2);
    if (sub == 0) {
      _Pragma("unroll 1") for (int spin = 0; spin < SPIN_CAP; ++spin) {
        if (__all((sv.x == tagA) & (sv.z == tagB))) break;
        __builtin_amdgcn_s_sleep(1);
        sv = __builtin_amdgcn_raw_buffer_load_b128(wsr, WS_STAT + (128 * r + 64 * rh + lane) * 16, 0, 16);
      }
      *(LAS u32x2*)(size_t)(LB + L_STAT + (64 * rh + lane) * 8) = (u32x2){sv.y, sv.w};
    }
    if (i16 == 0) {
      *(LAS f32x4*)(size_t)(LB + L_CS + (64 * sub + 32 * rh + 4 * g) * 4) = cs[0];
      *(LAS f32x4*)(size_t)(LB + L_CS + (64 * sub + 32 * rh + 16 + 4 * g) * 4) = cs[1];
    }
    barrier();
    const unsigned v2 = (lane >> 3) * 1024 + (((((lane & 7) >> 1) ^ (g & 3)) << 1) | (lane & 1)) * 16;
    const char* w2b = p.w2 + (size_t)(448 * q) * 1024 + ntile * 128;
    auto pageof = [&](int s) -> unsigned { return (unsigned)((s < 2 ? 20 + s : (s < 4 ? s - 2 : 6 + s)) * 4096); };
    const unsigned pq = LB + q * 8192;
    auto issueW = [&](int i, int s) {
#pragma unroll
      for (int ii = 0; ii < 4; ++ii) dma16(v2, w2b + (i * 32 + ii * 8) * 1024, pq + pageof(s) + ii * 1024);
    };
#pragma unroll
    for (int i = 0; i < 6; ++i) issueW(i, i);
    if (j < 14) {
      f32x2 st[4];
#pragma unroll
      for (int m = 0; m < 4; ++m) st[m] = ldsf2(LB + L_STAT + (64 * rh + 16 * (i16 >> 2) + 4 * m + (i16 & 3)) * 8);
      f32x4 csa[4];
      csa[0] = cs[0]; csa[1] = cs[1];
      csa[2] = ldsf4(LB + L_CS + (64 * sub + 32 * (rh ^ 1) + 4 * g) * 4);
      csa[3] = ldsf4(LB + L_CS + (64 * sub + 32 * (rh ^ 1) + 16 + 4 * g) * 4);
      const unsigned ho = WS_H + (unsigned)(2 * r + rh) * H_MT + (unsigned)(4 * j + 2 * sub) * 4096 + lane * 16;
#pragma unroll
      for (int pr = 0; pr < 2; ++pr) {
#pragma unroll
        for (int m = 0; m < 4; ++m) {
          u32x4 o;
#pragma unroll
          for (int h = 0; h < 2; ++h) {
            const int s = 2 * pr + h;
            float y[4];
#pragma unroll
            for (int e = 0; e < 4; ++e) {
              const float bias = (e & 1) ? bfhi(b1v[s][e >> 1]) : bflo(b1v[s][e >> 1]);
              const float v = fmaf(acc[s][m][e], st[m][0], fmaf(-st[m][1], csa[s][e], bias));
              const float z = v * fmaf(v * v, -0.10294324f, -2.30220820f);
              y[e] = v * __builtin_amdgcn_rcpf(1.f + __builtin_amdgcn_exp2f(z));
            }
            o[2 * h] = pk2(y[0], y[1]); o[2 * h + 1] = pk2(y[2], y[3]);
          }
          __builtin_amdgcn_raw_buffer_store_b128(o, wsr, ho + (pr ^ rh) * 4096 + m * 1024, 0, 16);
        }
      }
      STAMP(3);
      wait_vm<0>();
      const int kp = 2 * j + sub;
      if (lane == 0) __builtin_amdgcn_raw_buffer_store_b64((u32x2){tagA, tagB}, wsr, WS_FLAG + ((2 * r + rh) * 8 + kp / 7) * 128 + (kp % 7) * 8, 0, 16);
    } else {
      wait_vm<0>();
    }
    STAMP(4);
    unsigned r2[4];
#pragma unroll
    for (int n = 0; n < 4; ++n) r2[n] = pq + (4 * g + qq) * 128 + ((n ^ ((2 * g + (qq >> 1)) & 3)) * 32) + pp * 8;
    auto readW = [&](int s, int n) { return ldstr(r2[n] + pageof(s), r2[n] + pageof(s) + 2048); };
    s16x8 we[4][4];
#pragma unroll
    for (int i = 0; i < 4; ++i)
#pragma unroll
      for (int n = 0; n < 4; ++n) we[i][n] = readW(i, n);
    wait_lgkm0();
#pragma unroll
    for (int i = 0; i < 4; ++i) issueW(6 + i, i);
    STAMP(5);
    _Pragma("unroll 1") for (int spin = 0; spin < SPIN_CAP; ++spin) {
      const u32x2 f = __builtin_amdgcn_raw_buffer_load_b64(wsr, WS_FLAG + (mtile * 8 + q) * 128 + (lane < 7 ? lane : 0) * 8, 0, 16);
      if (__all((f.x == tagA) & (f.y == tagB))) break;
      __builtin_amdgcn_s_sleep(1);
    }
    asm volatile("" ::: "memory");
    STAMP(6);
    const unsigned hb = WS_H + (unsigned)mtile * H_MT + (unsigned)(14 * q) * 4096 + lane * 16;
    u32x4 hf[3][4];
#pragma unroll
    for (int i = 0; i < 3; ++i)
#pragma unroll
      for (int m = 0; m < 4; ++m) hf[i][m] = __builtin_amdgcn_raw_buffer_load_b128(wsr, hb + m * 1024, i * 4096, 16);
#pragma unroll
    for (int n = 0; n < 4; ++n)
#pragma unroll
      for (int m = 0; m < 4; ++m) acc2[n][m] = (f32x4){0, 0, 0, 0};
    SCHED();
    s16x8 wl[2][4];
#pragma unroll
    for (int i = 0; i < 14; ++i) {
      if (i + 1 >= 4 && i + 1 < 14) {
        asm volatile("" ::: "memory");
#pragma unroll
        for (int n = 0; n < 4; ++n) wl[(i + 1) & 1][n] = readW((i + 1) % 6, n);
      }
      SCHED();
#pragma unroll
      for (int n = 0; n < 4; ++n) {
        const s16x8 wf = i < 4 ? we[i & 3][n] : wl[i & 1][n];
#pragma unroll
        for (int m = 0; m < 4; ++m) acc2[n][m] = mfma(wf, __builtin_bit_cast(s16x8, hf[i % 3][m]), acc2[n][m]);
      }
      SCHED();
      if (i >= 4 && i < 8) issueW(i + 6, i % 6);
      if (i + 3 < 14) {
#pragma unroll
        for (int m = 0; m < 4; ++m) hf[i % 3][m] = __builtin_amdgcn_raw_buffer_load_b128(wsr, hb + m * 1024, (i + 3) * 4096, 16);
      }
      SCHED();
    }
    STAMP(7);
  }

  const int ck = tid & 15;
  u32x2 xv[2];
  int trow[2];
#pragma unroll
  for (int h = 0; h < 2; ++h) {
    const int rho = (tid >> 4) + 32 * h, ii = rho & 15;
    trow[h] = 16 * (ii >> 2) + 4 * (rho >> 4) + (ii & 3);
    xv[h] = *(const GAS u32x2*)(p.x + (size_t)(64 * mtile + trow[h]) * 1024 + ntile * 128 + ck * 8);
  }
  const u32x2 dv = *(const GAS u32x2*)(p.dsk + ntile * 128 + ck * 8), bv = *(const GAS u32x2*)(p.b2 + ntile * 128 + ck * 8);
  barrier();
#pragma unroll
  for (int n = 0; n < 4; ++n)
#pragma unroll
    for (int m = 0; m < 4; ++m) *(LAS f32x4*)(size_t)(LB + w * 16384 + (16 * m + i16) * 256 + (((4 * n + g) ^ i16) * 16)) = acc2[n][m];
  barrier();
#pragma unroll
  for (int h = 0; h < 2; ++h) {
    const int rho = (tid >> 4) + 32 * h;
    const unsigned a = LB + rho * 256 + ((ck ^ (rho & 15)) * 16);
    f32x4 s = ldsf4(a);
#pragma unroll
    for (int k = 1; k < 8; ++k) s += ldsf4(a + k * 16384);
    const f32x2 st = ldsf2(LB + L_STAT + (64 * (j >> 3) + trow[h]) * 8);
    f32x4 o;
#pragma unroll
    for (int e = 0; e < 4; ++e) {
      const float xx = (e & 1) ? bfhi(xv[h][e >> 1]) : bflo(xv[h][e >> 1]);
      const float dd = (e & 1) ? bfhi(dv[e >> 1]) : bflo(dv[e >> 1]);
      const float bb = (e & 1) ? bfhi(bv[e >> 1]) : bflo(bv[e >> 1]);
      o[e] = s[e] + xx + bb + fmaf(xx, st[0], -st[1]) * dd;
    }
    *(GAS f32x4*)(p.out + (size_t)(64 * mtile + trow[h]) * 2048 + ntile * 256 + ck * 16) = o;
  }
  if (r == 15 && j == 0 && tid == 0) __builtin_amdgcn_raw_buffer_store_b64((u32x2){tagA, tagB}, wsr, WS_EPOCH, 0, 16);
  STAMP(8);
}

extern "C" void kernel_launch(void* const* d_in, const int* in_sizes, int n_in, void* d_out, int out_size, void* d_ws,
                              size_t ws_size, hipStream_t stream) {
  Params p;
  p.x = (const char*)d_in[0]; p.lre = (const char*)d_in[1]; p.lim = (const char*)d_in[2];
  p.bre = (const char*)d_in[3]; p.bim = (const char*)d_in[4]; p.cre = (const char*)d_in[5]; p.cim = (const char*)d_in[6];
  p.logdt = (const char*)d_in[7]; p.dsk = (const char*)d_in[8];
  p.w1 = (const char*)d_in[10]; p.b1 = (const char*)d_in[11]; p.w2 = (const char*)d_in[12]; p.b2 = (const char*)d_in[13];
  p.out = (char*)d_out; p.ws = (char*)d_ws;
  fused_kernel<<<256, 512, 0, stream>>>(p);
}
```

```cpp
#include <hip/hip_runtime.h>
#include <stdint.h>

typedef short s16x4 __attribute__((ext_vector_type(4)));
typedef short s16x8 __attribute__((ext_vector_type(8)));
typedef __bf16 bf16x8 __attribute__((ext_vector_type(8)));
typedef __bf16 bf16x2 __attribute__((ext_vector_type(2)));
typedef float f32x4 __attribute__((ext_vector_type(4)));
typedef float f32x2 __attribute__((ext_vector_type(2)));
typedef unsigned u32x4 __attribute__((ext_vector_type(4)));
typedef unsigned u32x2 __attribute__((ext_vector_type(2)));
#define LAS __attribute__((address_space(3)))
#define GAS __attribute__((address_space(1)))
#define DI __device__ __forceinline__

#define STAMPS 0

struct Params {
  const char *x, *lre, *lim, *bre, *bim, *cre, *cim, *logdt, *dsk, *w1, *b1, *w2, *b2;
  char *out, *ws;
};

constexpr unsigned WS_EPOCH = 0;
constexpr unsigned WS_STAT = 4096;
constexpr unsigned WS_TOT = WS_STAT + 2048 * 16;
constexpr unsigned WS_FLAG = WS_TOT + 16 * 2 * 16 * 16 * 16;
constexpr unsigned WS_H = WS_FLAG + 32 * 8 * 128;
constexpr unsigned H_MT = 88 * 4096;
constexpr unsigned WS_END = WS_H + 32 * H_MT;
constexpr unsigned WS_STAMP = (WS_END + 4095) & ~4095u;
constexpr unsigned SLOT = 40960, SL_W = 16384, SL_B = 32768;
constexpr unsigned L_TRANSIT = 30 * 4096;
constexpr unsigned L_MISC = 38 * 4096;
constexpr unsigned L_STAT = L_MISC, L_CS = L_MISC + 1024, L_SEG = L_MISC + 2048, L_FLG = L_MISC + 4096, L_DUMMY = L_MISC + 4352;
constexpr unsigned LDS_BYTES = 163840;
constexpr int SPIN_CAP = 40000;
constexpr unsigned CODE_BYTES = 26624;

DI void dma16(unsigned voff, const char* sbase, unsigned ldsaddr) {
  asm volatile("s_mov_b32 m0, %2\n\ts_nop 0\n\tglobal_load_lds_dwordx4 %0, %1" ::"v"(voff), "s"(sbase), "s"(ldsaddr));
}
DI void dma4(unsigned voff, const char* sbase, unsigned ldsaddr) {
  asm volatile("s_mov_b32 m0, %2\n\ts_nop 0\n\tglobal_load_lds_dword %0, %1" ::"v"(voff), "s"(sbase), "s"(ldsaddr));
}
template <int N> DI void wait_vm() { asm volatile("s_waitcnt vmcnt(%0)" ::"i"(N) : "memory"); }
DI void wait_lgkm0() { asm volatile("" ::: "memory"); __builtin_amdgcn_s_waitcnt(0xC07F); asm volatile("" ::: "memory"); }
DI void barrier() {
  asm volatile("" ::: "memory");
  __builtin_amdgcn_s_waitcnt(0xC07F);
  __builtin_amdgcn_s_barrier();
  asm volatile("" ::: "memory");
}
#define SCHED() __builtin_amdgcn_sched_barrier(0)
DI s16x8 lds128(unsigned a) { return *(LAS s16x8*)(size_t)a; }
DI f32x4 ldsf4(unsigned a) { return *(LAS f32x4*)(size_t)a; }
DI f32x2 ldsf2(unsigned a) { return *(LAS f32x2*)(size_t)a; }
DI s16x8 ldstr(unsigned a0, unsigned a1) {
  s16x4 lo = __builtin_amdgcn_ds_read_tr16_b64_v4i16((LAS s16x4*)(size_t)a0);
  s16x4 hi = __builtin_amdgcn_ds_read_tr16_b64_v4i16((LAS s16x4*)(size_t)a1);
  return __builtin_shufflevector(lo, hi, 0, 1, 2, 3, 4, 5, 6, 7);
}
DI f32x4 mfma(s16x8 a, s16x8 b, f32x4 c) {
  return __builtin_amdgcn_mfma_f32_16x16x32_bf16(__builtin_bit_cast(bf16x8, a), __builtin_bit_cast(bf16x8, b), c, 0, 0, 0);
}
DI unsigned pk2(float a, float b) { f32x2 v = {a, b}; return __builtin_bit_cast(unsigned, __builtin_convertvector(v, bf16x2)); }
DI float u2f(unsigned u) { return __builtin_bit_cast(float, u); }
DI unsigned f2u(float u) { return __builtin_bit_cast(unsigned, u); }
DI float bflo(unsigned u) { return __builtin_bit_cast(float, u << 16); }
DI float bfhi(unsigned u) { return __builtin_bit_cast(float, u & 0xffff0000u); }
template <int CTRL> DI float dpp_add(float v) {
  return v + __builtin_bit_cast(float, __builtin_amdgcn_update_dpp(0, __builtin_bit_cast(int, v), CTRL, 0xf, 0xf, true));
}
DI float row16_sum(float v) {
  v = dpp_add<0xB1>(v);
  v = dpp_add<0x4E>(v);
  v = dpp_add<0x141>(v);
  v = dpp_add<0x140>(v);
  return v;
}
struct cf { float r, i; };
DI cf cmul(cf a, cf b) { return {a.r * b.r - a.i * b.i, a.r * b.i + a.i * b.r}; }
DI cf cfma(cf a, cf b, cf c) { return {fmaf(a.r, b.r, fmaf(-a.i, b.i, c.r)), fmaf(a.r, b.i, fmaf(a.i, b.r, c.i))}; }
DI cf csel(bool p, cf a, cf b) { return {p ? a.r : b.r, p ? a.i : b.i}; }
DI void lds_flag_set(unsigned a) { *(volatile LAS unsigned*)(size_t)a = 1u; }
DI void lds_flag_wait(unsigned a) {
#pragma unroll 1
  for (int spin = 0; spin < SPIN_CAP * 64; ++spin)
    if (__builtin_amdgcn_readfirstlane(*(volatile LAS unsigned*)(size_t)a) != 0u) break;
  asm volatile("" ::: "memory");
}

#define STAMP(k) do {} while (0)
#define STAMPC(k) do {} while (0)

__global__ void __launch_bounds__(512, 2) fused_kernel(Params p) {
  __shared__ __attribute__((aligned(16))) char lds_mem[LDS_BYTES];
  unsigned long long pc;
  asm volatile("s_getpc_b64 %0" : "=s"(pc));
  const unsigned LB = (unsigned)(size_t)(LAS char*)lds_mem;
  const int tid = threadIdx.x, lane = tid & 63;
  const int w = __builtin_amdgcn_readfirstlane(tid >> 6);
  if (w < 4) {
    const unsigned co = (unsigned)tid * 128u;
    dma4(co < CODE_BYTES ? co : 0u, (const char*)(pc & ~255ull), LB + L_DUMMY + w * 256);
  }
  const int b = blockIdx.x, idx = b >> 3;
  const int r = (b & 7) * 2 + (idx >> 4), j = idx & 15;
  const int mtile = 2 * r + (j >> 3), ntile = j & 7;
  const int i16 = lane & 15, g = lane >> 4;
  const int rh = (w >> 1) & 1, sub = w & 1, q = w & 3;
  const int rot = (j + 4 * (r & 1) + (r >> 1)) & 7;
  const __amdgpu_buffer_rsrc_t wsr = __builtin_amdgcn_make_buffer_rsrc(p.ws, 0, WS_END, 0x00020000);
  const s16x8 ones = {0x3f80, 0x3f80, 0x3f80, 0x3f80, 0x3f80, 0x3f80, 0x3f80, 0x3f80};
  f32x4 acc2[4][4];
  unsigned tagA, tagB;
  const unsigned v2 = (lane >> 3) * 1024 + (((((lane & 7) >> 1) ^ (g & 3)) << 1) | (lane & 1)) * 16;
  const char* w2b = p.w2 + (size_t)(448 * q) * 1024 + ntile * 128;
  auto pageof = [&](int s) -> unsigned { return (unsigned)((s < 2 ? 20 + s : (s < 4 ? s - 2 : 6 + s)) * 4096); };
  const unsigned pq = LB + q * 8192;
  const unsigned T = LB + L_TRANSIT + q * 8192;
  STAMP(0); STAMPC(14);

  if (__builtin_expect(w >= 4, 1)) {
    unsigned vx[4], vw[4], vb[2];
#pragma unroll
    for (int i = 0; i < 4; ++i) {
      const int mt = (2 * q + (i >> 1)) & 3, ii = 8 * (i & 1) + (lane >> 3);
      const int t = 64 * (q >> 1) + 16 * (ii >> 2) + 4 * mt + (ii & 3);
      vx[i] = t * 1024 + (((lane & 7) ^ ((ii >> 1) & 7)) * 16);
      const int row = 16 * q + 4 * i + g, sg = g | (((i >> 1) & 1) << 2);
      vw[i] = row * 3584 + ((((i16 >> 1) ^ sg) << 1) | (i16 & 1)) * 16;
    }
#pragma unroll
    for (int i = 0; i < 2; ++i) vb[i] = ((16 * q + 8 * i + (lane >> 3)) & 31) * 1024 + (((lane & 7) ^ ((4 * i + g) & 7)) * 16);
    const char* xb = p.x + (size_t)r * 131072;
    const char* wb = p.w1 + j * 256;
    const char* bb = (q < 2 ? p.bre : p.bim) + j * 32768;
    const bool mlp = j < 14;
#pragma unroll
    for (int c = 0; c < 2; ++c) {
      const int kc = (c + rot) & 7;
      const unsigned d = LB + c * SLOT + q * 4096;
      if (mlp) {
#pragma unroll
        for (int i = 0; i < 4; ++i) dma16(vw[i], wb + kc * (64 * 3584), d + SL_W + i * 1024);
      }
#pragma unroll
      for (int i = 0; i < 4; ++i) dma16(vx[i], xb + kc * 128, d + i * 1024);
#pragma unroll
      for (int i = 0; i < 2; ++i) dma16(vb[i], bb + kc * 128, LB + c * SLOT + SL_B + q * 2048 + i * 1024);
    }
    __builtin_amdgcn_s_setprio(1);
    const unsigned vc0 = (lane >> 3) * 1024 + (((lane & 7) ^ g) * 16), vc1 = (lane >> 3) * 1024 + (((lane & 7) ^ (4 + g)) * 16);
    auto issueC1 = [&](int hh, int nh, int k) {
      const size_t o = (size_t)(64 * ntile + 32 * nh) * 1024 + q * 256 + hh * 128;
      dma16((k & 1) ? vc1 : vc0, (k < 4 ? p.cre : p.cim) + o + (k & 3) * 8192, T + k * 1024);
    };
    auto issueC = [&](int hh, int nh) {
#pragma unroll
      for (int k = 0; k < 8; ++k) issueC1(hh, nh, k);
    };
    const unsigned crd = T + (g >> 1) * 4096 + i16 * 128;
    auto readC = [&](int ksl, int ntl) { return lds128(crd + ntl * 2048 + (((2 * ksl + (g & 1)) ^ (i16 >> 1)) * 16)); };

    f32x4 acc[4][2], csb[2];
#pragma unroll
    for (int m = 0; m < 4; ++m) { acc[m][0] = (f32x4){0, 0, 0, 0}; acc[m][1] = (f32x4){0, 0, 0, 0}; }
    csb[0] = (f32x4){0, 0, 0, 0}; csb[1] = (f32x4){0, 0, 0, 0};
    s16x8 xa[4], ba[2], xbf[4], bbf[2];
#pragma unroll
    for (int m = 0; m < 4; ++m) xbf[m] = (s16x8){0, 0, 0, 0, 0, 0, 0, 0};
    bbf[0] = (s16x8){0, 0, 0, 0, 0, 0, 0, 0}; bbf[1] = (s16x8){0, 0, 0, 0, 0, 0, 0, 0};
    const unsigned rx = LB + (64 * rh + i16) * 128, rbb = LB + SL_B + (16 * sub + i16) * 128;
    const unsigned k0 = (g ^ (i16 >> 1)) * 16, k1 = ((4 + g) ^ (i16 >> 1)) * 16;
    auto mma2 = [&](const s16x8* xf, const s16x8* bf, int m) {
      if (m < 4) { acc[m][0] = mfma(xf[m], bf[0], acc[m][0]); acc[m][1] = mfma(xf[m], bf[1], acc[m][1]); }
      else { csb[0] = mfma(ones, bf[0], csb[0]); csb[1] = mfma(ones, bf[1], csb[1]); }
    };
    unsigned slot = 0, pslot = 2 * SLOT;
    STAMP(1);
#pragma unroll 1
    for (int c = 0; c < 8; ++c) {
      wait_vm<6>();
      barrier();
      STAMP(32 + c);
      const bool ph1 = c < 6;
      const int kc = (c + 2 + rot) & 7;
      const char* s0 = ph1 ? wb + kc * (64 * 3584) : w2b + (c - 6) * 65536;
      const char* s1 = ph1 ? xb + kc * 128 : w2b + (c - 6) * 65536 + 32768;
      const unsigned d0 = ph1 ? LB + pslot + SL_W + q * 4096 : pq + pslot;
      const unsigned d1 = ph1 ? LB + pslot + q * 4096 : pq + pslot + 4096;
#pragma unroll
      for (int m = 0; m < 4; ++m) xa[m] = lds128(rx + slot + k0 + m * 2048);
      ba[0] = lds128(rbb + slot + k0); ba[1] = lds128(rbb + slot + k0 + 4096);
      SCHED();
      if (mlp | !ph1) {
#pragma unroll
        for (int i = 0; i < 4; ++i) { dma16(ph1 ? vw[i] : v2 + i * 8192, s0, d0 + i * 1024); mma2(xbf, bbf, i); SCHED(); }
      } else {
#pragma unroll
        for (int i = 0; i < 4; ++i) mma2(xbf, bbf, i);
      }
      dma16(ph1 ? vx[0] : v2, s1, d1);
      mma2(xbf, bbf, 4);
      SCHED();
#pragma unroll
      for (int m = 0; m < 4; ++m) xbf[m] = lds128(rx + slot + k1 + m * 2048);
      bbf[0] = lds128(rbb + slot + k1); bbf[1] = lds128(rbb + slot + k1 + 4096);
      SCHED();
#pragma unroll
      for (int i = 1; i < 4; ++i) { dma16(ph1 ? vx[i] : v2 + i * 8192, s1, d1 + i * 1024); mma2(xa, ba, i - 1); SCHED(); }
      if (ph1) {
        dma16(vb[0], bb + kc * 128, LB + pslot + SL_B + q * 2048);
        mma2(xa, ba, 3); SCHED();
        dma16(vb[1], bb + kc * 128, LB + pslot + SL_B + q * 2048 + 1024);
        mma2(xa, ba, 4);
      } else {
        mma2(xa, ba, 3); mma2(xa, ba, 4);
      }
      pslot = slot;
      slot = (slot == 2 * SLOT) ? 0 : slot + SLOT;
    }
    const f32x4 c0 = ldsf4(T + lane * 16), c1 = ldsf4(T + 1024 + lane * 16), c2 = ldsf4(T + 2048 + lane * 16);
    const u32x2 tg = *(LAS u32x2*)(size_t)(T + 3072);
#pragma unroll
    for (int i = 0; i < 5; ++i) mma2(xbf, bbf, i);
    STAMP(2);
    barrier();
    const cf a1 = {c0[0], c0[1]}, kk = {c0[2], c0[3]}, a16 = {c1[0], c1[1]}, a512 = {c1[2], c1[3]}, aseg = {c2[0], c2[1]}, acar = {c2[2], c2[3]};
    tagA = __builtin_amdgcn_readfirstlane(tg.x);
    tagB = __builtin_amdgcn_readfirstlane(tg.y);
    float ur[16], ui[16];
    f32x4 st[8];
#pragma unroll
    for (int s2 = 0; s2 < 8; ++s2) st[s2] = ldsf4(LB + L_STAT + (64 * rh + 16 * g + 2 * s2) * 8);
    cf E = {0.f, 0.f};
#pragma unroll
    for (int s = 0; s < 16; ++s) {
      if (s < 8) dma16(v2 + (s & 3) * 8192, w2b + (4 + (s >> 2)) * 32768, pq + pageof(4 + (s >> 2)) + (s & 3) * 1024);
      else issueC1(0, 0, s - 8);
      ur[s] = fmaf(acc[s >> 2][0][s & 3], st[s >> 1][2 * (s & 1)], -st[s >> 1][2 * (s & 1) + 1] * csb[0][0]);
      ui[s] = fmaf(acc[s >> 2][1][s & 3], st[s >> 1][2 * (s & 1)], -st[s >> 1][2 * (s & 1) + 1] * csb[1][0]);
      E = cfma(a1, E, cf{ur[s], ui[s]});
      SCHED();
    }
    *(LAS f32x2*)(size_t)(LB + L_SEG + sub * 1024 + (4 * rh + g) * 128 + i16 * 8) = (f32x2){E.r, E.i};
    wait_lgkm0();
    if (rh == 0) lds_flag_set(LB + L_FLG + sub * 4);
    else lds_flag_wait(LB + L_FLG + sub * 4);
    cf P = {0.f, 0.f}, Tt = {0.f, 0.f};
    {
      f32x2 e[8];
#pragma unroll
      for (int s = 0; s < 8; ++s) e[s] = ldsf2(LB + L_SEG + sub * 1024 + s * 128 + i16 * 8);
#pragma unroll
      for (int s = 0; s < 8; ++s) {
        P = csel(s == 4 * rh + g, Tt, P);
        Tt = cfma(a16, Tt, cf{e[s][0], e[s][1]});
      }
    }
    const unsigned totb = WS_TOT + (unsigned)(j * 2 + sub) * 4096;
    if (rh && g == 0) {
      const u32x4 v = {tagA, f2u(Tt.r), tagB, f2u(Tt.i)};
      __builtin_amdgcn_raw_buffer_store_b128(v, wsr, totb + r * 256 + i16 * 16, 0, 16);
    }
    STAMP(3);
    s16x8 cf0[4][2], cf1[4][2], cf2[4][2];
    wait_vm<0>();
    lds_flag_set(LB + L_FLG + 8 + q * 4);
#pragma unroll
    for (int k = 0; k < 4; ++k) { cf0[k][0] = readC(k, 0); cf0[k][1] = readC(k, 1); }
    wait_lgkm0();
    issueC(0, 1);
    wait_vm<0>();
#pragma unroll
    for (int k = 0; k < 4; ++k) { cf1[k][0] = readC(k, 0); cf1[k][1] = readC(k, 1); }
    wait_lgkm0();
    issueC(1, 0);
    cf car = {0.f, 0.f};
    if (r > 0) {
      u32x4 tv[4];
#pragma unroll
      for (int i = 0; i < 4; ++i) tv[i] = (u32x4){0, 0, 0, 0};
#pragma unroll 1
      for (int spin = 0; spin < SPIN_CAP; ++spin) {
        bool ok = true;
#pragma unroll
        for (int i = 0; i < 4; ++i)
          if (4 * i < r) tv[i] = __builtin_amdgcn_raw_buffer_load_b128(wsr, (4 * i + g < r) ? totb + i * 1024 + lane * 16 : 0xfffffff0u, 0, 16);
#pragma unroll
        for (int i = 0; i < 4; ++i) ok &= (4 * i + g >= r) | ((tv[i].x == tagA) & (tv[i].z == tagB));
        if (__all(ok)) break;
        __builtin_amdgcn_s_sleep(6);
      }
#pragma unroll
      for (int i = 0; i < 4; ++i) {
        const cf n = cfma(a512, car, cf{u2f(tv[i].y), u2f(tv[i].w)});
        car = csel(4 * i + g < r, n, car);
      }
      car = cmul(car, acar);
      car.r += __shfl_xor(car.r, 16); car.i += __shfl_xor(car.i, 16);
      car.r += __shfl_xor(car.r, 32); car.i += __shfl_xor(car.i, 32);
    }
    STAMP(4);
    wait_vm<0>();
#pragma unroll
    for (int k = 0; k < 4; ++k) { cf2[k][0] = readC(k, 0); cf2[k][1] = readC(k, 1); }
    wait_lgkm0();
    {
      cf h = cfma(aseg, car, P);
      const unsigned tp = LB + (q < 2 ? 28 + q : 6 + q) * 4096;
      const unsigned wa = tp + i16 * 32 + ((g ^ (i16 >> 2)) * 8);
#pragma unroll
      for (int m = 0; m < 4; ++m) {
        float o_r[4], o_i[4];
#pragma unroll
        for (int e = 0; e < 4; ++e) {
          if (!(e & 1)) issueC1(1, 1, 2 * m + (e >> 1));
          h = cfma(a1, h, cf{ur[4 * m + e], ui[4 * m + e]});
          o_r[e] = kk.r * h.r - kk.i * h.i;
          o_i[e] = -(kk.r * h.i + kk.i * h.r);
          if (e & 1) SCHED();
        }
        *(LAS u32x2*)(size_t)(wa + m * 1024) = (u32x2){pk2(o_r[0], o_r[1]), pk2(o_r[2], o_r[3])};
        *(LAS u32x2*)(size_t)(wa + m * 1024 + 512) = (u32x2){pk2(o_i[0], o_i[1]), pk2(o_i[2], o_i[3])};
      }
      wait_lgkm0();
      const int qq = i16 >> 2, pp = i16 & 3;
      const unsigned ra0 = tp + (8 * g + qq) * 32 + ((pp ^ ((2 * g) & 3)) * 8), ra1 = tp + (8 * g + 4 + qq) * 32 + ((pp ^ ((2 * g + 1) & 3)) * 8);
      const unsigned ho = WS_H + (unsigned)(2 * r + rh) * H_MT + (unsigned)(56 + 2 * j + sub) * 4096 + lane * 16;
#pragma unroll
      for (int m = 0; m < 4; ++m) {
        const s16x8 f = ldstr(ra0 + m * 1024, ra1 + m * 1024);
        __builtin_amdgcn_raw_buffer_store_b128(__builtin_bit_cast(u32x4, f), wsr, ho + m * 1024, 0, 16);
      }
      wait_vm<0>();
      const int ks = 2 * j + sub;
      if (lane == 0) __builtin_amdgcn_raw_buffer_store_b64((u32x2){tagA, tagB}, wsr, WS_FLAG + ((2 * r + rh) * 8 + 4 + (ks >> 3)) * 128 + (ks & 7) * 8, 0, 16);
    }
    STAMP(5);
#pragma unroll 1
    for (int spin = 0; spin < SPIN_CAP; ++spin) {
      const u32x2 f = __builtin_amdgcn_raw_buffer_load_b64(wsr, WS_FLAG + (mtile * 8 + 4 + q) * 128 + (lane & 7) * 8, 0, 16);
      if (__all((f.x == tagA) & (f.y == tagB))) break;
      __builtin_amdgcn_s_sleep(2);
    }
    asm volatile("" ::: "memory");
    STAMP(6);
    const unsigned hb = WS_H + (unsigned)mtile * H_MT + (unsigned)(56 + 8 * q) * 4096 + lane * 16;
    u32x4 hf[4][4];
#pragma unroll
    for (int i = 0; i < 4; ++i)
#pragma unroll
      for (int m = 0; m < 4; ++m) hf[i][m] = __builtin_amdgcn_raw_buffer_load_b128(wsr, hb + m * 1024, i * 4096, 16);
#pragma unroll
    for (int n = 0; n < 4; ++n)
#pragma unroll
      for (int m = 0; m < 4; ++m) acc2[n][m] = (f32x4){0, 0, 0, 0};
    SCHED();
    s16x8 cf3[4][2];
#pragma unroll
    for (int i = 0; i < 8; ++i) {
      if (i == 3) {
#pragma unroll
        for (int k = 0; k < 4; ++k) { cf3[k][0] = readC(k, 0); cf3[k][1] = readC(k, 1); }
      }
#pragma unroll
      for (int m = 0; m < 4; ++m) {
#pragma unroll
        for (int n = 0; n < 4; ++n) {
          const s16x8 cfr = i < 4 ? (n < 2 ? cf0[i & 3][n & 1] : cf1[i & 3][n & 1]) : (n < 2 ? cf2[i & 3][n & 1] : cf3[i & 3][n & 1]);
          acc2[n][m] = mfma(cfr, __builtin_bit_cast(s16x8, hf[i & 3][m]), acc2[n][m]);
        }
        SCHED();
        if (i + 4 < 8) hf[i & 3][m] = __builtin_amdgcn_raw_buffer_load_b128(wsr, hb + m * 1024, (i + 4) * 4096, 16);
        SCHED();
      }
    }
    STAMP(7);
  } else {
    const unsigned po = (unsigned)(32 * j + 16 * sub + i16) * 2u;
    const unsigned v_lre = *(const GAS unsigned short*)(p.lre + po), v_lim = *(const GAS unsigned short*)(p.lim + po),
                   v_ldt = *(const GAS unsigned short*)(p.logdt + po);
    const u32x2 ep = __builtin_amdgcn_raw_buffer_load_b64(wsr, WS_EPOCH, 0, 16);
    u32x2 b1v[4];
#pragma unroll
    for (int s = 0; s < 4; ++s) b1v[s] = *(const GAS u32x2*)(p.b1 + ((j < 14 ? j : 13) * 128 + 64 * sub + 16 * (s ^ (2 * rh)) + 4 * g) * 2);
    if (tid < 8) *(LAS unsigned*)(size_t)(LB + L_FLG + tid * 4) = 0u;
    tagA = __builtin_amdgcn_readfirstlane(ep.x) + 1u;
    tagB = __builtin_amdgcn_readfirstlane(ep.y) + 1u;
    {
      const float lr = bflo(v_lre), li = bflo(v_lim);
      const float dt = bflo(pk2(expf(bflo(v_ldt)), 0.f));
      float sn, cs;
      sincosf(dt * li, &sn, &cs);
      const float e = expf(dt * lr);
      const cf a1 = {e * cs, e * sn};
      const float nr = a1.r - 1.f, ni = a1.i, den = lr * lr + li * li;
      const cf kk = {2.f * (nr * lr + ni * li) / den, 2.f * (ni * lr - nr * li) / den};
      cf t = cmul(a1, a1); t = cmul(t, t); t = cmul(t, t);
      const cf a16 = cmul(t, t);
      const cf a32 = cmul(a16, a16), a64 = cmul(a32, a32), a128 = cmul(a64, a64), a256 = cmul(a128, a128);
      const cf a512 = cmul(a256, a256);
      const cf one = {1.f, 0.f};
      const cf aseg = cmul(cmul(csel(g & 1, a16, one), csel(g & 2, a32, one)), csel(rh, a64, one));
      const int e4 = (r - 1 - g) & 3;
      const cf acar = cmul(csel(e4 & 1, a128, one), csel(e4 & 2, a256, one));
      *(LAS f32x4*)(size_t)(T + lane * 16) = (f32x4){a1.r, a1.i, kk.r, kk.i};
      *(LAS f32x4*)(size_t)(T + 1024 + lane * 16) = (f32x4){a16.r, a16.i, a512.r, a512.i};
      *(LAS f32x4*)(size_t)(T + 2048 + lane * 16) = (f32x4){aseg.r, aseg.i, acar.r, acar.i};
      *(LAS u32x2*)(size_t)(T + 3072) = (u32x2){tagA, tagB};
    }
    asm volatile("" : "+v"(b1v[0]), "+v"(b1v[1]), "+v"(b1v[2]), "+v"(b1v[3]));
    {
      if (w == 0) { if (lane < 56) dma4(lane * 128, p.w2 + b * 7168, LB + L_DUMMY); }
      else if (w == 1) { if (lane < 16) dma4(lane * 128, p.cre + b * 2048, LB + L_DUMMY + 256); }
      else if (w == 2) { if (lane < 16) dma4(lane * 128, p.cim + b * 2048, LB + L_DUMMY + 512); }
      else { if (lane < 1) { dma4(0, p.dsk + ntile * 128, LB + L_DUMMY + 768); dma4(0, p.b2 + ntile * 128, LB + L_DUMMY + 768); } }
    }
    f32x4 acc[4][4], cs[2];
#pragma unroll
    for (int s = 0; s < 4; ++s)
#pragma unroll
      for (int m = 0; m < 4; ++m) acc[s][m] = (f32x4){0, 0, 0, 0};
    cs[0] = (f32x4){0, 0, 0, 0}; cs[1] = (f32x4){0, 0, 0, 0};
    const int qq = i16 >> 2, pp = i16 & 3;
    STAMP(1);
    if (j < 14) {
      s16x8 xa[4], wa[4], xbf[4], wbf[4];
      const unsigned rx = LB + (64 * rh + i16) * 128;
      const unsigned k0 = (g ^ (i16 >> 1)) * 16, k1 = ((4 + g) ^ (i16 >> 1)) * 16;
      unsigned rw[4];
#pragma unroll
      for (int s = 0; s < 4; ++s) rw[s] = LB + SL_W + (8 * g + qq) * 256 + (((4 * sub + (s ^ (2 * rh))) ^ (qq | ((g & 1) << 2))) * 32) + pp * 8;
      auto mma = [&](const s16x8* xf, const s16x8* wf) {
#pragma unroll
        for (int s = 0; s < 4; ++s)
#pragma unroll
          for (int m = 0; m < 4; ++m) acc[s][m] = mfma(wf[s], xf[m], acc[s][m]);
        cs[0] = mfma(wf[0], ones, cs[0]); cs[1] = mfma(wf[1], ones, cs[1]);
      };
      unsigned slot = 0;
#pragma unroll 1
      for (int c = 0; c < 8; ++c) {
        barrier();
#pragma unroll
        for (int m = 0; m < 4; ++m) xa[m] = lds128(rx + slot + k0 + m * 2048);
#pragma unroll
        for (int s = 0; s < 4; ++s) wa[s] = ldstr(rw[s] + slot, rw[s] + slot + 1024);
        if (c) mma(xbf, wbf);
        SCHED();
#pragma unroll
        for (int m = 0; m < 4; ++m) xbf[m] = lds128(rx + slot + k1 + m * 2048);
#pragma unroll
        for (int s = 0; s < 4; ++s) wbf[s] = ldstr(rw[s] + slot + 8192, rw[s] + slot + 8192 + 1024);
        SCHED();
        mma(xa, wa);
        slot = (slot == 2 * SLOT) ? 0 : slot + SLOT;
      }
      SCHED();
      mma(xbf, wbf);
      SCHED();
    } else {
      const int row0 = 128 * r + 64 * (j & 1) + 16 * w + g;
      u32x4 xr[4][4];
#pragma unroll
      for (int R = 0; R < 4; ++R)
#pragma unroll
        for (int i = 0; i < 4; ++i) xr[R][i] = *(const GAS u32x4*)(p.x + (size_t)(row0 + 4 * R) * 1024 + i * 256 + i16 * 16);
#pragma unroll
      for (int R = 0; R < 4; ++R) {
        float s = 0.f;
#pragma unroll
        for (int i = 0; i < 4; ++i)
#pragma unroll
          for (int e = 0; e < 4; ++e) s += bflo(xr[R][i][e]) + bfhi(xr[R][i][e]);
        const float mean = row16_sum(s) * (1.f / 512.f);
        float v = 0.f;
#pragma unroll
        for (int i = 0; i < 4; ++i)
#pragma unroll
          for (int e = 0; e < 4; ++e) {
            const float d0 = bflo(xr[R][i][e]) - mean, d1 = bfhi(xr[R][i][e]) - mean;
            v = fmaf(d0, d0, v); v = fmaf(d1, d1, v);
          }
        const float rs = __builtin_amdgcn_rsqf(row16_sum(v) * (1.f / 512.f) + 1e-5f);
        if (i16 == 0) __builtin_amdgcn_raw_buffer_store_b128((u32x4){tagA, f2u(rs), tagB, f2u(mean * rs)}, wsr, WS_STAT + (row0 + 4 * R) * 16, 0, 16);
        barrier();
        barrier();
      }
    }
    STAMP(2);
    if (sub == 0) {
      u32x4 sv;
#pragma unroll 1
      for (int spin = 0; spin < SPIN_CAP; ++spin) {
        sv = __builtin_amdgcn_raw_buffer_load_b128(wsr, WS_STAT + (128 * r + 64 * rh + lane) * 16, 0, 16);
        if (__all((sv.x == tagA) & (sv.z == tagB))) break;
        __builtin_amdgcn_s_sleep(2);
      }
      *(LAS u32x2*)(size_t)(LB + L_STAT + (64 * rh + lane) * 8) = (u32x2){sv.y, sv.w};
    }
    if (i16 == 0) {
      *(LAS f32x4*)(size_t)(LB + L_CS + (64 * sub + 32 * rh + 4 * g) * 4) = cs[0];
      *(LAS f32x4*)(size_t)(LB + L_CS + (64 * sub + 32 * rh + 16 + 4 * g) * 4) = cs[1];
    }
    barrier();
    STAMP(9);
    if (j < 14) {
      f32x2 st[4];
#pragma unroll
      for (int m = 0; m < 4; ++m) st[m] = ldsf2(LB + L_STAT + (64 * rh + 16 * (i16 >> 2) + 4 * m + (i16 & 3)) * 8);
      f32x4 csa[4];
      csa[0] = cs[0]; csa[1] = cs[1];
      csa[2] = ldsf4(LB + L_CS + (64 * sub + 32 * (rh ^ 1) + 4 * g) * 4);
      csa[3] = ldsf4(LB + L_CS + (64 * sub + 32 * (rh ^ 1) + 16 + 4 * g) * 4);
      const unsigned ho = WS_H + (unsigned)(2 * r + rh) * H_MT + (unsigned)(4 * j + 2 * sub) * 4096 + lane * 16;
#pragma unroll
      for (int pr = 0; pr < 2; ++pr) {
#pragma unroll
        for (int m = 0; m < 4; ++m) {
          u32x4 o;
#pragma unroll
          for (int h = 0; h < 2; ++h) {
            const int s = 2 * pr + h;
            float y[4];
#pragma unroll
            for (int e = 0; e < 4; ++e) {
              const float bias = (e & 1) ? bfhi(b1v[s][e >> 1]) : bflo(b1v[s][e >> 1]);
              const float v = fmaf(acc[s][m][e], st[m][0], fmaf(-st[m][1], csa[s][e], bias));
              const float z = v * fmaf(v * v, -0.10294324f, -2.30220820f);
              y[e] = v * __builtin_amdgcn_rcpf(1.f + __builtin_amdgcn_exp2f(z));
            }
            o[2 * h] = pk2(y[0], y[1]); o[2 * h + 1] = pk2(y[2], y[3]);
          }
          __builtin_amdgcn_raw_buffer_store_b128(o, wsr, ho + (pr ^ rh) * 4096 + m * 1024, 0, 16);
        }
      }
    }
    STAMP(3);
    unsigned r2[4];
#pragma unroll
    for (int n = 0; n < 4; ++n) r2[n] = pq + (4 * g + qq) * 128 + ((n ^ ((2 * g + (qq >> 1)) & 3)) * 32) + pp * 8;
    auto readW = [&](int s, int n) { return ldstr(r2[n] + pageof(s), r2[n] + pageof(s) + 2048); };
    auto issueW1 = [&](int i, int s, int ii) { dma16(v2, w2b + (i * 32 + ii * 8) * 1024, pq + pageof(s) + ii * 1024); };
    lds_flag_wait(LB + L_FLG + 8 + q * 4);
    s16x8 we[4][4];
#pragma unroll
    for (int i = 0; i < 4; ++i)
#pragma unroll
      for (int n = 0; n < 4; ++n) we[i][n] = readW(i, n);
    wait_vm<0>();
    if (j < 14) {
      const int kp = 2 * j + sub;
      if (lane == 0) __builtin_amdgcn_raw_buffer_store_b64((u32x2){tagA, tagB}, wsr, WS_FLAG + ((2 * r + rh) * 8 + kp / 7) * 128 + (kp % 7) * 8, 0, 16);
    }
    STAMP(4);
    wait_lgkm0();
#pragma unroll
    for (int i = 0; i < 4; ++i)
#pragma unroll
      for (int ii = 0; ii < 4; ++ii) issueW1(6 + i, i, ii);
    STAMP(5);
#pragma unroll 1
    for (int spin = 0; spin < SPIN_CAP; ++spin) {
      const u32x2 f = __builtin_amdgcn_raw_buffer_load_b64(wsr, WS_FLAG + (mtile * 8 + q) * 128 + (lane < 7 ? lane : 0) * 8, 0, 16);
      if (__all((f.x == tagA) & (f.y == tagB))) break;
      __builtin_amdgcn_s_sleep(2);
    }
    asm volatile("" ::: "memory");
    STAMP(6);
    const unsigned hb = WS_H + (unsigned)mtile * H_MT + (unsigned)(14 * q) * 4096 + lane * 16;
    u32x4 hf[4][4];
#pragma unroll
    for (int i = 0; i < 4; ++i)
#pragma unroll
      for (int m = 0; m < 4; ++m) hf[i][m] = __builtin_amdgcn_raw_buffer_load_b128(wsr, hb + m * 1024, i * 4096, 16);
#pragma unroll
    for (int n = 0; n < 4; ++n)
#pragma unroll
      for (int m = 0; m < 4; ++m) acc2[n][m] = (f32x4){0, 0, 0, 0};
    SCHED();
    s16x8 wl[2][4];
#pragma unroll
    for (int i = 0; i < 14; ++i) {
      if (i + 1 >= 4 && i + 1 < 14) {
        asm volatile("" ::: "memory");
#pragma unroll
        for (int n = 0; n < 4; ++n) wl[(i + 1) & 1][n] = readW((i + 1) % 6, n);
      }
      SCHED();
#pragma unroll
      for (int m = 0; m < 4; ++m) {
#pragma unroll
        for (int n = 0; n < 4; ++n) {
          const s16x8 wf = i < 4 ? we[i & 3][n] : wl[i & 1][n];
          acc2[n][m] = mfma(wf, __builtin_bit_cast(s16x8, hf[i & 3][m]), acc2[n][m]);
        }
        SCHED();
        if (i >= 4 && i < 8) issueW1(i + 6, i % 6, m);
        if (i + 4 < 14) hf[i & 3][m] = __builtin_amdgcn_raw_buffer_load_b128(wsr, hb + m * 1024, (i + 4) * 4096, 16);
        SCHED();
      }
    }
    STAMP(7);
  }

  const int ck = tid & 15;
  u32x2 xv[2];
  int trow[2];
#pragma unroll
  for (int h = 0; h < 2; ++h) {
    const int rho = (tid >> 4) + 32 * h, ii = rho & 15;
    trow[h] = 16 * (ii >> 2) + 4 * (rho >> 4) + (ii & 3);
    xv[h] = *(const GAS u32x2*)(p.x + (size_t)(64 * mtile + trow[h]) * 1024 + ntile * 128 + ck * 8);
  }
  const u32x2 dv = *(const GAS u32x2*)(p.dsk + ntile * 128 + ck * 8), bv = *(const GAS u32x2*)(p.b2 + ntile * 128 + ck * 8);
  barrier();
  STAMP(10);
#pragma unroll
  for (int n = 0; n < 4; ++n)
#pragma unroll
    for (int m = 0; m < 4; ++m) *(LAS f32x4*)(size_t)(LB + w * 16384 + (16 * m + i16) * 256 + (((4 * n + g) ^ i16) * 16)) = acc2[n][m];
  barrier();
  STAMP(11);
#pragma unroll
  for (int h = 0; h < 2; ++h) {
    const int rho = (tid >> 4) + 32 * h;
    const unsigned a = LB + rho * 256 + ((ck ^ (rho & 15)) * 16);
    f32x4 s = ldsf4(a);
#pragma unroll
    for (int k = 1; k < 8; ++k) s += ldsf4(a + k * 16384);
    const f32x2 st = ldsf2(LB + L_STAT + (64 * (j >> 3) + trow[h]) * 8);
    f32x4 o;
#pragma unroll
    for (int e = 0; e < 4; ++e) {
      const float xx = (e & 1) ? bfhi(xv[h][e >> 1]) : bflo(xv[h][e >> 1]);
      const float dd = (e & 1) ? bfhi(dv[e >> 1]) : bflo(dv[e >> 1]);
      const float bb = (e & 1) ? bfhi(bv[e >> 1]) : bflo(bv[e >> 1]);
      o[e] = s[e] + xx + bb + fmaf(xx, st[0], -st[1]) * dd;
    }
    *(GAS f32x4*)(p.out + (size_t)(64 * mtile + trow[h]) * 2048 + ntile * 256 + ck * 16) = o;
  }
  if (r == 15 && j == 0 && tid == 0) __builtin_amdgcn_raw_buffer_store_b64((u32x2){tagA, tagB}, wsr, WS_EPOCH, 0, 16);
  STAMP(8); STAMPC(15);
}

extern "C" void kernel_launch(void* const* d_in, const int* in_sizes, int n_in, void* d_out, int out_size, void* d_ws,
                              size_t ws_size, hipStream_t stream) {
  Params p;
  p.x = (const char*)d_in[0]; p.lre = (const char*)d_in[1]; p.lim = (const char*)d_in[2];
  p.bre = (const char*)d_in[3]; p.bim = (const char*)d_in[4]; p.cre = (const char*)d_in[5]; p.cim = (const char*)d_in[6];
  p.logdt = (const char*)d_in[7]; p.dsk = (const char*)d_in[8];
  p.w1 = (const char*)d_in[10]; p.b1 = (const char*)d_in[11]; p.w2 = (const char*)d_in[12]; p.b2 = (const char*)d_in[13];
  p.out = (char*)d_out; p.ws = (char*)d_ws;
  fused_kernel<<<256, 512, 0, stream>>>(p);
}
```

```cpp
#include <hip/hip_runtime.h>
#include <stdint.h>

typedef short s16x4 __attribute__((ext_vector_type(4)));
typedef short s16x8 __attribute__((ext_vector_type(8)));
typedef __bf16 bf16x8 __attribute__((ext_vector_type(8)));
typedef __bf16 bf16x2 __attribute__((ext_vector_type(2)));
typedef float f32x4 __attribute__((ext_vector_type(4)));
typedef float f32x2 __attribute__((ext_vector_type(2)));
typedef unsigned u32x4 __attribute__((ext_vector_type(4)));
typedef unsigned u32x2 __attribute__((ext_vector_type(2)));
#define LAS __attribute__((address_space(3)))
#define GAS __attribute__((address_space(1)))
#define DI __device__ __forceinline__

#define STAMPS 0
#define TRACING 0
#define GATHER_AT 5
#define LB_SLEEP 16
#define CHECK_AT 6
#define MAP 1

struct Params {
  const char *x, *lre, *lim, *bre, *bim, *cre, *cim, *logdt, *dsk, *w1, *b1, *w2, *b2;
  char *out, *ws;
};

constexpr unsigned WS_EPOCH = 0;
constexpr unsigned WS_STAT = 4096;
constexpr unsigned WS_TOT = WS_STAT + 2048 * 16;
constexpr unsigned WS_CS = WS_TOT + 16 * 2 * 16 * 256;
constexpr unsigned WS_FLAG = WS_CS + 16 * 2 * 16 * 1024;
constexpr unsigned WS_H = WS_FLAG + 32 * 1024;
constexpr unsigned H_MT = 88 * 4096;
constexpr unsigned WS_END = WS_H + 32 * H_MT;
constexpr unsigned WS_STAMP = 32u << 20;
constexpr unsigned SLOT = 40960, HALF = 20480, SL_W = 16384, SL_B = 32768;
constexpr unsigned L_ST1 = 3 * SLOT + 16384, L_ST2 = L_ST1 + 2048;
constexpr unsigned L_CS = 3 * SLOT + HALF + 16384, L_FLG = L_CS + 768, L_DUMMY = L_CS + 1024;
constexpr unsigned LDS_BYTES = 163840;
constexpr int SPIN_CAP = 40000;
constexpr unsigned CODE_BYTES = 37120;

DI void dma4w(unsigned voff, const char* sbase, unsigned ldsaddr) {
  asm volatile("s_mov_b32 m0, %2\n\ts_nop 0\n\tglobal_load_lds_dword %0, %1" ::"v"(voff), "s"(sbase), "s"(ldsaddr));
}
DI void dma_x4(const unsigned* v, const char* sbase, unsigned ldsaddr) {
  asm volatile("s_mov_b32 m0, %5\n\ts_nop 0\n\t"
               "global_load_lds_dwordx4 %0, %4\n\tglobal_load_lds_dwordx4 %1, %4 offset:1024\n\t"
               "global_load_lds_dwordx4 %2, %4 offset:2048\n\tglobal_load_lds_dwordx4 %3, %4 offset:3072"
               ::"v"(v[0]), "v"(v[1]), "v"(v[2]), "v"(v[3]), "s"(sbase), "s"(ldsaddr));
}
DI void dma_x8(const unsigned* v, const char* sbase, unsigned ldsaddr) {
  asm volatile("s_mov_b32 m0, %9\n\ts_nop 0\n\t"
               "global_load_lds_dwordx4 %0, %8 offset:-4096\n\tglobal_load_lds_dwordx4 %1, %8 offset:-3072\n\t"
               "global_load_lds_dwordx4 %2, %8 offset:-2048\n\tglobal_load_lds_dwordx4 %3, %8 offset:-1024\n\t"
               "global_load_lds_dwordx4 %4, %8\n\tglobal_load_lds_dwordx4 %5, %8 offset:1024\n\t"
               "global_load_lds_dwordx4 %6, %8 offset:2048\n\tglobal_load_lds_dwordx4 %7, %8 offset:3072"
               ::"v"(v[0]), "v"(v[1]), "v"(v[2]), "v"(v[3]), "v"(v[4]), "v"(v[5]), "v"(v[6]), "v"(v[7]), "s"(sbase), "s"(ldsaddr + 4096u));
}
template <int N> DI void wait_vm() { asm volatile("s_waitcnt vmcnt(%0)" ::"i"(N) : "memory"); }
DI void wait_lgkm0() { asm volatile("" ::: "memory"); __builtin_amdgcn_s_waitcnt(0xC07F); asm volatile("" ::: "memory"); }
DI void barrier() {
  asm volatile("" ::: "memory");
  __builtin_amdgcn_s_waitcnt(0xC07F);
  __builtin_amdgcn_s_barrier();
  asm volatile("" ::: "memory");
}
#define SCHED() __builtin_amdgcn_sched_barrier(0)
DI s16x8 lds128(unsigned a) { return *(LAS s16x8*)(size_t)a; }
DI f32x4 ldsf4(unsigned a) { return *(LAS f32x4*)(size_t)a; }
DI f32x2 ldsf2(unsigned a) { return *(LAS f32x2*)(size_t)a; }
DI float ldsf1(unsigned a) { return *(LAS float*)(size_t)a; }
DI s16x4 ldstr4(unsigned a) { return __builtin_amdgcn_ds_read_tr16_b64_v4i16((LAS s16x4*)(size_t)a); }
DI s16x8 cat(s16x4 lo, s16x4 hi) { return __builtin_shufflevector(lo, hi, 0, 1, 2, 3, 4, 5, 6, 7); }
DI s16x8 ldstr(unsigned a0, unsigned a1) { return cat(ldstr4(a0), ldstr4(a1)); }
DI f32x4 mfma(s16x8 a, s16x8 b, f32x4 c) {
  return __builtin_amdgcn_mfma_f32_16x16x32_bf16(__builtin_bit_cast(bf16x8, a), __builtin_bit_cast(bf16x8, b), c, 0, 0, 0);
}
DI unsigned pk2(float a, float b) { f32x2 v = {a, b}; return __builtin_bit_cast(unsigned, __builtin_convertvector(v, bf16x2)); }
DI float u2f(unsigned u) { return __builtin_bit_cast(float, u); }
DI unsigned f2u(float u) { return __builtin_bit_cast(unsigned, u); }
DI float bflo(unsigned u) { return __builtin_bit_cast(float, u << 16); }
DI float bfhi(unsigned u) { return __builtin_bit_cast(float, u & 0xffff0000u); }
template <int CTRL> DI float dpp_add(float v) {
  return v + __builtin_bit_cast(float, __builtin_amdgcn_update_dpp(0, __builtin_bit_cast(int, v), CTRL, 0xf, 0xf, true));
}
DI float row16_sum(float v) {
  v = dpp_add<0xB1>(v);
  v = dpp_add<0x4E>(v);
  v = dpp_add<0x141>(v);
  v = dpp_add<0x140>(v);
  return v;
}
DI float smul(float a, float b) { float d; asm("v_mul_f32 %0, %1, %2" : "=v"(d) : "v"(a), "v"(b)); return d; }
DI float sfma(float a, float b, float c) { float d; asm("v_fma_f32 %0, %1, %2, %3" : "=v"(d) : "v"(a), "v"(b), "v"(c)); return d; }
DI float sfms(float a, float b, float c) { float d; asm("v_fma_f32 %0, %1, %2, -%3" : "=v"(d) : "v"(a), "v"(b), "v"(c)); return d; }
DI f32x2 pfma(f32x2 a, f32x2 b, f32x2 c) { return __builtin_elementwise_fma(a, b, c); }
DI f32x2 bc(float a) { return (f32x2){a, a}; }
DI f32x2 bco(float a) { f32x2 v = {a, a}; asm("" : "+v"(v)); return v; }
DI f32x2 cmul(f32x2 a, f32x2 b) { return (f32x2){sfms(a[0], b[0], smul(a[1], b[1])), sfma(a[0], b[1], smul(a[1], b[0]))}; }
DI f32x2 cfma(f32x2 a, f32x2 b, f32x2 c) { return (f32x2){sfms(a[0], b[0], sfms(a[1], b[1], c[0])), sfma(a[0], b[1], sfma(a[1], b[0], c[1]))}; }
DI f32x2 csel(bool p, f32x2 a, f32x2 b) { return (f32x2){p ? a[0] : b[0], p ? a[1] : b[1]}; }
DI void prog_set(unsigned a, unsigned v) {
  asm volatile("s_mov_b64 exec, 1\n\tds_write_b32 %0, %1\n\ts_mov_b64 exec, -1" ::"v"(a), "v"(v) : "memory");
}
DI void prog_wait(unsigned a, unsigned v) {
#pragma unroll 1
  for (int spin = 0; spin < SPIN_CAP * 16; ++spin) {
    if (__builtin_amdgcn_readfirstlane(*(volatile LAS unsigned*)(size_t)a) >= v) break;
    __builtin_amdgcn_s_sleep(1);
  }
  asm volatile("" ::: "memory");
}
DI void bar() { asm volatile("" ::: "memory"); __builtin_amdgcn_s_barrier(); asm volatile("" ::: "memory"); }
DI void dma1_dev(unsigned voff, const char* sbase, unsigned ldsaddr) {
  asm volatile("s_mov_b32 m0, %2\n\ts_nop 0\n\tglobal_load_lds_dwordx4 %0, %1 sc1" ::"v"(voff), "s"(sbase), "s"(ldsaddr) : "memory");
}
DI int kstep_of(int kg, int i) {
  const int q = kg + 8 * i, cls = (q >= 14) + (q >= 28) + (q >= 42);
  return i < 7 ? 4 * (q - 14 * cls) + ((0x9C >> (2 * cls)) & 3) : 49 + 4 * kg + i;
}
DI f32x2 lo2(f32x4 v) { return __builtin_shufflevector(v, v, 0, 1); }
DI f32x2 hi2(f32x4 v) { return __builtin_shufflevector(v, v, 2, 3); }
DI void rows4(float v, float* o) {
  const unsigned u = f2u(v);
  const u32x2 h = __builtin_amdgcn_permlane32_swap(u, u, false, false);
  const u32x2 a = __builtin_amdgcn_permlane16_swap(h.x, h.x, false, false), b = __builtin_amdgcn_permlane16_swap(h.y, h.y, false, false);
  o[0] = u2f(a.x); o[1] = u2f(a.y); o[2] = u2f(b.x); o[3] = u2f(b.y);
}
struct Col { f32x2 cs[4], bias[4]; };
DI void mkcol(Col& c, f32x4 cs0, f32x4 cs1, u32x2 b0, u32x2 b1) {
  c.cs[0] = lo2(cs0); c.cs[1] = hi2(cs0); c.cs[2] = lo2(cs1); c.cs[3] = hi2(cs1);
  c.bias[0] = (f32x2){bflo(b0.x), bfhi(b0.x)}; c.bias[1] = (f32x2){bflo(b0.y), bfhi(b0.y)};
  c.bias[2] = (f32x2){bflo(b1.x), bfhi(b1.x)}; c.bias[3] = (f32x2){bflo(b1.y), bfhi(b1.y)};
}
DI u32x4 gelu8(f32x4 a0, f32x4 a1, const Col& c, f32x2 rs, f32x2 nmrs) {
  f32x2 v[4], z[4];
  u32x4 o;
#define ST4(stmt) _Pragma("unroll") for (int h = 0; h < 4; ++h) { stmt; } SCHED();
  ST4(v[h] = pfma(c.cs[h], nmrs, c.bias[h]))
  ST4(v[h] = pfma(h == 0 ? lo2(a0) : h == 1 ? hi2(a0) : h == 2 ? lo2(a1) : hi2(a1), rs, v[h]))
  ST4(z[h] = v[h] * v[h])
  ST4(z[h] = pfma(z[h], bc(-0.10294324f), bc(-2.30220820f)))
  ST4(z[h] = z[h] * v[h])
  ST4((z[h] = (f32x2){__builtin_amdgcn_exp2f(z[h][0]), __builtin_amdgcn_exp2f(z[h][1])}))
  ST4(z[h] = z[h] + bc(1.f))
  ST4((z[h] = (f32x2){__builtin_amdgcn_rcpf(z[h][0]), __builtin_amdgcn_rcpf(z[h][1])}))
  ST4(v[h] = v[h] * z[h])
  ST4(o[h] = pk2(v[h][0], v[h][1]))
#undef ST4
  return o;
}

#define TRACE(i) do {} while (0)
#define TRACE2(i) do {} while (0)
#define STAMP(k) do {} while (0)
#define STAMPC(k) do {} while (0)
#define COUNT(k) do {} while (0)

__global__ void __launch_bounds__(512, 2) fused_kernel(Params p) {
  __shared__ __attribute__((aligned(16))) char lds_mem[LDS_BYTES];
  STAMP(0); TRACE(0);
  unsigned long long pc;
  asm volatile("s_getpc_b64 %0" : "=s"(pc));
  const unsigned LB = (unsigned)(size_t)(LAS char*)lds_mem;
  const int tid = threadIdx.x, lane = tid & 63;
  const int w = __builtin_amdgcn_readfirstlane(tid >> 6);
  if (w < 6) {
    const unsigned co = (unsigned)tid * 128u;
    dma4w(co < CODE_BYTES ? co : 0u, (const char*)(pc & ~255ull), LB + L_DUMMY + w * 256);
  }
  const int b = blockIdx.x, idx = b >> 3, xcd = b & 7;
  const int r = 8 * (xcd >> 2) + (idx >> 2), j = 4 * (xcd & 3) + (idx & 3);
  const int rot = (r + 2 * j) & 7;
  const int cs_r0 = r & 8, cs_dr = 1;
  const int mtile = 4 * xcd + (idx >> 3), ntile = idx & 7;
  const int i16 = lane & 15, g = lane >> 4, qq = i16 >> 2, pp = i16 & 3;
  const int d = w & 1;
  const bool mlp = j < 14;
  const int kgo = 2 * (idx >> 3), kg = (w + kgo) & 7;
  const __amdgpu_buffer_rsrc_t wsr = __builtin_amdgcn_make_buffer_rsrc(p.ws, 0, WS_END, 0x00020000);
  const unsigned HB = LB + d * HALF;
  const unsigned PW = LB + L_FLG;
  unsigned tagA, tagB;
  s16x8 we[7][4];
  f32x4 acc2[4][4];
  unsigned home;
  const unsigned r2l = (4 * g + qq) * 128 + pp * 8, r2x = (2 * g + (qq >> 1)) & 3;
  auto absorb = [&](int i, unsigned page) {
#pragma unroll
    for (int n = 0; n < 4; ++n) { const unsigned a = page + r2l + ((n ^ r2x) * 32); we[i][n] = ldstr(a, a + 2048); }
  };
  auto mkvc = [&](unsigned* vc) {
#pragma unroll
    for (int i = 0; i < 8; ++i) vc[i] = (lane >> 3) * 1024 + (((lane & 7) ^ (4 * (i & 1) + g)) * 16) + 4096 + i * 7168;
  };
  auto mkv2 = [&](unsigned* v2) {
#pragma unroll
    for (int i = 0; i < 4; ++i) v2[i] = (lane >> 3) * 1024 + (((((lane & 7) >> 1) ^ g) << 1) | (lane & 1)) * 16 + i * 7168;
  };
  auto loadC = [&](const unsigned* vc, int kgc, unsigned dst) {
    const unsigned o = ntile * 65536 + kgc * 128;
    dma_x8(vc, p.cre + o, dst);
    dma_x8(vc, p.cim + o, dst + 8192);
  };
  const char* w2b = p.w2 + ntile * 128;
  auto adv = [&](unsigned& a, int dl) { a += dl; asm volatile("" : "+v"(a)); };
  const int tok = 32 * qq + 16 * (i16 & 1) + ((i16 >> 1) & 1);
  const unsigned kx = (g ^ (i16 >> 1)) * 16;
  const unsigned awb = LB + SL_W + (8 * g + qq) * 256 + pp * 8, awx = qq | ((g & 1) << 2);
  auto second_round = [&](unsigned ovf) {
#pragma unroll
    for (int i = 0; i < 5; ++i) absorb(i, home + i * 4096);
    absorb(5, ovf);
    wait_lgkm0();
    prog_set(PW + w * 4, 1);
  };
  auto fetch_round2 = [&]() {
    unsigned vc[8], v2[4];
    mkvc(vc); mkv2(v2);
    dma_x4(v2, w2b + kstep_of(kg, 6) * 32768, home + 16384);
    loadC(vc, kg, home);
  };

  if (__builtin_expect(w >= 6, 1)) {
    unsigned vx[8], vw[8], vb[4], v2[4];
#pragma unroll
    for (int i = 0; i < 8; ++i) vw[i] = g * 3584 + ((((i16 >> 1) ^ (g | (((i >> 1) & 1) << 2))) << 1) | (i16 & 1)) * 16 + 4096 + i * (14336 - 1024);
    const char* wb = p.w1 + j * 256 + d * (32 * 3584);
    auto part1 = [&](int c, unsigned slot) { if (mlp) dma_x8(vw, wb + ((c + rot) & 7) * (64 * 3584), LB + slot + SL_W + d * 8192); };
    part1(0, 0);
#pragma unroll
    for (int i = 0; i < 8; ++i)
      vx[i] = (32 * (g >> 1) + 16 * ((lane >> 3) & 1) + (g & 1)) * 1024 + (((lane & 7) ^ (4 * (i & 1) + g)) * 16) + 4096 + (i & 1) * 65536 + (i >> 1) * 2048 - i * 1024;
#pragma unroll
    for (int i = 0; i < 4; ++i) vb[i] = (lane >> 3) * 1024 + (((lane & 7) ^ (4 * (i & 1) + g)) * 16) + i * 7168;
    const char* xb = p.x + (r * 128 + d * 8) * 1024;
    const char* bb = (d ? p.bim : p.bre) + j * 32768;
    auto part0 = [&](int c, unsigned slot) {
      const int kc = (c + rot) & 7;
      dma_x8(vx, xb + kc * 128, LB + slot + d * 8192);
      dma_x4(vb, bb + kc * 128, LB + slot + SL_B + d * 4096);
    };
    part0(0, 0);
    STAMP(1);
    part0(1, SLOT);
    wait_vm<12>();
    bar();
    STAMP(2);
    {
      const unsigned to = d ? (lane & 15) * 128 : (lane < 56 ? lane : 0) * 128;
      dma4w(to, d ? p.cre + b * 2048 : p.w2 + b * 7168, LB + L_DUMMY + d * 256);
      if (d) dma4w(to, p.cim + b * 2048, LB + L_DUMMY + d * 256);
    }
    mkv2(v2);
    const int kt = kstep_of((2 * (lane >> 3) + d + kgo) & 7, lane & 7) * 32768;
    auto page = [&](int role, int i, int grp, int pg) { dma_x4(v2, w2b + __builtin_amdgcn_readlane(kt, 8 * role + i), HB + grp * SLOT + pg * 4096); };
    const unsigned me = PW + w * 4;
    home = HB + 3 * SLOT;
    const unsigned co = ntile * 65536 + kg * 128;
    part1(1, SLOT); part0(2, 2 * SLOT);
    TRACE(1); wait_vm<12>(); TRACE(2); bar(); TRACE(3);
    part1(2, 2 * SLOT); part0(3, 0); page(3, 0, 3, 0); page(3, 1, 3, 1);
    TRACE(4); wait_vm<20>(); TRACE(5); bar(); TRACE(6);
    STAMPC(21);
    part1(3, 0); part0(4, SLOT); page(3, 2, 3, 2); page(3, 3, 3, 3);
    TRACE(7); wait_vm<20>(); TRACE(8); bar(); TRACE(9);
    absorb(0, home); absorb(1, home + 4096);
    part1(4, SLOT); part0(5, 2 * SLOT); wait_lgkm0(); page(3, 4, 3, 0); page(3, 5, 3, 1);
    TRACE(10); wait_vm<20>(); TRACE(11); bar(); TRACE(12);
    absorb(2, home + 8192); absorb(3, home + 12288);
    part1(5, 2 * SLOT); part0(6, 0); wait_lgkm0(); page(3, 6, 3, 2); dma4w(lane * 1024, p.cre + co, LB + L_DUMMY + d * 256);
    TRACE(13); wait_vm<17>(); TRACE(14); bar(); TRACE(15);
    absorb(4, home); absorb(5, home + 4096);
    part1(6, 0); part0(7, SLOT); dma4w(lane * 1024, p.cim + co, LB + L_DUMMY + d * 256);
    TRACE(16); wait_vm<13>(); TRACE(17); barrier(); TRACE(18);
    STAMP(3); STAMPC(22);
    absorb(6, home + 8192);
    part1(7, SLOT);
    page(0, 0, 2, 0); page(0, 1, 2, 1); page(0, 2, 2, 2);
    wait_vm<12>();
    barrier();
    page(0, 3, 2, 3); page(0, 4, 2, 4); page(0, 5, 3, 0); page(2, 5, 3, 1); page(2, 0, 0, 0);
    bar();
    STAMP(4);
    page(2, 1, 0, 1); page(2, 2, 0, 2); page(2, 3, 0, 3); page(2, 4, 0, 4);
    page(1, 5, 3, 2); page(1, 0, 1, 0); page(1, 1, 1, 1);
    wait_vm<12>(); prog_set(me, 1);
    STAMP(5);
    page(1, 2, 1, 2); page(1, 3, 1, 3); page(1, 4, 1, 4);
    STAMP(6);
    unsigned vc[8];
    mkvc(vc);
    prog_wait(PW + (4 + d) * 4, 1);
    page(2, 6, 0, 4);
    {
      const unsigned o = ntile * 65536 + ((4 + d + kgo) & 7) * 128;
      dma_x8(vc, p.cre + o, HB);
      wait_vm<12>(); prog_set(me, 2);
      dma_x8(vc, p.cim + o, HB + 8192);
    }
    STAMP(7);
    prog_wait(PW + d * 4, 1);
    prog_wait(PW + (2 + d) * 4, 1);
    loadC(vc, kg, home);
    wait_vm<16>(); prog_set(me, 3);
    const u32x4 tg = *(LAS u32x4*)(size_t)(LB + L_ST1);
    tagA = __builtin_amdgcn_readfirstlane(tg.x);
    tagB = __builtin_amdgcn_readfirstlane(tg.z);
    STAMP(8);
  } else if (w >= 4) {
    const unsigned po = (unsigned)(32 * j + 16 * d + i16) * 2u;
    const unsigned v_lre = *(const GAS unsigned short*)(p.lre + po), v_lim = *(const GAS unsigned short*)(p.lim + po),
                   v_ldt = *(const GAS unsigned short*)(p.logdt + po);
    f32x4 acr[8], aci[8];
#pragma unroll
    for (int m = 0; m < 8; ++m) { acr[m] = (f32x4){0, 0, 0, 0}; aci[m] = (f32x4){0, 0, 0, 0}; asm volatile("" : "+v"(acr[m]), "+v"(aci[m])); }
    unsigned ax[2], ab[2];
    ax[0] = LB + i16 * 128 + kx; ax[1] = ax[0] ^ 64;
    ab[0] = LB + SL_B + (16 * d + i16) * 128 + kx; ab[1] = ab[0] ^ 64;
    unsigned sa = LB + L_ST1 + g * 512, csa = LB + L_CS + (96 * d + 64 + i16) * 4;
    asm volatile("" : "+v"(sa), "+v"(csa), "+v"(ax[0]), "+v"(ax[1]), "+v"(ab[0]), "+v"(ab[1]));
    s16x8 xf[2][8], bf[2][2];
    auto rd = [&](int set, int i) {
      if (i < 2) bf[set][i] = lds128(ab[set] + i * 4096);
      else xf[set][i - 2] = lds128(ax[set] + (i - 2) * 2048);
    };
    auto mm = [&](int set, int i) {
      if (i & 1) aci[i >> 1] = mfma(xf[set][i >> 1], bf[set][1], aci[i >> 1]);
      else acr[i >> 1] = mfma(xf[set][i >> 1], bf[set][0], acr[i >> 1]);
    };
    __builtin_amdgcn_s_setprio(1);
    STAMP(1);
    bar();
    STAMP(2);
#pragma unroll
    for (int i = 0; i < 10; ++i) rd(0, i);
    wait_lgkm0();
    auto stage1 = [&](int dl) {
      SCHED();
#pragma unroll
      for (int i = 0; i < 16; ++i) {
        mm(0, i); SCHED();
        if (i < 10) rd(1, i);
        if (i == 11) { adv(ax[0], dl); adv(ab[0], dl); }
        SCHED();
      }
    };
    stage1(SLOT);
    const float lr = bflo(v_lre), li = bflo(v_lim);
    const float dt = bflo(pk2(expf(bflo(v_ldt)), 0.f));
    const float ang = smul(dt, li);
    const float rv = smul(ang, 0.15915494f);
    const float rev = (rv - __builtin_rintf(rv)) + sfma(ang, 6.4206382e-9f, sfms(ang, 0.15915494f, rv));
    const float sn = __builtin_amdgcn_sinf(rev), cn = __builtin_amdgcn_cosf(rev);
    const float ex = expf(smul(dt, lr));
    f32x2 a1 = {smul(ex, cn), smul(ex, sn)};
    const float nr = a1[0] - 1.f, ni = a1[1], iden = 2.f / sfma(lr, lr, smul(li, li));
    f32x2 kk = {smul(sfma(nr, lr, smul(ni, li)), iden), smul(sfms(ni, lr, smul(nr, li)), iden)};
    f32x2 t = cmul(a1, a1); t = cmul(t, t); t = cmul(t, t);
    f32x2 a16 = cmul(t, t);
    f32x2 a32 = cmul(a16, a16);
    const f32x2 a64 = cmul(a32, a32), a128 = cmul(a64, a64), a256 = cmul(a128, a128);
    f32x2 a512 = cmul(a256, a256);
    const f32x2 one = {1.f, 0.f};
    f32x2 aseg = cmul(kk, cmul(csel(g & 1, a32, one), csel(g & 2, a64, one)));
    const int e4 = (r - 1 - g) & 3;
    f32x2 acar = cmul(csel(e4 & 1, a128, one), csel(e4 & 2, a256, one));
    const f32x2 ar = bco(a1[0]), ai = bco(a1[1]);
    asm volatile("" :: "v"(ar), "v"(ai));
    asm volatile("" : "+v"(kk), "+v"(a16), "+v"(a32), "+v"(a512), "+v"(aseg), "+v"(acar));
    barrier();
    int ph = 0;
#pragma unroll 1
    for (int c = 2; c <= 8; ++c) {
      const int dl = ph == 2 ? -2 * (int)SLOT : (int)SLOT;
      ph = ph == 2 ? 0 : ph + 1;
      SCHED();
#pragma unroll
      for (int i = 0; i < 16; ++i) {
        mm(1, i); SCHED();
        if (i < 10) rd(0, i);
        if (i == 11) { adv(ax[1], dl); adv(ab[1], dl); }
        SCHED();
      }
      wait_lgkm0();
      stage1(ph == 2 ? -2 * (int)SLOT : (int)SLOT);
      barrier();
    }
    STAMP(4);
    __builtin_amdgcn_s_setprio(3);
    f32x2 rs[16], mrs[16];
    float csr, csi;
    u32x4 tg;
#pragma unroll
    for (int i = 0; i < 16; ++i) {
      mm(1, i); SCHED();
      if (i == 0) { csr = ldsf1(csa); csi = ldsf1(csa + 64); tg = *(LAS u32x4*)(size_t)sa; }
      mrs[i] = (f32x2){ldsf1(sa + i * 16 + 12), ldsf1(sa + i * 16 + 268)};
      rs[i] = (f32x2){ldsf1(sa + i * 16 + 4), ldsf1(sa + i * 16 + 260)};
      if ((i & 3) == 3) TRACE2(20 + (i >> 2));
      SCHED();
    }
    const f32x2 ncr = bco(-csr), nci = bco(-csi);
    tagA = __builtin_amdgcn_readfirstlane(tg.x);
    tagB = __builtin_amdgcn_readfirstlane(tg.z);
    STAMP(5);
    f32x2 ur[16], ui[16], Er, Ei;
    SCHED();
#pragma unroll
    for (int s = 0; s < 16; ++s) {
      ur[s] = mrs[s] * ncr; SCHED();
      ui[s] = mrs[s] * nci; SCHED();
      ur[s] = pfma((s & 1) ? hi2(acr[s >> 1]) : lo2(acr[s >> 1]), rs[s], ur[s]); SCHED();
      ui[s] = pfma((s & 1) ? hi2(aci[s >> 1]) : lo2(aci[s >> 1]), rs[s], ui[s]); SCHED();
      if (s == 0) { Er = ur[0]; Ei = ui[0]; }
      else {
        const f32x2 t0 = pfma(Ei, -ai, ur[s]); SCHED();
        const f32x2 t1 = pfma(Er, ai, ui[s]); SCHED();
        Er = pfma(Er, ar, t0); SCHED();
        Ei = pfma(Ei, ar, t1); SCHED();
      }
    }
    TRACE2(1);
    const f32x2 E0 = {Er[0], Ei[0]};
    const f32x2 E = cfma(a16, E0, (f32x2){Er[1], Ei[1]});
    f32x2 P = {0.f, 0.f}, Tt = {0.f, 0.f};
    float er4[4], ei4[4];
    rows4(E[0], er4); rows4(E[1], ei4);
#pragma unroll
    for (int s = 0; s < 4; ++s) {
      const f32x2 e = {er4[s], ei4[s]};
      P = csel(s == g, Tt, P);
      Tt = s ? cfma(a32, Tt, e) : e;
    }
    const unsigned totb = WS_TOT + (unsigned)(j * 2 + d) * 4096;
    if (g == 0) __builtin_amdgcn_raw_buffer_store_b128((u32x4){tagA, f2u(Tt[0]), tagB, f2u(Tt[1])}, wsr, totb + r * 256 + i16 * 16, 0, 16);
    STAMP(6);
    {
      const f32x2 kr = bco(kk[0]), nki = bco(-kk[1]);
#pragma unroll
      for (int s = 0; s < 16; ++s) {
        const f32x2 t0 = ur[s] * kr; SCHED();
        const f32x2 t1 = ur[s] * nki; SCHED();
        ur[s] = pfma(ui[s], nki, t0); SCHED();
        ui[s] = pfma(ui[s], -kr, t1); SCHED();
      }
    }
#pragma unroll
    for (int s = 0; s < 16; s += 4) asm volatile("" : "+v"(ur[s]), "+v"(ui[s]), "+v"(ur[s + 1]), "+v"(ui[s + 1]), "+v"(ur[s + 2]), "+v"(ui[s + 2]), "+v"(ur[s + 3]), "+v"(ui[s + 3]));
    f32x2 kP = cmul(kk, P), kE = cmul(kk, E0);
    asm volatile("" : "+v"(kP), "+v"(kE));
    home = HB;
    TRACE2(2);
    prog_wait(PW + (6 + d) * 4, 1);
    TRACE2(3);
    second_round(HB + 3 * SLOT + 4096);
    __builtin_amdgcn_s_sleep(LB_SLEEP);
    STAMP(7);
    f32x2 car = {0.f, 0.f};
    if (r > 0) {
      u32x4 tv[4];
#pragma unroll
      for (int i = 0; i < 4; ++i) tv[i] = (u32x4){0, 0, 0, 0};
#pragma unroll 1
      for (int spin = 0; spin < SPIN_CAP; ++spin) {
        bool ok = true;
#pragma unroll
        for (int i = 0; i < 4; ++i)
          if (4 * i < r) tv[i] = __builtin_amdgcn_raw_buffer_load_b128(wsr, (4 * i + g < r) ? totb + i * 1024 + lane * 16 : 0xfffffff0u, 0, 16);
#pragma unroll
        for (int i = 0; i < 4; ++i) ok &= (4 * i + g >= r) | ((tv[i].x == tagA) & (tv[i].z == tagB));
        if (__all(ok)) break;
        COUNT(20);
        __builtin_amdgcn_s_sleep(8);
      }
#pragma unroll
      for (int i = 0; i < 4; ++i) car = csel(4 * i + g < r, cfma(a512, car, (f32x2){u2f(tv[i].y), u2f(tv[i].w)}), car);
      car = cmul(car, acar);
      float cr4[4], ci4[4];
      rows4(car[0], cr4); rows4(car[1], ci4);
      car = (f32x2){(cr4[0] + cr4[1]) + (cr4[2] + cr4[3]), (ci4[0] + ci4[1]) + (ci4[2] + ci4[3])};
    }
    STAMP(8);
    {
      const f32x2 h0 = cfma(aseg, car, kP), h1 = cfma(a16, h0, kE);
      f32x2 hr = {h0[0], h1[0]}, hi = {-h0[1], -h1[1]};
      const unsigned on = (qq == g) ? ((pp & 1) ? 0x3f800000u : 0x3f80u) : 0u;
      const u32x4 z0 = {(pp & 2) ? 0u : on, (pp & 2) ? on : 0u, 0u, 0u}, z1 = {0u, 0u, z0.x, z0.y};
      const s16x8 sel0 = __builtin_bit_cast(s16x8, z0), sel1 = __builtin_bit_cast(s16x8, z1);
      const unsigned ho = WS_H + (unsigned)(2 * r) * H_MT + (unsigned)(56 + 2 * j + d) * 4096 + lane * 16;
      u32x4 fr[4], fi[4];
      f32x4 tr[2], ti[2];
      const f32x4 zz = {0, 0, 0, 0};
      auto put = [&](int mp, int k) {
        const u32x4 o = {pk2(tr[k][0], tr[k][1]), pk2(tr[k][2], tr[k][3]), pk2(ti[k][0], ti[k][1]), pk2(ti[k][2], ti[k][3])};
        __builtin_amdgcn_raw_buffer_store_b128(o, wsr, ho + (mp >> 1) * H_MT + (2 * (mp & 1) + k) * 1024, 0, 16);
        SCHED();
      };
      SCHED();
#pragma unroll
      for (int mp = 0; mp < 5; ++mp) {
        if (mp > 0) {
#pragma unroll
          for (int k = 0; k < 2; ++k) {
            tr[k] = mfma(__builtin_bit_cast(s16x8, fr[mp - 1]), k ? sel1 : sel0, zz);
            ti[k] = mfma(__builtin_bit_cast(s16x8, fi[mp - 1]), k ? sel1 : sel0, zz);
          }
          SCHED();
        }
        if (mp < 4) {
#pragma unroll
          for (int e = 0; e < 4; ++e) {
            const int s = 4 * mp + e;
            const f32x2 t1 = pfma(hr, -ai, ui[s]); SCHED();
            const f32x2 t0 = pfma(hi, ai, ur[s]); SCHED();
            hi = pfma(hi, ar, t1); SCHED();
            hr = pfma(hr, ar, t0); SCHED();
            fi[mp][e] = pk2(hi[0], hi[1]); SCHED();
            fr[mp][e] = pk2(hr[0], hr[1]); SCHED();
          }
        }
        TRACE2(4 + 2 * mp);
        if (mp > 0) { put(mp - 1, 0); put(mp - 1, 1); }
        TRACE2(5 + 2 * mp);
      }
      STAMP(19);
      wait_vm<0>();
      if (lane < 2) __builtin_amdgcn_raw_buffer_store_b64((u32x2){tagA, tagB}, wsr, WS_FLAG + (2 * r + lane) * 1024 + (56 + 2 * j + d) * 8, 0, 16);
    }
    __builtin_amdgcn_s_setprio(0);
    STAMP(18);
  } else if (w >= 2) {
    home = HB + SLOT;
    if (mlp) {
      u32x2 b1v[6];
#pragma unroll
      for (int s = 0; s < 6; ++s) b1v[s] = *(const GAS u32x2*)(p.b1 + (j * 128 + 16 * s + 4 * g) * 2);
      f32x4 acc[6][4];
#pragma unroll
      for (int s = 0; s < 6; ++s) {
#pragma unroll
        for (int m = 0; m < 4; ++m) acc[s][m] = (f32x4){0, 0, 0, 0};
        asm volatile("" : "+v"(acc[s][0]), "+v"(acc[s][1]), "+v"(acc[s][2]), "+v"(acc[s][3]));
      }
      unsigned ax[2], aw[6];
      ax[0] = LB + (64 * d + i16) * 128 + kx; ax[1] = ax[0] ^ 64;
#pragma unroll
      for (int s = 0; s < 6; ++s) aw[s] = awb + ((s ^ awx) * 32);
      unsigned sta = LB + L_ST1 + (tok + 8 * d) * 16, csa = LB + L_CS + g * 16;
      asm volatile("" : "+v"(ax[0]), "+v"(ax[1]), "+v"(aw[0]), "+v"(aw[1]), "+v"(aw[2]), "+v"(aw[3]), "+v"(aw[4]), "+v"(aw[5]), "+v"(sta), "+v"(csa));
      s16x8 xf[2][4];
      s16x4 wf[2][12];
      auto rd = [&](int set, int i) {
        if (i < 4) xf[set][i] = lds128(ax[set] + i * 2048);
        else wf[set][i - 4] = ldstr4(aw[(i - 4) >> 1] + set * 8192 + (i & 1) * 1024);
      };
      auto mm = [&](int set, int i) { const int s = i >> 2, m = i & 3; acc[s][m] = mfma(cat(wf[set][2 * s], wf[set][2 * s + 1]), xf[set][m], acc[s][m]); };
      __builtin_amdgcn_s_setprio(1);
      STAMP(1);
      bar();
      STAMP(2); STAMPC(21);
#pragma unroll
      for (int i = 0; i < 16; ++i) rd(0, i);
      wait_lgkm0();
      auto stage1 = [&](int c, int dl) {
        SCHED();
#pragma unroll
        for (int i = 0; i < 24; ++i) {
          mm(0, i); SCHED();
          if (i < 16) rd(1, i);
          else if (i < 22) adv(aw[i - 16], dl);
          else if (i == 22) adv(ax[0], dl);
          SCHED();
        }
        TRACE(2 * c);
        barrier();
        TRACE(2 * c + 1);
        SCHED();
      };
      int ph = 0;
#pragma unroll 1
      for (int c = 1; c < 8; ++c) {
        const int dl = ph == 2 ? -2 * (int)SLOT : (int)SLOT;
        ph = ph == 2 ? 0 : ph + 1;
        stage1(c, dl);
#pragma unroll
        for (int i = 0; i < 24; ++i) {
          mm(1, i); SCHED();
          if (i < 16) rd(0, i);
          else if (i == 16) adv(ax[1], dl);
          else if (i == 23) wait_lgkm0();
          SCHED();
        }
      }
      stage1(8, 0);
      STAMP(4); STAMPC(22);
      f32x2 rs[4], nm[4];
      f32x4 cs[6];
      u32x4 st[4];
#pragma unroll
      for (int i = 0; i < 24; ++i) {
        mm(1, i); SCHED();
        if (i < 4) st[i] = *(LAS u32x4*)(size_t)(sta + i * 32);
        else if (i < 10) cs[i - 4] = ldsf4(csa + (i < 8 ? 64 * (i - 4) : 384 + 64 * (i - 8)));
        else if (i >= 14 && i < 18) rs[i - 14] = bco(u2f(st[i - 14].y));
        else if (i >= 18 && i < 22) nm[i - 18] = bco(-u2f(st[i - 18].w));
        SCHED();
      }
      tagA = __builtin_amdgcn_readfirstlane(st[0].x);
      tagB = __builtin_amdgcn_readfirstlane(st[0].z);
      STAMP(5);
      const unsigned fo = WS_FLAG + (2 * r + d) * 1024 + 4 * j * 8;
      const unsigned ho = WS_H + (unsigned)(2 * r + d) * H_MT + (unsigned)(4 * j) * 4096 + lane * 16;
#pragma unroll
      for (int pr = 0; pr < 3; ++pr) {
        Col col;
        mkcol(col, cs[2 * pr], cs[2 * pr + 1], b1v[2 * pr], b1v[2 * pr + 1]);
        SCHED();
#pragma unroll
        for (int m = 0; m < 4; ++m) {
          __builtin_amdgcn_raw_buffer_store_b128(gelu8(acc[2 * pr][m], acc[2 * pr + 1][m], col, rs[m], nm[m]), wsr, ho + pr * 4096 + m * 1024, 0, 16);
          TRACE2(1 + 4 * pr + m);
          if (pr > 0 && m == 3) {
            if (pr == 1) wait_vm<4>(); else wait_vm<5>();
            if (lane == 0) __builtin_amdgcn_raw_buffer_store_b64((u32x2){tagA, tagB}, wsr, fo + (pr - 1) * 8, 0, 16);
          }
          SCHED();
        }
      }
      STAMP(6);
      prog_wait(PW + (6 + d) * 4, 2);
      STAMP(7);
      second_round(HB + 3 * SLOT + 8192);
      wait_vm<0>();
      if (lane == 0) __builtin_amdgcn_raw_buffer_store_b64((u32x2){tagA, tagB}, wsr, fo + 16, 0, 16);
      STAMP(8);
      __builtin_amdgcn_s_setprio(0);
    } else {
#pragma unroll 1
      for (int c = 0; c < 9; ++c) bar();
      const u32x4 tg = *(LAS u32x4*)(size_t)(LB + L_ST1);
      tagA = __builtin_amdgcn_readfirstlane(tg.x);
      tagB = __builtin_amdgcn_readfirstlane(tg.z);
      prog_wait(PW + (6 + d) * 4, 2);
      second_round(HB + 3 * SLOT + 8192);
    }
    fetch_round2();
  } else {
    home = HB + 2 * SLOT;
    const u32x2 ep = __builtin_amdgcn_raw_buffer_load_b64(wsr, WS_EPOCH, 0, 16);
    {
      unsigned vr[4];
#pragma unroll
      for (int i = 0; i < 4; ++i)
        vr[i] = (32 * (g >> 1) + 16 * ((lane >> 3) & 1) + (g & 1)) * 1024 + (((lane & 7) ^ (4 * (i & 1) + g)) * 16) + (i & 1) * 65536 + (i >> 1) * 2048 - i * 1024;
      dma_x4(vr, p.x + (128 * (mtile >> 1) + 8 * (mtile & 1) + 4 * d) * 1024 + ntile * 128, HB + 3 * SLOT);
    }
    const int srow = 128 * r + 8 * j + 4 * d + g;
    u32x4 xr[4];
#pragma unroll
    for (int i = 0; i < 4; ++i) xr[i] = *(const GAS u32x4*)(p.x + srow * 1024 + i * 256 + i16 * 16);
    u32x2 b1v[2], dv[4], bv[4];
#pragma unroll
    for (int s = 0; s < 2; ++s) b1v[s] = *(const GAS u32x2*)(p.b1 + ((mlp ? j : 13) * 128 + 96 + 16 * s + 4 * g) * 2);
#pragma unroll
    for (int n = 0; n < 4; ++n) { dv[n] = *(const GAS u32x2*)(p.dsk + ntile * 128 + n * 32 + g * 8); bv[n] = *(const GAS u32x2*)(p.b2 + ntile * 128 + n * 32 + g * 8); }
    if (tid < 8) *(LAS unsigned*)(size_t)(PW + tid * 4) = 0u;
    f32x4 acc[2][4];
#pragma unroll
    for (int m = 0; m < 4; ++m) { acc[0][m] = (f32x4){0, 0, 0, 0}; acc[1][m] = (f32x4){0, 0, 0, 0}; asm volatile("" : "+v"(acc[0][m]), "+v"(acc[1][m])); }
    unsigned ax0 = LB + (64 * d + i16) * 128 + kx, ax1 = ax0 ^ 64, aw0 = awb + ((6 ^ awx) * 32), aw1 = awb + ((7 ^ awx) * 32);
    unsigned sta = LB + L_ST1 + (tok + 8 * d) * 16, csa = LB + L_CS + 512 + g * 16;
    asm volatile("" : "+v"(ax0), "+v"(ax1), "+v"(aw0), "+v"(aw1), "+v"(sta), "+v"(csa));
    int ph = 0;
    auto chunk = [&]() {
      if (!mlp) return;
      s16x8 xf[2][4], wf[2][2];
#pragma unroll
      for (int k = 0; k < 2; ++k) {
        wf[k][0] = ldstr(aw0 + k * 8192, aw0 + k * 8192 + 1024);
        wf[k][1] = ldstr(aw1 + k * 8192, aw1 + k * 8192 + 1024);
#pragma unroll
        for (int m = 0; m < 4; ++m) xf[k][m] = lds128((k ? ax1 : ax0) + m * 2048);
      }
      SCHED();
      const int dl = ph == 2 ? -2 * (int)SLOT : (int)SLOT;
      ph = ph == 2 ? 0 : ph + 1;
      adv(ax0, dl); adv(ax1, dl); adv(aw0, dl); adv(aw1, dl);
      SCHED();
#pragma unroll
      for (int k = 0; k < 2; ++k)
#pragma unroll
        for (int m = 0; m < 4; ++m) { acc[0][m] = mfma(wf[k][0], xf[k][m], acc[0][m]); acc[1][m] = mfma(wf[k][1], xf[k][m], acc[1][m]); }
      SCHED();
    };
    __builtin_amdgcn_s_setprio(2);
    STAMP(1);
    barrier();
    STAMP(2);
    chunk();
    tagA = __builtin_amdgcn_readfirstlane(ep.x) + 1u;
    tagB = __builtin_amdgcn_readfirstlane(ep.y) + 1u;
    {
      const s16x8 ones = {0x3f80, 0x3f80, 0x3f80, 0x3f80, 0x3f80, 0x3f80, 0x3f80, 0x3f80};
      const unsigned rb0 = LB + SL_B + (16 * d + i16) * 128 + kx;
      f32x4 c[6];
#pragma unroll
      for (int t = 0; t < 6; ++t) {
        s16x8 f0, f1;
        if (t < 4) {
          const unsigned a = awb + (((4 * d + t) ^ awx) * 32);
          f0 = ldstr(a, a + 1024); f1 = ldstr(a + 8192, a + 9216);
        } else {
          f0 = lds128(rb0 + (t - 4) * 4096); f1 = lds128((rb0 ^ 64) + (t - 4) * 4096);
        }
        c[t] = mfma(ones, f1, mfma(ones, f0, (f32x4){0, 0, 0, 0}));
      }
      const float v0 = g == 0 ? c[0][0] : (g == 1 ? c[2][0] : c[4][0]), v1 = g == 0 ? c[1][0] : (g == 1 ? c[3][0] : c[5][0]);
      __builtin_amdgcn_raw_buffer_store_b128((u32x4){tagA, f2u(v0), tagB, f2u(v1)}, wsr, WS_CS + ((j * 2 + d) * 16 + r) * 1024 + lane * 16, 0, 16);
    }
    float mean;
    {
      float s = 0.f;
#pragma unroll
      for (int i = 0; i < 4; ++i)
#pragma unroll
        for (int e = 0; e < 4; ++e) s += bflo(xr[i][e]) + bfhi(xr[i][e]);
      mean = row16_sum(s) * (1.f / 512.f);
      asm volatile("" : "+v"(mean), "+v"(b1v[0]), "+v"(b1v[1]), "+v"(dv[0]), "+v"(dv[1]), "+v"(dv[2]), "+v"(dv[3]), "+v"(bv[0]), "+v"(bv[1]), "+v"(bv[2]), "+v"(bv[3]));
    }
    u32x2 xv[2][4];
#pragma unroll
    for (int m = 0; m < 2; ++m)
#pragma unroll
      for (int n = 0; n < 4; ++n) xv[m][n] = *(LAS u32x2*)(size_t)(HB + 3 * SLOT + (16 * m + i16) * 128 + (g & 1) * 8 + (((2 * n + (g >> 1)) ^ (i16 >> 1)) * 16));
    barrier();
    chunk();
    {
      float v = 0.f;
#pragma unroll
      for (int i = 0; i < 4; ++i)
#pragma unroll
        for (int e = 0; e < 4; ++e) {
          const float d0 = bflo(xr[i][e]) - mean, d1 = bfhi(xr[i][e]) - mean;
          v = sfma(d0, d0, v); v = sfma(d1, d1, v);
        }
      const float rsd = __builtin_amdgcn_rsqf(row16_sum(v) * (1.f / 512.f) + 1e-5f);
      if (i16 == 0) __builtin_amdgcn_raw_buffer_store_b128((u32x4){tagA, f2u(rsd), tagB, f2u(mean * rsd)}, wsr, WS_STAT + srow * 16, 0, 16);
    }
    barrier();
    chunk();
    barrier();
    u32x4 cv[8];
    auto gather = [&]() {
      dma1_dev(lane * 16, p.ws + WS_STAT + (128 * r + 64 * d) * 16, LB + L_ST1 + d * 1024);
      dma1_dev(lane * 16, p.ws + WS_STAT + (128 * (mtile >> 1) + 64 * d) * 16, LB + L_ST2 + d * 1024);
#pragma unroll
      for (int i = 0; i < 8; ++i) cv[i] = __builtin_amdgcn_raw_buffer_load_b128(wsr, g < 3 ? WS_CS + ((j * 2 + d) * 16 + cs_r0 + i * cs_dr) * 1024 + lane * 16 : 0xfffffff0u, 0, 16);
    };
#pragma unroll 1
    for (int c = 3; c < GATHER_AT; ++c) { chunk(); barrier(); }
    chunk();
    gather();
    barrier();
#pragma unroll 1
    for (int c = GATHER_AT + 1; c < CHECK_AT; ++c) { chunk(); barrier(); }
    chunk();
    STAMP(3);
#pragma unroll 1
    for (int spin = 0; spin < SPIN_CAP; ++spin) {
      wait_vm<0>();
      const u32x4 sv = *(volatile LAS u32x4*)(size_t)(LB + L_ST1 + d * 1024 + lane * 16), s2 = *(volatile LAS u32x4*)(size_t)(LB + L_ST2 + d * 1024 + lane * 16);
      bool ok = true;
#pragma unroll
      for (int i = 0; i < 8; ++i) ok &= (cv[i].x == tagA) & (cv[i].z == tagB);
      ok = (ok | (g == 3)) & (sv.x == tagA) & (sv.z == tagB) & (s2.x == tagA) & (s2.z == tagB);
      if (__all(ok)) break;
      COUNT(20);
      __builtin_amdgcn_s_sleep(4);
      gather();
    }
    {
      float s0 = 0.f, s1 = 0.f;
#pragma unroll
      for (int i = 0; i < 8; ++i) { s0 += u2f(cv[i].y); s1 += u2f(cv[i].w); }
      if (g < 3) {
        unsigned ca = LB + L_CS + (96 * d + 32 * g + i16) * 4;
        asm volatile("" : "+v"(ca));
        *(LAS float*)(size_t)ca = s0;
        *(LAS float*)(size_t)(ca + 64) = s1;
      }
    }
    if (CHECK_AT < 7) bar(); else barrier();
#pragma unroll 1
    for (int c = CHECK_AT + 1; c < 8; ++c) { chunk(); barrier(); }
    STAMP(4);
#pragma unroll
    for (int m = 0; m < 2; ++m) {
      const u32x4 st = *(LAS u32x4*)(size_t)(LB + L_ST2 + (tok + 8 * (mtile & 1) + 4 * d + 2 * m) * 16);
#pragma unroll
      for (int n = 0; n < 4; ++n) {
        f32x4 o;
#pragma unroll
        for (int e = 0; e < 4; ++e) {
          const float xx = (e & 1) ? bfhi(xv[m][n][e >> 1]) : bflo(xv[m][n][e >> 1]);
          const float dd = (e & 1) ? bfhi(dv[n][e >> 1]) : bflo(dv[n][e >> 1]);
          const float bb = (e & 1) ? bfhi(bv[n][e >> 1]) : bflo(bv[n][e >> 1]);
          o[e] = sfma(sfms(xx, u2f(st.y), u2f(st.w)), dd, xx + bb);
        }
        const f32x4 zz = {0, 0, 0, 0};
        acc2[n][m] = d ? zz : o;
        acc2[n][2 + m] = d ? o : zz;
      }
    }
#pragma unroll
    for (int n = 0; n < 4; ++n) asm volatile("" : "+v"(acc2[n][0]), "+v"(acc2[n][1]), "+v"(acc2[n][2]), "+v"(acc2[n][3]));
    STAMP(5);
    if (mlp) {
      Col col;
      mkcol(col, ldsf4(csa), ldsf4(csa + 64), b1v[0], b1v[1]);
      u32x4 st[4];
#pragma unroll
      for (int m = 0; m < 4; ++m) st[m] = *(LAS u32x4*)(size_t)(sta + m * 32);
      const unsigned ho = WS_H + (unsigned)(2 * r + d) * H_MT + (unsigned)(4 * j + 3) * 4096 + lane * 16;
      SCHED();
#pragma unroll
      for (int m = 0; m < 4; ++m)
        __builtin_amdgcn_raw_buffer_store_b128(gelu8(acc[0][m], acc[1][m], col, bco(u2f(st[m].y)), bco(-u2f(st[m].w))), wsr, ho + m * 1024, 0, 16);
    }
    STAMP(6);
    wait_vm<0>();
    if (mlp && lane == 0) __builtin_amdgcn_raw_buffer_store_b64((u32x2){tagA, tagB}, wsr, WS_FLAG + (2 * r + d) * 1024 + (4 * j + 3) * 8, 0, 16);
    STAMP(18);
    prog_wait(PW + (6 + d) * 4, 1);
    STAMP(7);
    second_round(HB + 3 * SLOT);
    STAMP(8);
    fetch_round2();
    __builtin_amdgcn_s_setprio(0);
  }

  STAMP(9);
  if (w >= 2) {
#pragma unroll
    for (int n = 0; n < 4; ++n)
#pragma unroll
      for (int m = 0; m < 4; ++m) acc2[n][m] = (f32x4){0, 0, 0, 0};
  }
#pragma unroll
  for (int n = 0; n < 4; ++n) asm volatile("" : "+v"(acc2[n][0]), "+v"(acc2[n][1]), "+v"(acc2[n][2]), "+v"(acc2[n][3]));
  const int ksl = kstep_of(kg, lane < 11 ? lane : 0);
  const unsigned fa = WS_FLAG + mtile * 1024 + ksl * 8;
  auto ready = [&](u32x2 f) -> unsigned { return (unsigned)__ballot((f.x == tagA) & (f.y == tagB)); };
  unsigned have = 0;
  auto waitflags = [&](unsigned need) {
#pragma unroll 1
    for (int spin = 0; spin < SPIN_CAP; ++spin) {
      if ((have & need) == need) break;
      have = ready(__builtin_amdgcn_raw_buffer_load_b64(wsr, fa, 0, 16));
      if ((have & need) != need) __builtin_amdgcn_s_sleep(2);
    }
    asm volatile("" ::: "memory");
  };
  if (w < 6) prog_wait(PW + (6 + d) * 4, 3);
  STAMP(10);
  waitflags(0x7u);
  STAMP(11);
  const unsigned hb = WS_H + (unsigned)mtile * H_MT + lane * 16;
  auto loadH = [&](int i, int m) { return __builtin_amdgcn_raw_buffer_load_b128(wsr, hb + m * 1024, __builtin_amdgcn_readlane(ksl, i) * 4096, 16); };
  u32x4 hf[2][4];
#pragma unroll
  for (int i = 0; i < 2; ++i)
#pragma unroll
    for (int m = 0; m < 4; ++m) hf[i][m] = loadH(i, m);
  const unsigned crd = home + i16 * 128 + (g & 1) * 8;
  s16x8 cfr[2][4];
  u32x2 pf;
  SCHED();
#pragma unroll
  for (int i = 0; i < 11; ++i) {
    if (i == 1) { have = ready(pf); waitflags(0x18u); STAMP(12); }
    if (i == 3) { have = ready(pf); waitflags(0x60u); }
    if (i == 5) { STAMP(13); have = ready(pf); waitflags(0x780u); STAMP(14); }
    if (i == 0 || i == 2 || i == 4) pf = __builtin_amdgcn_raw_buffer_load_b64(wsr, fa, 0, 16);
    if (i == 4 && w < 6) absorb(6, home + 16384);
    if (i >= 6 && i < 10) {
#pragma unroll
      for (int n = 0; n < 4; ++n) {
        const unsigned a = crd + n * 2048 + (((2 * (i - 6) + (g >> 1)) ^ (i16 >> 1)) * 16);
        cfr[i & 1][n] = cat(*(LAS s16x4*)(size_t)a, *(LAS s16x4*)(size_t)(a + 8192));
      }
    }
    SCHED();
#pragma unroll
    for (int m = 0; m < 4; ++m) {
#pragma unroll
      for (int n = 0; n < 4; ++n) acc2[n][m] = mfma(i < 7 ? we[i < 7 ? i : 0][n] : cfr[(i + 1) & 1][n], __builtin_bit_cast(s16x8, hf[i & 1][m]), acc2[n][m]);
      SCHED();
      if (i + 2 < 11) hf[i & 1][m] = loadH(i + 2, m);
      SCHED();
    }
  }
  STAMP(15);
#pragma unroll
  for (int m = 0; m < 4; ++m)
#pragma unroll
    for (int n = 0; n < 4; ++n) *(LAS f32x4*)(size_t)(home + (16 * m + i16) * 256 + (((4 * n + g) ^ i16) * 16)) = acc2[n][m];
  const int ck = tid & 15;
  const int orow = 32 * ((tid >> 6) & 3) + 16 * ((tid >> 4) & 1) + ((tid >> 5) & 1) + 2 * (tid >> 8) + 8 * (mtile & 1);
  char* op = p.out + (128 * (mtile >> 1) + orow) * 2048 + ntile * 256 + ck * 16;
  unsigned ra[4];
#pragma unroll
  for (int k = 0; k < 4; ++k) ra[k] = LB + (tid >> 4) * 256 + ((ck ^ ((tid >> 4) & 15)) * 16) + k * SLOT;
  asm volatile("" : "+v"(ra[0]), "+v"(ra[1]), "+v"(ra[2]), "+v"(ra[3]), "+v"(op));
  barrier();
  STAMP(16);
  {
    f32x4 pt[2][8];
#pragma unroll
    for (int h = 0; h < 2; ++h)
#pragma unroll
      for (int k = 0; k < 8; ++k) pt[h][k] = ldsf4(ra[k >> 1] + (k & 1) * HALF + h * 8192);
#pragma unroll
    for (int h = 0; h < 2; ++h) {
      const f32x4 s = ((pt[h][0] + pt[h][1]) + (pt[h][2] + pt[h][3])) + ((pt[h][4] + pt[h][5]) + (pt[h][6] + pt[h][7]));
      __builtin_nontemporal_store(s, (GAS f32x4*)(op + h * 8192));
    }
  }
  if (b == 255 && tid == 0) __builtin_amdgcn_raw_buffer_store_b64((u32x2){tagA, tagB}, wsr, WS_EPOCH, 0, 16);
}

extern "C" void kernel_launch(void* const* d_in, const int* in_sizes, int n_in, void* d_out, int out_size, void* d_ws,
                              size_t ws_size, hipStream_t stream) {
  Params p;
  p.x = (const char*)d_in[0]; p.lre = (const char*)d_in[1]; p.lim = (const char*)d_in[2];
  p.bre = (const char*)d_in[3]; p.bim = (const char*)d_in[4]; p.cre = (const char*)d_in[5]; p.cim = (const char*)d_in[6];
  p.logdt = (const char*)d_in[7]; p.dsk = (const char*)d_in[8];
  p.w1 = (const char*)d_in[10]; p.b1 = (const char*)d_in[11]; p.w2 = (const char*)d_in[12]; p.b2 = (const char*)d_in[13];
  p.out = (char*)d_out; p.ws = (char*)d_ws;
  fused_kernel<<<256, 512, 0, stream>>>(p);
}
```

```cpp
#include <hip/hip_runtime.h>
#include <stdint.h>
#include <type_traits>

typedef short s16x4 __attribute__((ext_vector_type(4)));
typedef short s16x8 __attribute__((ext_vector_type(8)));
typedef __bf16 bf16x8 __attribute__((ext_vector_type(8)));
typedef __bf16 bf16x2 __attribute__((ext_vector_type(2)));
typedef float f32x4 __attribute__((ext_vector_type(4)));
typedef float f32x2 __attribute__((ext_vector_type(2)));
typedef unsigned u32x4 __attribute__((ext_vector_type(4)));
typedef unsigned u32x2 __attribute__((ext_vector_type(2)));
typedef int i32x4 __attribute__((ext_vector_type(4)));
typedef unsigned long long u64;
typedef u64 u64x4 __attribute__((ext_vector_type(4)));
typedef u64 u64x8 __attribute__((ext_vector_type(8)));
typedef int i32x8 __attribute__((ext_vector_type(8)));
typedef int i32x16 __attribute__((ext_vector_type(16)));
__device__ i32x8 sbl8(i32x4, int, int) __asm("llvm.amdgcn.s.buffer.load.v8i32");
__device__ i32x16 sbl16(i32x4, int, int) __asm("llvm.amdgcn.s.buffer.load.v16i32");
#define LAS __attribute__((address_space(3)))
#define GAS __attribute__((address_space(1)))
#define DI __device__ __forceinline__

#define STAMPS 0
#define TRACING 0
#define GATHER_AT 4
#define ROT 1
#define TOUCH 1
#define TLBW 0
#define NOFF 0
#define PSLEEP 1
#define BFLAG 2
#define ABAR 0
#define KROT 0
#define LPRIO 3
#define CHK 0
#define BSPLIT 8
#define APRIO 0
#define ADELAY 0
#define DEPE 2
#define DEPL 2
#define LB_SLEEP 8
#define CHECK_AT 5
#define CPF 1
#define MAP 1

struct Params {
  const char *x, *lre, *lim, *bre, *bim, *cre, *cim, *logdt, *dsk, *w1, *b1, *w2, *b2;
  char *out, *ws;
};

constexpr unsigned WS_EPOCH = 0;
constexpr unsigned WS_STAT = 4096;
constexpr unsigned WS_TOT = WS_STAT + 2048 * 16;
constexpr unsigned WS_CS = WS_TOT + 16 * 2 * 16 * 256;
constexpr unsigned WS_BS = WS_CS + 16 * 16 * 512;
constexpr unsigned WS_FLAG = WS_BS + 16 * 1024;
constexpr unsigned WS_H = WS_FLAG + 32 * 2048;
constexpr unsigned H_MT = 88 * 4096;
constexpr unsigned WS_END = WS_H + 32 * H_MT;
constexpr unsigned WS_STAMP = 32u << 20;
constexpr unsigned SLOT = 40960, HALF = 20480, SL_W = 16384, SL_B = 32768;
constexpr unsigned L_ST1 = 3 * SLOT + 16384, L_ST2 = L_ST1 + 2048;
constexpr unsigned L_CS = 3 * SLOT + HALF + 16384, L_FLG = L_CS + 512, L_B1 = L_CS + 768, L_BS = L_CS + 1024, L_LS = L_CS + 2048, L_DUMMY = L_CS + 3328;
constexpr unsigned LDS_BYTES = 163840;
constexpr int SPIN_CAP = 40000;
constexpr unsigned CODE_BYTES = 44032;

DI void dma4w(unsigned voff, const char* sbase, unsigned ldsaddr) {
  asm volatile("s_mov_b32 m0, %2\n\ts_nop 0\n\tglobal_load_lds_dword %0, %1" ::"v"(voff), "s"(sbase), "s"(ldsaddr));
}
DI void dma_x4(const unsigned* v, const char* sbase, unsigned ldsaddr) {
  asm volatile("s_mov_b32 m0, %5\n\ts_nop 0\n\t"
               "global_load_lds_dwordx4 %0, %4\n\tglobal_load_lds_dwordx4 %1, %4 offset:1024\n\t"
               "global_load_lds_dwordx4 %2, %4 offset:2048\n\tglobal_load_lds_dwordx4 %3, %4 offset:3072"
               ::"v"(v[0]), "v"(v[1]), "v"(v[2]), "v"(v[3]), "s"(sbase), "s"(ldsaddr));
}
DI void dma_x8(const unsigned* v, const char* sbase, unsigned ldsaddr) {
  asm volatile("s_mov_b32 m0, %9\n\ts_nop 0\n\t"
               "global_load_lds_dwordx4 %0, %8 offset:-4096\n\tglobal_load_lds_dwordx4 %1, %8 offset:-3072\n\t"
               "global_load_lds_dwordx4 %2, %8 offset:-2048\n\tglobal_load_lds_dwordx4 %3, %8 offset:-1024\n\t"
               "global_load_lds_dwordx4 %4, %8\n\tglobal_load_lds_dwordx4 %5, %8 offset:1024\n\t"
               "global_load_lds_dwordx4 %6, %8 offset:2048\n\tglobal_load_lds_dwordx4 %7, %8 offset:3072"
               ::"v"(v[0]), "v"(v[1]), "v"(v[2]), "v"(v[3]), "v"(v[4]), "v"(v[5]), "v"(v[6]), "v"(v[7]), "s"(sbase), "s"(ldsaddr + 4096u));
}
constexpr int PG(int i) { return i == 7 ? 3 : (i + 2) % 3; }
template <int N> DI void wait_vm() { asm volatile("s_waitcnt vmcnt(%0)" ::"i"(N) : "memory"); }
DI void wait_lgkm0() { asm volatile("" ::: "memory"); __builtin_amdgcn_s_waitcnt(0xC07F); asm volatile("" ::: "memory"); }
DI void barrier() {
  asm volatile("" ::: "memory");
  __builtin_amdgcn_s_waitcnt(0xC07F);
  __builtin_amdgcn_s_barrier();
  asm volatile("" ::: "memory");
}
#define SCHED() __builtin_amdgcn_sched_barrier(0)
DI s16x8 lds128(unsigned a) { return *(LAS s16x8*)(size_t)a; }
DI f32x4 ldsf4(unsigned a) { return *(LAS f32x4*)(size_t)a; }
DI f32x2 ldsf2(unsigned a) { return *(LAS f32x2*)(size_t)a; }
DI float ldsf1(unsigned a) { return *(LAS float*)(size_t)a; }
DI s16x4 ldstr4(unsigned a) { return __builtin_amdgcn_ds_read_tr16_b64_v4i16((LAS s16x4*)(size_t)a); }
DI s16x8 cat(s16x4 lo, s16x4 hi) { return __builtin_shufflevector(lo, hi, 0, 1, 2, 3, 4, 5, 6, 7); }
DI s16x8 ldstr(unsigned a0, unsigned a1) { return cat(ldstr4(a0), ldstr4(a1)); }
DI f32x4 mfma(s16x8 a, s16x8 b, f32x4 c) {
  return __builtin_amdgcn_mfma_f32_16x16x32_bf16(__builtin_bit_cast(bf16x8, a), __builtin_bit_cast(bf16x8, b), c, 0, 0, 0);
}
DI unsigned pk2(float a, float b) { f32x2 v = {a, b}; return __builtin_bit_cast(unsigned, __builtin_convertvector(v, bf16x2)); }
DI float u2f(unsigned u) { return __builtin_bit_cast(float, u); }
DI unsigned f2u(float u) { return __builtin_bit_cast(unsigned, u); }
DI float bflo(unsigned u) { return __builtin_bit_cast(float, u << 16); }
DI float bfhi(unsigned u) { return __builtin_bit_cast(float, u & 0xffff0000u); }
template <int CTRL> DI float dpp_add(float v) {
  return v + __builtin_bit_cast(float, __builtin_amdgcn_update_dpp(0, __builtin_bit_cast(int, v), CTRL, 0xf, 0xf, true));
}
DI float row16_sum(float v) {
  v = dpp_add<0xB1>(v);
  v = dpp_add<0x4E>(v);
  v = dpp_add<0x141>(v);
  v = dpp_add<0x140>(v);
  return v;
}
DI float smul(float a, float b) { float d; asm("v_mul_f32 %0, %1, %2" : "=v"(d) : "v"(a), "v"(b)); return d; }
DI float sadd(float a, float b) { float d; asm("v_add_f32 %0, %1, %2" : "=v"(d) : "v"(a), "v"(b)); return d; }
DI float sfma(float a, float b, float c) { float d; asm("v_fma_f32 %0, %1, %2, %3" : "=v"(d) : "v"(a), "v"(b), "v"(c)); return d; }
DI float sfms(float a, float b, float c) { float d; asm("v_fma_f32 %0, %1, %2, -%3" : "=v"(d) : "v"(a), "v"(b), "v"(c)); return d; }
DI void zero4(f32x4& v) {
  float a, b, c, d;
  asm volatile("v_mov_b32 %0, 0\n\tv_mov_b32 %1, 0\n\tv_mov_b32 %2, 0\n\tv_mov_b32 %3, 0" : "=v"(a), "=v"(b), "=v"(c), "=v"(d));
  v = (f32x4){a, b, c, d};
}
DI u32x2 swap32(float a, float b) { asm volatile("s_nop 1" : "+v"(a), "+v"(b)); return __builtin_amdgcn_permlane32_swap(f2u(a), f2u(b), false, false); }
DI u32x2 swap16(float a, float b) { asm volatile("s_nop 1" : "+v"(a), "+v"(b)); return __builtin_amdgcn_permlane16_swap(f2u(a), f2u(b), false, false); }
DI f32x2 pfma(f32x2 a, f32x2 b, f32x2 c) { return __builtin_elementwise_fma(a, b, c); }
DI f32x2 bc(float a) { return (f32x2){a, a}; }
DI f32x2 bco(float a) { f32x2 v = {a, a}; asm("" : "+v"(v)); return v; }
DI f32x2 cmul(f32x2 a, f32x2 b) { return (f32x2){sfms(a[0], b[0], smul(a[1], b[1])), sfma(a[0], b[1], smul(a[1], b[0]))}; }
DI f32x2 cfma(f32x2 a, f32x2 b, f32x2 c) { return (f32x2){sfms(a[0], b[0], sfms(a[1], b[1], c[0])), sfma(a[0], b[1], sfma(a[1], b[0], c[1]))}; }
DI f32x2 csel(bool p, f32x2 a, f32x2 b) { return (f32x2){p ? a[0] : b[0], p ? a[1] : b[1]}; }
DI void prog_set(unsigned a, unsigned v) {
  asm volatile("s_mov_b64 exec, 1\n\tds_write_b32 %0, %1\n\ts_mov_b64 exec, -1" ::"v"(a), "v"(v) : "memory");
}
DI void prog_wait(unsigned a, unsigned v) {
#pragma unroll 1
  for (int spin = 0; spin < SPIN_CAP * 16; ++spin) {
    if (__builtin_amdgcn_readfirstlane(*(volatile LAS unsigned*)(size_t)a) >= v) break;
    __builtin_amdgcn_s_sleep(1);
  }
  asm volatile("" ::: "memory");
}
DI void bar() { asm volatile("" ::: "memory"); __builtin_amdgcn_s_barrier(); asm volatile("" ::: "memory"); }
DI void dma1_dev(unsigned voff, const char* sbase, unsigned ldsaddr) {
  asm volatile("s_mov_b32 m0, %2\n\ts_nop 0\n\tglobal_load_lds_dwordx4 %0, %1 sc1" ::"v"(voff), "s"(sbase), "s"(ldsaddr) : "memory");
}
DI void dma4_dev(unsigned voff, const char* s0, const char* s1, unsigned ldsaddr) {
  asm volatile("s_mov_b32 m0, %3\n\ts_nop 0\n\tglobal_load_lds_dwordx4 %0, %1 sc1\n\tglobal_load_lds_dwordx4 %0, %1 offset:1024 sc1\n\t"
               "global_load_lds_dwordx4 %0, %2 offset:2048 sc1\n\tglobal_load_lds_dwordx4 %0, %2 offset:3072 sc1" ::"v"(voff), "s"(s0), "s"(s1), "s"(ldsaddr) : "memory");
}
DI int kstep_of(int slot, int i) {
  const int q = slot + 4 * i + (slot >= 4 ? 28 : 0), cls = (q >= 14) + (q >= 28) + (q >= 42);
  return 4 * (q - 14 * cls) + ((0x9C >> (2 * cls)) & 3);
}
DI unsigned flag_of(int j, int c) {
  const int q = 14 * ((0x78 >> (2 * c)) & 3) + j;
  return q < 32 ? (q & 3) * 128 + (q >> 2) * 8 : 512 + (q & 3) * 128 + ((q - 32) >> 2) * 8;
}
DI void cnt_add(unsigned a) {
  asm volatile("s_mov_b64 exec, 1\n\tds_add_u32 %0, %1\n\ts_mov_b64 exec, -1" ::"v"(a), "v"(1u) : "memory");
}
DI f32x2 lo2(f32x4 v) { return __builtin_shufflevector(v, v, 0, 1); }
DI f32x2 hi2(f32x4 v) { return __builtin_shufflevector(v, v, 2, 3); }
DI void rows4(float v, float* o) {
  asm volatile("s_nop 1" : "+v"(v));
  const unsigned u = f2u(v);
  const u32x2 h = __builtin_amdgcn_permlane32_swap(u, u, false, false);
  const u32x2 a = __builtin_amdgcn_permlane16_swap(h.x, h.x, false, false), b = __builtin_amdgcn_permlane16_swap(h.y, h.y, false, false);
  o[0] = u2f(a.x); o[1] = u2f(a.y); o[2] = u2f(b.x); o[3] = u2f(b.y);
}
struct Col { f32x2 cs[4], bias[4]; };
DI void mkcol(Col& c, f32x4 cs0, f32x4 cs1, u32x2 b0, u32x2 b1) {
  c.cs[0] = lo2(cs0); c.cs[1] = hi2(cs0); c.cs[2] = lo2(cs1); c.cs[3] = hi2(cs1);
  c.bias[0] = (f32x2){bflo(b0.x), bfhi(b0.x)}; c.bias[1] = (f32x2){bflo(b0.y), bfhi(b0.y)};
  c.bias[2] = (f32x2){bflo(b1.x), bfhi(b1.x)}; c.bias[3] = (f32x2){bflo(b1.y), bfhi(b1.y)};
}
DI u32x4 gelu8(f32x4 a0, f32x4 a1, const Col& c, f32x2 rs, f32x2 nmrs) {
  f32x2 v[4], z[4];
  u32x4 o;
#define ST4(stmt) _Pragma("unroll") for (int h = 0; h < 4; ++h) { stmt; } SCHED();
  ST4(v[h] = pfma(c.cs[h], nmrs, c.bias[h]))
  ST4(v[h] = pfma(h == 0 ? lo2(a0) : h == 1 ? hi2(a0) : h == 2 ? lo2(a1) : hi2(a1), rs, v[h]))
  ST4(z[h] = v[h] * v[h])
  ST4(z[h] = pfma(z[h], bc(-0.10294324f), bc(-2.30220820f)))
  ST4(z[h] = z[h] * v[h])
  ST4((z[h] = (f32x2){__builtin_amdgcn_exp2f(z[h][0]), __builtin_amdgcn_exp2f(z[h][1])}))
  ST4(z[h] = z[h] + bc(1.f))
  ST4((z[h] = (f32x2){__builtin_amdgcn_rcpf(z[h][0]), __builtin_amdgcn_rcpf(z[h][1])}))
  ST4(v[h] = v[h] * z[h])
  ST4(o[h] = pk2(v[h][0], v[h][1]))
#undef ST4
  return o;
}

#define TRACE(i) do {} while (0)
#define TRACE2(i) do {} while (0)
#define TRACE3(i) do {} while (0)
#define STAMP(k) do {} while (0)
#define STAMPC(k) do {} while (0)
#define COUNT(k) do {} while (0)

__global__ void __launch_bounds__(512, 2) fused_kernel(Params p) {
  __shared__ __attribute__((aligned(16))) char lds_mem[LDS_BYTES];
  unsigned long long pc;
  asm volatile("s_getpc_b64 %0" : "=s"(pc));
  const unsigned LB = (unsigned)(size_t)(LAS char*)lds_mem;
  const int tid = threadIdx.x, lane = tid & 63;
  const int w = __builtin_amdgcn_readfirstlane(tid >> 6);
  if (w == 5) {
    const unsigned co = ((unsigned)(lane & 15) * 32u + (blockIdx.x >> 3)) * 128u;
    dma4w(co < CODE_BYTES ? co : 0u, (const char*)(pc & ~255ull), LB + L_DUMMY + 512);
  }
  const int b = blockIdx.x, idx = b >> 3, xcd = b & 7;
  const int r = 8 * (xcd >> 2) + (idx >> 2), j = 4 * (xcd & 3) + (idx & 3);
  const int rot = (2 * (xcd & 3) + 4 * (xcd >> 2)) & 7;
  const int mtile = 4 * xcd + (idx >> 3), ntile = (idx + NOFF) & 7;
  const int i16 = lane & 15, g = lane >> 4, qq = i16 >> 2, pp = i16 & 3;
  const int d = w & 1;
  const bool mlp = j < 14;
  u32x4 pre[4];
  unsigned v_lre, v_lim, v_ldt;
  if ((w >> 1) == 2) {
    const unsigned po = (unsigned)(32 * j + 16 * d + i16) * 2u;
    v_lre = *(const GAS unsigned short*)(p.lre + po); v_lim = *(const GAS unsigned short*)(p.lim + po); v_ldt = *(const GAS unsigned short*)(p.logdt + po);
    asm volatile("" ::: "memory");
  }
  u32x4 prb[2];
  if (w < 4) {
    const bool isb = w >= 2;
    const char* pb = isb ? p.w1 + (64 * (r & 7) + 32 * d) * 3584 + (mlp ? j : 13) * 256 + (r >> 3) * 128 : p.x + (128 * r + 8 * j + 4 * d) * 1024;
    const unsigned po = isb ? (lane >> 3) * 3584 + (lane & 7) * 16 : g * 1024 + i16 * 16, ps = isb ? 28672 : 256;
#pragma unroll
    for (int i = 0; i < 4; ++i) pre[i] = *(const GAS u32x4*)(pb + i * ps + po);
    if (isb) {
#pragma unroll
      for (int i = 0; i < 2; ++i) prb[i] = *(const GAS u32x4*)((r & 8 ? p.bim : p.bre) + (32 * j + 4 * (r & 7) + 2 * d) * 1024 + i * 512 + (lane >> 5) * 1024 + (lane & 31) * 16);
    }
    asm volatile("" ::: "memory");
  }
  const int kgo = 2 * (idx >> 3), kg = (w + KROT * kgo) & 7;
  auto slot_of = [&](int role) { return (((role + 1) & 2) << 1) + (((((role + 1) & 2) && !KROT ? 0 : kgo) + ((role & 1) ? 0 : 2) + d) & 3); };
  const int slot = slot_of(w >> 1);
  const u32x2 ep = *(const __attribute__((address_space(4))) u32x2*)(p.ws + WS_EPOCH);
  unsigned tagA, tagB;
  auto mktag = [&]() { tagA = ep.x; tagB = ep.y; asm volatile("" : "+s"(tagA), "+s"(tagB)); tagA += 1u; tagB += 1u; };
  const __amdgpu_buffer_rsrc_t wsr = __builtin_amdgcn_make_buffer_rsrc(p.ws, 0, WS_END, 0x00020000);
  const i32x4 wss = {(int)(size_t)p.ws, (int)((size_t)p.ws >> 32) & 0xffff, (int)WS_END, 0x00020000};
  auto fresh = [](int o) { asm volatile("" : "+s"(o) :: "memory"); return o; };
  const unsigned HB = LB + d * HALF;
  const unsigned PW = LB + L_FLG;
  s16x8 we[8][4];
  f32x4 acc2[4][4];
  unsigned home;
  const unsigned r2l = (4 * g + qq) * 128 + pp * 8, r2x = (2 * g + (qq >> 1)) & 3;
  auto absorb = [&](int i, unsigned page) {
#pragma unroll
    for (int n = 0; n < 4; ++n) { const unsigned a = page + r2l + ((n ^ r2x) * 32); we[i][n] = ldstr(a, a + 2048); }
  };
  auto mkvc = [&](unsigned* vc) {
#pragma unroll
    for (int i = 0; i < 8; ++i) vc[i] = (lane >> 3) * 1024 + (((lane & 7) ^ (4 * (i & 1) + g)) * 16) + 4096 + i * 7168;
  };
  auto mkv2 = [&](unsigned* v2) {
#pragma unroll
    for (int i = 0; i < 4; ++i) v2[i] = (lane >> 3) * 1024 + (((((lane & 7) >> 1) ^ g) << 1) | (lane & 1)) * 16 + i * 7168;
  };
  auto loadC = [&](const unsigned* vc, int kgc, unsigned dst) {
    const unsigned o = ntile * 65536 + kgc * 128;
    dma_x8(vc, p.cre + o, dst);
    dma_x8(vc, p.cim + o, dst + 8192);
  };
  const char* w2b = p.w2 + ntile * 128;
  auto adv = [&](unsigned& a, int dl) { a += dl; asm volatile("" : "+v"(a)); };
  const int tok = 32 * qq + 16 * (i16 & 1) + ((i16 >> 1) & 1);
  const unsigned kx = (g ^ (i16 >> 1)) * 16;
  const unsigned awb = LB + SL_W + (8 * g + qq) * 256 + pp * 8, awx = qq | ((g & 1) << 2);
  auto second_round = [&]() {
#pragma unroll
    for (int i = 0; i < 5; ++i) absorb(i, home + i * 4096);
    unsigned vc[8], v2[4];
    mkvc(vc); mkv2(v2);
    wait_lgkm0();
    dma_x4(v2, w2b + kstep_of(slot, 5) * 32768, home + 16384);
    loadC(vc, kg, home);
  };
  auto sum32 = [&](const u32x4* v) {
    float s0 = 0.f, s1 = 0.f;
#pragma unroll
    for (int i = 0; i < 4; ++i)
#pragma unroll
      for (int e = 0; e < 4; ++e) { s0 += bflo(v[i][e]); s1 += bfhi(v[i][e]); }
    return row16_sum(s0 + s1);
  };
  auto touch = [&]() {
    dma4w(d ? (lane & 15) * 128 : (lane < 56 ? lane : 0) * 128, d ? p.cre + b * 2048 : p.w2 + b * 7168, LB + L_DUMMY + d * 256);
    if (d) dma4w((lane & 15) * 128, p.cim + b * 2048, LB + L_DUMMY + d * 256);
  };
  auto zero2 = [&]() {
#pragma unroll
    for (int n = 0; n < 4; ++n) {
#pragma unroll
      for (int m = 0; m < 4; ++m) acc2[n][m] = (f32x4){0, 0, 0, 0};
      asm volatile("" : "+v"(acc2[n][0]), "+v"(acc2[n][1]), "+v"(acc2[n][2]), "+v"(acc2[n][3]));
    }
  };
  int ksl;
  unsigned hb, crd;
  auto setup2 = [&](int nm) {
    ksl = (lane < nm ? kstep_of(slot, lane) : 56 + 4 * kg + ((lane - nm) & 3)) * 4096;
    ksl += w == 1 ? 2048 : 0;
    hb = WS_H + (unsigned)mtile * H_MT + lane * 16;
    crd = home + i16 * 128 + (g & 1) * 8 + (((g >> 1) ^ (i16 >> 1)) * 16);
    asm volatile("" : "+v"(ksl), "+v"(hb), "+v"(crd));
  };

  auto roleD = [&]() __attribute__((always_inline)) {
    __builtin_amdgcn_s_setprio(3);
    unsigned vx[8], vw[8], vb[4], v2[4];
#pragma unroll
    for (int i = 0; i < 8; ++i) vw[i] = g * 3584 + ((((i16 >> 1) ^ (g | (((i >> 1) & 1) << 2))) << 1) | (i16 & 1)) * 16 + 4096 + i * (14336 - 1024);
    const char* wb = p.w1 + j * 256 + d * (32 * 3584);
    auto part1 = [&](int c, unsigned slot) { if (mlp) dma_x8(vw, wb + ((c + rot) & 7) * (64 * 3584), LB + slot + SL_W + d * 8192); };
    if (TOUCH == 5) touch();
    part1(0, 0);
#pragma unroll
    for (int i = 0; i < 8; ++i)
      vx[i] = (32 * (g >> 1) + 16 * ((lane >> 3) & 1) + (g & 1)) * 1024 + (((lane & 7) ^ (4 * (i & 1) + g)) * 16) + 4096 + (i & 1) * 65536 + (i >> 1) * 2048 - i * 1024;
#pragma unroll
    for (int i = 0; i < 4; ++i) vb[i] = (lane >> 3) * 1024 + (((lane & 7) ^ (4 * (i & 1) + g)) * 16) + i * 7168;
    const char* xb = p.x + (r * 128 + d * 8) * 1024;
    const char* bb = (d ? p.bim : p.bre) + j * 32768;
    auto partx = [&](int c, unsigned slot) { dma_x8(vx, xb + ((c + rot) & 7) * 128, LB + slot + d * 8192); };
    auto partb = [&](int c, unsigned slot) { dma_x4(vb, bb + ((c + rot) & 7) * 128, LB + slot + SL_B + d * 4096); };
    auto part0 = [&](int c, unsigned slot) { partx(c, slot); partb(c, slot); };
    mkv2(v2);
    unsigned vc[8], ra[4];
    mkvc(vc);
    home = HB + 3 * SLOT;
#pragma unroll
    for (int n = 0; n < 4; ++n) ra[n] = home + r2l + ((n ^ r2x) * 32);
    int kt = kstep_of(slot_of((lane >> 3) & 3), lane & 7) * 32768;
    asm volatile("" : "+v"(kt), "+v"(ra[0]), "+v"(ra[1]), "+v"(ra[2]), "+v"(ra[3]), "+v"(v2[0]), "+v"(v2[1]), "+v"(v2[2]), "+v"(v2[3]));
    asm volatile("" : "+v"(vc[0]), "+v"(vc[1]), "+v"(vc[2]), "+v"(vc[3]), "+v"(vc[4]), "+v"(vc[5]), "+v"(vc[6]), "+v"(vc[7]));
    auto page = [&](int role, int i, int grp, int pg) { dma_x4(v2, w2b + (unsigned)__builtin_amdgcn_readlane(kt, 8 * role + i), HB + grp * SLOT + pg * 4096); };
    auto take = [&](int i) {
      SCHED();
#pragma unroll
      for (int n = 0; n < 4; ++n) we[i][n] = ldstr(ra[n] + PG(i) * 4096, ra[n] + PG(i) * 4096 + 2048);
      SCHED();
    };
    auto own = [&](int i) { page(3, i, 3, PG(i)); };
    const unsigned PT = LB + 3 * SLOT + 12288;
    auto gather = [&]() {
      if (d) dma4_dev(lane * 16, p.ws + WS_STAT + r * 2048, p.ws + WS_STAT + (mtile >> 1) * 2048 - 2048, LB + L_ST1);
      else {
        const char* cp = p.ws + WS_CS + j * 8192;
        dma1_dev(lane * 16, p.ws + WS_BS + j * 1024, LB + L_BS);
        dma4_dev(lane * 16, cp, cp, PT);
        dma4_dev(lane * 16, cp + 4096, cp + 4096, PT + HALF);
      }
    };
    auto ld16 = [](unsigned a) { return *(volatile LAS u32x4*)(size_t)a; };
    auto bad = [&](unsigned a, u32x4 v) { return a | (v.x ^ tagA) | (v.z ^ tagB); };
    auto again = [&]() { COUNT(20); __builtin_amdgcn_s_sleep(10); gather(); wait_vm<0>(); };
    auto check0 = [&](auto between) {
      u32x4 cv[9];
      auto look = [&]() {
#pragma unroll
        for (int i = 0; i < 8; ++i) cv[i] = ld16(PT + (i >> 2) * HALF + (i & 3) * 1024 + lane * 16);
        cv[8] = ld16(LB + L_BS + lane * 16);
      };
      look(); SCHED(); between(); SCHED();
#pragma unroll 1
      for (int spin = 0; spin < SPIN_CAP; ++spin) {
        unsigned e = 0;
#pragma unroll
        for (int i = 0; i < 8; ++i) e = bad(e, cv[i]);
        if (__builtin_expect(__all(bad(mlp ? e : 0u, cv[8]) == 0), 1)) break;
        again(); look();
      }
      float t[4];
#pragma unroll
      for (int k = 0; k < 4; ++k) {
        const int i = 4 * (k & 1), e = 1 + (k & 2);
        t[k] = sadd(sadd(u2f(cv[i][e]), u2f(cv[i + 1][e])), sadd(u2f(cv[i + 2][e]), u2f(cv[i + 3][e])));
      }
      const u32x2 h0 = swap32(t[0], t[1]), h1 = swap32(t[2], t[3]);
      *(LAS f32x2*)(size_t)(LB + L_CS + lane * 8) = (f32x2){sadd(u2f(h0.x), u2f(h0.y)), sadd(u2f(h1.x), u2f(h1.y))};
      SCHED();
    };
    auto check1 = [&](auto between) {
      u32x4 cv[4];
      auto look = [&]() {
        cv[0] = ld16(LB + L_ST1 + g * 512 + i16 * 16); cv[1] = ld16(LB + L_ST1 + g * 512 + i16 * 16 + 256);
        cv[2] = ld16(LB + L_ST2 + lane * 16); cv[3] = ld16(LB + L_ST2 + lane * 16 + 1024);
      };
      look(); SCHED(); between(); SCHED();
#pragma unroll 1
      for (int spin = 0; spin < SPIN_CAP; ++spin) {
        if (__builtin_expect(__all(bad(bad(bad(bad(0u, cv[0]), cv[1]), cv[2]), cv[3]) == 0), 1)) break;
        again(); look();
      }
      *(LAS u32x4*)(size_t)(LB + L_LS + g * 288 + i16 * 16) = (u32x4){cv[0].y, cv[1].y, cv[0].w, cv[1].w};
      SCHED();
    };
    const unsigned me = PW + w * 4;
    const unsigned co = ntile * 65536 + kg * 128;
    zero2();
    setup2(8);
    auto landed = [&]() { if (mlp) wait_vm<24>(); else wait_vm<16>(); };
    SCHED();
    part0(0, 0);
    if (TOUCH == 4) touch();
    part1(1, SLOT); part0(1, SLOT);
    STAMP(1);
    mktag();
    if (mlp) wait_vm<20 + (TOUCH == 4)>(); else wait_vm<12 + (TOUCH == 4)>();
    STAMP(19);
    bar();
    STAMP(2);
    dma_x4(vx, p.x + (128 * (mtile >> 1) + 8 * (mtile & 1) + 4 * d) * 1024 + ntile * 128 - 4096, home + 12288);
    if (d) dma4w(lane * 4, p.b1 + (mlp ? j : 13) * 256, LB + L_B1);
    part1(2, 2 * SLOT); part0(2, 2 * SLOT); if (TOUCH == 1) touch(); own(0);
    TRACE(1); landed(); TRACE(2); bar(); TRACE(3);
    part1(3, 0); part0(3, 0);
    dma4w((lane < 28 ? lane : 0) * 1024, p.w2 + (2 * idx + d) * 28672 + ((3u - ((unsigned)(size_t)p.w2 >> 7)) & 7u) * 128, LB + L_DUMMY + d * 256);
    own(1);
    TRACE(4); landed(); TRACE(5); bar(); TRACE(6);
    STAMPC(21);
    part1(4, SLOT); take(0); part0(4, SLOT); own(2);
    TRACE(7); landed(); TRACE(8); barrier(); TRACE(9);
    if (GATHER_AT == 3) gather();
    part1(5, 2 * SLOT); take(1); part0(5, 2 * SLOT); own(3);
    TRACE(10); landed(); TRACE(11); barrier(); TRACE(12);
    if (GATHER_AT == 4) gather();
    part1(6, 0); take(2); part0(6, 0); own(4);
    TRACE(13); landed(); TRACE(14); barrier(); TRACE(15);
    part1(7, SLOT);
    if (d) { partx(7, SLOT); check1([&]() { partb(7, SLOT); own(5); }); take(3); }
    else { check0([&]() { partb(7, SLOT); }); partx(7, SLOT); take(3); own(5); }
    TRACE(16); landed(); TRACE(17); barrier(); TRACE(18);
    STAMP(3); STAMPC(22);
    page(0, 0, 2, 0); page(0, 1, 2, 1); take(4); page(0, 2, 2, 2); page(0, 3, 2, 3); page(0, 4, 2, 4); own(6); own(7);
    wait_vm<8>();
    bar();
    STAMP(4);
    page(2, 0, 0, 0); take(5); page(2, 1, 0, 1); page(2, 2, 0, 2); page(2, 3, 0, 3); page(2, 4, 0, 4);
    STAMP(5);
    barrier();
    __builtin_amdgcn_s_setprio(0);
    STAMP(6);
    page(0, 5, 2, 2); page(0, 6, 2, 3); page(0, 7, 2, 4);
    dma_x8(vc, p.cre + ntile * 65536 + ((d + KROT * kgo) & 7) * 128, HB + 2 * SLOT);
    dma_x8(vc, p.cre + co, home);
    page(1, 0, 1, 0); page(1, 1, 1, 1);
    wait_vm<16>(); prog_set(me, 1);
    take(6); page(1, 2, 1, 2); take(7); page(1, 3, 1, 3); page(1, 4, 1, 4);
    wait_lgkm0();
    dma_x8(vc, p.cim + co, home + 8192);
    STAMP(7);
    wait_vm<8>(); prog_set(me, 2);
    STAMP(8);
  };
  auto roleL = [&]() __attribute__((always_inline)) {
    if (TOUCH == 0) touch();
    if (TLBW && d) { dma4w(0, p.cre + ntile * 65536, LB + L_DUMMY + 256); dma4w(0, p.cim + ntile * 65536, LB + L_DUMMY + 256); dma4w((lane & 1) * 0x1bf000, p.w2, LB + L_DUMMY + 256); }
    f32x4 acr[8], aci[8];
#pragma unroll
    for (int m = 0; m < 8; ++m) { acr[m] = (f32x4){0, 0, 0, 0}; aci[m] = (f32x4){0, 0, 0, 0}; asm volatile("" : "+v"(acr[m]), "+v"(aci[m])); }
    unsigned ax[2], ab[2];
    ax[0] = LB + i16 * 128 + kx; ax[1] = ax[0] ^ 64;
    ab[0] = LB + SL_B + (16 * d + i16) * 128 + kx; ab[1] = ab[0] ^ 64;
    unsigned sa = LB + L_LS + g * 288, csa = LB + L_BS + (16 * d + i16) * 16 + 4;
    asm volatile("" : "+v"(sa), "+v"(csa), "+v"(ax[0]), "+v"(ax[1]), "+v"(ab[0]), "+v"(ab[1]));
    s16x8 xf[2][8], bf[2][2];
    auto rd = [&](int set, int i) {
      if (i < 2) bf[set][i] = lds128(ab[set] + i * 4096);
      else xf[set][i - 2] = lds128(ax[set] + (i - 2) * 2048);
    };
    auto mm = [&](int set, int i) {
      if (i & 1) aci[i >> 1] = mfma(xf[set][i >> 1], bf[set][1], aci[i >> 1]);
      else acr[i >> 1] = mfma(xf[set][i >> 1], bf[set][0], acr[i >> 1]);
    };
    asm volatile("" : "+v"(v_lre), "+v"(v_lim), "+v"(v_ldt));
    const float lr = bflo(v_lre), li = bflo(v_lim);
    const float dt = bflo(pk2(expf(bflo(v_ldt)), 0.f));
    const float ang = smul(dt, li);
    const float rv = smul(ang, 0.15915494f);
    const float rev = (rv - __builtin_rintf(rv)) + sfma(ang, 6.4206382e-9f, sfms(ang, 0.15915494f, rv));
    float sn = __builtin_amdgcn_sinf(rev), cn = __builtin_amdgcn_cosf(rev);
    asm volatile("s_nop 0" : "+v"(sn), "+v"(cn));
    const float ex = expf(smul(dt, lr));
    f32x2 a1 = {smul(ex, cn), smul(ex, sn)};
    const float nr = a1[0] - 1.f, ni = a1[1], iden = 2.f / sfma(lr, lr, smul(li, li));
    f32x2 kk = {smul(sfma(nr, lr, smul(ni, li)), iden), smul(sfms(ni, lr, smul(nr, li)), iden)};
    f32x2 t = cmul(a1, a1); t = cmul(t, t); t = cmul(t, t);
    f32x2 a16 = cmul(t, t);
    f32x2 a32 = cmul(a16, a16);
    const f32x2 a64 = cmul(a32, a32), a128 = cmul(a64, a64), a256 = cmul(a128, a128);
    f32x2 a512 = cmul(a256, a256);
    const f32x2 one = {1.f, 0.f};
    f32x2 aseg = cmul(kk, cmul(csel(g & 1, a32, one), csel(g & 2, a64, one)));
    const int e4 = (r - 1 - g) & 3;
    f32x2 acar = cmul(csel(e4 & 1, a128, one), csel(e4 & 2, a256, one));
    const f32x2 ar = bco(a1[0]), ai = bco(a1[1]);
    asm volatile("" :: "v"(ar), "v"(ai));
    asm volatile("" : "+v"(kk), "+v"(a16), "+v"(a32), "+v"(a512), "+v"(aseg), "+v"(acar));
    mktag();
    __builtin_amdgcn_s_setprio(1);
    STAMP(1);
    bar();
    STAMP(2); TRACE3(1);
#pragma unroll
    for (int i = 0; i < 10; ++i) rd(0, i);
    wait_lgkm0();
    auto stage1 = [&](int dl) {
      SCHED();
#pragma unroll
      for (int i = 0; i < 16; ++i) {
        mm(0, i); SCHED();
        if (i < 10) rd(1, i);
        if (i == 11) { adv(ax[0], dl); adv(ab[0], dl); }
        SCHED();
      }
    };
    stage1(SLOT);
    barrier();
    TRACE3(2);
    int ph = 0;
#pragma unroll 1
    for (int c = 2; c <= 8; ++c) {
      const int dl = ph == 2 ? -2 * (int)SLOT : (int)SLOT;
      ph = ph == 2 ? 0 : ph + 1;
      SCHED();
#pragma unroll
      for (int i = 0; i < 16; ++i) {
        mm(1, i); SCHED();
        if (i < 10) rd(0, i);
        if (i == 11) { adv(ax[1], dl); adv(ab[1], dl); }
        SCHED();
      }
      wait_lgkm0();
      stage1(ph == 2 ? -2 * (int)SLOT : (int)SLOT);
      TRACE(2 * c);
      barrier();
      TRACE(2 * c + 1); TRACE3(c + 1);
    }
    __builtin_amdgcn_s_setprio(LPRIO);
    STAMP(4);
    f32x2 rs[16], mrs[16];
    float csr, csi;
#pragma unroll
    for (int i = 0; i < 16; ++i) {
      mm(1, i); SCHED();
      if (i == 0) { csr = ldsf1(csa); csi = ldsf1(csa + 512); }
      const f32x4 t = ldsf4(sa + i * 16);
      rs[i] = lo2(t); mrs[i] = hi2(t);
      if ((i & 3) == 3) TRACE2(20 + (i >> 2));
      SCHED();
    }
    const f32x2 ncr = bco(-csr), nci = bco(-csi);
    STAMP(5);
    f32x2 ur[16], ui[16], Er, Ei;
    SCHED();
#pragma unroll
    for (int s = 0; s < 16; ++s) {
      ur[s] = mrs[s] * ncr; SCHED();
      ui[s] = mrs[s] * nci; SCHED();
      ur[s] = pfma((s & 1) ? hi2(acr[s >> 1]) : lo2(acr[s >> 1]), rs[s], ur[s]); SCHED();
      ui[s] = pfma((s & 1) ? hi2(aci[s >> 1]) : lo2(aci[s >> 1]), rs[s], ui[s]); SCHED();
      if (s == 0) { Er = ur[0]; Ei = ui[0]; }
      else {
        const f32x2 t0 = pfma(Ei, -ai, ur[s]); SCHED();
        const f32x2 t1 = pfma(Er, ai, ui[s]); SCHED();
        Er = pfma(Er, ar, t0); SCHED();
        Ei = pfma(Ei, ar, t1); SCHED();
      }
    }
    TRACE2(1);
    const f32x2 E0 = {Er[0], Ei[0]};
    const f32x2 E = cfma(a16, E0, (f32x2){Er[1], Ei[1]});
    f32x2 P = {0.f, 0.f}, Tt = {0.f, 0.f};
    float er4[4], ei4[4];
    rows4(E[0], er4); rows4(E[1], ei4);
#pragma unroll
    for (int s = 0; s < 4; ++s) {
      const f32x2 e = {er4[s], ei4[s]};
      P = csel(s == g, Tt, P);
      Tt = s ? cfma(a32, Tt, e) : e;
    }
    const unsigned totb = WS_TOT + (unsigned)(j * 2 + d) * 4096;
    if (g == 0) __builtin_amdgcn_raw_buffer_store_b128((u32x4){tagA, f2u(Tt[0]), tagB, f2u(Tt[1])}, wsr, totb + r * 256 + i16 * 16, 0, 16);
    STAMP(6);
    {
      const f32x2 kr = bco(kk[0]), nki = bco(-kk[1]);
#pragma unroll
      for (int s = 0; s < 16; ++s) {
        const f32x2 t0 = ur[s] * kr; SCHED();
        const f32x2 t1 = ur[s] * nki; SCHED();
        ur[s] = pfma(ui[s], nki, t0); SCHED();
        ui[s] = pfma(ui[s], -kr, t1); SCHED();
      }
    }
#pragma unroll
    for (int s = 0; s < 16; s += 4) asm volatile("" : "+v"(ur[s]), "+v"(ui[s]), "+v"(ur[s + 1]), "+v"(ui[s + 1]), "+v"(ur[s + 2]), "+v"(ui[s + 2]), "+v"(ur[s + 3]), "+v"(ui[s + 3]));
    f32x2 kP = cmul(kk, P), kE = cmul(kk, E0);
    asm volatile("" : "+v"(kP), "+v"(kE));
    home = HB;
    TRACE2(2);
    prog_wait(PW + (6 + d) * 4, 1);
#pragma unroll
    for (int i = 0; i < 5; ++i) absorb(i, home + i * 4096);
    unsigned vc[8], v2[4], vt[4];
    mkvc(vc); mkv2(v2);
#pragma unroll
    for (int i = 0; i < 4; ++i) { vt[i] = (4 * i + g < r ? i * 1024 + g * 256 : 0) + i16 * 16; asm volatile("" : "+v"(vt[i]), "+v"(v2[i]), "+v"(vc[i]), "+v"(vc[i + 4])); }
    setup2(6);
    TRACE2(3);
    {
      const i32x4 tr = {wss[0], wss[1], (int)(totb + r * 256), wss[3]};
#pragma unroll 1
      for (int spin = 0; spin < 64; ++spin) {
        int n, t[15];
        asm volatile("s_buffer_load_dword %1, %16, %17 offset:0 glc\n\ts_buffer_load_dword %2, %16, %17 offset:256 glc\n\ts_buffer_load_dword %3, %16, %17 offset:512 glc\n\ts_buffer_load_dword %4, %16, %17 offset:768 glc\n\ts_buffer_load_dword %5, %16, %17 offset:1024 glc\n\ts_buffer_load_dword %6, %16, %17 offset:1280 glc\n\ts_buffer_load_dword %7, %16, %17 offset:1536 glc\n\ts_buffer_load_dword %8, %16, %17 offset:1792 glc\n\ts_buffer_load_dword %9, %16, %17 offset:2048 glc\n\ts_buffer_load_dword %10, %16, %17 offset:2304 glc\n\ts_buffer_load_dword %11, %16, %17 offset:2560 glc\n\ts_buffer_load_dword %12, %16, %17 offset:2816 glc\n\ts_buffer_load_dword %13, %16, %17 offset:3072 glc\n\ts_buffer_load_dword %14, %16, %17 offset:3328 glc\n\ts_buffer_load_dword %15, %16, %17 offset:3584 glc\n\t"
                     "s_mov_b32 %0, 0\n\ts_waitcnt lgkmcnt(0)\n\t"
                     "s_cmp_eq_u32 %1, %18\n\ts_addc_u32 %0, %0, 0\n\ts_cmp_eq_u32 %2, %18\n\ts_addc_u32 %0, %0, 0\n\ts_cmp_eq_u32 %3, %18\n\ts_addc_u32 %0, %0, 0\n\ts_cmp_eq_u32 %4, %18\n\ts_addc_u32 %0, %0, 0\n\ts_cmp_eq_u32 %5, %18\n\ts_addc_u32 %0, %0, 0\n\ts_cmp_eq_u32 %6, %18\n\ts_addc_u32 %0, %0, 0\n\ts_cmp_eq_u32 %7, %18\n\ts_addc_u32 %0, %0, 0\n\ts_cmp_eq_u32 %8, %18\n\ts_addc_u32 %0, %0, 0\n\ts_cmp_eq_u32 %9, %18\n\ts_addc_u32 %0, %0, 0\n\ts_cmp_eq_u32 %10, %18\n\ts_addc_u32 %0, %0, 0\n\ts_cmp_eq_u32 %11, %18\n\ts_addc_u32 %0, %0, 0\n\ts_cmp_eq_u32 %12, %18\n\ts_addc_u32 %0, %0, 0\n\ts_cmp_eq_u32 %13, %18\n\ts_addc_u32 %0, %0, 0\n\ts_cmp_eq_u32 %14, %18\n\ts_addc_u32 %0, %0, 0\n\ts_cmp_eq_u32 %15, %18\n\ts_addc_u32 %0, %0, 0"
                     : "=&s"(n), "=&s"(t[0]), "=&s"(t[1]), "=&s"(t[2]), "=&s"(t[3]), "=&s"(t[4]), "=&s"(t[5]), "=&s"(t[6]), "=&s"(t[7]), "=&s"(t[8]), "=&s"(t[9]), "=&s"(t[10]), "=&s"(t[11]), "=&s"(t[12]), "=&s"(t[13]), "=&s"(t[14]) : "s"(tr), "s"(totb), "s"(tagA) : "scc", "memory");
        if (n >= r) break;
        COUNT(20);
      }
    }
    STAMP(7);
    f32x2 car = {0.f, 0.f};
    auto fetch = [&]() {
#pragma unroll
      for (int i = 0; i < 4; ++i)
        if (4 * i < r) dma1_dev(vt[i], p.ws + totb, home + 16384 + i * 1024);
    };
    fetch();
    loadC(vc, kg, home);
    wait_vm<16>();
    if (r > 0) {
      u32x4 tv[4];
#pragma unroll 1
      for (int spin = 0; spin < SPIN_CAP; ++spin) {
        bool ok = true;
#pragma unroll
        for (int i = 0; i < 4; ++i) {
          tv[i] = *(volatile LAS u32x4*)(size_t)(home + 16384 + i * 1024 + lane * 16);
          ok &= (4 * i + g >= r) | ((tv[i].x == tagA) & (tv[i].z == tagB));
        }
        if (__all(ok)) break;
        COUNT(20);
        __builtin_amdgcn_s_sleep(8);
        fetch();
        wait_vm<0>();
      }
#pragma unroll
      for (int i = 0; i < 4; ++i) car = csel(4 * i + g < r, cfma(a512, car, (f32x2){u2f(tv[i].y), u2f(tv[i].w)}), car);
      car = cmul(car, acar);
      float cr4[4], ci4[4];
      rows4(car[0], cr4); rows4(car[1], ci4);
      car = (f32x2){(cr4[0] + cr4[1]) + (cr4[2] + cr4[3]), (ci4[0] + ci4[1]) + (ci4[2] + ci4[3])};
    }
    dma_x4(v2, w2b + kstep_of(slot, 5) * 32768, home + 16384);
    STAMP(8);
    {
      const f32x2 h0 = cfma(aseg, car, kP), h1 = cfma(a16, h0, kE);
      f32x2 hr = {h0[0], h1[0]}, hi = {-h0[1], -h1[1]};
      const unsigned on = (qq == g) ? ((pp & 1) ? 0x3f800000u : 0x3f80u) : 0u;
      const u32x4 z0 = {(pp & 2) ? 0u : on, (pp & 2) ? on : 0u, 0u, 0u}, z1 = {0u, 0u, z0.x, z0.y};
      const s16x8 sel0 = __builtin_bit_cast(s16x8, z0), sel1 = __builtin_bit_cast(s16x8, z1);
      const unsigned ho = WS_H + (unsigned)(2 * r) * H_MT + (unsigned)(56 + 2 * j + d) * 4096 + lane * 16;
      u32x4 fr[4], fi[4];
      f32x4 tr[2], ti[2];
      const f32x4 zz = {0, 0, 0, 0};
      auto put = [&](int mp, int k) {
        const u32x4 o = {pk2(tr[k][0], tr[k][1]), pk2(tr[k][2], tr[k][3]), pk2(ti[k][0], ti[k][1]), pk2(ti[k][2], ti[k][3])};
        __builtin_amdgcn_raw_buffer_store_b128(o, wsr, ho + (mp >> 1) * H_MT + (2 * (mp & 1) + k) * 1024, 0, 16);
        SCHED();
      };
      SCHED();
#pragma unroll
      for (int mp = 0; mp < 5; ++mp) {
        if (mp > 0) {
#pragma unroll
          for (int k = 0; k < 2; ++k) {
            tr[k] = mfma(__builtin_bit_cast(s16x8, fr[mp - 1]), k ? sel1 : sel0, zz);
            ti[k] = mfma(__builtin_bit_cast(s16x8, fi[mp - 1]), k ? sel1 : sel0, zz);
          }
          SCHED();
        }
        if (mp < 4) {
#pragma unroll
          for (int e = 0; e < 4; ++e) {
            const int s = 4 * mp + e;
            const f32x2 t1 = pfma(hr, -ai, ui[s]); SCHED();
            const f32x2 t0 = pfma(hi, ai, ur[s]); SCHED();
            hi = pfma(hi, ar, t1); SCHED();
            hr = pfma(hr, ar, t0); SCHED();
            fi[mp][e] = pk2(hi[0], hi[1]); SCHED();
            fr[mp][e] = pk2(hr[0], hr[1]); SCHED();
          }
        }
        TRACE2(4 + 2 * mp);
        if (mp > 0) { put(mp - 1, 0); put(mp - 1, 1); }
        TRACE2(5 + 2 * mp);
      }
      STAMP(19);
      wait_vm<0>();
      if (lane < 2) __builtin_amdgcn_raw_buffer_store_b64((u32x2){tagA, tagB}, wsr, WS_FLAG + (2 * r + lane) * 2048 + 1024 + (2 * j + d) * 8, 0, 16);
    }
    __builtin_amdgcn_s_setprio(0);
    STAMP(18);
  };
  auto roleB = [&]() __attribute__((always_inline)) {
    home = HB + SLOT;
    mktag();
    {
      float s0 = 0.f, s1 = 0.f;
#pragma unroll
      for (int e = 0; e < 8; ++e) { s0 = sadd(s0, bflo(prb[e >> 2][e & 3])); s1 = sadd(s1, bfhi(prb[e >> 2][e & 3])); }
      *(LAS float*)(size_t)(LB + L_LS + 512 + (4 * d + g) * 4) = row16_sum(s0 + s1);
    }
    if (mlp) {
      f32x4 acc[6][4];
#pragma unroll
      for (int s = 0; s < 6; ++s) {
#pragma unroll
        for (int m = 0; m < 4; ++m) acc[s][m] = (f32x4){0, 0, 0, 0};
        asm volatile("" : "+v"(acc[s][0]), "+v"(acc[s][1]), "+v"(acc[s][2]), "+v"(acc[s][3]));
      }
      unsigned ax[2], aw[6];
      ax[0] = LB + (64 * d + i16) * 128 + kx; ax[1] = ax[0] ^ 64;
#pragma unroll
      for (int s = 0; s < 6; ++s) aw[s] = awb + ((s ^ awx) * 32);
      unsigned sta = LB + L_LS + qq * 288 + (8 * d + ((i16 >> 1) & 1)) * 16 + (i16 & 1) * 4, csa = LB + L_CS + g * 16;
      asm volatile("" : "+v"(ax[0]), "+v"(ax[1]), "+v"(aw[0]), "+v"(aw[1]), "+v"(aw[2]), "+v"(aw[3]), "+v"(aw[4]), "+v"(aw[5]), "+v"(sta), "+v"(csa));
      s16x8 xf[2][4];
      s16x4 wf[2][12];
      auto rd = [&](int set, int i) {
        if (i < 4) xf[set][i] = lds128(ax[set] + i * 2048);
        else wf[set][i - 4] = ldstr4(aw[(i - 4) >> 1] + set * 8192 + (i & 1) * 1024);
      };
      auto mm = [&](int set, int i) { const int s = i >> 2, m = i & 3; acc[s][m] = mfma(cat(wf[set][2 * s], wf[set][2 * s + 1]), xf[set][m], acc[s][m]); };
      {
        float t[8];
#pragma unroll
        for (int e = 0; e < 8; ++e)
          t[e] = (e & 1) ? sadd(sadd(bfhi(pre[0][e >> 1]), bfhi(pre[1][e >> 1])), sadd(bfhi(pre[2][e >> 1]), bfhi(pre[3][e >> 1])))
                         : sadd(sadd(bflo(pre[0][e >> 1]), bflo(pre[1][e >> 1])), sadd(bflo(pre[2][e >> 1]), bflo(pre[3][e >> 1])));
#pragma unroll
        for (int e = 0; e < 4; ++e) { const u32x2 h = swap32(t[e], t[e + 4]); t[e] = sadd(u2f(h.x), u2f(h.y)); }
#pragma unroll
        for (int e = 0; e < 2; ++e) { const u32x2 h = swap16(t[e], t[e + 2]); t[e] = dpp_add<0x128>(sadd(u2f(h.x), u2f(h.y))); }
        *(LAS f32x2*)(size_t)(LB + L_LS + d * 256 + (4 * (lane & 7) + g) * 8) = (f32x2){t[0], t[1]};
      }
      __builtin_amdgcn_s_setprio(1);
      STAMP(1);
      barrier();
      STAMP(2); STAMPC(21);
#pragma unroll
      for (int i = 0; i < 16; ++i) rd(0, i);
      wait_lgkm0();
      auto stage1 = [&](int c, int dl) {
        SCHED();
#pragma unroll
        for (int i = 0; i < 24; ++i) {
          mm(0, i); SCHED();
          if (i < 16) rd(1, i);
          else if (i < 22) adv(aw[i - 16], dl);
          else if (i == 22) adv(ax[0], dl);
          SCHED();
        }
        TRACE(2 * c);
        barrier();
        TRACE(2 * c + 1);
        SCHED();
      };
      int ph = 0;
#pragma unroll 1
      for (int c = 1; c < 8; ++c) {
        const int dl = ph == 2 ? -2 * (int)SLOT : (int)SLOT;
        ph = ph == 2 ? 0 : ph + 1;
        stage1(c, dl);
#pragma unroll
        for (int i = 0; i < 24; ++i) {
          mm(1, i); SCHED();
          if (i < 16) rd(0, i);
          else if (i == 16) adv(ax[1], dl);
          else if (i == 23) wait_lgkm0();
          SCHED();
        }
      }
      stage1(8, 0);
      unsigned b1a = LB + L_B1 + g * 8;
      asm volatile("" : "+v"(b1a));
      STAMP(4); STAMPC(22);
      f32x2 rs[4], nm[4];
      f32x4 cs[6];
      u32x2 b1v[6];
      f32x2 st[4];
      auto colrd = [&](int s) { cs[s] = ldsf4(csa + 64 * s); b1v[s] = *(LAS u32x2*)(size_t)(b1a + 32 * s); };
#pragma unroll
      for (int i = 0; i < 4; ++i) st[i] = (f32x2){ldsf1(sta + i * 32), ldsf1(sta + i * 32 + 8)};
      colrd(0); colrd(1);
      SCHED();
#pragma unroll
      for (int i = 0; i < BSPLIT; ++i) mm(1, i);
      SCHED();
#pragma unroll
      for (int i = 0; i < 4; ++i) { rs[i] = bco(st[i][0]); nm[i] = bco(-st[i][1]); }
      STAMP(5);
      const unsigned fo = WS_FLAG + (2 * r + d) * 2048;
      const unsigned ho = WS_H + (unsigned)(2 * r + d) * H_MT + (unsigned)(4 * j) * 4096 + lane * 16;
#pragma unroll
      for (int pr = 0; pr < 3; ++pr) {
        Col col;
        mkcol(col, cs[2 * pr], cs[2 * pr + 1], b1v[2 * pr], b1v[2 * pr + 1]);
        if (pr < 2) { colrd(2 * pr + 2); colrd(2 * pr + 3); }
        SCHED();
        if (pr == 1) {
#pragma unroll
          for (int i = BSPLIT; i < 24; ++i) mm(1, i);
          SCHED();
        }
#pragma unroll
        for (int m = 0; m < 4; ++m) {
          __builtin_amdgcn_raw_buffer_store_b128(gelu8(acc[2 * pr][m], acc[2 * pr + 1][m], col, rs[m], nm[m]), wsr, ho + pr * 4096 + m * 1024, 0, 16);
          TRACE2(1 + 4 * pr + m);
          if (pr > 0 && m == BFLAG) {
            if (BFLAG == 3 && pr == 2) wait_vm<5>(); else wait_vm<BFLAG + 1>();
            if (lane == 0) __builtin_amdgcn_raw_buffer_store_b64((u32x2){tagA, tagB}, wsr, fo + flag_of(j, pr - 1), 0, 16);
          }
          SCHED();
        }
      }
      STAMP(6);
      prog_wait(PW + (6 + d) * 4, 2);
      STAMP(7);
      second_round();
      setup2(6);
      wait_vm<(BFLAG == 3 ? 21 : 20)>();
      if (lane == 0) __builtin_amdgcn_raw_buffer_store_b64((u32x2){tagA, tagB}, wsr, fo + flag_of(j, 2), 0, 16);
      STAMP(8);
      __builtin_amdgcn_s_setprio(0);
    } else {
#pragma unroll 1
      for (int c = 0; c < 9; ++c) barrier();
      prog_wait(PW + (6 + d) * 4, 2);
      second_round();
      setup2(6);
    }
  };
  auto roleA = [&]() __attribute__((always_inline)) {
    home = HB + 2 * SLOT;
    const int srow = 128 * r + 8 * j + 4 * d + g;
    u32x2 dv[4], bv[4];
#pragma unroll
    for (int n = 0; n < 4; ++n) { dv[n] = *(const GAS u32x2*)(p.dsk + ntile * 128 + n * 32 + g * 8); bv[n] = *(const GAS u32x2*)(p.b2 + ntile * 128 + n * 32 + g * 8); }
    if (tid < 16) *(LAS unsigned*)(size_t)(PW + tid * 4) = 0u;
    f32x4 acc[2][4];
#pragma unroll
    for (int m = 0; m < 4; ++m) { acc[0][m] = (f32x4){0, 0, 0, 0}; acc[1][m] = (f32x4){0, 0, 0, 0}; asm volatile("" : "+v"(acc[0][m]), "+v"(acc[1][m])); }
    unsigned ax0 = LB + (64 * d + i16) * 128 + kx, ax1 = ax0 ^ 64, aw0 = awb + ((6 ^ awx) * 32), aw1 = awb + ((7 ^ awx) * 32);
    unsigned sta = LB + L_LS + qq * 288 + (8 * d + ((i16 >> 1) & 1)) * 16 + (i16 & 1) * 4, csa = LB + L_CS + 384 + g * 16;
    asm volatile("" : "+v"(ax0), "+v"(ax1), "+v"(aw0), "+v"(aw1), "+v"(sta), "+v"(csa));
    int ph = 0;
    auto chunk = [&](auto side) {
      s16x8 xf[2][4], wf[2][2];
      if (ADELAY) __builtin_amdgcn_s_sleep(ADELAY);
#pragma unroll
      for (int k = 0; k < 2; ++k) {
        wf[k][0] = ldstr(aw0 + k * 8192, aw0 + k * 8192 + 1024);
        wf[k][1] = ldstr(aw1 + k * 8192, aw1 + k * 8192 + 1024);
#pragma unroll
        for (int m = 0; m < 4; ++m) xf[k][m] = lds128((k ? ax1 : ax0) + m * 2048);
      }
      SCHED();
      const int dl = ph == 2 ? -2 * (int)SLOT : (int)SLOT;
      ph = ph == 2 ? 0 : ph + 1;
      adv(ax0, dl); adv(ax1, dl); adv(aw0, dl); adv(aw1, dl);
      side(8);
      SCHED();
#pragma unroll
      for (int k = 0; k < 2; ++k)
#pragma unroll
        for (int m = 0; m < 4; ++m) {
          acc[0][m] = mfma(wf[k][0], xf[k][m], acc[0][m]); acc[1][m] = mfma(wf[k][1], xf[k][m], acc[1][m]);
          SCHED();
          side(4 * k + m);
          SCHED();
        }
    };
    auto none = [](int) {};
    mktag();
    asm volatile("" : "+v"(pre[3]));
    STAMP(19);
    {
      const float mean = sum32(pre) * (1.f / 512.f);
      float v0 = 0.f, v1 = 0.f;
#pragma unroll
      for (int i = 0; i < 4; ++i)
#pragma unroll
        for (int e = 0; e < 4; ++e) {
          const float d0 = bflo(pre[i][e]) - mean, d1 = bfhi(pre[i][e]) - mean;
          v0 = sfma(d0, d0, v0); v1 = sfma(d1, d1, v1);
        }
      const float rsd = __builtin_amdgcn_rsqf(row16_sum(v0 + v1) * (1.f / 512.f) + 1e-5f);
      if (i16 == 0) __builtin_amdgcn_raw_buffer_store_b128((u32x4){tagA, f2u(rsd), tagB, f2u(mean * rsd)}, wsr, WS_STAT + srow * 16, 0, 16);
    }
    __builtin_amdgcn_s_setprio(APRIO);
    STAMP(1);
    barrier();
    STAMP(2);
    chunk(none);
    if (d) {
      const f32x2 c0 = ldsf2(LB + L_LS + 512 + (lane & 3) * 8);
      if (lane < 4) __builtin_amdgcn_raw_buffer_store_b128((u32x4){tagA, f2u(sadd(c0[0], c0[1])), tagB, 0u}, wsr, WS_BS + j * 1024 + (4 * r + lane) * 16, 0, 16);
    } else if (mlp) {
      const f32x2 c0 = ldsf2(LB + L_LS + (lane & 31) * 8), c1 = ldsf2(LB + L_LS + 256 + (lane & 31) * 8);
      if (lane < 32) __builtin_amdgcn_raw_buffer_store_b128((u32x4){tagA, f2u(sadd(c0[0], c1[0])), tagB, f2u(sadd(c0[1], c1[1]))}, wsr, WS_CS + (j * 16 + r) * 512 + lane * 16, 0, 16);
    }
    TRACE(2); barrier(); TRACE(3);
    chunk(none);
    asm volatile("" : "+v"(dv[0]), "+v"(dv[1]), "+v"(dv[2]), "+v"(dv[3]), "+v"(bv[0]), "+v"(bv[1]), "+v"(bv[2]), "+v"(bv[3]));
    u32x2 xv[2][4];
#pragma unroll
    for (int m = 0; m < 2; ++m)
#pragma unroll
      for (int n = 0; n < 4; ++n) xv[m][n] = *(LAS u32x2*)(size_t)(HB + 3 * SLOT + 12288 + (16 * m + i16) * 128 + (g & 1) * 8 + (((2 * n + (g >> 1)) ^ (i16 >> 1)) * 16));
    TRACE(4); barrier(); TRACE(5);
#pragma unroll 1
    for (int c = 2; c < 6; ++c) { chunk(none); TRACE(2 * c + 2); barrier(); TRACE(2 * c + 3); }
    f32x2 st[4];
#pragma unroll
    for (int m = 0; m < 4; ++m) st[m] = (f32x2){ldsf1(sta + m * 32), ldsf1(sta + m * 32 + 8)};
    unsigned ra[4], vc[8], ho = mlp ? WS_H + (unsigned)(2 * r + d) * H_MT + (unsigned)(4 * j + 3) * 4096 + lane * 16 : 0xffff0000u;
    f32x2 rs[4], nm[4];
    chunk([&](int k) {
      if (k < 4) { rs[k] = bco(st[k][0]); nm[k] = bco(-st[k][1]); asm volatile("" : "+v"(rs[k]), "+v"(nm[k])); }
      if (k < 8) return;
#pragma unroll
      for (int n = 0; n < 4; ++n) ra[n] = home + r2l + ((n ^ r2x) * 32);
      asm volatile("" : "+v"(ra[0]), "+v"(ra[1]), "+v"(ra[2]), "+v"(ra[3]), "+v"(ho));
    });
    TRACE(14); barrier(); TRACE(15);
    STAMP(4);
    const f32x4 cs0 = ldsf4(csa), cs1 = ldsf4(csa + 64);
    const u32x2 b10 = *(LAS u32x2*)(size_t)(LB + L_B1 + 192 + g * 8), b11 = *(LAS u32x2*)(size_t)(LB + L_B1 + 224 + g * 8);
    Col col;
    auto take = [&](int i, int pg) {
#pragma unroll
      for (int n = 0; n < 4; ++n) we[i][n] = ldstr(ra[n] + pg * 4096, ra[n] + pg * 4096 + 2048);
    };
    chunk([&](int k) {
      if (k == 2) { mkcol(col, cs0, cs1, b10, b11); asm volatile("" : "+v"(col.bias[0]), "+v"(col.bias[1]), "+v"(col.bias[2]), "+v"(col.bias[3])); }
      if (k >= 3 && k < 8) take(k - 3, k - 3);
    });
    if (ABAR == 0) barrier();
    __builtin_amdgcn_s_setprio(2);
    STAMP(5);
#pragma unroll
    for (int m = 0; m < 4; ++m) {
      TRACE2(1 + 2 * m);
      const u32x4 o = gelu8(acc[0][m], acc[1][m], col, rs[m], nm[m]);
      TRACE2(2 + 2 * m);
      __builtin_amdgcn_raw_buffer_store_b128(o, wsr, ho + m * 1024, 0, 16);
      if (m + 1 == ABAR) barrier();
      SCHED();
    }
    STAMP(6);
#pragma unroll
    for (int m = 0; m < 2; ++m) {
      const u32x4 s2 = *(LAS u32x4*)(size_t)(LB + L_ST2 + (tok + 8 * (mtile & 1) + 4 * d + 2 * m) * 16);
      const f32x2 r2 = bco(u2f(s2.y)), n2 = bco(-u2f(s2.w));
#pragma unroll
      for (int n = 0; n < 4; ++n) {
        f32x2 o[2];
#pragma unroll
        for (int h = 0; h < 2; ++h) {
          const f32x2 xx = {bflo(xv[m][n][h]), bfhi(xv[m][n][h])};
          o[h] = pfma(pfma(xx, r2, n2), (f32x2){bflo(dv[n][h]), bfhi(dv[n][h])}, xx + (f32x2){bflo(bv[n][h]), bfhi(bv[n][h])});
        }
        acc2[n][m] = __builtin_shufflevector(o[0], o[1], 0, 1, 2, 3);
        acc2[n][2 + m] = (f32x4){0, 0, 0, 0};
        asm volatile("" : "+v"(acc2[n][m]), "+v"(acc2[n][2 + m]));
      }
    }
    STAMP(7);
    wait_vm<0>();
    if (mlp && lane == 0) __builtin_amdgcn_raw_buffer_store_b64((u32x2){tagA, tagB}, wsr, WS_FLAG + (2 * r + d) * 2048 + flag_of(j, 3), 0, 16);
    STAMP(18);
    mkvc(vc);
    setup2(8);
    prog_wait(PW + (6 + d) * 4, 1);
#pragma unroll
    for (int i = 5; i < 8; ++i) take(i, i - 3);
    wait_lgkm0();
    dma_x8(vc, p.cim + ntile * 65536 + kg * 128, home + 8192);
    STAMP(8);
    __builtin_amdgcn_s_setprio(0);
  };

  auto phase2 = [&](auto nm_, auto dep_) __attribute__((always_inline)) {
    STAMP(9);
    constexpr int NM = decltype(nm_)::value, N = NM + 4, DEP = decltype(dep_)::value;
    const int fm = WS_FLAG + mtile * 2048 + slot * 128, fs = WS_FLAG + mtile * 2048 + 1024 + kg * 32;
    const u64 tag = ((u64)tagB << 32) | tagA;
    u64x8 flm;
    u64x4 fls;
    auto snap = [&](bool ssm) { if (ssm) fls = __builtin_bit_cast(u64x4, sbl8(wss, fresh(fs), 1)); else flm = __builtin_bit_cast(u64x8, sbl16(wss, fresh(fm), 1)); };
    auto has = [&](int k) { return (k < NM ? flm[k < NM ? k : 0] : fls[k < NM ? 0 : k - NM]) == tag; };
    auto need = [&](int k, bool ok) {
      if (__builtin_expect(!ok, 0)) {
#pragma unroll 1
        for (int spin = 1; spin < SPIN_CAP; ++spin) {
          snap(k >= NM);
          if (has(k)) break;
          if (!(spin & 31)) {
            const u32x2 f = __builtin_amdgcn_raw_buffer_load_b64(wsr, 0, k < NM ? fm + 8 * k : fs + 8 * (k - NM), 16);
            if (__builtin_amdgcn_readfirstlane(f.x) == tagA && __builtin_amdgcn_readfirstlane(f.y) == tagB) break;
          }
          __builtin_amdgcn_s_sleep(PSLEEP);
        }
      }
      asm volatile("" ::: "memory");
    };
    STAMP(10);
    snap(false);
    auto loadH = [&](int i, int m) {
      if (m == 0) asm volatile("" : "+v"(hb));
      return __builtin_amdgcn_raw_buffer_load_b128(wsr, hb + (m & 1) * 1024, __builtin_amdgcn_readlane(ksl, i) ^ ((m & 2) * 1024), 16);
    };
    u32x4 hf[DEP][4];
#pragma unroll
    for (int i = 0; i < DEP; ++i) {
      need(i, has(i));
#pragma unroll
      for (int m = 0; m < 4; ++m) hf[i][m] = loadH(i, m);
    }
    STAMP(11);
    s16x8 cfr[2][4];
    bool okn = false;
    SCHED();
#pragma unroll
    for (int i = 0; i < N; ++i) {
      if (i == 1) STAMP(12);
      if (i + DEP == NM) STAMP(13);
      if (i + DEP < N) need(i + DEP, (i & 1) && i + DEP < NM ? okn : has(i + DEP));
      if (i + DEP == NM) STAMP(14);
      if (!(i & 1) && i + DEP + 1 < NM) {
        okn = has(i + DEP + 1);
        if (i + DEP + 2 < NM) snap(false);
      }
      if (i + DEP + 2 == NM) snap(true);
      if (NM == 6 && i == 3) absorb(5, home + 16384);
      if (i >= NM - 1 && i < N - 1) {
#pragma unroll
        for (int n = 0; n < 4; ++n) {
          const unsigned a = (crd ^ (32 * (i - NM + 1))) + n * 2048;
          cfr[(i + 1) & 1][n] = cat(*(LAS s16x4*)(size_t)a, *(LAS s16x4*)(size_t)(a + 8192));
        }
      }
      SCHED();
#pragma unroll
      for (int m = 0; m < 4; ++m) {
#pragma unroll
        for (int n = 0; n < 4; ++n) acc2[n][m] = mfma(i < NM ? we[i < NM ? i : 0][n] : cfr[i & 1][n], __builtin_bit_cast(s16x8, hf[i % DEP][m]), NM == 6 && i == 0 ? (f32x4){0, 0, 0, 0} : acc2[n][m]);
        SCHED();
        if (i + DEP < N) hf[i % DEP][m] = loadH(i + DEP, m);
        SCHED();
      }
    }
  };
  if (__builtin_expect(w >= 6 || w < 2, 1)) {
    if (__builtin_expect(w >= 6, 1)) roleD(); else roleA();
    phase2(std::integral_constant<int, 8>{}, std::integral_constant<int, DEPE>{});
  } else {
    if (w >= 4) roleL(); else roleB();
    phase2(std::integral_constant<int, 6>{}, std::integral_constant<int, DEPL>{});
  }
  STAMP(15);
  int tl = threadIdx.x;
  asm volatile("" : "+v"(tl));
  const int i16t = tl & 15, gt = (tl >> 4) & 3;
#pragma unroll
  for (int m = 0; m < 4; ++m)
#pragma unroll
    for (int n = 0; n < 4; ++n) *(LAS f32x4*)(size_t)(home + ((16 * m + i16t) ^ (w == 1 ? 32 : 0)) * 256 + (((4 * n + gt) ^ i16t) * 16)) = acc2[n][m];
  const int ck = tl & 15;
  const int orow = 32 * ((tl >> 6) & 3) + 16 * ((tl >> 4) & 1) + ((tl >> 5) & 1) + 2 * (tl >> 8) + 8 * (mtile & 1);
  char* op = p.out + (128 * (mtile >> 1) + orow) * 2048 + ntile * 256 + ck * 16;
  unsigned ra[4];
#pragma unroll
  for (int k = 0; k < 4; ++k) ra[k] = LB + (tl >> 4) * 256 + ((ck ^ ((tl >> 4) & 15)) * 16) + k * SLOT;
  asm volatile("" : "+v"(ra[0]), "+v"(ra[1]), "+v"(ra[2]), "+v"(ra[3]), "+v"(op));
  barrier();
  STAMP(16);
  {
    f32x4 pt[2][8];
#pragma unroll
    for (int h = 0; h < 2; ++h)
#pragma unroll
      for (int k = 0; k < 8; ++k) pt[h][k] = ldsf4(ra[k >> 1] + (k & 1) * HALF + h * 8192);
#pragma unroll
    for (int h = 0; h < 2; ++h) {
      const f32x4 s = ((pt[h][0] + pt[h][1]) + (pt[h][2] + pt[h][3])) + ((pt[h][4] + pt[h][5]) + (pt[h][6] + pt[h][7]));
      __builtin_nontemporal_store(s, (GAS f32x4*)(op + h * 8192));
    }
  }
  if (b == 255 && tl == 0) __builtin_amdgcn_raw_buffer_store_b64((u32x2){tagA, tagB}, wsr, WS_EPOCH, 0, 16);
}

extern "C" void kernel_launch(void* const* d_in, const int* in_sizes, int n_in, void* d_out, int out_size, void* d_ws,
                              size_t ws_size, hipStream_t stream) {
  Params p;
  p.x = (const char*)d_in[0]; p.lre = (const char*)d_in[1]; p.lim = (const char*)d_in[2];
  p.bre = (const char*)d_in[3]; p.bim = (const char*)d_in[4]; p.cre = (const char*)d_in[5]; p.cim = (const char*)d_in[6];
  p.logdt = (const char*)d_in[7]; p.dsk = (const char*)d_in[8];
  p.w1 = (const char*)d_in[10]; p.b1 = (const char*)d_in[11]; p.w2 = (const char*)d_in[12]; p.b2 = (const char*)d_in[13];
  p.out = (char*)d_out; p.ws = (char*)d_ws;
  fused_kernel<<<256, 512, 0, stream>>>(p);
}
```

```cpp
#include <hip/hip_runtime.h>
#include <stdint.h>
#include <type_traits>

typedef short s16x4 __attribute__((ext_vector_type(4)));
typedef short s16x8 __attribute__((ext_vector_type(8)));
typedef __bf16 bf16x8 __attribute__((ext_vector_type(8)));
typedef __bf16 bf16x2 __attribute__((ext_vector_type(2)));
typedef float f32x4 __attribute__((ext_vector_type(4)));
typedef float f32x2 __attribute__((ext_vector_type(2)));
typedef unsigned u32x4 __attribute__((ext_vector_type(4)));
typedef unsigned u32x2 __attribute__((ext_vector_type(2)));
typedef int i32x4 __attribute__((ext_vector_type(4)));
typedef unsigned long long u64;
typedef u64 u64x4 __attribute__((ext_vector_type(4)));
typedef u64 u64x8 __attribute__((ext_vector_type(8)));
typedef int i32x8 __attribute__((ext_vector_type(8)));
typedef int i32x16 __attribute__((ext_vector_type(16)));
__device__ i32x8 sbl8(i32x4, int, int) __asm("llvm.amdgcn.s.buffer.load.v8i32");
__device__ i32x16 sbl16(i32x4, int, int) __asm("llvm.amdgcn.s.buffer.load.v16i32");
#define LAS __attribute__((address_space(3)))
#define GAS __attribute__((address_space(1)))
#define DI __device__ __forceinline__

#define STAMPS 0
#define TRACING 0
#define GATHER_AT 4
#define ROT 1
#define TOUCH 1
#define TLBW 0
#define NOFF 0
#define PSLEEP 1
#define BFLAG 2
#define ABAR 0
#define KROT 0
#define LPRIO 3
#define CHK 0
#define BSPLIT 8
#define APRIO 0
#define ADELAY 0
#define BFLAG0 -1
#define DEPE 2
#define DEPL 2
#define LB_SLEEP 8
#define CHECK_AT 5
#define CPF 1
#define MAP 1

struct Params {
  const char *x, *lre, *lim, *bre, *bim, *cre, *cim, *logdt, *dsk, *w1, *b1, *w2, *b2;
  char *out, *ws;
};

constexpr unsigned WS_EPOCH = 0;
constexpr unsigned WS_STAT = 4096;
constexpr unsigned WS_TOT = WS_STAT + 2048 * 16;
constexpr unsigned WS_CS = WS_TOT + 16 * 2 * 16 * 256;
constexpr unsigned WS_BS = WS_CS + 16 * 16 * 512;
constexpr unsigned WS_FLAG = WS_BS + 16 * 1024;
constexpr unsigned WS_H = WS_FLAG + 32 * 2048;
constexpr unsigned H_MT = 88 * 4096;
constexpr unsigned WS_END = WS_H + 32 * H_MT;
constexpr unsigned WS_STAMP = 32u << 20;
constexpr unsigned SLOT = 40960, HALF = 20480, SL_W = 16384, SL_B = 32768;
constexpr unsigned L_ST1 = 3 * SLOT + 16384, L_ST2 = L_ST1 + 2048;
constexpr unsigned L_CS = 3 * SLOT + HALF + 16384, L_FLG = L_CS + 512, L_B1 = L_CS + 768, L_BS = L_CS + 1024, L_LS = L_CS + 2048, L_DUMMY = L_CS + 3328;
constexpr unsigned LDS_BYTES = 163840;
constexpr int SPIN_CAP = 40000;
constexpr unsigned CODE_BYTES = 44032;

DI void dma4w(unsigned voff, const char* sbase, unsigned ldsaddr) {
  asm volatile("s_mov_b32 m0, %2\n\ts_nop 0\n\tglobal_load_lds_dword %0, %1" ::"v"(voff), "s"(sbase), "s"(ldsaddr));
}
DI void dma_x4(const unsigned* v, const char* sbase, unsigned ldsaddr, bool low = false) {
  if (low) {
    asm volatile("s_mov_b32 m0, %5\n\ts_setprio 0\n\t"
               "global_load_lds_dwordx4 %0, %4\n\tglobal_load_lds_dwordx4 %1, %4 offset:1024\n\t"
               "global_load_lds_dwordx4 %2, %4 offset:2048\n\tglobal_load_lds_dwordx4 %3, %4 offset:3072\n\ts_setprio 3"
               ::"v"(v[0]), "v"(v[1]), "v"(v[2]), "v"(v[3]), "s"(sbase), "s"(ldsaddr));
    return;
  }
  asm volatile("s_mov_b32 m0, %5\n\ts_nop 0\n\t"
               "global_load_lds_dwordx4 %0, %4\n\tglobal_load_lds_dwordx4 %1, %4 offset:1024\n\t"
               "global_load_lds_dwordx4 %2, %4 offset:2048\n\tglobal_load_lds_dwordx4 %3, %4 offset:3072"
               ::"v"(v[0]), "v"(v[1]), "v"(v[2]), "v"(v[3]), "s"(sbase), "s"(ldsaddr));
}
DI void dma_x8(const unsigned* v, const char* sbase, unsigned ldsaddr) {
  asm volatile("s_mov_b32 m0, %9\n\ts_nop 0\n\t"
               "global_load_lds_dwordx4 %0, %8 offset:-4096\n\tglobal_load_lds_dwordx4 %1, %8 offset:-3072\n\t"
               "global_load_lds_dwordx4 %2, %8 offset:-2048\n\tglobal_load_lds_dwordx4 %3, %8 offset:-1024\n\t"
               "global_load_lds_dwordx4 %4, %8\n\tglobal_load_lds_dwordx4 %5, %8 offset:1024\n\t"
               "global_load_lds_dwordx4 %6, %8 offset:2048\n\tglobal_load_lds_dwordx4 %7, %8 offset:3072"
               ::"v"(v[0]), "v"(v[1]), "v"(v[2]), "v"(v[3]), "v"(v[4]), "v"(v[5]), "v"(v[6]), "v"(v[7]), "s"(sbase), "s"(ldsaddr + 4096u));
}
constexpr int PG(int i) { return i & 3; }
template <int N> DI void wait_vm() { asm volatile("s_waitcnt vmcnt(%0)" ::"i"(N) : "memory"); }
DI void wait_lgkm0() { asm volatile("" ::: "memory"); __builtin_amdgcn_s_waitcnt(0xC07F); asm volatile("" ::: "memory"); }
DI void barrier() {
  asm volatile("" ::: "memory");
  __builtin_amdgcn_s_waitcnt(0xC07F);
  __builtin_amdgcn_s_barrier();
  asm volatile("" ::: "memory");
}
#define SCHED() __builtin_amdgcn_sched_barrier(0)
DI s16x8 lds128(unsigned a) { return *(LAS s16x8*)(size_t)a; }
DI f32x4 ldsf4(unsigned a) { return *(LAS f32x4*)(size_t)a; }
DI f32x2 ldsf2(unsigned a) { return *(LAS f32x2*)(size_t)a; }
DI float ldsf1(unsigned a) { return *(LAS float*)(size_t)a; }
DI s16x4 ldstr4(unsigned a) { return __builtin_amdgcn_ds_read_tr16_b64_v4i16((LAS s16x4*)(size_t)a); }
DI s16x8 cat(s16x4 lo, s16x4 hi) { return __builtin_shufflevector(lo, hi, 0, 1, 2, 3, 4, 5, 6, 7); }
DI s16x8 ldstr(unsigned a0, unsigned a1) { return cat(ldstr4(a0), ldstr4(a1)); }
DI f32x4 mfma(s16x8 a, s16x8 b, f32x4 c) {
  return __builtin_amdgcn_mfma_f32_16x16x32_bf16(__builtin_bit_cast(bf16x8, a), __builtin_bit_cast(bf16x8, b), c, 0, 0, 0);
}
DI unsigned pk2(float a, float b) { f32x2 v = {a, b}; return __builtin_bit_cast(unsigned, __builtin_convertvector(v, bf16x2)); }
DI float u2f(unsigned u) { return __builtin_bit_cast(float, u); }
DI unsigned f2u(float u) { return __builtin_bit_cast(unsigned, u); }
DI float bflo(unsigned u) { return __builtin_bit_cast(float, u << 16); }
DI float bfhi(unsigned u) { return __builtin_bit_cast(float, u & 0xffff0000u); }
template <int CTRL> DI float dpp_add(float v) {
  return v + __builtin_bit_cast(float, __builtin_amdgcn_update_dpp(0, __builtin_bit_cast(int, v), CTRL, 0xf, 0xf, true));
}
DI float row16_sum(float v) {
  v = dpp_add<0xB1>(v);
  v = dpp_add<0x4E>(v);
  v = dpp_add<0x141>(v);
  v = dpp_add<0x140>(v);
  return v;
}
DI float smul(float a, float b) { float d; asm("v_mul_f32 %0, %1, %2" : "=v"(d) : "v"(a), "v"(b)); return d; }
DI float sadd(float a, float b) { float d; asm("v_add_f32 %0, %1, %2" : "=v"(d) : "v"(a), "v"(b)); return d; }
DI float sfma(float a, float b, float c) { float d; asm("v_fma_f32 %0, %1, %2, %3" : "=v"(d) : "v"(a), "v"(b), "v"(c)); return d; }
DI float sfms(float a, float b, float c) { float d; asm("v_fma_f32 %0, %1, %2, -%3" : "=v"(d) : "v"(a), "v"(b), "v"(c)); return d; }
DI void zero4(f32x4& v) {
  float a, b, c, d;
  asm volatile("v_mov_b32 %0, 0\n\tv_mov_b32 %1, 0\n\tv_mov_b32 %2, 0\n\tv_mov_b32 %3, 0" : "=v"(a), "=v"(b), "=v"(c), "=v"(d));
  v = (f32x4){a, b, c, d};
}
DI u32x2 swap32(float a, float b) { asm volatile("s_nop 1" : "+v"(a), "+v"(b)); return __builtin_amdgcn_permlane32_swap(f2u(a), f2u(b), false, false); }
DI u32x2 swap16(float a, float b) { asm volatile("s_nop 1" : "+v"(a), "+v"(b)); return __builtin_amdgcn_permlane16_swap(f2u(a), f2u(b), false, false); }
DI f32x2 pfma(f32x2 a, f32x2 b, f32x2 c) { return __builtin_elementwise_fma(a, b, c); }
DI f32x2 bc(float a) { return (f32x2){a, a}; }
DI f32x2 bco(float a) { f32x2 v = {a, a}; asm("" : "+v"(v)); return v; }
DI f32x2 cmul(f32x2 a, f32x2 b) { return (f32x2){sfms(a[0], b[0], smul(a[1], b[1])), sfma(a[0], b[1], smul(a[1], b[0]))}; }
DI f32x2 cfma(f32x2 a, f32x2 b, f32x2 c) { return (f32x2){sfms(a[0], b[0], sfms(a[1], b[1], c[0])), sfma(a[0], b[1], sfma(a[1], b[0], c[1]))}; }
DI f32x2 csel(bool p, f32x2 a, f32x2 b) { return (f32x2){p ? a[0] : b[0], p ? a[1] : b[1]}; }
DI void prog_set(unsigned a, unsigned v) {
  asm volatile("s_mov_b64 exec, 1\n\tds_write_b32 %0, %1\n\ts_mov_b64 exec, -1" ::"v"(a), "v"(v) : "memory");
}
DI void prog_wait(unsigned a, unsigned v) {
#pragma unroll 1
  for (int spin = 0; spin < SPIN_CAP * 16; ++spin) {
    if (__builtin_amdgcn_readfirstlane(*(volatile LAS unsigned*)(size_t)a) >= v) break;
    __builtin_amdgcn_s_sleep(1);
  }
  asm volatile("" ::: "memory");
}
DI void bar() { asm volatile("" ::: "memory"); __builtin_amdgcn_s_barrier(); asm volatile("" ::: "memory"); }
DI void dma1_dev(unsigned voff, const char* sbase, unsigned ldsaddr) {
  asm volatile("s_mov_b32 m0, %2\n\ts_nop 0\n\tglobal_load_lds_dwordx4 %0, %1 sc1" ::"v"(voff), "s"(sbase), "s"(ldsaddr) : "memory");
}
DI void dma4_dev(unsigned voff, const char* s0, const char* s1, unsigned ldsaddr) {
  asm volatile("s_mov_b32 m0, %3\n\ts_nop 0\n\tglobal_load_lds_dwordx4 %0, %1 sc1\n\tglobal_load_lds_dwordx4 %0, %1 offset:1024 sc1\n\t"
               "global_load_lds_dwordx4 %0, %2 offset:2048 sc1\n\tglobal_load_lds_dwordx4 %0, %2 offset:3072 sc1" ::"v"(voff), "s"(s0), "s"(s1), "s"(ldsaddr) : "memory");
}
DI int kstep_of(int slot, int i) {
  const int q = slot + 4 * i + (slot >= 4 ? 28 : 0), cls = (q >= 14) + (q >= 28) + (q >= 42);
  return 4 * (q - 14 * cls) + ((0x9C >> (2 * cls)) & 3);
}
DI unsigned flag_of(int j, int c) {
  const int q = 14 * ((0x78 >> (2 * c)) & 3) + j;
  return q < 32 ? (q & 3) * 128 + (q >> 2) * 8 : 512 + (q & 3) * 128 + ((q - 32) >> 2) * 8;
}
DI void cnt_add(unsigned a) {
  asm volatile("s_mov_b64 exec, 1\n\tds_add_u32 %0, %1\n\ts_mov_b64 exec, -1" ::"v"(a), "v"(1u) : "memory");
}
DI f32x2 lo2(f32x4 v) { return __builtin_shufflevector(v, v, 0, 1); }
DI f32x2 hi2(f32x4 v) { return __builtin_shufflevector(v, v, 2, 3); }
DI void rows4(float v, float* o) {
  asm volatile("s_nop 1" : "+v"(v));
  const unsigned u = f2u(v);
  const u32x2 h = __builtin_amdgcn_permlane32_swap(u, u, false, false);
  const u32x2 a = __builtin_amdgcn_permlane16_swap(h.x, h.x, false, false), b = __builtin_amdgcn_permlane16_swap(h.y, h.y, false, false);
  o[0] = u2f(a.x); o[1] = u2f(a.y); o[2] = u2f(b.x); o[3] = u2f(b.y);
}
struct Col { f32x2 cs[4], bias[4]; };
DI void mkcol(Col& c, f32x4 cs0, f32x4 cs1, u32x2 b0, u32x2 b1) {
  c.cs[0] = lo2(cs0); c.cs[1] = hi2(cs0); c.cs[2] = lo2(cs1); c.cs[3] = hi2(cs1);
  c.bias[0] = (f32x2){bflo(b0.x), bfhi(b0.x)}; c.bias[1] = (f32x2){bflo(b0.y), bfhi(b0.y)};
  c.bias[2] = (f32x2){bflo(b1.x), bfhi(b1.x)}; c.bias[3] = (f32x2){bflo(b1.y), bfhi(b1.y)};
}
DI u32x4 gelu8(f32x4 a0, f32x4 a1, const Col& c, f32x2 rs, f32x2 nmrs) {
  f32x2 v[4], z[4];
  u32x4 o;
#define ST4(stmt) _Pragma("unroll") for (int h = 0; h < 4; ++h) { stmt; } SCHED();
  ST4(v[h] = pfma(c.cs[h], nmrs, c.bias[h]))
  ST4(v[h] = pfma(h == 0 ? lo2(a0) : h == 1 ? hi2(a0) : h == 2 ? lo2(a1) : hi2(a1), rs, v[h]))
  ST4(z[h] = v[h] * v[h])
  ST4(z[h] = pfma(z[h], bc(-0.10294324f), bc(-2.30220820f)))
  ST4(z[h] = z[h] * v[h])
  ST4((z[h] = (f32x2){__builtin_amdgcn_exp2f(z[h][0]), __builtin_amdgcn_exp2f(z[h][1])}))
  ST4(z[h] = z[h] + bc(1.f))
  ST4((z[h] = (f32x2){__builtin_amdgcn_rcpf(z[h][0]), __builtin_amdgcn_rcpf(z[h][1])}))
  ST4(v[h] = v[h] * z[h])
  ST4(o[h] = pk2(v[h][0], v[h][1]))
#undef ST4
  return o;
}

#define TRACE(i) do {} while (0)
#define TRACE2(i) do {} while (0)
#define TRACE3(i) do {} while (0)
#define STAMP(k) do {} while (0)
#define STAMPC(k) do {} while (0)
#define COUNT(k) do {} while (0)

__global__ void __launch_bounds__(512, 2) fused_kernel(Params p) {
  __shared__ __attribute__((aligned(16))) char lds_mem[LDS_BYTES];
  unsigned long long pc;
  asm volatile("s_getpc_b64 %0" : "=s"(pc));
  const unsigned LB = (unsigned)(size_t)(LAS char*)lds_mem;
  const int tid = threadIdx.x, lane = tid & 63;
  const int w = __builtin_amdgcn_readfirstlane(tid >> 6);
  if (w == 5) {
    const unsigned co = ((unsigned)(lane & 15) * 32u + (blockIdx.x >> 3)) * 128u;
    dma4w(co < CODE_BYTES ? co : 0u, (const char*)(pc & ~255ull), LB + L_DUMMY + 512);
  }
  const int b = blockIdx.x, idx = b >> 3, xcd = b & 7;
  const int r = 8 * (xcd >> 2) + (idx >> 2), j = 4 * (xcd & 3) + (idx & 3);
  const int rot = (2 * (xcd & 3) + 4 * (xcd >> 2)) & 7;
  const int mtile = 4 * xcd + (idx >> 3), ntile = (idx + NOFF) & 7;
  const int i16 = lane & 15, g = lane >> 4, qq = i16 >> 2, pp = i16 & 3;
  const int d = w & 1;
  const bool mlp = j < 14;
  u32x4 pre[4];
  unsigned v_lre, v_lim, v_ldt;
  if ((w >> 1) == 2) {
    const unsigned po = (unsigned)(32 * j + 16 * d + i16) * 2u;
    v_lre = *(const GAS unsigned short*)(p.lre + po); v_lim = *(const GAS unsigned short*)(p.lim + po); v_ldt = *(const GAS unsigned short*)(p.logdt + po);
    asm volatile("" ::: "memory");
  }
  u32x4 prb[2];
  if (w < 4) {
    const bool isb = w >= 2;
    const char* pb = isb ? p.w1 + (64 * (r & 7) + 32 * d) * 3584 + (mlp ? j : 13) * 256 + (r >> 3) * 128 : p.x + (128 * r + 8 * j + 4 * d) * 1024;
    const unsigned po = isb ? (lane >> 3) * 3584 + (lane & 7) * 16 : g * 1024 + i16 * 16, ps = isb ? 28672 : 256;
#pragma unroll
    for (int i = 0; i < 4; ++i) pre[i] = *(const GAS u32x4*)(pb + i * ps + po);
    if (isb) {
#pragma unroll
      for (int i = 0; i < 2; ++i) prb[i] = *(const GAS u32x4*)((r & 8 ? p.bim : p.bre) + (32 * j + 4 * (r & 7) + 2 * d) * 1024 + i * 512 + (lane >> 5) * 1024 + (lane & 31) * 16);
    }
    asm volatile("" ::: "memory");
  }
  const int kgo = 2 * (idx >> 3), kg = (w + KROT * kgo) & 7;
  auto slot_of = [&](int role) { return (((role + 1) & 2) << 1) + (((((role + 1) & 2) && !KROT ? 0 : kgo) + ((role & 1) ? 0 : 2) + d) & 3); };
  const int slot = slot_of(w >> 1);
  const u32x2 ep = *(const __attribute__((address_space(4))) u32x2*)(p.ws + WS_EPOCH);
  unsigned tagA, tagB;
  auto mktag = [&]() { tagA = ep.x; tagB = ep.y; asm volatile("" : "+s"(tagA), "+s"(tagB)); tagA += 1u; tagB += 1u; };
  const __amdgpu_buffer_rsrc_t wsr = __builtin_amdgcn_make_buffer_rsrc(p.ws, 0, WS_END, 0x00020000);
  const i32x4 wss = {(int)(size_t)p.ws, (int)((size_t)p.ws >> 32) & 0xffff, (int)WS_END, 0x00020000};
  auto fresh = [](int o) { asm volatile("" : "+s"(o) :: "memory"); return o; };
  const unsigned HB = LB + d * HALF;
  const unsigned PW = LB + L_FLG;
  s16x8 we[8][4];
  f32x4 acc2[4][4];
  unsigned home;
  const unsigned r2l = (4 * g + qq) * 128 + pp * 8, r2x = (2 * g + (qq >> 1)) & 3;
  auto absorb = [&](int i, unsigned page) {
#pragma unroll
    for (int n = 0; n < 4; ++n) { const unsigned a = page + r2l + ((n ^ r2x) * 32); we[i][n] = ldstr(a, a + 2048); }
  };
  auto mkvc = [&](unsigned* vc) {
#pragma unroll
    for (int i = 0; i < 8; ++i) vc[i] = (lane >> 3) * 1024 + (((lane & 7) ^ (4 * (i & 1) + g)) * 16) + 4096 + i * 7168;
  };
  auto mkv2 = [&](unsigned* v2) {
#pragma unroll
    for (int i = 0; i < 4; ++i) v2[i] = (lane >> 3) * 1024 + (((((lane & 7) >> 1) ^ g) << 1) | (lane & 1)) * 16 + i * 7168;
  };
  auto loadC = [&](const unsigned* vc, int kgc, unsigned dst) {
    const unsigned o = ntile * 65536 + kgc * 128;
    dma_x8(vc, p.cre + o, dst);
    dma_x8(vc, p.cim + o, dst + 8192);
  };
  const char* w2b = p.w2 + ntile * 128;
  auto adv = [&](unsigned& a, int dl) { a += dl; asm volatile("" : "+v"(a)); };
  const int tok = 32 * qq + 16 * (i16 & 1) + ((i16 >> 1) & 1);
  const unsigned kx = (g ^ (i16 >> 1)) * 16;
  const unsigned awb = LB + SL_W + (8 * g + qq) * 256 + pp * 8, awx = qq | ((g & 1) << 2);
  auto sum32 = [&](const u32x4* v) {
    float s0 = 0.f, s1 = 0.f;
#pragma unroll
    for (int i = 0; i < 4; ++i)
#pragma unroll
      for (int e = 0; e < 4; ++e) { s0 += bflo(v[i][e]); s1 += bfhi(v[i][e]); }
    return row16_sum(s0 + s1);
  };
  auto touch = [&]() {
    dma4w(d ? (lane & 15) * 128 : (lane < 56 ? lane : 0) * 128, d ? p.cre + b * 2048 : p.w2 + b * 7168, LB + L_DUMMY + d * 256);
    if (d) dma4w((lane & 15) * 128, p.cim + b * 2048, LB + L_DUMMY + d * 256);
  };
  auto zero2 = [&]() {
#pragma unroll
    for (int n = 0; n < 4; ++n) {
#pragma unroll
      for (int m = 0; m < 4; ++m) acc2[n][m] = (f32x4){0, 0, 0, 0};
      asm volatile("" : "+v"(acc2[n][0]), "+v"(acc2[n][1]), "+v"(acc2[n][2]), "+v"(acc2[n][3]));
    }
  };
  int ksl;
  unsigned hb, crd, pubo = 0xffff0000u;
  const int fm = WS_FLAG + mtile * 2048 + slot * 128, fs = WS_FLAG + mtile * 2048 + 1024 + kg * 32;
  u64x8 flm;
  u64x4 fls;
  auto snap = [&](bool ssm) { if (ssm) fls = __builtin_bit_cast(u64x4, sbl8(wss, fresh(fs), 1)); else flm = __builtin_bit_cast(u64x8, sbl16(wss, fresh(fm), 1)); };
  auto setup2 = [&](int nm) {
    ksl = (lane < nm ? kstep_of(slot, lane) : 56 + 4 * kg + ((lane - nm) & 3)) * 4096;
    ksl += w == 1 ? 2048 : 0;
    hb = WS_H + (unsigned)mtile * H_MT + lane * 16;
    crd = home + i16 * 128 + (g & 1) * 8 + (((g >> 1) ^ (i16 >> 1)) * 16);
    asm volatile("" : "+v"(ksl), "+v"(hb), "+v"(crd));
  };

  auto roleD = [&]() __attribute__((always_inline)) {
    __builtin_amdgcn_s_setprio(3);
    unsigned vx[8], vw[8], vb[4], v2[4];
#pragma unroll
    for (int i = 0; i < 8; ++i) vw[i] = g * 3584 + ((((i16 >> 1) ^ (g | (((i >> 1) & 1) << 2))) << 1) | (i16 & 1)) * 16 + 4096 + i * (14336 - 1024);
    const char* wb = p.w1 + j * 256 + d * (32 * 3584);
    auto part1 = [&](int c, unsigned slot) { if (mlp) dma_x8(vw, wb + ((c + rot) & 7) * (64 * 3584), LB + slot + SL_W + d * 8192); };
    if (TOUCH == 5) touch();
    part1(0, 0);
#pragma unroll
    for (int i = 0; i < 8; ++i)
      vx[i] = (32 * (g >> 1) + 16 * ((lane >> 3) & 1) + (g & 1)) * 1024 + (((lane & 7) ^ (4 * (i & 1) + g)) * 16) + 4096 + (i & 1) * 65536 + (i >> 1) * 2048 - i * 1024;
#pragma unroll
    for (int i = 0; i < 4; ++i) vb[i] = (lane >> 3) * 1024 + (((lane & 7) ^ (4 * (i & 1) + g)) * 16) + i * 7168;
    const char* xb = p.x + (r * 128 + d * 8) * 1024;
    const char* bb = (d ? p.bim : p.bre) + j * 32768;
    auto partx = [&](int c, unsigned slot) { dma_x8(vx, xb + ((c + rot) & 7) * 128, LB + slot + d * 8192); };
    auto partb = [&](int c, unsigned slot) { dma_x4(vb, bb + ((c + rot) & 7) * 128, LB + slot + SL_B + d * 4096); };
    auto part0 = [&](int c, unsigned slot) { partx(c, slot); partb(c, slot); };
    mkv2(v2);
    unsigned ra[4];
    home = HB + 3 * SLOT;
#pragma unroll
    for (int n = 0; n < 4; ++n) ra[n] = home + r2l + ((n ^ r2x) * 32);
    int kt = kstep_of(slot_of((lane >> 3) & 3), lane & 7) * 32768;
    asm volatile("" : "+v"(kt), "+v"(ra[0]), "+v"(ra[1]), "+v"(ra[2]), "+v"(ra[3]), "+v"(v2[0]), "+v"(v2[1]), "+v"(v2[2]), "+v"(v2[3]));
    bool low = false;
    auto page = [&](int role, int i, int grp, int pg) { dma_x4(v2, w2b + (unsigned)__builtin_amdgcn_readlane(kt, 8 * role + i), HB + grp * SLOT + pg * 4096, low); };
    auto c8 = [&](const char* src, unsigned dst) { dma_x4(vb, src, dst, true); dma_x4(vb, src + 32768, dst + 4096, true); };
    auto take = [&](int i) {
      SCHED();
#pragma unroll
      for (int n = 0; n < 4; ++n) we[i][n] = ldstr(ra[n] + PG(i) * 4096, ra[n] + PG(i) * 4096 + 2048);
      SCHED();
    };
    auto own = [&](int i) { page(3, i, 3, PG(i)); };
    const unsigned PT = LB + 3 * SLOT + 12288;
    auto gather = [&]() {
      const bool all = false;
      const char* cp = p.ws + WS_CS + j * 8192;
      if (d || all) { dma4_dev(lane * 16, p.ws + WS_STAT + r * 2048, p.ws + WS_STAT + (mtile >> 1) * 2048 - 2048, LB + L_ST1); dma1_dev(lane * 16, p.ws + WS_BS + j * 1024, LB + L_BS); }
      if (!d) { dma4_dev(lane * 16, cp, cp, PT); dma4_dev(lane * 16, cp + 4096, cp + 4096, PT + HALF); }
    };
    auto ld16 = [](unsigned a) { return *(volatile LAS u32x4*)(size_t)a; };
    auto bad = [&](unsigned a, u32x4 v) { return a | (v.x ^ tagA) | (v.z ^ tagB); };
    auto again = [&]() { COUNT(20); __builtin_amdgcn_s_sleep(10); gather(); wait_vm<0>(); };
    auto check0 = [&](auto between) {
      u32x4 cv[8];
      auto look = [&]() {
#pragma unroll
        for (int i = 0; i < 8; ++i) cv[i] = ld16(PT + (i >> 2) * HALF + (i & 3) * 1024 + lane * 16);
      };
      look(); SCHED(); between(); SCHED();
#pragma unroll 1
      for (int spin = 0; spin < SPIN_CAP; ++spin) {
        unsigned e = 0;
#pragma unroll
        for (int i = 0; i < 8; ++i) e = bad(e, cv[i]);
        if (__builtin_expect(!mlp || __all(e == 0), 1)) break;
        again(); look();
      }
      float t[4];
#pragma unroll
      for (int k = 0; k < 4; ++k) {
        const int i = 4 * (k & 1), e = 1 + (k & 2);
        t[k] = sadd(sadd(u2f(cv[i][e]), u2f(cv[i + 1][e])), sadd(u2f(cv[i + 2][e]), u2f(cv[i + 3][e])));
      }
      const u32x2 h0 = swap32(t[0], t[1]), h1 = swap32(t[2], t[3]);
      *(LAS f32x2*)(size_t)(LB + L_CS + lane * 8) = (f32x2){sadd(u2f(h0.x), u2f(h0.y)), sadd(u2f(h1.x), u2f(h1.y))};
      SCHED();
    };
    auto check1 = [&](auto between) {
      u32x4 cv[5];
      auto look = [&]() {
        cv[0] = ld16(LB + L_ST1 + g * 512 + i16 * 16); cv[1] = ld16(LB + L_ST1 + g * 512 + i16 * 16 + 256);
        cv[2] = ld16(LB + L_ST2 + lane * 16); cv[3] = ld16(LB + L_ST2 + lane * 16 + 1024); cv[4] = ld16(LB + L_BS + lane * 16);
      };
      look(); SCHED(); between(); SCHED();
#pragma unroll 1
      for (int spin = 0; spin < SPIN_CAP; ++spin) {
        if (__builtin_expect(__all(bad(bad(bad(bad(bad(0u, cv[0]), cv[1]), cv[2]), cv[3]), cv[4]) == 0), 1)) break;
        again(); look();
      }
      *(LAS u32x4*)(size_t)(LB + L_LS + g * 288 + i16 * 16) = (u32x4){cv[0].y, cv[1].y, cv[0].w, cv[1].w};
      SCHED();
    };
    const unsigned me = PW + w * 4;
    const unsigned co = ntile * 65536 + kg * 128;
    zero2();
    setup2(8);
    auto landed = [&]() { if (mlp) wait_vm<20>(); else wait_vm<12>(); };
    SCHED();
    part0(0, 0);
    if (TOUCH == 4) touch();
    part1(1, SLOT); part0(1, SLOT);
    STAMP(1);
    mktag();
    if (mlp) wait_vm<20 + (TOUCH == 4)>(); else wait_vm<12 + (TOUCH == 4)>();
    STAMP(19);
    bar();
    STAMP(2);
    part1(2, 2 * SLOT); part0(2, 2 * SLOT); landed(); if (TOUCH == 1) touch();
    bar();
    if (d) dma4w(lane * 4, p.b1 + (mlp ? j : 13) * 256, LB + L_B1);
    part1(3, 0); part0(3, 0); landed();
    dma4w((lane < 28 ? lane : 0) * 1024, p.w2 + (2 * idx + d) * 28672 + ((3u - ((unsigned)(size_t)p.w2 >> 7)) & 7u) * 128, LB + L_DUMMY + d * 256);
    bar();
    STAMPC(21);
    dma_x4(vx, p.x + (128 * (mtile >> 1) + 8 * (mtile & 1) + 4 * d) * 1024 + ntile * 128 - 4096, home + 12288);
    part1(4, SLOT); part0(4, SLOT); landed();
    bar();
    own(0); part1(5, 2 * SLOT); part0(5, 2 * SLOT); landed();
    bar();
    gather(); part1(6, 0); take(0); part0(6, 0); landed();
    bar();
    part1(7, SLOT);
    if (d) { partx(7, SLOT); check1([&]() { partb(7, SLOT); }); }
    else { check0([&]() { partb(7, SLOT); }); partx(7, SLOT); }
    landed();
    barrier();
    STAMP(3); STAMPC(22);
    page(0, 0, 2, 0); page(0, 1, 2, 1); page(0, 2, 2, 2); page(0, 3, 2, 3); page(0, 4, 2, 4); own(1);
    wait_vm<12>();
    bar();
    STAMP(4);
    own(2); page(2, 0, 0, 0); page(2, 1, 0, 1); page(2, 2, 0, 2); page(2, 3, 0, 3); page(2, 4, 0, 4);
    wait_vm<20>();
    STAMP(5);
    bar();
    low = true;
    STAMP(6);
    page(1, 0, 1, 0); page(1, 1, 1, 1); take(1); page(1, 2, 1, 2); take(2); page(1, 3, 1, 3); page(1, 4, 1, 4);
    own(3); own(4); wait_lgkm0(); own(5); own(6);
    wait_vm<16>(); prog_set(me, 1);
    page(0, 5, 2, 0); page(0, 6, 2, 1); page(0, 7, 2, 2);
    wait_vm<16>(); take(3); take(4); take(5); wait_lgkm0();
    own(7);
    c8(p.cre + co, home);
    STAMP(7);
    prog_wait(PW + (2 + d) * 4, 1);
    page(1, 5, 1, 4);
    wait_vm<16>(); prog_set(me, 2);
    snap(false);
    { const unsigned o = ntile * 65536 + ((2 + d + KROT * kgo) & 7) * 128; c8(p.cre + o, HB + SLOT); c8(p.cim + o, HB + SLOT + 8192); }
    wait_vm<28>(); take(6); take(7); wait_lgkm0();
    c8(p.cim + co, home + 8192);
    __builtin_amdgcn_s_setprio(0);
    STAMP(8);
  };
  auto roleL = [&]() __attribute__((always_inline)) {
    if (TOUCH == 0) touch();
    if (TLBW && d) { dma4w(0, p.cre + ntile * 65536, LB + L_DUMMY + 256); dma4w(0, p.cim + ntile * 65536, LB + L_DUMMY + 256); dma4w((lane & 1) * 0x1bf000, p.w2, LB + L_DUMMY + 256); }
    f32x4 acr[8], aci[8];
#pragma unroll
    for (int m = 0; m < 8; ++m) { acr[m] = (f32x4){0, 0, 0, 0}; aci[m] = (f32x4){0, 0, 0, 0}; asm volatile("" : "+v"(acr[m]), "+v"(aci[m])); }
    unsigned ax[2], ab[2];
    ax[0] = LB + i16 * 128 + kx; ax[1] = ax[0] ^ 64;
    ab[0] = LB + SL_B + (16 * d + i16) * 128 + kx; ab[1] = ab[0] ^ 64;
    unsigned sa = LB + L_LS + g * 288, csa = LB + L_BS + (16 * d + i16) * 16 + 4;
    asm volatile("" : "+v"(sa), "+v"(csa), "+v"(ax[0]), "+v"(ax[1]), "+v"(ab[0]), "+v"(ab[1]));
    s16x8 xf[2][8], bf[2][2];
    auto rd = [&](int set, int i) {
      if (i < 2) bf[set][i] = lds128(ab[set] + i * 4096);
      else xf[set][i - 2] = lds128(ax[set] + (i - 2) * 2048);
    };
    auto mm = [&](int set, int i) {
      if (i & 1) aci[i >> 1] = mfma(xf[set][i >> 1], bf[set][1], aci[i >> 1]);
      else acr[i >> 1] = mfma(xf[set][i >> 1], bf[set][0], acr[i >> 1]);
    };
    asm volatile("" : "+v"(v_lre), "+v"(v_lim), "+v"(v_ldt));
    const float lr = bflo(v_lre), li = bflo(v_lim);
    const float dt = bflo(pk2(expf(bflo(v_ldt)), 0.f));
    const float ang = smul(dt, li);
    const float rv = smul(ang, 0.15915494f);
    const float rev = (rv - __builtin_rintf(rv)) + sfma(ang, 6.4206382e-9f, sfms(ang, 0.15915494f, rv));
    float sn = __builtin_amdgcn_sinf(rev), cn = __builtin_amdgcn_cosf(rev);
    asm volatile("s_nop 0" : "+v"(sn), "+v"(cn));
    const float ex = expf(smul(dt, lr));
    f32x2 a1 = {smul(ex, cn), smul(ex, sn)};
    const float nr = a1[0] - 1.f, ni = a1[1], iden = 2.f / sfma(lr, lr, smul(li, li));
    f32x2 kk = {smul(sfma(nr, lr, smul(ni, li)), iden), smul(sfms(ni, lr, smul(nr, li)), iden)};
    f32x2 t = cmul(a1, a1); t = cmul(t, t); t = cmul(t, t);
    f32x2 a16 = cmul(t, t);
    f32x2 a32 = cmul(a16, a16);
    const f32x2 a64 = cmul(a32, a32), a128 = cmul(a64, a64), a256 = cmul(a128, a128);
    f32x2 a512 = cmul(a256, a256);
    const f32x2 one = {1.f, 0.f};
    f32x2 aseg = cmul(kk, cmul(csel(g & 1, a32, one), csel(g & 2, a64, one)));
    const int e4 = (r - 1 - g) & 3;
    f32x2 acar = cmul(csel(e4 & 1, a128, one), csel(e4 & 2, a256, one));
    const f32x2 ar = bco(a1[0]), ai = bco(a1[1]);
    asm volatile("" :: "v"(ar), "v"(ai));
    asm volatile("" : "+v"(kk), "+v"(a16), "+v"(a32), "+v"(a512), "+v"(aseg), "+v"(acar));
    mktag();
    __builtin_amdgcn_s_setprio(1);
    STAMP(1);
    bar();
    STAMP(2); TRACE3(1);
#pragma unroll
    for (int i = 0; i < 10; ++i) rd(0, i);
    wait_lgkm0();
    auto stage1 = [&](int dl) {
      SCHED();
#pragma unroll
      for (int i = 0; i < 16; ++i) {
        mm(0, i); SCHED();
        if (i < 10) rd(1, i);
        if (i == 11) { adv(ax[0], dl); adv(ab[0], dl); }
        SCHED();
      }
    };
    stage1(SLOT);
    barrier();
    TRACE3(2);
    int ph = 0;
#pragma unroll 1
    for (int c = 2; c <= 8; ++c) {
      const int dl = ph == 2 ? -2 * (int)SLOT : (int)SLOT;
      ph = ph == 2 ? 0 : ph + 1;
      SCHED();
#pragma unroll
      for (int i = 0; i < 16; ++i) {
        mm(1, i); SCHED();
        if (i < 10) rd(0, i);
        if (i == 11) { adv(ax[1], dl); adv(ab[1], dl); }
        SCHED();
      }
      wait_lgkm0();
      stage1(ph == 2 ? -2 * (int)SLOT : (int)SLOT);
      TRACE(2 * c);
      barrier();
      TRACE(2 * c + 1); TRACE3(c + 1);
    }
    __builtin_amdgcn_s_setprio(LPRIO);
    STAMP(4);
    f32x2 rs[16], mrs[16];
    float csr, csi;
#pragma unroll
    for (int i = 0; i < 16; ++i) {
      mm(1, i); SCHED();
      if (i == 0) { csr = ldsf1(csa); csi = ldsf1(csa + 512); }
      const f32x4 t = ldsf4(sa + i * 16);
      rs[i] = lo2(t); mrs[i] = hi2(t);
      if ((i & 3) == 3) TRACE2(20 + (i >> 2));
      SCHED();
    }
    const f32x2 ncr = bco(-csr), nci = bco(-csi);
    STAMP(5);
    f32x2 ur[16], ui[16], Er, Ei;
    SCHED();
#pragma unroll
    for (int s = 0; s < 16; ++s) {
      ur[s] = mrs[s] * ncr; SCHED();
      ui[s] = mrs[s] * nci; SCHED();
      ur[s] = pfma((s & 1) ? hi2(acr[s >> 1]) : lo2(acr[s >> 1]), rs[s], ur[s]); SCHED();
      ui[s] = pfma((s & 1) ? hi2(aci[s >> 1]) : lo2(aci[s >> 1]), rs[s], ui[s]); SCHED();
      if (s == 0) { Er = ur[0]; Ei = ui[0]; }
      else {
        const f32x2 t0 = pfma(Ei, -ai, ur[s]); SCHED();
        const f32x2 t1 = pfma(Er, ai, ui[s]); SCHED();
        Er = pfma(Er, ar, t0); SCHED();
        Ei = pfma(Ei, ar, t1); SCHED();
      }
    }
    TRACE2(1);
    const f32x2 E0 = {Er[0], Ei[0]};
    const f32x2 E = cfma(a16, E0, (f32x2){Er[1], Ei[1]});
    f32x2 P = {0.f, 0.f}, Tt = {0.f, 0.f};
    float er4[4], ei4[4];
    rows4(E[0], er4); rows4(E[1], ei4);
#pragma unroll
    for (int s = 0; s < 4; ++s) {
      const f32x2 e = {er4[s], ei4[s]};
      P = csel(s == g, Tt, P);
      Tt = s ? cfma(a32, Tt, e) : e;
    }
    const unsigned totb = WS_TOT + (unsigned)(j * 2 + d) * 4096;
    if (g == 0) __builtin_amdgcn_raw_buffer_store_b128((u32x4){tagA, f2u(Tt[0]), tagB, f2u(Tt[1])}, wsr, totb + r * 256 + i16 * 16, 0, 16);
    STAMP(6);
    home = HB;
    TRACE2(2);
    prog_wait(PW + (6 + d) * 4, 1);
#pragma unroll
    for (int i = 0; i < 5; ++i) absorb(i, home + i * 4096);
    unsigned vc[8], v2[4], vt[4];
    mkvc(vc); mkv2(v2);
#pragma unroll
    for (int i = 0; i < 4; ++i) { vt[i] = (4 * i + g < r ? i * 1024 + g * 256 : 0) + i16 * 16; asm volatile("" : "+v"(vt[i]), "+v"(v2[i]), "+v"(vc[i]), "+v"(vc[i + 4])); }
    wait_lgkm0();
    loadC(vc, kg, home);
    TRACE2(3);
    {
      const i32x4 tr = {wss[0], wss[1], (int)(totb + r * 256), wss[3]};
#pragma unroll 1
      for (int spin = 0; spin < 64; ++spin) {
        int n, t[15];
        asm volatile("s_buffer_load_dword %1, %16, %17 offset:0 glc\n\ts_buffer_load_dword %2, %16, %17 offset:256 glc\n\ts_buffer_load_dword %3, %16, %17 offset:512 glc\n\ts_buffer_load_dword %4, %16, %17 offset:768 glc\n\ts_buffer_load_dword %5, %16, %17 offset:1024 glc\n\ts_buffer_load_dword %6, %16, %17 offset:1280 glc\n\ts_buffer_load_dword %7, %16, %17 offset:1536 glc\n\ts_buffer_load_dword %8, %16, %17 offset:1792 glc\n\ts_buffer_load_dword %9, %16, %17 offset:2048 glc\n\ts_buffer_load_dword %10, %16, %17 offset:2304 glc\n\ts_buffer_load_dword %11, %16, %17 offset:2560 glc\n\ts_buffer_load_dword %12, %16, %17 offset:2816 glc\n\ts_buffer_load_dword %13, %16, %17 offset:3072 glc\n\ts_buffer_load_dword %14, %16, %17 offset:3328 glc\n\ts_buffer_load_dword %15, %16, %17 offset:3584 glc\n\t"
                     "s_mov_b32 %0, 0\n\ts_waitcnt lgkmcnt(0)\n\t"
                     "s_cmp_eq_u32 %1, %18\n\ts_addc_u32 %0, %0, 0\n\ts_cmp_eq_u32 %2, %18\n\ts_addc_u32 %0, %0, 0\n\ts_cmp_eq_u32 %3, %18\n\ts_addc_u32 %0, %0, 0\n\ts_cmp_eq_u32 %4, %18\n\ts_addc_u32 %0, %0, 0\n\ts_cmp_eq_u32 %5, %18\n\ts_addc_u32 %0, %0, 0\n\ts_cmp_eq_u32 %6, %18\n\ts_addc_u32 %0, %0, 0\n\ts_cmp_eq_u32 %7, %18\n\ts_addc_u32 %0, %0, 0\n\ts_cmp_eq_u32 %8, %18\n\ts_addc_u32 %0, %0, 0\n\ts_cmp_eq_u32 %9, %18\n\ts_addc_u32 %0, %0, 0\n\ts_cmp_eq_u32 %10, %18\n\ts_addc_u32 %0, %0, 0\n\ts_cmp_eq_u32 %11, %18\n\ts_addc_u32 %0, %0, 0\n\ts_cmp_eq_u32 %12, %18\n\ts_addc_u32 %0, %0, 0\n\ts_cmp_eq_u32 %13, %18\n\ts_addc_u32 %0, %0, 0\n\ts_cmp_eq_u32 %14, %18\n\ts_addc_u32 %0, %0, 0\n\ts_cmp_eq_u32 %15, %18\n\ts_addc_u32 %0, %0, 0"
                     : "=&s"(n), "=&s"(t[0]), "=&s"(t[1]), "=&s"(t[2]), "=&s"(t[3]), "=&s"(t[4]), "=&s"(t[5]), "=&s"(t[6]), "=&s"(t[7]), "=&s"(t[8]), "=&s"(t[9]), "=&s"(t[10]), "=&s"(t[11]), "=&s"(t[12]), "=&s"(t[13]), "=&s"(t[14]) : "s"(tr), "s"(totb), "s"(tagA) : "scc", "memory");
        if (n >= r) break;
        COUNT(20);
      }
    }
    STAMP(7);
    f32x2 car = {0.f, 0.f};
    auto fetch = [&]() {
#pragma unroll
      for (int i = 0; i < 4; ++i)
        if (4 * i < r) dma1_dev(vt[i], p.ws + totb, home + 16384 + i * 1024);
    };
    fetch();
    {
      const f32x2 kr = bco(kk[0]), nki = bco(-kk[1]);
#pragma unroll
      for (int s = 0; s < 16; ++s) {
        const f32x2 t0 = ur[s] * kr; SCHED();
        const f32x2 t1 = ur[s] * nki; SCHED();
        ur[s] = pfma(ui[s], nki, t0); SCHED();
        ui[s] = pfma(ui[s], -kr, t1); SCHED();
      }
    }
#pragma unroll
    for (int s = 0; s < 16; s += 4) asm volatile("" : "+v"(ur[s]), "+v"(ui[s]), "+v"(ur[s + 1]), "+v"(ui[s + 1]), "+v"(ur[s + 2]), "+v"(ui[s + 2]), "+v"(ur[s + 3]), "+v"(ui[s + 3]));
    f32x2 kP = cmul(kk, P), kE = cmul(kk, E0);
    asm volatile("" : "+v"(kP), "+v"(kE));
    setup2(6);
    wait_vm<0>();
    if (r > 0) {
      u32x4 tv[4];
#pragma unroll 1
      for (int spin = 0; spin < SPIN_CAP; ++spin) {
        bool ok = true;
#pragma unroll
        for (int i = 0; i < 4; ++i) {
          tv[i] = *(volatile LAS u32x4*)(size_t)(home + 16384 + i * 1024 + lane * 16);
          ok &= (4 * i + g >= r) | ((tv[i].x == tagA) & (tv[i].z == tagB));
        }
        if (__all(ok)) break;
        COUNT(20);
        __builtin_amdgcn_s_sleep(8);
        fetch();
        wait_vm<0>();
      }
#pragma unroll
      for (int i = 0; i < 4; ++i) car = csel(4 * i + g < r, cfma(a512, car, (f32x2){u2f(tv[i].y), u2f(tv[i].w)}), car);
      car = cmul(car, acar);
      float cr4[4], ci4[4];
      rows4(car[0], cr4); rows4(car[1], ci4);
      car = (f32x2){(cr4[0] + cr4[1]) + (cr4[2] + cr4[3]), (ci4[0] + ci4[1]) + (ci4[2] + ci4[3])};
    }
    dma_x4(v2, w2b + kstep_of(slot, 5) * 32768, home + 16384);
    STAMP(8);
    {
      const f32x2 h0 = cfma(aseg, car, kP), h1 = cfma(a16, h0, kE);
      f32x2 hr = {h0[0], h1[0]}, hi = {-h0[1], -h1[1]};
      const unsigned on = (qq == g) ? ((pp & 1) ? 0x3f800000u : 0x3f80u) : 0u;
      const u32x4 z0 = {(pp & 2) ? 0u : on, (pp & 2) ? on : 0u, 0u, 0u}, z1 = {0u, 0u, z0.x, z0.y};
      const s16x8 sel0 = __builtin_bit_cast(s16x8, z0), sel1 = __builtin_bit_cast(s16x8, z1);
      const unsigned ho = WS_H + (unsigned)(2 * r) * H_MT + (unsigned)(56 + 2 * j + d) * 4096 + lane * 16;
      u32x4 fr[4], fi[4];
      f32x4 tr[2], ti[2];
      const f32x4 zz = {0, 0, 0, 0};
      auto put = [&](int mp, int k) {
        const u32x4 o = {pk2(tr[k][0], tr[k][1]), pk2(tr[k][2], tr[k][3]), pk2(ti[k][0], ti[k][1]), pk2(ti[k][2], ti[k][3])};
        __builtin_amdgcn_raw_buffer_store_b128(o, wsr, ho + (mp >> 1) * H_MT + (2 * (mp & 1) + k) * 1024, 0, 16);
        SCHED();
      };
      SCHED();
#pragma unroll
      for (int mp = 0; mp < 5; ++mp) {
        if (mp > 0) {
#pragma unroll
          for (int k = 0; k < 2; ++k) {
            tr[k] = mfma(__builtin_bit_cast(s16x8, fr[mp - 1]), k ? sel1 : sel0, zz);
            ti[k] = mfma(__builtin_bit_cast(s16x8, fi[mp - 1]), k ? sel1 : sel0, zz);
          }
          SCHED();
        }
        if (mp < 4) {
#pragma unroll
          for (int e = 0; e < 4; ++e) {
            const int s = 4 * mp + e;
            const f32x2 t1 = pfma(hr, -ai, ui[s]); SCHED();
            const f32x2 t0 = pfma(hi, ai, ur[s]); SCHED();
            hi = pfma(hi, ar, t1); SCHED();
            hr = pfma(hr, ar, t0); SCHED();
            fi[mp][e] = pk2(hi[0], hi[1]); SCHED();
            fr[mp][e] = pk2(hr[0], hr[1]); SCHED();
          }
        }
        TRACE2(4 + 2 * mp);
        if (mp == 2) snap(false);
        if (mp > 0) { put(mp - 1, 0); put(mp - 1, 1); }
        TRACE2(5 + 2 * mp);
      }
      STAMP(19);
      if (lane < 2) pubo = WS_FLAG + (2 * r + lane) * 2048 + 1024 + (2 * j + d) * 8;
    }
    __builtin_amdgcn_s_setprio(0);
    STAMP(18);
  };
  auto roleB = [&]() __attribute__((always_inline)) {
    home = HB + SLOT;
    mktag();
    {
      float s0 = 0.f, s1 = 0.f;
#pragma unroll
      for (int e = 0; e < 8; ++e) { s0 = sadd(s0, bflo(prb[e >> 2][e & 3])); s1 = sadd(s1, bfhi(prb[e >> 2][e & 3])); }
      *(LAS float*)(size_t)(LB + L_LS + 512 + (4 * d + g) * 4) = row16_sum(s0 + s1);
    }
    if (mlp) {
      f32x4 acc[6][4];
#pragma unroll
      for (int s = 0; s < 6; ++s) {
#pragma unroll
        for (int m = 0; m < 4; ++m) acc[s][m] = (f32x4){0, 0, 0, 0};
        asm volatile("" : "+v"(acc[s][0]), "+v"(acc[s][1]), "+v"(acc[s][2]), "+v"(acc[s][3]));
      }
      unsigned ax[2], aw[6];
      ax[0] = LB + (64 * d + i16) * 128 + kx; ax[1] = ax[0] ^ 64;
#pragma unroll
      for (int s = 0; s < 6; ++s) aw[s] = awb + ((s ^ awx) * 32);
      unsigned sta = LB + L_LS + qq * 288 + (8 * d + ((i16 >> 1) & 1)) * 16 + (i16 & 1) * 4, csa = LB + L_CS + g * 16;
      asm volatile("" : "+v"(ax[0]), "+v"(ax[1]), "+v"(aw[0]), "+v"(aw[1]), "+v"(aw[2]), "+v"(aw[3]), "+v"(aw[4]), "+v"(aw[5]), "+v"(sta), "+v"(csa));
      s16x8 xf[2][4];
      s16x4 wf[2][12];
      auto rd = [&](int set, int i) {
        if (i < 4) xf[set][i] = lds128(ax[set] + i * 2048);
        else wf[set][i - 4] = ldstr4(aw[(i - 4) >> 1] + set * 8192 + (i & 1) * 1024);
      };
      auto mm = [&](int set, int i) { const int s = i >> 2, m = i & 3; acc[s][m] = mfma(cat(wf[set][2 * s], wf[set][2 * s + 1]), xf[set][m], acc[s][m]); };
      {
        float t[8];
#pragma unroll
        for (int e = 0; e < 8; ++e)
          t[e] = (e & 1) ? sadd(sadd(bfhi(pre[0][e >> 1]), bfhi(pre[1][e >> 1])), sadd(bfhi(pre[2][e >> 1]), bfhi(pre[3][e >> 1])))
                         : sadd(sadd(bflo(pre[0][e >> 1]), bflo(pre[1][e >> 1])), sadd(bflo(pre[2][e >> 1]), bflo(pre[3][e >> 1])));
#pragma unroll
        for (int e = 0; e < 4; ++e) { const u32x2 h = swap32(t[e], t[e + 4]); t[e] = sadd(u2f(h.x), u2f(h.y)); }
#pragma unroll
        for (int e = 0; e < 2; ++e) { const u32x2 h = swap16(t[e], t[e + 2]); t[e] = dpp_add<0x128>(sadd(u2f(h.x), u2f(h.y))); }
        *(LAS f32x2*)(size_t)(LB + L_LS + d * 256 + (4 * (lane & 7) + g) * 8) = (f32x2){t[0], t[1]};
      }
      __builtin_amdgcn_s_setprio(1);
      STAMP(1);
      barrier();
      STAMP(2); STAMPC(21);
#pragma unroll
      for (int i = 0; i < 16; ++i) rd(0, i);
      wait_lgkm0();
      auto stage1 = [&](int c, int dl) {
        SCHED();
#pragma unroll
        for (int i = 0; i < 24; ++i) {
          mm(0, i); SCHED();
          if (i < 16) rd(1, i);
          else if (i < 22) adv(aw[i - 16], dl);
          else if (i == 22) adv(ax[0], dl);
          SCHED();
        }
        TRACE(2 * c);
        barrier();
        TRACE(2 * c + 1);
        SCHED();
      };
      int ph = 0;
#pragma unroll 1
      for (int c = 1; c < 8; ++c) {
        const int dl = ph == 2 ? -2 * (int)SLOT : (int)SLOT;
        ph = ph == 2 ? 0 : ph + 1;
        stage1(c, dl);
#pragma unroll
        for (int i = 0; i < 24; ++i) {
          mm(1, i); SCHED();
          if (i < 16) rd(0, i);
          else if (i == 16) adv(ax[1], dl);
          else if (i == 23) wait_lgkm0();
          SCHED();
        }
      }
      stage1(8, 0);
      unsigned b1a = LB + L_B1 + g * 8;
      asm volatile("" : "+v"(b1a));
      STAMP(4); STAMPC(22);
      f32x2 rs[4], nm[4];
      f32x4 cs[6];
      u32x2 b1v[6];
      f32x2 st[4];
      auto colrd = [&](int s) { cs[s] = ldsf4(csa + 64 * s); b1v[s] = *(LAS u32x2*)(size_t)(b1a + 32 * s); };
#pragma unroll
      for (int i = 0; i < 4; ++i) st[i] = (f32x2){ldsf1(sta + i * 32), ldsf1(sta + i * 32 + 8)};
      colrd(0); colrd(1);
      SCHED();
#pragma unroll
      for (int i = 0; i < BSPLIT; ++i) mm(1, i);
      SCHED();
#pragma unroll
      for (int i = 0; i < 4; ++i) { rs[i] = bco(st[i][0]); nm[i] = bco(-st[i][1]); }
      STAMP(5);
      const unsigned fo = WS_FLAG + (2 * r + d) * 2048;
      const unsigned ho = WS_H + (unsigned)(2 * r + d) * H_MT + (unsigned)(4 * j) * 4096 + lane * 16;
#pragma unroll
      for (int pr = 0; pr < 3; ++pr) {
        Col col;
        mkcol(col, cs[2 * pr], cs[2 * pr + 1], b1v[2 * pr], b1v[2 * pr + 1]);
        if (pr < 2) { colrd(2 * pr + 2); colrd(2 * pr + 3); }
        SCHED();
        if (pr == 1) {
#pragma unroll
          for (int i = BSPLIT; i < 24; ++i) mm(1, i);
          SCHED();
          STAMP(7);
          prog_wait(PW + (6 + d) * 4, 1);
#pragma unroll
          for (int i = 0; i < 5; ++i) absorb(i, home + i * 4096);
          SCHED();
          if (BFLAG0 < 0) {
            wait_vm<0>();
            if (lane == 0) __builtin_amdgcn_raw_buffer_store_b64((u32x2){tagA, tagB}, wsr, fo + flag_of(j, 0), 0, 16);
          }
          STAMP(8);
        }
#pragma unroll
        for (int m = 0; m < 4; ++m) {
          __builtin_amdgcn_raw_buffer_store_b128(gelu8(acc[2 * pr][m], acc[2 * pr + 1][m], col, rs[m], nm[m]), wsr, ho + pr * 4096 + m * 1024, 0, 16);
          TRACE2(1 + 4 * pr + m);
          if (pr == 1 && m == 0) { wait_lgkm0(); prog_set(PW + w * 4, 1); }
          if (pr > 0 && m == (pr == 1 ? BFLAG0 : BFLAG)) {
            if (pr == 1) wait_vm<(BFLAG0 < 0 ? 0 : BFLAG0 + 1)>(); else wait_vm<BFLAG + 1>();
            if (lane == 0) __builtin_amdgcn_raw_buffer_store_b64((u32x2){tagA, tagB}, wsr, fo + flag_of(j, pr - 1), 0, 16);
          }
          if (pr == 2 && m == 1) snap(false);
          SCHED();
        }
      }
      STAMP(6);
      setup2(6);
      if (lane == 0) pubo = fo + flag_of(j, 2);
      __builtin_amdgcn_s_setprio(0);
    } else {
#pragma unroll 1
      for (int c = 0; c < 9; ++c) barrier();
      prog_wait(PW + (6 + d) * 4, 1);
#pragma unroll
      for (int i = 0; i < 5; ++i) absorb(i, home + i * 4096);
      wait_lgkm0();
      prog_set(PW + w * 4, 1);
      setup2(6);
      snap(false);
    }
  };
  auto roleA = [&]() __attribute__((always_inline)) {
    home = HB + 2 * SLOT;
    const int srow = 128 * r + 8 * j + 4 * d + g;
    u32x2 dv[4], bv[4];
#pragma unroll
    for (int n = 0; n < 4; ++n) { dv[n] = *(const GAS u32x2*)(p.dsk + ntile * 128 + n * 32 + g * 8); bv[n] = *(const GAS u32x2*)(p.b2 + ntile * 128 + n * 32 + g * 8); }
    if (tid < 16) *(LAS unsigned*)(size_t)(PW + tid * 4) = 0u;
    f32x4 acc[2][4];
#pragma unroll
    for (int m = 0; m < 4; ++m) { acc[0][m] = (f32x4){0, 0, 0, 0}; acc[1][m] = (f32x4){0, 0, 0, 0}; asm volatile("" : "+v"(acc[0][m]), "+v"(acc[1][m])); }
    unsigned ax0 = LB + (64 * d + i16) * 128 + kx, ax1 = ax0 ^ 64, aw0 = awb + ((6 ^ awx) * 32), aw1 = awb + ((7 ^ awx) * 32);
    unsigned sta = LB + L_LS + qq * 288 + (8 * d + ((i16 >> 1) & 1)) * 16 + (i16 & 1) * 4, csa = LB + L_CS + 384 + g * 16;
    asm volatile("" : "+v"(ax0), "+v"(ax1), "+v"(aw0), "+v"(aw1), "+v"(sta), "+v"(csa));
    int ph = 0;
    auto chunk = [&](auto side) {
      s16x8 xf[2][4], wf[2][2];
      if (ADELAY) __builtin_amdgcn_s_sleep(ADELAY);
#pragma unroll
      for (int k = 0; k < 2; ++k) {
        wf[k][0] = ldstr(aw0 + k * 8192, aw0 + k * 8192 + 1024);
        wf[k][1] = ldstr(aw1 + k * 8192, aw1 + k * 8192 + 1024);
#pragma unroll
        for (int m = 0; m < 4; ++m) xf[k][m] = lds128((k ? ax1 : ax0) + m * 2048);
      }
      SCHED();
      const int dl = ph == 2 ? -2 * (int)SLOT : (int)SLOT;
      ph = ph == 2 ? 0 : ph + 1;
      adv(ax0, dl); adv(ax1, dl); adv(aw0, dl); adv(aw1, dl);
      side(8);
      SCHED();
#pragma unroll
      for (int k = 0; k < 2; ++k)
#pragma unroll
        for (int m = 0; m < 4; ++m) {
          acc[0][m] = mfma(wf[k][0], xf[k][m], acc[0][m]); acc[1][m] = mfma(wf[k][1], xf[k][m], acc[1][m]);
          SCHED();
          side(4 * k + m);
          SCHED();
        }
    };
    auto none = [](int) {};
    mktag();
    asm volatile("" : "+v"(pre[3]));
    STAMP(19);
    {
      const float mean = sum32(pre) * (1.f / 512.f);
      float v0 = 0.f, v1 = 0.f;
#pragma unroll
      for (int i = 0; i < 4; ++i)
#pragma unroll
        for (int e = 0; e < 4; ++e) {
          const float d0 = bflo(pre[i][e]) - mean, d1 = bfhi(pre[i][e]) - mean;
          v0 = sfma(d0, d0, v0); v1 = sfma(d1, d1, v1);
        }
      const float rsd = __builtin_amdgcn_rsqf(row16_sum(v0 + v1) * (1.f / 512.f) + 1e-5f);
      if (i16 == 0) __builtin_amdgcn_raw_buffer_store_b128((u32x4){tagA, f2u(rsd), tagB, f2u(mean * rsd)}, wsr, WS_STAT + srow * 16, 0, 16);
    }
    __builtin_amdgcn_s_setprio(APRIO);
    STAMP(1);
    barrier();
    STAMP(2);
    chunk(none);
    if (d) {
      const f32x2 c0 = ldsf2(LB + L_LS + 512 + (lane & 3) * 8);
      if (lane < 4) __builtin_amdgcn_raw_buffer_store_b128((u32x4){tagA, f2u(sadd(c0[0], c0[1])), tagB, 0u}, wsr, WS_BS + j * 1024 + (4 * r + lane) * 16, 0, 16);
    } else if (mlp) {
      const f32x2 c0 = ldsf2(LB + L_LS + (lane & 31) * 8), c1 = ldsf2(LB + L_LS + 256 + (lane & 31) * 8);
      if (lane < 32) __builtin_amdgcn_raw_buffer_store_b128((u32x4){tagA, f2u(sadd(c0[0], c1[0])), tagB, f2u(sadd(c0[1], c1[1]))}, wsr, WS_CS + (j * 16 + r) * 512 + lane * 16, 0, 16);
    }
    barrier();
#pragma unroll 1
    for (int c = 1; c < 3; ++c) { chunk(none); barrier(); }
    chunk(none);
    asm volatile("" : "+v"(dv[0]), "+v"(dv[1]), "+v"(dv[2]), "+v"(dv[3]), "+v"(bv[0]), "+v"(bv[1]), "+v"(bv[2]), "+v"(bv[3]));
    u32x2 xv[2][4];
#pragma unroll
    for (int m = 0; m < 2; ++m)
#pragma unroll
      for (int n = 0; n < 4; ++n) xv[m][n] = *(LAS u32x2*)(size_t)(HB + 3 * SLOT + 12288 + (16 * m + i16) * 128 + (g & 1) * 8 + (((2 * n + (g >> 1)) ^ (i16 >> 1)) * 16));
    barrier();
#pragma unroll 1
    for (int c = 4; c < 6; ++c) { chunk(none); barrier(); }
    f32x2 st[4];
#pragma unroll
    for (int m = 0; m < 4; ++m) st[m] = (f32x2){ldsf1(sta + m * 32), ldsf1(sta + m * 32 + 8)};
    unsigned ra[4], vc[8], ho = mlp ? WS_H + (unsigned)(2 * r + d) * H_MT + (unsigned)(4 * j + 3) * 4096 + lane * 16 : 0xffff0000u;
    f32x2 rs[4], nm[4];
    chunk([&](int k) {
      if (k < 4) { rs[k] = bco(st[k][0]); nm[k] = bco(-st[k][1]); asm volatile("" : "+v"(rs[k]), "+v"(nm[k])); }
      if (k < 8) return;
#pragma unroll
      for (int n = 0; n < 4; ++n) ra[n] = home + r2l + ((n ^ r2x) * 32);
      asm volatile("" : "+v"(ra[0]), "+v"(ra[1]), "+v"(ra[2]), "+v"(ra[3]), "+v"(ho));
    });
    TRACE(14); barrier(); TRACE(15);
    STAMP(4);
    const f32x4 cs0 = ldsf4(csa), cs1 = ldsf4(csa + 64);
    const u32x2 b10 = *(LAS u32x2*)(size_t)(LB + L_B1 + 192 + g * 8), b11 = *(LAS u32x2*)(size_t)(LB + L_B1 + 224 + g * 8);
    Col col;
    auto take = [&](int i, int pg) {
#pragma unroll
      for (int n = 0; n < 4; ++n) we[i][n] = ldstr(ra[n] + pg * 4096, ra[n] + pg * 4096 + 2048);
    };
    chunk([&](int k) {
      if (k == 2) { mkcol(col, cs0, cs1, b10, b11); asm volatile("" : "+v"(col.bias[0]), "+v"(col.bias[1]), "+v"(col.bias[2]), "+v"(col.bias[3])); }
      if (k >= 3 && k < 6) take(k - 3, k - 3);
    });
    barrier();
    __builtin_amdgcn_s_setprio(2);
    STAMP(5);
#pragma unroll
    for (int m = 0; m < 4; ++m) {
      TRACE2(1 + 2 * m);
      const u32x4 o = gelu8(acc[0][m], acc[1][m], col, rs[m], nm[m]);
      TRACE2(2 + 2 * m);
      __builtin_amdgcn_raw_buffer_store_b128(o, wsr, ho + m * 1024, 0, 16);
      if (m == 0) { take(3, 3); take(4, 4); }
      SCHED();
    }
    STAMP(6);
#pragma unroll
    for (int m = 0; m < 2; ++m) {
      const u32x4 s2 = *(LAS u32x4*)(size_t)(LB + L_ST2 + (tok + 8 * (mtile & 1) + 4 * d + 2 * m) * 16);
      const f32x2 r2 = bco(u2f(s2.y)), n2 = bco(-u2f(s2.w));
#pragma unroll
      for (int n = 0; n < 4; ++n) {
        f32x2 o[2];
#pragma unroll
        for (int h = 0; h < 2; ++h) {
          const f32x2 xx = {bflo(xv[m][n][h]), bfhi(xv[m][n][h])};
          o[h] = pfma(pfma(xx, r2, n2), (f32x2){bflo(dv[n][h]), bfhi(dv[n][h])}, xx + (f32x2){bflo(bv[n][h]), bfhi(bv[n][h])});
        }
        acc2[n][m] = __builtin_shufflevector(o[0], o[1], 0, 1, 2, 3);
        acc2[n][2 + m] = (f32x4){0, 0, 0, 0};
        asm volatile("" : "+v"(acc2[n][m]), "+v"(acc2[n][2 + m]));
      }
    }
    STAMP(7);
    wait_vm<0>();
    if (mlp && lane == 0) __builtin_amdgcn_raw_buffer_store_b64((u32x2){tagA, tagB}, wsr, WS_FLAG + (2 * r + d) * 2048 + flag_of(j, 3), 0, 16);
    STAMP(18);
    mkvc(vc);
    setup2(8);
    prog_wait(PW + (6 + d) * 4, 2);
#pragma unroll
    for (int i = 5; i < 8; ++i) take(i, i - 5);
    wait_lgkm0();
    snap(false);
    loadC(vc, kg, home);
    STAMP(8);
    __builtin_amdgcn_s_setprio(0);
  };

  auto phase2 = [&](auto nm_, auto dep_) __attribute__((always_inline)) {
    STAMP(9);
    constexpr int NM = decltype(nm_)::value, N = NM + 4, DEP = decltype(dep_)::value;
    const u64 tag = ((u64)tagB << 32) | tagA;
    auto has = [&](int k) { return (k < NM ? flm[k < NM ? k : 0] : fls[k < NM ? 0 : k - NM]) == tag; };
    auto need = [&](int k, bool ok) {
      if (__builtin_expect(!ok, 0)) {
#pragma unroll 1
        for (int spin = 1; spin < SPIN_CAP; ++spin) {
          snap(k >= NM);
          if (has(k)) break;
          if (!(spin & 31)) {
            const u32x2 f = __builtin_amdgcn_raw_buffer_load_b64(wsr, 0, k < NM ? fm + 8 * k : fs + 8 * (k - NM), 16);
            if (__builtin_amdgcn_readfirstlane(f.x) == tagA && __builtin_amdgcn_readfirstlane(f.y) == tagB) break;
          }
          __builtin_amdgcn_s_sleep(PSLEEP);
        }
      }
      asm volatile("" ::: "memory");
    };
    auto publish = [&]() { __builtin_amdgcn_raw_buffer_store_b64((u32x2){tagA, tagB}, wsr, pubo, 0, 16); };
    auto loadH = [&](int i, int m) {
      if (m == 0) asm volatile("" : "+v"(hb));
      return __builtin_amdgcn_raw_buffer_load_b128(wsr, hb + (m & 1) * 1024, __builtin_amdgcn_readlane(ksl, i) ^ ((m & 2) * 1024), 16);
    };
    u32x4 hf[N][4];
    auto load4 = [&](int i) {
#pragma unroll
      for (int m = 0; m < 4; ++m) hf[i][m] = loadH(i, m);
    };
    STAMP(10);
    if (NM == 6 && __builtin_expect(!(has(0) && has(1)), 0)) { wait_vm<0>(); publish(); pubo = 0xffff0000u; }
    need(0, has(0)); load4(0);
    need(1, has(1)); load4(1);
    if (NM == 6) { wait_vm<8>(); publish(); }
    STAMP(11);
    s16x8 cfr[2][4];
    bool okn = false;
    SCHED();
#pragma unroll
    for (int i = 0; i < N; ++i) {
      const int k = i == 0 ? 2 : i + DEP;
      if (i == 1) STAMP(12);
      if (k == NM) STAMP(13);
      if (k < N) need(k, (i & 1) && k < NM ? okn : has(k));
      if (k == NM) STAMP(14);
      if (i > 0 || DEP == 2) {
        if (!(i & 1) && k + 1 < NM) {
          okn = has(k + 1);
          if (k + 2 < NM) snap(false);
        }
        if (k + 2 == NM) snap(true);
      }
      if (NM == 8 && i == 1 && w >= 6) prog_set(PW + w * 4, 3);
      if (NM == 6 && i == 3) { prog_wait(PW + (6 + d) * 4, 3); absorb(5, home + 16384); }
      if (i >= NM - 1 && i < N - 1) {
#pragma unroll
        for (int n = 0; n < 4; ++n) {
          const unsigned a = (crd ^ (32 * (i - NM + 1))) + n * 2048;
          cfr[(i + 1) & 1][n] = cat(*(LAS s16x4*)(size_t)a, *(LAS s16x4*)(size_t)(a + 8192));
        }
      }
      SCHED();
#pragma unroll
      for (int m = 0; m < 4; ++m) {
#pragma unroll
        for (int n = 0; n < 4; ++n) acc2[n][m] = mfma(i < NM ? we[i < NM ? i : 0][n] : cfr[i & 1][n], __builtin_bit_cast(s16x8, hf[i][m]), NM == 6 && i == 0 ? (f32x4){0, 0, 0, 0} : acc2[n][m]);
        SCHED();
        if (k < N) hf[k < N ? k : 0][m] = loadH(k, m);
        SCHED();
      }
      if (i == 0 && DEP == 3) {
        need(3, has(3)); load4(3);
        okn = has(4);
        snap(false);
        SCHED();
      }
    }
  };
  if (__builtin_expect(w >= 6 || w < 2, 1)) {
    if (__builtin_expect(w >= 6, 1)) roleD(); else roleA();
    phase2(std::integral_constant<int, 8>{}, std::integral_constant<int, DEPE>{});
  } else {
    if (w >= 4) roleL(); else roleB();
    phase2(std::integral_constant<int, 6>{}, std::integral_constant<int, DEPL>{});
  }
  STAMP(15);
  int tl = threadIdx.x;
  asm volatile("" : "+v"(tl));
  const int i16t = tl & 15, gt = (tl >> 4) & 3;
#pragma unroll
  for (int m = 0; m < 4; ++m)
#pragma unroll
    for (int n = 0; n < 4; ++n) *(LAS f32x4*)(size_t)(home + ((16 * m + i16t) ^ (w == 1 ? 32 : 0)) * 256 + (((4 * n + gt) ^ i16t) * 16)) = acc2[n][m];
  const int ck = tl & 15;
  const int orow = 32 * ((tl >> 6) & 3) + 16 * ((tl >> 4) & 1) + ((tl >> 5) & 1) + 2 * (tl >> 8) + 8 * (mtile & 1);
  char* op = p.out + (128 * (mtile >> 1) + orow) * 2048 + ntile * 256 + ck * 16;
  unsigned ra[4];
#pragma unroll
  for (int k = 0; k < 4; ++k) ra[k] = LB + (tl >> 4) * 256 + ((ck ^ ((tl >> 4) & 15)) * 16) + k * SLOT;
  asm volatile("" : "+v"(ra[0]), "+v"(ra[1]), "+v"(ra[2]), "+v"(ra[3]), "+v"(op));
  barrier();
  STAMP(16);
  {
    f32x4 pt[2][8];
#pragma unroll
    for (int h = 0; h < 2; ++h)
#pragma unroll
      for (int k = 0; k < 8; ++k) pt[h][k] = ldsf4(ra[k >> 1] + (k & 1) * HALF + h * 8192);
#pragma unroll
    for (int h = 0; h < 2; ++h) {
      const f32x4 s = ((pt[h][0] + pt[h][1]) + (pt[h][2] + pt[h][3])) + ((pt[h][4] + pt[h][5]) + (pt[h][6] + pt[h][7]));
      __builtin_nontemporal_store(s, (GAS f32x4*)(op + h * 8192));
    }
  }
  if (b == 255 && tl == 0) __builtin_amdgcn_raw_buffer_store_b64((u32x2){tagA, tagB}, wsr, WS_EPOCH, 0, 16);
}

extern "C" void kernel_launch(void* const* d_in, const int* in_sizes, int n_in, void* d_out, int out_size, void* d_ws,
                              size_t ws_size, hipStream_t stream) {
  Params p;
  p.x = (const char*)d_in[0]; p.lre = (const char*)d_in[1]; p.lim = (const char*)d_in[2];
  p.bre = (const char*)d_in[3]; p.bim = (const char*)d_in[4]; p.cre = (const char*)d_in[5]; p.cim = (const char*)d_in[6];
  p.logdt = (const char*)d_in[7]; p.dsk = (const char*)d_in[8];
  p.w1 = (const char*)d_in[10]; p.b1 = (const char*)d_in[11]; p.w2 = (const char*)d_in[12]; p.b2 = (const char*)d_in[13];
  p.out = (char*)d_out; p.ws = (char*)d_ws;
  fused_kernel<<<256, 512, 0, stream>>>(p);
}
```

```cpp
#include <hip/hip_runtime.h>
#include <stdint.h>
#include <type_traits>

typedef short s16x4 __attribute__((ext_vector_type(4)));
typedef short s16x8 __attribute__((ext_vector_type(8)));
typedef __bf16 bf16x8 __attribute__((ext_vector_type(8)));
typedef __bf16 bf16x2 __attribute__((ext_vector_type(2)));
typedef float f32x4 __attribute__((ext_vector_type(4)));
typedef float f32x2 __attribute__((ext_vector_type(2)));
typedef unsigned u32x4 __attribute__((ext_vector_type(4)));
typedef unsigned u32x2 __attribute__((ext_vector_type(2)));
typedef int i32x4 __attribute__((ext_vector_type(4)));
typedef unsigned long long u64;
typedef u64 u64x4 __attribute__((ext_vector_type(4)));
typedef u64 u64x8 __attribute__((ext_vector_type(8)));
typedef int i32x8 __attribute__((ext_vector_type(8)));
typedef int i32x16 __attribute__((ext_vector_type(16)));
__device__ i32x8 sbl8(i32x4, int, int) __asm("llvm.amdgcn.s.buffer.load.v8i32");
__device__ i32x16 sbl16(i32x4, int, int) __asm("llvm.amdgcn.s.buffer.load.v16i32");
#define LAS __attribute__((address_space(3)))
#define GAS __attribute__((address_space(1)))
#define DI __device__ __forceinline__

#define STAMPS 0
#define TRACING 0
#define GATHER_AT 4
#define ROT 1
#define TOUCH 1
#define TLBW 0
#define NOFF 0
#define PSLEEP 4
#define BFLAG 2
#define ABAR 0
#define KROT 0
#define LPRIO 3
#define CHK 0
#define BSPLIT 8
#define APRIO 0
#define ADELAY 0
#define BFLAG0 -1
#define DEPE 2
#define DEPL 2
#define LB_SLEEP 8
#define CHECK_AT 5
#define CPF 1
#define MAP 1

struct Params {
  const char *x, *lre, *lim, *bre, *bim, *cre, *cim, *logdt, *dsk, *w1, *b1, *w2, *b2;
  char *out, *ws;
};

constexpr unsigned WS_EPOCH = 0;
constexpr unsigned WS_STAT = 4096;
constexpr unsigned WS_TOT = WS_STAT + 2048 * 16;
constexpr unsigned WS_CS = WS_TOT + 16 * 2 * 16 * 256;
constexpr unsigned WS_BS = WS_CS + 16 * 16 * 512;
constexpr unsigned WS_FLAG = WS_BS + 16 * 1024;
constexpr unsigned WS_H = WS_FLAG + 32 * 2048;
constexpr unsigned H_MT = 88 * 4096;
constexpr unsigned WS_END = WS_H + 32 * H_MT;
constexpr unsigned WS_STAMP = 32u << 20;
constexpr unsigned WS_B2B = WS_STAMP + (4u << 20);
constexpr unsigned SLOT = 40960, HALF = 20480, SL_W = 16384, SL_B = 32768;
constexpr unsigned L_ST1 = 3 * SLOT + 16384, L_ST2 = L_ST1 + 2048;
constexpr unsigned L_CS = 3 * SLOT + HALF + 16384, L_FLG = L_CS + 512, L_B1 = L_CS + 768, L_BS = L_CS + 1024, L_LS = L_CS + 2048, L_DUMMY = L_CS + 3328;
constexpr unsigned LDS_BYTES = 163840;
constexpr int SPIN_CAP = 40000;
constexpr unsigned CODE_BYTES = 43520;

DI void dma4w(unsigned voff, const char* sbase, unsigned ldsaddr) {
  asm volatile("s_mov_b32 m0, %2\n\ts_nop 0\n\tglobal_load_lds_dword %0, %1" ::"v"(voff), "s"(sbase), "s"(ldsaddr));
}
DI void dma_x4(const unsigned* v, const char* sbase, unsigned ldsaddr, bool low = false) {
  if (low) {
    asm volatile("s_mov_b32 m0, %5\n\ts_setprio 0\n\t"
               "global_load_lds_dwordx4 %0, %4\n\tglobal_load_lds_dwordx4 %1, %4 offset:1024\n\t"
               "global_load_lds_dwordx4 %2, %4 offset:2048\n\tglobal_load_lds_dwordx4 %3, %4 offset:3072\n\ts_setprio 3"
               ::"v"(v[0]), "v"(v[1]), "v"(v[2]), "v"(v[3]), "s"(sbase), "s"(ldsaddr));
    return;
  }
  asm volatile("s_mov_b32 m0, %5\n\ts_nop 0\n\t"
               "global_load_lds_dwordx4 %0, %4\n\tglobal_load_lds_dwordx4 %1, %4 offset:1024\n\t"
               "global_load_lds_dwordx4 %2, %4 offset:2048\n\tglobal_load_lds_dwordx4 %3, %4 offset:3072"
               ::"v"(v[0]), "v"(v[1]), "v"(v[2]), "v"(v[3]), "s"(sbase), "s"(ldsaddr));
}
DI void dma_x8(const unsigned* v, const char* sbase, unsigned ldsaddr) {
  asm volatile("s_mov_b32 m0, %9\n\ts_nop 0\n\t"
               "global_load_lds_dwordx4 %0, %8 offset:-4096\n\tglobal_load_lds_dwordx4 %1, %8 offset:-3072\n\t"
               "global_load_lds_dwordx4 %2, %8 offset:-2048\n\tglobal_load_lds_dwordx4 %3, %8 offset:-1024\n\t"
               "global_load_lds_dwordx4 %4, %8\n\tglobal_load_lds_dwordx4 %5, %8 offset:1024\n\t"
               "global_load_lds_dwordx4 %6, %8 offset:2048\n\tglobal_load_lds_dwordx4 %7, %8 offset:3072"
               ::"v"(v[0]), "v"(v[1]), "v"(v[2]), "v"(v[3]), "v"(v[4]), "v"(v[5]), "v"(v[6]), "v"(v[7]), "s"(sbase), "s"(ldsaddr + 4096u));
}
constexpr int PG(int i) { return i & 3; }
template <int N> DI void wait_vm() { asm volatile("s_waitcnt vmcnt(%0)" ::"i"(N) : "memory"); }
DI void wait_lgkm0() { asm volatile("" ::: "memory"); __builtin_amdgcn_s_waitcnt(0xC07F); asm volatile("" ::: "memory"); }
DI void barrier() {
  asm volatile("" ::: "memory");
  __builtin_amdgcn_s_waitcnt(0xC07F);
  __builtin_amdgcn_s_barrier();
  asm volatile("" ::: "memory");
}
#define SCHED() __builtin_amdgcn_sched_barrier(0)
DI s16x8 lds128(unsigned a) { return *(LAS s16x8*)(size_t)a; }
DI f32x4 ldsf4(unsigned a) { return *(LAS f32x4*)(size_t)a; }
DI f32x2 ldsf2(unsigned a) { return *(LAS f32x2*)(size_t)a; }
DI float ldsf1(unsigned a) { return *(LAS float*)(size_t)a; }
DI s16x4 ldstr4(unsigned a) { return __builtin_amdgcn_ds_read_tr16_b64_v4i16((LAS s16x4*)(size_t)a); }
DI s16x8 cat(s16x4 lo, s16x4 hi) { return __builtin_shufflevector(lo, hi, 0, 1, 2, 3, 4, 5, 6, 7); }
DI s16x8 ldstr(unsigned a0, unsigned a1) { return cat(ldstr4(a0), ldstr4(a1)); }
DI f32x4 mfma(s16x8 a, s16x8 b, f32x4 c) {
  return __builtin_amdgcn_mfma_f32_16x16x32_bf16(__builtin_bit_cast(bf16x8, a), __builtin_bit_cast(bf16x8, b), c, 0, 0, 0);
}
DI unsigned pk2(float a, float b) { f32x2 v = {a, b}; return __builtin_bit_cast(unsigned, __builtin_convertvector(v, bf16x2)); }
DI float u2f(unsigned u) { return __builtin_bit_cast(float, u); }
DI unsigned f2u(float u) { return __builtin_bit_cast(unsigned, u); }
DI float bflo(unsigned u) { return __builtin_bit_cast(float, u << 16); }
DI float bfhi(unsigned u) { return __builtin_bit_cast(float, u & 0xffff0000u); }
template <int CTRL> DI float dpp_add(float v) {
  return v + __builtin_bit_cast(float, __builtin_amdgcn_update_dpp(0, __builtin_bit_cast(int, v), CTRL, 0xf, 0xf, true));
}
DI float row16_sum(float v) {
  v = dpp_add<0xB1>(v);
  v = dpp_add<0x4E>(v);
  v = dpp_add<0x141>(v);
  v = dpp_add<0x140>(v);
  return v;
}
DI float smul(float a, float b) { float d; asm("v_mul_f32 %0, %1, %2" : "=v"(d) : "v"(a), "v"(b)); return d; }
DI float sadd(float a, float b) { float d; asm("v_add_f32 %0, %1, %2" : "=v"(d) : "v"(a), "v"(b)); return d; }
DI float sfma(float a, float b, float c) { float d; asm("v_fma_f32 %0, %1, %2, %3" : "=v"(d) : "v"(a), "v"(b), "v"(c)); return d; }
DI float sfms(float a, float b, float c) { float d; asm("v_fma_f32 %0, %1, %2, -%3" : "=v"(d) : "v"(a), "v"(b), "v"(c)); return d; }
DI void zero4(f32x4& v) {
  float a, b, c, d;
  asm volatile("v_mov_b32 %0, 0\n\tv_mov_b32 %1, 0\n\tv_mov_b32 %2, 0\n\tv_mov_b32 %3, 0" : "=v"(a), "=v"(b), "=v"(c), "=v"(d));
  v = (f32x4){a, b, c, d};
}
DI u32x2 swap32(float a, float b) { asm volatile("s_nop 1" : "+v"(a), "+v"(b)); return __builtin_amdgcn_permlane32_swap(f2u(a), f2u(b), false, false); }
DI u32x2 swap16(float a, float b) { asm volatile("s_nop 1" : "+v"(a), "+v"(b)); return __builtin_amdgcn_permlane16_swap(f2u(a), f2u(b), false, false); }
DI f32x2 pfma(f32x2 a, f32x2 b, f32x2 c) { return __builtin_elementwise_fma(a, b, c); }
DI f32x2 bc(float a) { return (f32x2){a, a}; }
DI f32x2 bco(float a) { f32x2 v = {a, a}; asm("" : "+v"(v)); return v; }
DI f32x2 cmul(f32x2 a, f32x2 b) { return (f32x2){sfms(a[0], b[0], smul(a[1], b[1])), sfma(a[0], b[1], smul(a[1], b[0]))}; }
DI f32x2 cfma(f32x2 a, f32x2 b, f32x2 c) { return (f32x2){sfms(a[0], b[0], sfms(a[1], b[1], c[0])), sfma(a[0], b[1], sfma(a[1], b[0], c[1]))}; }
DI f32x2 csel(bool p, f32x2 a, f32x2 b) { return (f32x2){p ? a[0] : b[0], p ? a[1] : b[1]}; }
DI void prog_set(unsigned a, unsigned v) {
  asm volatile("s_mov_b64 exec, 1\n\tds_write_b32 %0, %1\n\ts_mov_b64 exec, -1" ::"v"(a), "v"(v) : "memory");
}
DI void prog_wait(unsigned a, unsigned v) {
#pragma unroll 1
  for (int spin = 0; spin < SPIN_CAP * 16; ++spin) {
    if (__builtin_amdgcn_readfirstlane(*(volatile LAS unsigned*)(size_t)a) >= v) break;
    __builtin_amdgcn_s_sleep(1);
  }
  asm volatile("" ::: "memory");
}
DI void bar() { asm volatile("" ::: "memory"); __builtin_amdgcn_s_barrier(); asm volatile("" ::: "memory"); }
DI void dma1_dev(unsigned voff, const char* sbase, unsigned ldsaddr) {
  asm volatile("s_mov_b32 m0, %2\n\ts_nop 0\n\tglobal_load_lds_dwordx4 %0, %1 sc1" ::"v"(voff), "s"(sbase), "s"(ldsaddr) : "memory");
}
DI void dma2_dev(unsigned voff, const char* s0, const char* s1, unsigned ldsaddr) {
  asm volatile("s_mov_b32 m0, %3\n\ts_nop 0\n\tglobal_load_lds_dwordx4 %0, %1 sc1\n\tglobal_load_lds_dwordx4 %0, %2 offset:2048 sc1" ::"v"(voff), "s"(s0), "s"(s1), "s"(ldsaddr) : "memory");
}
DI void dma4_dev(unsigned voff, const char* s0, const char* s1, unsigned ldsaddr) {
  asm volatile("s_mov_b32 m0, %3\n\ts_nop 0\n\tglobal_load_lds_dwordx4 %0, %1 sc1\n\tglobal_load_lds_dwordx4 %0, %1 offset:1024 sc1\n\t"
               "global_load_lds_dwordx4 %0, %2 offset:2048 sc1\n\tglobal_load_lds_dwordx4 %0, %2 offset:3072 sc1" ::"v"(voff), "s"(s0), "s"(s1), "s"(ldsaddr) : "memory");
}
DI int kstep_of(int slot, int i) {
  const int q = slot + 4 * i + (slot >= 4 ? 28 : 0), cls = (q >= 14) + (q >= 28) + (q >= 42);
  return 4 * (q - 14 * cls) + ((0x9C >> (2 * cls)) & 3);
}
DI unsigned flag_of(int j, int c) {
  const int q = 14 * ((0x78 >> (2 * c)) & 3) + j;
  return q < 32 ? (q & 3) * 128 + (q >> 2) * 8 : 512 + (q & 3) * 128 + ((q - 32) >> 2) * 8;
}
DI void cnt_add(unsigned a) {
  asm volatile("s_mov_b64 exec, 1\n\tds_add_u32 %0, %1\n\ts_mov_b64 exec, -1" ::"v"(a), "v"(1u) : "memory");
}
DI f32x2 lo2(f32x4 v) { return __builtin_shufflevector(v, v, 0, 1); }
DI f32x2 hi2(f32x4 v) { return __builtin_shufflevector(v, v, 2, 3); }
DI void rows4(float v, float* o) {
  asm volatile("s_nop 1" : "+v"(v));
  const unsigned u = f2u(v);
  const u32x2 h = __builtin_amdgcn_permlane32_swap(u, u, false, false);
  const u32x2 a = __builtin_amdgcn_permlane16_swap(h.x, h.x, false, false), b = __builtin_amdgcn_permlane16_swap(h.y, h.y, false, false);
  o[0] = u2f(a.x); o[1] = u2f(a.y); o[2] = u2f(b.x); o[3] = u2f(b.y);
}
struct Col { f32x2 cs[4], bias[4]; };
DI void mkcol(Col& c, f32x4 cs0, f32x4 cs1, u32x2 b0, u32x2 b1) {
  c.cs[0] = lo2(cs0); c.cs[1] = hi2(cs0); c.cs[2] = lo2(cs1); c.cs[3] = hi2(cs1);
  c.bias[0] = (f32x2){bflo(b0.x), bfhi(b0.x)}; c.bias[1] = (f32x2){bflo(b0.y), bfhi(b0.y)};
  c.bias[2] = (f32x2){bflo(b1.x), bfhi(b1.x)}; c.bias[3] = (f32x2){bflo(b1.y), bfhi(b1.y)};
}
DI u32x4 gelu8(f32x4 a0, f32x4 a1, const Col& c, f32x2 rs, f32x2 nmrs) {
  f32x2 v[4], z[4];
  u32x4 o;
#define ST4(stmt) _Pragma("unroll") for (int h = 0; h < 4; ++h) { stmt; } SCHED();
  ST4(v[h] = pfma(c.cs[h], nmrs, c.bias[h]))
  ST4(v[h] = pfma(h == 0 ? lo2(a0) : h == 1 ? hi2(a0) : h == 2 ? lo2(a1) : hi2(a1), rs, v[h]))
  ST4(z[h] = v[h] * v[h])
  ST4(z[h] = pfma(z[h], bc(-0.10294324f), bc(-2.30220820f)))
  ST4(z[h] = z[h] * v[h])
  ST4((z[h] = (f32x2){__builtin_amdgcn_exp2f(z[h][0]), __builtin_amdgcn_exp2f(z[h][1])}))
  ST4(z[h] = z[h] + bc(1.f))
  ST4((z[h] = (f32x2){__builtin_amdgcn_rcpf(z[h][0]), __builtin_amdgcn_rcpf(z[h][1])}))
  ST4(v[h] = v[h] * z[h])
  ST4(o[h] = pk2(v[h][0], v[h][1]))
#undef ST4
  return o;
}

#define TRACE(i) do {} while (0)
#define TRACE2(i) do {} while (0)
#define TRACE3(i) do {} while (0)
#define STAMP(k) do {} while (0)
#define STAMPC(k) do {} while (0)
#define COUNT(k) do {} while (0)

__global__ void __launch_bounds__(512, 2) fused_kernel(Params p) {
  __shared__ __attribute__((aligned(16))) char lds_mem[LDS_BYTES];
  const unsigned long long t_start = __builtin_amdgcn_s_memrealtime();
  unsigned long long pc;
  asm volatile("s_getpc_b64 %0" : "=s"(pc));
  const unsigned LB = (unsigned)(size_t)(LAS char*)lds_mem;
  const int tid = threadIdx.x, lane = tid & 63;
  const int w = __builtin_amdgcn_readfirstlane(tid >> 6);
  if (w == 5) {
    const unsigned co = ((unsigned)(lane & 15) * 32u + (blockIdx.x >> 3)) * 128u;
    dma4w(co < CODE_BYTES ? co : 0u, (const char*)(pc & ~255ull), LB + L_DUMMY + 512);
  }
  const int b = blockIdx.x, idx = b >> 3, xcd = b & 7;
  const int r = 8 * (xcd >> 2) + (idx >> 2), j = 4 * (xcd & 3) + (idx & 3);
  const int rot = (2 * (xcd & 3) + 4 * (xcd >> 2)) & 7;
  const int mtile = 4 * xcd + (idx >> 3), ntile = (idx + NOFF) & 7;
  const int i16 = lane & 15, g = lane >> 4, qq = i16 >> 2, pp = i16 & 3;
  const int d = w & 1;
  const bool mlp = j < 14;
  u32x4 pre[4];
  unsigned v_lre, v_lim, v_ldt;
  if ((w >> 1) == 2) {
    const unsigned po = (unsigned)(32 * j + 16 * d + i16) * 2u;
    v_lre = *(const GAS unsigned short*)(p.lre + po); v_lim = *(const GAS unsigned short*)(p.lim + po); v_ldt = *(const GAS unsigned short*)(p.logdt + po);
    asm volatile("" ::: "memory");
  }
  u32x4 prb[2];
  if (w < 4) {
    const bool isb = w >= 2;
    const char* pb = isb ? p.w1 + (64 * (r & 7) + 32 * d) * 3584 + (mlp ? j : 13) * 256 + (r >> 3) * 128 : p.x + (128 * r + 8 * j + 4 * d) * 1024;
    const unsigned po = isb ? (lane >> 3) * 3584 + (lane & 7) * 16 : g * 1024 + i16 * 16, ps = isb ? 28672 : 256;
#pragma unroll
    for (int i = 0; i < 4; ++i) pre[i] = *(const GAS u32x4*)(pb + i * ps + po);
    if (isb) {
#pragma unroll
      for (int i = 0; i < 2; ++i) prb[i] = *(const GAS u32x4*)((r & 8 ? p.bim : p.bre) + (32 * j + 4 * (r & 7) + 2 * d) * 1024 + i * 512 + (lane >> 5) * 1024 + (lane & 31) * 16);
    }
    asm volatile("" ::: "memory");
  }
  const int kgo = 2 * (idx >> 3), kg = (w + KROT * kgo) & 7;
  auto slot_of = [&](int role) { return (((role + 1) & 2) << 1) + (((((role + 1) & 2) && !KROT ? 0 : kgo) + ((role & 1) ? 0 : 2) + d) & 3); };
  const int slot = slot_of(w >> 1);
  const u32x2 ep = *(const __attribute__((address_space(4))) u32x2*)(p.ws + WS_EPOCH);
  unsigned tagA, tagB;
  auto mktag = [&]() { tagA = ep.x; tagB = ep.y; asm volatile("" : "+s"(tagA), "+s"(tagB)); tagA += 1u; tagB += 1u; };
  const __amdgpu_buffer_rsrc_t wsr = __builtin_amdgcn_make_buffer_rsrc(p.ws, 0, WS_END, 0x00020000);
  const i32x4 wss = {(int)(size_t)p.ws, (int)((size_t)p.ws >> 32) & 0xffff, (int)WS_END, 0x00020000};
  auto fresh = [](int o) { asm volatile("" : "+s"(o) :: "memory"); return o; };
  const unsigned HB = LB + d * HALF;
  const unsigned PW = LB + L_FLG;
  s16x8 we[8][4];
  f32x4 acc2[4][4];
  unsigned home;
  const unsigned r2l = (4 * g + qq) * 128 + pp * 8, r2x = (2 * g + (qq >> 1)) & 3;
  auto absorb = [&](int i, unsigned page) {
#pragma unroll
    for (int n = 0; n < 4; ++n) { const unsigned a = page + r2l + ((n ^ r2x) * 32); we[i][n] = ldstr(a, a + 2048); }
  };
  auto mkvc = [&](unsigned* vc) {
#pragma unroll
    for (int i = 0; i < 8; ++i) vc[i] = (lane >> 3) * 1024 + (((lane & 7) ^ (4 * (i & 1) + g)) * 16) + 4096 + i * 7168;
  };
  auto mkv2 = [&](unsigned* v2) {
#pragma unroll
    for (int i = 0; i < 4; ++i) v2[i] = (lane >> 3) * 1024 + (((((lane & 7) >> 1) ^ g) << 1) | (lane & 1)) * 16 + i * 7168;
  };
  auto loadC = [&](const unsigned* vc, int kgc, unsigned dst) {
    const unsigned o = ntile * 65536 + kgc * 128;
    dma_x8(vc, p.cre + o, dst);
    dma_x8(vc, p.cim + o, dst + 8192);
  };
  const char* w2b = p.w2 + ntile * 128;
  auto adv = [&](unsigned& a, int dl) { a += dl; asm volatile("" : "+v"(a)); };
  const int tok = 32 * qq + 16 * (i16 & 1) + ((i16 >> 1) & 1);
  const unsigned kx = (g ^ (i16 >> 1)) * 16;
  const unsigned awb = LB + SL_W + (8 * g + qq) * 256 + pp * 8, awx = qq | ((g & 1) << 2);
  auto sum32 = [&](const u32x4* v) {
    float s0 = 0.f, s1 = 0.f;
#pragma unroll
    for (int i = 0; i < 4; ++i)
#pragma unroll
      for (int e = 0; e < 4; ++e) { s0 += bflo(v[i][e]); s1 += bfhi(v[i][e]); }
    return row16_sum(s0 + s1);
  };
  auto touch = [&]() {
    dma4w(d ? (lane & 15) * 128 : (lane < 56 ? lane : 0) * 128, d ? p.cre + b * 2048 : p.w2 + b * 7168, LB + L_DUMMY + d * 256);
    if (d) dma4w((lane & 15) * 128, p.cim + b * 2048, LB + L_DUMMY + d * 256);
  };
  auto zero2 = [&]() {
#pragma unroll
    for (int n = 0; n < 4; ++n) {
#pragma unroll
      for (int m = 0; m < 4; ++m) acc2[n][m] = (f32x4){0, 0, 0, 0};
      asm volatile("" : "+v"(acc2[n][0]), "+v"(acc2[n][1]), "+v"(acc2[n][2]), "+v"(acc2[n][3]));
    }
  };
  int ksl;
  unsigned hb, crd, pubo = 0xffff0000u;
  const int fm = WS_FLAG + mtile * 2048 + slot * 128, fs = WS_FLAG + mtile * 2048 + 1024 + kg * 32;
  u64x8 flm;
  u64x4 fls;
  auto snap = [&](bool ssm) { if (ssm) fls = __builtin_bit_cast(u64x4, sbl8(wss, fresh(fs), 1)); else flm = __builtin_bit_cast(u64x8, sbl16(wss, fresh(fm), 1)); };
  auto setup2 = [&](int nm) {
    ksl = (lane < nm ? kstep_of(slot, lane) : 56 + 4 * kg + ((lane - nm) & 3)) * 4096;
    ksl += w == 1 ? 2048 : 0;
    hb = WS_H + (unsigned)mtile * H_MT + lane * 16;
    crd = home + i16 * 128 + (g & 1) * 8 + (((g >> 1) ^ (i16 >> 1)) * 16);
    asm volatile("" : "+v"(ksl), "+v"(hb), "+v"(crd));
  };

  auto roleD = [&]() __attribute__((always_inline)) {
    __builtin_amdgcn_s_setprio(3);
    unsigned vx[8], vw[8], vb[4], v2[4];
#pragma unroll
    for (int i = 0; i < 8; ++i) vw[i] = g * 3584 + ((((i16 >> 1) ^ (g | (((i >> 1) & 1) << 2))) << 1) | (i16 & 1)) * 16 + 4096 + i * (14336 - 1024);
    const char* wb = p.w1 + j * 256 + d * (32 * 3584);
    auto part1 = [&](int c, unsigned slot) { if (mlp) dma_x8(vw, wb + ((c + rot) & 7) * (64 * 3584), LB + slot + SL_W + d * 8192); };
    if (TOUCH == 5) touch();
    part1(0, 0);
#pragma unroll
    for (int i = 0; i < 8; ++i)
      vx[i] = (32 * (g >> 1) + 16 * ((lane >> 3) & 1) + (g & 1)) * 1024 + (((lane & 7) ^ (4 * (i & 1) + g)) * 16) + 4096 + (i & 1) * 65536 + (i >> 1) * 2048 - i * 1024;
#pragma unroll
    for (int i = 0; i < 4; ++i) vb[i] = (lane >> 3) * 1024 + (((lane & 7) ^ (4 * (i & 1) + g)) * 16) + i * 7168;
    const char* xb = p.x + (r * 128 + d * 8) * 1024;
    const char* bb = (d ? p.bim : p.bre) + j * 32768;
    auto partx = [&](int c, unsigned slot) { dma_x8(vx, xb + ((c + rot) & 7) * 128, LB + slot + d * 8192); };
    auto partb = [&](int c, unsigned slot) { dma_x4(vb, bb + ((c + rot) & 7) * 128, LB + slot + SL_B + d * 4096); };
    auto part0 = [&](int c, unsigned slot) { partx(c, slot); partb(c, slot); };
    mkv2(v2);
    unsigned ra[4];
    home = HB + 3 * SLOT;
#pragma unroll
    for (int n = 0; n < 4; ++n) ra[n] = home + r2l + ((n ^ r2x) * 32);
    int kt = kstep_of(slot_of((lane >> 3) & 3), lane & 7) * 32768;
    asm volatile("" : "+v"(kt), "+v"(ra[0]), "+v"(ra[1]), "+v"(ra[2]), "+v"(ra[3]), "+v"(v2[0]), "+v"(v2[1]), "+v"(v2[2]), "+v"(v2[3]));
    bool low = false;
    auto page = [&](int role, int i, int grp, int pg) { dma_x4(v2, w2b + (unsigned)__builtin_amdgcn_readlane(kt, 8 * role + i), HB + grp * SLOT + pg * 4096, low); };
    auto c8 = [&](const char* src, unsigned dst) { dma_x4(vb, src, dst, true); dma_x4(vb, src + 32768, dst + 4096, true); };
    auto take = [&](int i) {
      SCHED();
#pragma unroll
      for (int n = 0; n < 4; ++n) we[i][n] = ldstr(ra[n] + PG(i) * 4096, ra[n] + PG(i) * 4096 + 2048);
      SCHED();
    };
    auto own = [&](int i) { page(3, i, 3, PG(i)); };
    unsigned l16 = lane * 16, la = home + 12288 + lane * 16, lb = LB + L_ST1 + d * 1024 + lane * 16, lc = LB + L_BS + d * 512 + (lane & 31) * 16;
    unsigned wa = LB + L_CS + d * 256 + (lane & 31) * 8 + (lane >> 5) * 4, wb2 = LB + L_LS + (2 * d + (lane >> 5)) * 288 + i16 * 16 + (g & 1) * 4;
    asm volatile("" : "+v"(l16), "+v"(la), "+v"(lb), "+v"(lc), "+v"(wa), "+v"(wb2));
    auto gather = [&](bool bs) {
      const char* cp = p.ws + WS_CS + j * 8192 + d * 4096;
      dma4_dev(l16, cp, cp, home + 12288);
      dma2_dev(l16, p.ws + WS_STAT + r * 2048 + d * 1024, p.ws + WS_STAT + (mtile >> 1) * 2048 + d * 1024 - 2048, LB + L_ST1 + d * 1024);
      if (bs) dma1_dev(l16, p.ws + WS_BS + j * 1024, LB + L_BS);
    };
    auto check = [&](auto between) -> bool {
      auto ld16 = [](unsigned a) { return *(volatile LAS u32x4*)(size_t)a; };
      auto bad = [&](unsigned a, u32x4 v) { return __builtin_amdgcn_bitop3_b32(v.z, tagB, __builtin_amdgcn_bitop3_b32(v.x, tagA, a, 0xBE), 0xBE); };
      const u32x4 c0 = ld16(la), c1 = ld16(la + 1024), c2 = ld16(la + 2048), c3 = ld16(la + 3072), s1 = ld16(lb), s2 = ld16(lb + 2048), s3 = ld16(lc);
      SCHED(); between(); SCHED();
      const u32x2 h = swap32(sadd(sadd(u2f(c0.y), u2f(c1.y)), sadd(u2f(c2.y), u2f(c3.y))), sadd(sadd(u2f(c0.w), u2f(c1.w)), sadd(u2f(c2.w), u2f(c3.w))));
      *(LAS float*)(size_t)wa = sadd(u2f(h.x), u2f(h.y));
      *(LAS unsigned*)(size_t)wb2 = s1.y;
      *(LAS unsigned*)(size_t)(wb2 + 8) = s1.w;
      const unsigned e = bad(bad(bad(bad(bad(bad(bad(0u, c0), c1), c2), c3) & (mlp ? ~0u : 0u), s1), s2), s3);
      return __builtin_amdgcn_ballot_w64(e != 0) == 0;
    };
    const unsigned me = PW + w * 4;
    const unsigned co = ntile * 65536 + kg * 128;
    zero2();
    setup2(8);
    auto landed = [&]() { if (mlp) wait_vm<20>(); else wait_vm<12>(); };
    SCHED();
    part0(0, 0);
    if (TOUCH == 4) touch();
    part1(1, SLOT); part0(1, SLOT);
    STAMP(1);
    mktag();
    if (mlp) wait_vm<20 + (TOUCH == 4)>(); else wait_vm<12 + (TOUCH == 4)>();
    STAMP(19);
    bar();
    STAMP(2);
    part1(2, 2 * SLOT); part0(2, 2 * SLOT); landed(); if (TOUCH == 1) touch();
    bar();
    if (d) dma4w(lane * 4, p.b1 + (mlp ? j : 13) * 256, LB + L_B1);
    part1(3, 0); part0(3, 0); landed();
    dma4w((lane < 28 ? lane : 0) * 1024, p.w2 + (2 * idx + d) * 28672 + ((3u - ((unsigned)(size_t)p.w2 >> 7)) & 7u) * 128, LB + L_DUMMY + d * 256);
    bar();
    STAMPC(21);
    dma_x4(vx, p.x + (128 * (mtile >> 1) + 8 * (mtile & 1) + 4 * d) * 1024 + ntile * 128 - 4096, home + 12288);
    part1(4, SLOT); part0(4, SLOT); landed();
    bar();
    own(0); part1(5, 2 * SLOT); part0(5, 2 * SLOT); landed();
    bar();
    gather(d); part1(6, 0); take(0); part0(6, 0); landed();
    bar();
    part1(7, SLOT);
    const bool ok = check([&]() { partx(7, SLOT); });
    SCHED();
    partb(7, SLOT);
    if (__builtin_expect(!ok, 0)) {
#pragma unroll 1
      for (int spin = 0; spin < SPIN_CAP; ++spin) {
        COUNT(20);
        __builtin_amdgcn_s_sleep(10);
        gather(true);
        wait_vm<0>();
        if (check([]() {})) break;
      }
    }
    landed();
    barrier();
    STAMP(3); STAMPC(22);
    page(0, 0, 2, 0); page(0, 1, 2, 1); page(0, 2, 2, 2); page(0, 3, 2, 3); page(0, 4, 2, 4); own(1);
    wait_vm<12>();
    bar();
    STAMP(4);
    own(2); page(2, 0, 0, 0); page(2, 1, 0, 1); page(2, 2, 0, 2); page(2, 3, 0, 3); page(2, 4, 0, 4);
    wait_vm<20>();
    STAMP(5);
    bar();
    low = true;
    STAMP(6);
    page(1, 0, 1, 0); page(1, 1, 1, 1); take(1); page(1, 2, 1, 2); take(2); page(1, 3, 1, 3); page(1, 4, 1, 4);
    own(3); own(4); wait_lgkm0(); own(5); own(6);
    wait_vm<16>(); prog_set(me, 1);
    page(0, 5, 2, 0); page(0, 6, 2, 1); page(0, 7, 2, 2);
    wait_vm<16>(); take(3); take(4); take(5); wait_lgkm0();
    own(7);
    c8(p.cre + co, home);
    wait_vm<8>(); prog_set(me, 2);
    take(6); take(7); wait_lgkm0();
    c8(p.cim + co, home + 8192);
    STAMP(7);
    prog_wait(PW + (2 + d) * 4, 1);
    snap(false);
    page(1, 5, 1, 4);
    { const unsigned o = ntile * 65536 + ((2 + d + KROT * kgo) & 7) * 128; c8(p.cre + o, HB + SLOT); c8(p.cim + o, HB + SLOT + 8192); }
    __builtin_amdgcn_s_setprio(0);
    STAMP(8);
  };
  auto roleL = [&]() __attribute__((always_inline)) {
    if (TOUCH == 0) touch();
    if (TLBW && d) { dma4w(0, p.cre + ntile * 65536, LB + L_DUMMY + 256); dma4w(0, p.cim + ntile * 65536, LB + L_DUMMY + 256); dma4w((lane & 1) * 0x1bf000, p.w2, LB + L_DUMMY + 256); }
    f32x4 acr[8], aci[8];
#pragma unroll
    for (int m = 0; m < 8; ++m) { acr[m] = (f32x4){0, 0, 0, 0}; aci[m] = (f32x4){0, 0, 0, 0}; asm volatile("" : "+v"(acr[m]), "+v"(aci[m])); }
    unsigned ax[2], ab[2];
    ax[0] = LB + i16 * 128 + kx; ax[1] = ax[0] ^ 64;
    ab[0] = LB + SL_B + (16 * d + i16) * 128 + kx; ab[1] = ab[0] ^ 64;
    unsigned sa = LB + L_LS + g * 288, csa = LB + L_BS + (16 * d + i16) * 16 + 4;
    asm volatile("" : "+v"(sa), "+v"(csa), "+v"(ax[0]), "+v"(ax[1]), "+v"(ab[0]), "+v"(ab[1]));
    s16x8 xf[2][8], bf[2][2];
    auto rd = [&](int set, int i) {
      if (i < 2) bf[set][i] = lds128(ab[set] + i * 4096);
      else xf[set][i - 2] = lds128(ax[set] + (i - 2) * 2048);
    };
    auto mm = [&](int set, int i) {
      if (i & 1) aci[i >> 1] = mfma(xf[set][i >> 1], bf[set][1], aci[i >> 1]);
      else acr[i >> 1] = mfma(xf[set][i >> 1], bf[set][0], acr[i >> 1]);
    };
    asm volatile("" : "+v"(v_lre), "+v"(v_lim), "+v"(v_ldt));
    const float lr = bflo(v_lre), li = bflo(v_lim);
    const float dt = bflo(pk2(expf(bflo(v_ldt)), 0.f));
    const float ang = smul(dt, li);
    const float rv = smul(ang, 0.15915494f);
    const float rev = (rv - __builtin_rintf(rv)) + sfma(ang, 6.4206382e-9f, sfms(ang, 0.15915494f, rv));
    float sn = __builtin_amdgcn_sinf(rev), cn = __builtin_amdgcn_cosf(rev);
    asm volatile("s_nop 0" : "+v"(sn), "+v"(cn));
    const float ex = expf(smul(dt, lr));
    f32x2 a1 = {smul(ex, cn), smul(ex, sn)};
    const float nr = a1[0] - 1.f, ni = a1[1], iden = 2.f / sfma(lr, lr, smul(li, li));
    f32x2 kk = {smul(sfma(nr, lr, smul(ni, li)), iden), smul(sfms(ni, lr, smul(nr, li)), iden)};
    f32x2 t = cmul(a1, a1); t = cmul(t, t); t = cmul(t, t);
    f32x2 a16 = cmul(t, t);
    f32x2 a32 = cmul(a16, a16);
    const f32x2 a64 = cmul(a32, a32), a128 = cmul(a64, a64), a256 = cmul(a128, a128);
    f32x2 a512 = cmul(a256, a256);
    const f32x2 one = {1.f, 0.f};
    f32x2 aseg = cmul(kk, cmul(csel(g & 1, a32, one), csel(g & 2, a64, one)));
    const int e4 = (r - 1 - g) & 3;
    f32x2 acar = cmul(csel(e4 & 1, a128, one), csel(e4 & 2, a256, one));
    const f32x2 ar = bco(a1[0]), ai = bco(a1[1]);
    asm volatile("" :: "v"(ar), "v"(ai));
    asm volatile("" : "+v"(kk), "+v"(a16), "+v"(a32), "+v"(a512), "+v"(aseg), "+v"(acar));
    mktag();
    __builtin_amdgcn_s_setprio(1);
    STAMP(1);
    bar();
    STAMP(2); TRACE3(1);
#pragma unroll
    for (int i = 0; i < 10; ++i) rd(0, i);
    wait_lgkm0();
    auto stage1 = [&](int dl) {
      SCHED();
#pragma unroll
      for (int i = 0; i < 16; ++i) {
        mm(0, i); SCHED();
        if (i < 10) rd(1, i);
        if (i == 11) { adv(ax[0], dl); adv(ab[0], dl); }
        SCHED();
      }
    };
    stage1(SLOT);
    barrier();
    TRACE3(2);
    int ph = 0;
#pragma unroll 1
    for (int c = 2; c <= 8; ++c) {
      const int dl = ph == 2 ? -2 * (int)SLOT : (int)SLOT;
      ph = ph == 2 ? 0 : ph + 1;
      SCHED();
#pragma unroll
      for (int i = 0; i < 16; ++i) {
        mm(1, i); SCHED();
        if (i < 10) rd(0, i);
        if (i == 11) { adv(ax[1], dl); adv(ab[1], dl); }
        SCHED();
      }
      wait_lgkm0();
      stage1(ph == 2 ? -2 * (int)SLOT : (int)SLOT);
      TRACE(2 * c);
      barrier();
      TRACE(2 * c + 1); TRACE3(c + 1);
    }
    __builtin_amdgcn_s_setprio(LPRIO);
    STAMP(4);
    f32x2 rs[16], mrs[16];
    float csr, csi;
#pragma unroll
    for (int i = 0; i < 16; ++i) {
      mm(1, i); SCHED();
      if (i == 0) { csr = ldsf1(csa); csi = ldsf1(csa + 512); }
      const f32x4 t = ldsf4(sa + i * 16);
      rs[i] = lo2(t); mrs[i] = hi2(t);
      if ((i & 3) == 3) TRACE2(20 + (i >> 2));
      SCHED();
    }
    const f32x2 ncr = bco(-csr), nci = bco(-csi);
    STAMP(5);
    f32x2 ur[16], ui[16], Er, Ei;
    SCHED();
#pragma unroll
    for (int s = 0; s < 16; ++s) {
      ur[s] = mrs[s] * ncr; SCHED();
      ui[s] = mrs[s] * nci; SCHED();
      ur[s] = pfma((s & 1) ? hi2(acr[s >> 1]) : lo2(acr[s >> 1]), rs[s], ur[s]); SCHED();
      ui[s] = pfma((s & 1) ? hi2(aci[s >> 1]) : lo2(aci[s >> 1]), rs[s], ui[s]); SCHED();
      if (s == 0) { Er = ur[0]; Ei = ui[0]; }
      else {
        const f32x2 t0 = pfma(Ei, -ai, ur[s]); SCHED();
        const f32x2 t1 = pfma(Er, ai, ui[s]); SCHED();
        Er = pfma(Er, ar, t0); SCHED();
        Ei = pfma(Ei, ar, t1); SCHED();
      }
    }
    TRACE2(1);
    const f32x2 E0 = {Er[0], Ei[0]};
    const f32x2 E = cfma(a16, E0, (f32x2){Er[1], Ei[1]});
    f32x2 P = {0.f, 0.f}, Tt = {0.f, 0.f};
    float er4[4], ei4[4];
    rows4(E[0], er4); rows4(E[1], ei4);
#pragma unroll
    for (int s = 0; s < 4; ++s) {
      const f32x2 e = {er4[s], ei4[s]};
      P = csel(s == g, Tt, P);
      Tt = s ? cfma(a32, Tt, e) : e;
    }
    const unsigned totb = WS_TOT + (unsigned)(j * 2 + d) * 4096;
    if (g == 0) __builtin_amdgcn_raw_buffer_store_b128((u32x4){tagA, f2u(Tt[0]), tagB, f2u(Tt[1])}, wsr, totb + r * 256 + i16 * 16, 0, 16);
    STAMP(6);
    home = HB;
    TRACE2(2);
    prog_wait(PW + (6 + d) * 4, 1);
#pragma unroll
    for (int i = 0; i < 5; ++i) absorb(i, home + i * 4096);
    unsigned vc[8], v2[4], vt[4];
    mkvc(vc); mkv2(v2);
#pragma unroll
    for (int i = 0; i < 4; ++i) { vt[i] = (4 * i + g < r ? i * 1024 + g * 256 : 0) + i16 * 16; asm volatile("" : "+v"(vt[i]), "+v"(v2[i]), "+v"(vc[i]), "+v"(vc[i + 4])); }
    wait_lgkm0();
    loadC(vc, kg, home);
    TRACE2(3);
    {
      const i32x4 tr = {wss[0], wss[1], (int)(totb + r * 256), wss[3]};
#pragma unroll 1
      for (int spin = 0; spin < 64; ++spin) {
        int n, t[15];
        asm volatile("s_buffer_load_dword %1, %16, %17 offset:0 glc\n\ts_buffer_load_dword %2, %16, %17 offset:256 glc\n\ts_buffer_load_dword %3, %16, %17 offset:512 glc\n\ts_buffer_load_dword %4, %16, %17 offset:768 glc\n\ts_buffer_load_dword %5, %16, %17 offset:1024 glc\n\ts_buffer_load_dword %6, %16, %17 offset:1280 glc\n\ts_buffer_load_dword %7, %16, %17 offset:1536 glc\n\ts_buffer_load_dword %8, %16, %17 offset:1792 glc\n\ts_buffer_load_dword %9, %16, %17 offset:2048 glc\n\ts_buffer_load_dword %10, %16, %17 offset:2304 glc\n\ts_buffer_load_dword %11, %16, %17 offset:2560 glc\n\ts_buffer_load_dword %12, %16, %17 offset:2816 glc\n\ts_buffer_load_dword %13, %16, %17 offset:3072 glc\n\ts_buffer_load_dword %14, %16, %17 offset:3328 glc\n\ts_buffer_load_dword %15, %16, %17 offset:3584 glc\n\t"
                     "s_mov_b32 %0, 0\n\ts_waitcnt lgkmcnt(0)\n\t"
                     "s_cmp_eq_u32 %1, %18\n\ts_addc_u32 %0, %0, 0\n\ts_cmp_eq_u32 %2, %18\n\ts_addc_u32 %0, %0, 0\n\ts_cmp_eq_u32 %3, %18\n\ts_addc_u32 %0, %0, 0\n\ts_cmp_eq_u32 %4, %18\n\ts_addc_u32 %0, %0, 0\n\ts_cmp_eq_u32 %5, %18\n\ts_addc_u32 %0, %0, 0\n\ts_cmp_eq_u32 %6, %18\n\ts_addc_u32 %0, %0, 0\n\ts_cmp_eq_u32 %7, %18\n\ts_addc_u32 %0, %0, 0\n\ts_cmp_eq_u32 %8, %18\n\ts_addc_u32 %0, %0, 0\n\ts_cmp_eq_u32 %9, %18\n\ts_addc_u32 %0, %0, 0\n\ts_cmp_eq_u32 %10, %18\n\ts_addc_u32 %0, %0, 0\n\ts_cmp_eq_u32 %11, %18\n\ts_addc_u32 %0, %0, 0\n\ts_cmp_eq_u32 %12, %18\n\ts_addc_u32 %0, %0, 0\n\ts_cmp_eq_u32 %13, %18\n\ts_addc_u32 %0, %0, 0\n\ts_cmp_eq_u32 %14, %18\n\ts_addc_u32 %0, %0, 0\n\ts_cmp_eq_u32 %15, %18\n\ts_addc_u32 %0, %0, 0"
                     : "=&s"(n), "=&s"(t[0]), "=&s"(t[1]), "=&s"(t[2]), "=&s"(t[3]), "=&s"(t[4]), "=&s"(t[5]), "=&s"(t[6]), "=&s"(t[7]), "=&s"(t[8]), "=&s"(t[9]), "=&s"(t[10]), "=&s"(t[11]), "=&s"(t[12]), "=&s"(t[13]), "=&s"(t[14]) : "s"(tr), "s"(totb), "s"(tagA) : "scc", "memory");
        if (n >= r) break;
        COUNT(20);
      }
    }
    STAMP(7);
    f32x2 car = {0.f, 0.f};
    auto fetch = [&]() {
#pragma unroll
      for (int i = 0; i < 4; ++i)
        if (4 * i < r) dma1_dev(vt[i], p.ws + totb, home + 16384 + i * 1024);
    };
    fetch();
    {
      const f32x2 kr = bco(kk[0]), nki = bco(-kk[1]);
#pragma unroll
      for (int s = 0; s < 16; ++s) {
        const f32x2 t0 = ur[s] * kr; SCHED();
        const f32x2 t1 = ur[s] * nki; SCHED();
        ur[s] = pfma(ui[s], nki, t0); SCHED();
        ui[s] = pfma(ui[s], -kr, t1); SCHED();
      }
    }
#pragma unroll
    for (int s = 0; s < 16; s += 4) asm volatile("" : "+v"(ur[s]), "+v"(ui[s]), "+v"(ur[s + 1]), "+v"(ui[s + 1]), "+v"(ur[s + 2]), "+v"(ui[s + 2]), "+v"(ur[s + 3]), "+v"(ui[s + 3]));
    f32x2 kP = cmul(kk, P), kE = cmul(kk, E0);
    asm volatile("" : "+v"(kP), "+v"(kE));
    setup2(6);
    wait_vm<0>();
    if (r > 0) {
      u32x4 tv[4];
#pragma unroll 1
      for (int spin = 0; spin < SPIN_CAP; ++spin) {
        bool ok = true;
#pragma unroll
        for (int i = 0; i < 4; ++i) {
          tv[i] = *(volatile LAS u32x4*)(size_t)(home + 16384 + i * 1024 + lane * 16);
          ok &= (4 * i + g >= r) | ((tv[i].x == tagA) & (tv[i].z == tagB));
        }
        if (__all(ok)) break;
        COUNT(20);
        __builtin_amdgcn_s_sleep(8);
        fetch();
        wait_vm<0>();
      }
#pragma unroll
      for (int i = 0; i < 4; ++i) car = csel(4 * i + g < r, cfma(a512, car, (f32x2){u2f(tv[i].y), u2f(tv[i].w)}), car);
      car = cmul(car, acar);
      float cr4[4], ci4[4];
      rows4(car[0], cr4); rows4(car[1], ci4);
      car = (f32x2){(cr4[0] + cr4[1]) + (cr4[2] + cr4[3]), (ci4[0] + ci4[1]) + (ci4[2] + ci4[3])};
    }
    dma_x4(v2, w2b + kstep_of(slot, 5) * 32768, home + 16384);
    STAMP(8);
    {
      const f32x2 h0 = cfma(aseg, car, kP), h1 = cfma(a16, h0, kE);
      f32x2 hr = {h0[0], h1[0]}, hi = {-h0[1], -h1[1]};
      const unsigned on = (qq == g) ? ((pp & 1) ? 0x3f800000u : 0x3f80u) : 0u;
      const u32x4 z0 = {(pp & 2) ? 0u : on, (pp & 2) ? on : 0u, 0u, 0u}, z1 = {0u, 0u, z0.x, z0.y};
      const s16x8 sel0 = __builtin_bit_cast(s16x8, z0), sel1 = __builtin_bit_cast(s16x8, z1);
      const unsigned ho = WS_H + (unsigned)(2 * r) * H_MT + (unsigned)(56 + 2 * j + d) * 4096 + lane * 16;
      u32x4 fr[4], fi[4];
      f32x4 tr[2], ti[2];
      const f32x4 zz = {0, 0, 0, 0};
      auto put = [&](int mp, int k) {
        const u32x4 o = {pk2(tr[k][0], tr[k][1]), pk2(tr[k][2], tr[k][3]), pk2(ti[k][0], ti[k][1]), pk2(ti[k][2], ti[k][3])};
        __builtin_amdgcn_raw_buffer_store_b128(o, wsr, ho + (mp >> 1) * H_MT + (2 * (mp & 1) + k) * 1024, 0, 16);
        SCHED();
      };
      SCHED();
#pragma unroll
      for (int mp = 0; mp < 5; ++mp) {
        if (mp > 0) {
#pragma unroll
          for (int k = 0; k < 2; ++k) {
            tr[k] = mfma(__builtin_bit_cast(s16x8, fr[mp - 1]), k ? sel1 : sel0, zz);
            ti[k] = mfma(__builtin_bit_cast(s16x8, fi[mp - 1]), k ? sel1 : sel0, zz);
          }
          SCHED();
        }
        if (mp < 4) {
#pragma unroll
          for (int e = 0; e < 4; ++e) {
            const int s = 4 * mp + e;
            const f32x2 t1 = pfma(hr, -ai, ui[s]); SCHED();
            const f32x2 t0 = pfma(hi, ai, ur[s]); SCHED();
            hi = pfma(hi, ar, t1); SCHED();
            hr = pfma(hr, ar, t0); SCHED();
            fi[mp][e] = pk2(hi[0], hi[1]); SCHED();
            fr[mp][e] = pk2(hr[0], hr[1]); SCHED();
          }
        }
        TRACE2(4 + 2 * mp);
        if (mp == 0) snap(false);
        if (mp > 0) { put(mp - 1, 0); put(mp - 1, 1); }
        TRACE2(5 + 2 * mp);
      }
      STAMP(19);
      if (lane < 2) pubo = WS_FLAG + (2 * r + lane) * 2048 + 1024 + (2 * j + d) * 8;
    }
    __builtin_amdgcn_s_setprio(0);
    STAMP(18);
  };
  auto roleB = [&]() __attribute__((always_inline)) {
    home = HB + SLOT;
    mktag();
    {
      float s0 = 0.f, s1 = 0.f;
#pragma unroll
      for (int e = 0; e < 8; ++e) { s0 = sadd(s0, bflo(prb[e >> 2][e & 3])); s1 = sadd(s1, bfhi(prb[e >> 2][e & 3])); }
      *(LAS float*)(size_t)(LB + L_LS + 512 + (4 * d + g) * 4) = row16_sum(s0 + s1);
    }
    if (mlp) {
      f32x4 acc[6][4];
#pragma unroll
      for (int s = 0; s < 6; ++s) {
#pragma unroll
        for (int m = 0; m < 4; ++m) acc[s][m] = (f32x4){0, 0, 0, 0};
        asm volatile("" : "+v"(acc[s][0]), "+v"(acc[s][1]), "+v"(acc[s][2]), "+v"(acc[s][3]));
      }
      unsigned ax[2], aw[6];
      ax[0] = LB + (64 * d + i16) * 128 + kx; ax[1] = ax[0] ^ 64;
#pragma unroll
      for (int s = 0; s < 6; ++s) aw[s] = awb + ((s ^ awx) * 32);
      unsigned sta = LB + L_LS + qq * 288 + (8 * d + ((i16 >> 1) & 1)) * 16 + (i16 & 1) * 4, csa = LB + L_CS + g * 16;
      asm volatile("" : "+v"(ax[0]), "+v"(ax[1]), "+v"(aw[0]), "+v"(aw[1]), "+v"(aw[2]), "+v"(aw[3]), "+v"(aw[4]), "+v"(aw[5]), "+v"(sta), "+v"(csa));
      s16x8 xf[2][4];
      s16x4 wf[2][12];
      auto rd = [&](int set, int i) {
        if (i < 4) xf[set][i] = lds128(ax[set] + i * 2048);
        else wf[set][i - 4] = ldstr4(aw[(i - 4) >> 1] + set * 8192 + (i & 1) * 1024);
      };
      auto mm = [&](int set, int i) { const int s = i >> 2, m = i & 3; acc[s][m] = mfma(cat(wf[set][2 * s], wf[set][2 * s + 1]), xf[set][m], acc[s][m]); };
      {
        float t[8];
#pragma unroll
        for (int e = 0; e < 8; ++e)
          t[e] = (e & 1) ? sadd(sadd(bfhi(pre[0][e >> 1]), bfhi(pre[1][e >> 1])), sadd(bfhi(pre[2][e >> 1]), bfhi(pre[3][e >> 1])))
                         : sadd(sadd(bflo(pre[0][e >> 1]), bflo(pre[1][e >> 1])), sadd(bflo(pre[2][e >> 1]), bflo(pre[3][e >> 1])));
#pragma unroll
        for (int e = 0; e < 4; ++e) { const u32x2 h = swap32(t[e], t[e + 4]); t[e] = sadd(u2f(h.x), u2f(h.y)); }
#pragma unroll
        for (int e = 0; e < 2; ++e) { const u32x2 h = swap16(t[e], t[e + 2]); t[e] = dpp_add<0x128>(sadd(u2f(h.x), u2f(h.y))); }
        *(LAS f32x2*)(size_t)(LB + L_LS + d * 256 + (4 * (lane & 7) + g) * 8) = (f32x2){t[0], t[1]};
      }
      __builtin_amdgcn_s_setprio(1);
      STAMP(1);
      barrier();
      STAMP(2); STAMPC(21);
#pragma unroll
      for (int i = 0; i < 16; ++i) rd(0, i);
      wait_lgkm0();
      auto stage1 = [&](int c, int dl) {
        SCHED();
#pragma unroll
        for (int i = 0; i < 24; ++i) {
          mm(0, i); SCHED();
          if (i < 16) rd(1, i);
          else if (i < 22) adv(aw[i - 16], dl);
          else if (i == 22) adv(ax[0], dl);
          SCHED();
        }
        TRACE(2 * c);
        barrier();
        TRACE(2 * c + 1);
        SCHED();
      };
      int ph = 0;
#pragma unroll 1
      for (int c = 1; c < 8; ++c) {
        const int dl = ph == 2 ? -2 * (int)SLOT : (int)SLOT;
        ph = ph == 2 ? 0 : ph + 1;
        stage1(c, dl);
#pragma unroll
        for (int i = 0; i < 24; ++i) {
          mm(1, i); SCHED();
          if (i < 16) rd(0, i);
          else if (i == 16) adv(ax[1], dl);
          else if (i == 23) wait_lgkm0();
          SCHED();
        }
      }
      stage1(8, 0);
      unsigned b1a = LB + L_B1 + g * 8;
      asm volatile("" : "+v"(b1a));
      STAMP(4); STAMPC(22);
      f32x2 rs[4], nm[4];
      f32x4 cs[6];
      u32x2 b1v[6];
      f32x2 st[4];
      auto colrd = [&](int s) { cs[s] = ldsf4(csa + 64 * s); b1v[s] = *(LAS u32x2*)(size_t)(b1a + 32 * s); };
#pragma unroll
      for (int i = 0; i < 4; ++i) st[i] = (f32x2){ldsf1(sta + i * 32), ldsf1(sta + i * 32 + 8)};
      colrd(0); colrd(1);
      SCHED();
#pragma unroll
      for (int i = 0; i < BSPLIT; ++i) mm(1, i);
      SCHED();
#pragma unroll
      for (int i = 0; i < 4; ++i) { rs[i] = bco(st[i][0]); nm[i] = bco(-st[i][1]); }
      STAMP(5);
      const unsigned fo = WS_FLAG + (2 * r + d) * 2048;
      const unsigned ho = WS_H + (unsigned)(2 * r + d) * H_MT + (unsigned)(4 * j) * 4096 + lane * 16;
#pragma unroll
      for (int pr = 0; pr < 3; ++pr) {
        Col col;
        mkcol(col, cs[2 * pr], cs[2 * pr + 1], b1v[2 * pr], b1v[2 * pr + 1]);
        if (pr < 2) { colrd(2 * pr + 2); colrd(2 * pr + 3); }
        SCHED();
        if (pr == 1) {
#pragma unroll
          for (int i = BSPLIT; i < 24; ++i) mm(1, i);
          SCHED();
          STAMP(7);
          prog_wait(PW + (6 + d) * 4, 1);
#pragma unroll
          for (int i = 0; i < 5; ++i) absorb(i, home + i * 4096);
          wait_lgkm0();
          prog_set(PW + w * 4, 1);
          if (BFLAG0 < 0) {
            wait_vm<0>();
            if (lane == 0) __builtin_amdgcn_raw_buffer_store_b64((u32x2){tagA, tagB}, wsr, fo + flag_of(j, 0), 0, 16);
          }
          STAMP(8);
        }
#pragma unroll
        for (int m = 0; m < 4; ++m) {
          __builtin_amdgcn_raw_buffer_store_b128(gelu8(acc[2 * pr][m], acc[2 * pr + 1][m], col, rs[m], nm[m]), wsr, ho + pr * 4096 + m * 1024, 0, 16);
          TRACE2(1 + 4 * pr + m);
          if (pr > 0 && m == (pr == 1 ? BFLAG0 : BFLAG)) {
            if (pr == 1) wait_vm<(BFLAG0 < 0 ? 0 : BFLAG0 + 1)>(); else wait_vm<BFLAG + 1>();
            if (lane == 0) __builtin_amdgcn_raw_buffer_store_b64((u32x2){tagA, tagB}, wsr, fo + flag_of(j, pr - 1), 0, 16);
          }
          if (pr == 2 && m == 0) snap(false);
          SCHED();
        }
      }
      STAMP(6);
      setup2(6);
      if (lane == 0) pubo = fo + flag_of(j, 2);
      __builtin_amdgcn_s_setprio(0);
    } else {
#pragma unroll 1
      for (int c = 0; c < 9; ++c) barrier();
      prog_wait(PW + (6 + d) * 4, 1);
#pragma unroll
      for (int i = 0; i < 5; ++i) absorb(i, home + i * 4096);
      wait_lgkm0();
      prog_set(PW + w * 4, 1);
      setup2(6);
      snap(false);
    }
  };
  auto roleA = [&]() __attribute__((always_inline)) {
    home = HB + 2 * SLOT;
    const int srow = 128 * r + 8 * j + 4 * d + g;
    u32x2 dv[4], bv[4];
#pragma unroll
    for (int n = 0; n < 4; ++n) { dv[n] = *(const GAS u32x2*)(p.dsk + ntile * 128 + n * 32 + g * 8); bv[n] = *(const GAS u32x2*)(p.b2 + ntile * 128 + n * 32 + g * 8); }
    if (tid < 16) *(LAS unsigned*)(size_t)(PW + tid * 4) = 0u;
    f32x4 acc[2][4];
#pragma unroll
    for (int m = 0; m < 4; ++m) { acc[0][m] = (f32x4){0, 0, 0, 0}; acc[1][m] = (f32x4){0, 0, 0, 0}; asm volatile("" : "+v"(acc[0][m]), "+v"(acc[1][m])); }
    unsigned ax0 = LB + (64 * d + i16) * 128 + kx, ax1 = ax0 ^ 64, aw0 = awb + ((6 ^ awx) * 32), aw1 = awb + ((7 ^ awx) * 32);
    unsigned sta = LB + L_LS + qq * 288 + (8 * d + ((i16 >> 1) & 1)) * 16 + (i16 & 1) * 4, csa = LB + L_CS + 384 + g * 16;
    asm volatile("" : "+v"(ax0), "+v"(ax1), "+v"(aw0), "+v"(aw1), "+v"(sta), "+v"(csa));
    int ph = 0;
    auto chunk = [&](auto side) {
      s16x8 xf[2][4], wf[2][2];
      if (ADELAY) __builtin_amdgcn_s_sleep(ADELAY);
#pragma unroll
      for (int k = 0; k < 2; ++k) {
        wf[k][0] = ldstr(aw0 + k * 8192, aw0 + k * 8192 + 1024);
        wf[k][1] = ldstr(aw1 + k * 8192, aw1 + k * 8192 + 1024);
#pragma unroll
        for (int m = 0; m < 4; ++m) xf[k][m] = lds128((k ? ax1 : ax0) + m * 2048);
      }
      SCHED();
      const int dl = ph == 2 ? -2 * (int)SLOT : (int)SLOT;
      ph = ph == 2 ? 0 : ph + 1;
      adv(ax0, dl); adv(ax1, dl); adv(aw0, dl); adv(aw1, dl);
      side(8);
      SCHED();
#pragma unroll
      for (int k = 0; k < 2; ++k)
#pragma unroll
        for (int m = 0; m < 4; ++m) {
          acc[0][m] = mfma(wf[k][0], xf[k][m], acc[0][m]); acc[1][m] = mfma(wf[k][1], xf[k][m], acc[1][m]);
          SCHED();
          side(4 * k + m);
          SCHED();
        }
    };
    auto none = [](int) {};
    mktag();
    asm volatile("" : "+v"(pre[3]));
    STAMP(19);
    {
      const float mean = sum32(pre) * (1.f / 512.f);
      float v0 = 0.f, v1 = 0.f;
#pragma unroll
      for (int i = 0; i < 4; ++i)
#pragma unroll
        for (int e = 0; e < 4; ++e) {
          const float d0 = bflo(pre[i][e]) - mean, d1 = bfhi(pre[i][e]) - mean;
          v0 = sfma(d0, d0, v0); v1 = sfma(d1, d1, v1);
        }
      const float rsd = __builtin_amdgcn_rsqf(row16_sum(v0 + v1) * (1.f / 512.f) + 1e-5f);
      if (i16 == 0) __builtin_amdgcn_raw_buffer_store_b128((u32x4){tagA, f2u(rsd), tagB, f2u(mean * rsd)}, wsr, WS_STAT + srow * 16, 0, 16);
    }
    __builtin_amdgcn_s_setprio(APRIO);
    STAMP(1);
    barrier();
    STAMP(2);
    chunk(none);
    if (d) {
      const f32x2 c0 = ldsf2(LB + L_LS + 512 + (lane & 3) * 8);
      if (lane < 4) __builtin_amdgcn_raw_buffer_store_b128((u32x4){tagA, f2u(sadd(c0[0], c0[1])), tagB, 0u}, wsr, WS_BS + j * 1024 + (4 * r + lane) * 16, 0, 16);
    } else if (mlp) {
      const f32x2 c0 = ldsf2(LB + L_LS + (lane & 31) * 8), c1 = ldsf2(LB + L_LS + 256 + (lane & 31) * 8);
      if (lane < 32) __builtin_amdgcn_raw_buffer_store_b128((u32x4){tagA, f2u(sadd(c0[0], c1[0])), tagB, f2u(sadd(c0[1], c1[1]))}, wsr, WS_CS + (j * 16 + r) * 512 + lane * 16, 0, 16);
    }
    barrier();
#pragma unroll 1
    for (int c = 1; c < 3; ++c) { chunk(none); barrier(); }
    chunk(none);
    asm volatile("" : "+v"(dv[0]), "+v"(dv[1]), "+v"(dv[2]), "+v"(dv[3]), "+v"(bv[0]), "+v"(bv[1]), "+v"(bv[2]), "+v"(bv[3]));
    u32x2 xv[2][4];
#pragma unroll
    for (int m = 0; m < 2; ++m)
#pragma unroll
      for (int n = 0; n < 4; ++n) xv[m][n] = *(LAS u32x2*)(size_t)(HB + 3 * SLOT + 12288 + (16 * m + i16) * 128 + (g & 1) * 8 + (((2 * n + (g >> 1)) ^ (i16 >> 1)) * 16));
    barrier();
#pragma unroll 1
    for (int c = 4; c < 6; ++c) { chunk(none); barrier(); }
    f32x2 st[4];
#pragma unroll
    for (int m = 0; m < 4; ++m) st[m] = (f32x2){ldsf1(sta + m * 32), ldsf1(sta + m * 32 + 8)};
    unsigned ra[4], vc[8], ho = mlp ? WS_H + (unsigned)(2 * r + d) * H_MT + (unsigned)(4 * j + 3) * 4096 + lane * 16 : 0xffff0000u;
    f32x2 rs[4], nm[4];
    chunk([&](int k) {
      if (k < 4) { rs[k] = bco(st[k][0]); nm[k] = bco(-st[k][1]); asm volatile("" : "+v"(rs[k]), "+v"(nm[k])); }
      if (k < 8) return;
#pragma unroll
      for (int n = 0; n < 4; ++n) ra[n] = home + r2l + ((n ^ r2x) * 32);
      asm volatile("" : "+v"(ra[0]), "+v"(ra[1]), "+v"(ra[2]), "+v"(ra[3]), "+v"(ho));
    });
    TRACE(14); barrier(); TRACE(15);
    STAMP(4);
    const f32x4 cs0 = ldsf4(csa), cs1 = ldsf4(csa + 64);
    const u32x2 b10 = *(LAS u32x2*)(size_t)(LB + L_B1 + 192 + g * 8), b11 = *(LAS u32x2*)(size_t)(LB + L_B1 + 224 + g * 8);
    Col col;
    auto take = [&](int i, int pg) {
#pragma unroll
      for (int n = 0; n < 4; ++n) we[i][n] = ldstr(ra[n] + pg * 4096, ra[n] + pg * 4096 + 2048);
    };
    chunk([&](int k) {
      if (k == 2) { mkcol(col, cs0, cs1, b10, b11); asm volatile("" : "+v"(col.bias[0]), "+v"(col.bias[1]), "+v"(col.bias[2]), "+v"(col.bias[3])); }
      if (k >= 3 && k < 6) take(k - 3, k - 3);
    });
    barrier();
    __builtin_amdgcn_s_setprio(2);
    STAMP(5);
#pragma unroll
    for (int m = 0; m < 4; ++m) {
      TRACE2(1 + 2 * m);
      const u32x4 o = gelu8(acc[0][m], acc[1][m], col, rs[m], nm[m]);
      TRACE2(2 + 2 * m);
      __builtin_amdgcn_raw_buffer_store_b128(o, wsr, ho + m * 1024, 0, 16);
      if (m == 0) { take(3, 3); take(4, 4); }
      SCHED();
    }
    STAMP(6);
#pragma unroll
    for (int m = 0; m < 2; ++m) {
      const u32x4 s2 = *(LAS u32x4*)(size_t)(LB + L_ST2 + (tok + 8 * (mtile & 1) + 4 * d + 2 * m) * 16);
      const f32x2 r2 = bco(u2f(s2.y)), n2 = bco(-u2f(s2.w));
#pragma unroll
      for (int n = 0; n < 4; ++n) {
        f32x2 o[2];
#pragma unroll
        for (int h = 0; h < 2; ++h) {
          const f32x2 xx = {bflo(xv[m][n][h]), bfhi(xv[m][n][h])};
          o[h] = pfma(pfma(xx, r2, n2), (f32x2){bflo(dv[n][h]), bfhi(dv[n][h])}, xx + (f32x2){bflo(bv[n][h]), bfhi(bv[n][h])});
        }
        acc2[n][m] = __builtin_shufflevector(o[0], o[1], 0, 1, 2, 3);
        acc2[n][2 + m] = (f32x4){0, 0, 0, 0};
        asm volatile("" : "+v"(acc2[n][m]), "+v"(acc2[n][2 + m]));
      }
    }
    STAMP(7);
    wait_vm<0>();
    if (mlp && lane == 0) __builtin_amdgcn_raw_buffer_store_b64((u32x2){tagA, tagB}, wsr, WS_FLAG + (2 * r + d) * 2048 + flag_of(j, 3), 0, 16);
    STAMP(18);
    mkvc(vc);
    setup2(8);
    prog_wait(PW + (6 + d) * 4, 2);
#pragma unroll
    for (int i = 5; i < 8; ++i) take(i, i - 5);
    wait_lgkm0();
    snap(false);
    loadC(vc, kg, home);
    STAMP(8);
    __builtin_amdgcn_s_setprio(0);
  };

  auto phase2 = [&](auto nm_, auto dep_) __attribute__((always_inline)) {
    STAMP(9);
    constexpr int NM = decltype(nm_)::value, N = NM + 4, DEP = decltype(dep_)::value;
    const u64 tag = ((u64)tagB << 32) | tagA;
    auto has = [&](int k) { return (k < NM ? flm[k < NM ? k : 0] : fls[k < NM ? 0 : k - NM]) == tag; };
    auto need = [&](int k, bool ok) {
      if (__builtin_expect(!ok, 0)) {
#pragma unroll 1
        for (int spin = 1; spin < SPIN_CAP; ++spin) {
          snap(k >= NM);
          if (has(k)) break;
          if (!(spin & 31)) {
            const u32x2 f = __builtin_amdgcn_raw_buffer_load_b64(wsr, 0, k < NM ? fm + 8 * k : fs + 8 * (k - NM), 16);
            if (__builtin_amdgcn_readfirstlane(f.x) == tagA && __builtin_amdgcn_readfirstlane(f.y) == tagB) break;
          }
          __builtin_amdgcn_s_sleep(PSLEEP);
        }
      }
      asm volatile("" ::: "memory");
    };
    auto publish = [&]() { __builtin_amdgcn_raw_buffer_store_b64((u32x2){tagA, tagB}, wsr, pubo, 0, 16); };
    auto loadH = [&](int i, int m) {
      if (m == 0) asm volatile("" : "+v"(hb));
      return __builtin_amdgcn_raw_buffer_load_b128(wsr, hb + (m & 1) * 1024, __builtin_amdgcn_readlane(ksl, i) ^ ((m & 2) * 1024), 16);
    };
    u32x4 hf[N][4];
    auto load4 = [&](int i) {
#pragma unroll
      for (int m = 0; m < 4; ++m) hf[i][m] = loadH(i, m);
    };
    STAMP(10);
    if (NM == 6 && __builtin_expect(!(has(0) && has(1)), 0)) { wait_vm<0>(); publish(); pubo = 0xffff0000u; }
    need(0, has(0)); load4(0);
    need(1, has(1)); load4(1);
    if (NM == 6) { wait_vm<8>(); publish(); }
    STAMP(11);
    s16x8 cfr[2][4];
    bool okn = false;
    SCHED();
#pragma unroll
    for (int i = 0; i < N; ++i) {
      const int k = i == 0 ? 2 : i + DEP;
      if (i == 1) STAMP(12);
      if (k == NM) STAMP(13);
      if (k < N) need(k, (i & 1) && k < NM ? okn : has(k));
      if (k == NM) STAMP(14);
      if (i > 0 || DEP == 2) {
        if (!(i & 1) && k + 1 < NM) {
          okn = has(k + 1);
          if (k + 2 < NM) snap(false);
        }
        if (k + 2 == NM) snap(true);
      }
      if (NM == 8 && i == 1 && w >= 6) prog_set(PW + w * 4, 3);
      if (NM == 6 && i == 3) { prog_wait(PW + (6 + d) * 4, 3); absorb(5, home + 16384); }
      if (i >= NM - 1 && i < N - 1) {
#pragma unroll
        for (int n = 0; n < 4; ++n) {
          const unsigned a = (crd ^ (32 * (i - NM + 1))) + n * 2048;
          cfr[(i + 1) & 1][n] = cat(*(LAS s16x4*)(size_t)a, *(LAS s16x4*)(size_t)(a + 8192));
        }
      }
      SCHED();
#pragma unroll
      for (int m = 0; m < 4; ++m) {
#pragma unroll
        for (int n = 0; n < 4; ++n) acc2[n][m] = mfma(i < NM ? we[i < NM ? i : 0][n] : cfr[i & 1][n], __builtin_bit_cast(s16x8, hf[i][m]), NM == 6 && i == 0 ? (f32x4){0, 0, 0, 0} : acc2[n][m]);
        SCHED();
        if (k < N) hf[k < N ? k : 0][m] = loadH(k, m);
        SCHED();
      }
      if (i == 0 && DEP == 3) {
        need(3, has(3)); load4(3);
        okn = has(4);
        snap(false);
        SCHED();
      }
    }
  };
  if (__builtin_expect(w >= 6 || w < 2, 1)) {
    if (__builtin_expect(w >= 6, 1)) roleD(); else roleA();
    phase2(std::integral_constant<int, 8>{}, std::integral_constant<int, DEPE>{});
  } else {
    if (w >= 4) roleL(); else roleB();
    phase2(std::integral_constant<int, 6>{}, std::integral_constant<int, DEPL>{});
  }
  STAMP(15);
  int tl = threadIdx.x;
  asm volatile("" : "+v"(tl));
  const int i16t = tl & 15, gt = (tl >> 4) & 3;
#pragma unroll
  for (int m = 0; m < 4; ++m)
#pragma unroll
    for (int n = 0; n < 4; ++n) *(LAS f32x4*)(size_t)(home + ((16 * m + i16t) ^ (w == 1 ? 32 : 0)) * 256 + (((4 * n + gt) ^ i16t) * 16)) = acc2[n][m];
  const int ck = tl & 15;
  const int orow = 32 * ((tl >> 6) & 3) + 16 * ((tl >> 4) & 1) + ((tl >> 5) & 1) + 2 * (tl >> 8) + 8 * (mtile & 1);
  char* op = p.out + (128 * (mtile >> 1) + orow) * 2048 + ntile * 256 + ck * 16;
  unsigned ra[4];
#pragma unroll
  for (int k = 0; k < 4; ++k) ra[k] = LB + (tl >> 4) * 256 + ((ck ^ ((tl >> 4) & 15)) * 16) + k * SLOT;
  asm volatile("" : "+v"(ra[0]), "+v"(ra[1]), "+v"(ra[2]), "+v"(ra[3]), "+v"(op));
  barrier();
  STAMP(16);
  {
    f32x4 pt[2][8];
#pragma unroll
    for (int h = 0; h < 2; ++h)
#pragma unroll
      for (int k = 0; k < 8; ++k) pt[h][k] = ldsf4(ra[k >> 1] + (k & 1) * HALF + h * 8192);
#pragma unroll
    for (int h = 0; h < 2; ++h) {
      const f32x4 s = ((pt[h][0] + pt[h][1]) + (pt[h][2] + pt[h][3])) + ((pt[h][4] + pt[h][5]) + (pt[h][6] + pt[h][7]));
      __builtin_nontemporal_store(s, (GAS f32x4*)(op + h * 8192));
    }
  }
  if (b == 255 && tl == 0) __builtin_amdgcn_raw_buffer_store_b64((u32x2){tagA, tagB}, wsr, WS_EPOCH, 0, 16);
  while (__builtin_amdgcn_s_memrealtime() - t_start < 3000) __builtin_amdgcn_s_sleep(2);
}

extern "C" void kernel_launch(void* const* d_in, const int* in_sizes, int n_in, void* d_out, int out_size, void* d_ws,
                              size_t ws_size, hipStream_t stream) {
  Params p;
  p.x = (const char*)d_in[0]; p.lre = (const char*)d_in[1]; p.lim = (const char*)d_in[2];
  p.bre = (const char*)d_in[3]; p.bim = (const char*)d_in[4]; p.cre = (const char*)d_in[5]; p.cim = (const char*)d_in[6];
  p.logdt = (const char*)d_in[7]; p.dsk = (const char*)d_in[8];
  p.w1 = (const char*)d_in[10]; p.b1 = (const char*)d_in[11]; p.w2 = (const char*)d_in[12]; p.b2 = (const char*)d_in[13];
  p.out = (char*)d_out; p.ws = (char*)d_ws;
  fused_kernel<<<256, 512, 0, stream>>>(p);
}
```

```cpp
#include <hip/hip_runtime.h>
#include <stdint.h>
#include <type_traits>

typedef short s16x4 __attribute__((ext_vector_type(4)));
typedef short s16x8 __attribute__((ext_vector_type(8)));
typedef __bf16 bf16x8 __attribute__((ext_vector_type(8)));
typedef __bf16 bf16x2 __attribute__((ext_vector_type(2)));
typedef float f32x4 __attribute__((ext_vector_type(4)));
typedef float f32x2 __attribute__((ext_vector_type(2)));
typedef unsigned u32x4 __attribute__((ext_vector_type(4)));
typedef unsigned u32x2 __attribute__((ext_vector_type(2)));
typedef int i32x4 __attribute__((ext_vector_type(4)));
typedef unsigned long long u64;
typedef u64 u64x4 __attribute__((ext_vector_type(4)));
typedef u64 u64x8 __attribute__((ext_vector_type(8)));
typedef int i32x8 __attribute__((ext_vector_type(8)));
typedef int i32x16 __attribute__((ext_vector_type(16)));
__device__ i32x8 sbl8(i32x4, int, int) __asm("llvm.amdgcn.s.buffer.load.v8i32");
__device__ i32x16 sbl16(i32x4, int, int) __asm("llvm.amdgcn.s.buffer.load.v16i32");
#define LAS __attribute__((address_space(3)))
#define GAS __attribute__((address_space(1)))
#define DI __device__ __forceinline__

#define STAMPS 0
#define TRACING 0
#define GATHER_AT 4
#define ROT 1
#define TOUCH 1
#define TLBW 0
#define NOFF 0
#define PSLEEP 4
#define BFLAG 2
#define ABAR 0
#define KROT 0
#define LPRIO 3
#define CHK 0
#define BSPLIT 8
#define APRIO 0
#define ADELAY 0
#define BFLAG0 -1
#define DEPE 2
#define DEPL 2
#define LB_SLEEP 8
#define CHECK_AT 5
#define CPF 1
#define MAP 1

struct Params {
  const char *x, *lre, *lim, *bre, *bim, *cre, *cim, *logdt, *dsk, *w1, *b1, *w2, *b2;
  char *out, *ws;
};

constexpr unsigned WS_EPOCH = 0;
constexpr unsigned WS_STAT = 4096;
constexpr unsigned WS_TOT = WS_STAT + 2048 * 16;
constexpr unsigned WS_CS = WS_TOT + 16 * 2 * 16 * 256;
constexpr unsigned WS_BS = WS_CS + 16 * 16 * 512;
constexpr unsigned WS_FLAG = WS_BS + 16 * 1024;
constexpr unsigned WS_H = WS_FLAG + 32 * 2048;
constexpr unsigned H_MT = 88 * 4096;
constexpr unsigned WS_END = WS_H + 32 * H_MT;
constexpr unsigned WS_STAMP = 32u << 20;
constexpr unsigned WS_B2B = WS_STAMP + (4u << 20);
constexpr unsigned SLOT = 40960, HALF = 20480, SL_W = 16384, SL_B = 32768;
constexpr unsigned L_ST1 = 3 * SLOT + 16384, L_ST2 = L_ST1 + 2048;
constexpr unsigned L_CS = 3 * SLOT + HALF + 16384, L_FLG = L_CS + 512, L_B1 = L_CS + 768, L_BS = L_CS + 1024, L_LS = L_CS + 2048, L_DUMMY = L_CS + 3328;
constexpr unsigned LDS_BYTES = 163840;
constexpr int SPIN_CAP = 40000;
constexpr unsigned CODE_BYTES = 43520;

DI void dma4w(unsigned voff, const char* sbase, unsigned ldsaddr) {
  asm volatile("s_mov_b32 m0, %2\n\ts_nop 0\n\tglobal_load_lds_dword %0, %1" ::"v"(voff), "s"(sbase), "s"(ldsaddr));
}
DI void dma_x4(const unsigned* v, const char* sbase, unsigned ldsaddr, bool low = false) {
  if (low) {
    asm volatile("s_mov_b32 m0, %5\n\ts_setprio 0\n\t"
               "global_load_lds_dwordx4 %0, %4\n\tglobal_load_lds_dwordx4 %1, %4 offset:1024\n\t"
               "global_load_lds_dwordx4 %2, %4 offset:2048\n\tglobal_load_lds_dwordx4 %3, %4 offset:3072\n\ts_setprio 3"
               ::"v"(v[0]), "v"(v[1]), "v"(v[2]), "v"(v[3]), "s"(sbase), "s"(ldsaddr));
    return;
  }
  asm volatile("s_mov_b32 m0, %5\n\ts_nop 0\n\t"
               "global_load_lds_dwordx4 %0, %4\n\tglobal_load_lds_dwordx4 %1, %4 offset:1024\n\t"
               "global_load_lds_dwordx4 %2, %4 offset:2048\n\tglobal_load_lds_dwordx4 %3, %4 offset:3072"
               ::"v"(v[0]), "v"(v[1]), "v"(v[2]), "v"(v[3]), "s"(sbase), "s"(ldsaddr));
}
DI void dma_x8(const unsigned* v, const char* sbase, unsigned ldsaddr) {
  asm volatile("s_mov_b32 m0, %9\n\ts_nop 0\n\t"
               "global_load_lds_dwordx4 %0, %8 offset:-4096\n\tglobal_load_lds_dwordx4 %1, %8 offset:-3072\n\t"
               "global_load_lds_dwordx4 %2, %8 offset:-2048\n\tglobal_load_lds_dwordx4 %3, %8 offset:-1024\n\t"
               "global_load_lds_dwordx4 %4, %8\n\tglobal_load_lds_dwordx4 %5, %8 offset:1024\n\t"
               "global_load_lds_dwordx4 %6, %8 offset:2048\n\tglobal_load_lds_dwordx4 %7, %8 offset:3072"
               ::"v"(v[0]), "v"(v[1]), "v"(v[2]), "v"(v[3]), "v"(v[4]), "v"(v[5]), "v"(v[6]), "v"(v[7]), "s"(sbase), "s"(ldsaddr + 4096u));
}
constexpr int PG(int i) { return i & 3; }
template <int N> DI void wait_vm() { asm volatile("s_waitcnt vmcnt(%0)" ::"i"(N) : "memory"); }
DI void wait_lgkm0() { asm volatile("" ::: "memory"); __builtin_amdgcn_s_waitcnt(0xC07F); asm volatile("" ::: "memory"); }
DI void barrier() {
  asm volatile("" ::: "memory");
  __builtin_amdgcn_s_waitcnt(0xC07F);
  __builtin_amdgcn_s_barrier();
  asm volatile("" ::: "memory");
}
#define SCHED() __builtin_amdgcn_sched_barrier(0)
DI s16x8 lds128(unsigned a) { return *(LAS s16x8*)(size_t)a; }
DI f32x4 ldsf4(unsigned a) { return *(LAS f32x4*)(size_t)a; }
DI f32x2 ldsf2(unsigned a) { return *(LAS f32x2*)(size_t)a; }
DI float ldsf1(unsigned a) { return *(LAS float*)(size_t)a; }
DI s16x4 ldstr4(unsigned a) { return __builtin_amdgcn_ds_read_tr16_b64_v4i16((LAS s16x4*)(size_t)a); }
DI s16x8 cat(s16x4 lo, s16x4 hi) { return __builtin_shufflevector(lo, hi, 0, 1, 2, 3, 4, 5, 6, 7); }
DI s16x8 ldstr(unsigned a0, unsigned a1) { return cat(ldstr4(a0), ldstr4(a1)); }
DI f32x4 mfma(s16x8 a, s16x8 b, f32x4 c) {
  return __builtin_amdgcn_mfma_f32_16x16x32_bf16(__builtin_bit_cast(bf16x8, a), __builtin_bit_cast(bf16x8, b), c, 0, 0, 0);
}
DI unsigned pk2(float a, float b) { f32x2 v = {a, b}; return __builtin_bit_cast(unsigned, __builtin_convertvector(v, bf16x2)); }
DI float u2f(unsigned u) { return __builtin_bit_cast(float, u); }
DI unsigned f2u(float u) { return __builtin_bit_cast(unsigned, u); }
DI float bflo(unsigned u) { return __builtin_bit_cast(float, u << 16); }
DI float bfhi(unsigned u) { return __builtin_bit_cast(float, u & 0xffff0000u); }
template <int CTRL> DI float dpp_add(float v) {
  return v + __builtin_bit_cast(float, __builtin_amdgcn_update_dpp(0, __builtin_bit_cast(int, v), CTRL, 0xf, 0xf, true));
}
DI float row16_sum(float v) {
  v = dpp_add<0xB1>(v);
  v = dpp_add<0x4E>(v);
  v = dpp_add<0x141>(v);
  v = dpp_add<0x140>(v);
  return v;
}
DI float smul(float a, float b) { float d; asm("v_mul_f32 %0, %1, %2" : "=v"(d) : "v"(a), "v"(b)); return d; }
DI float sadd(float a, float b) { float d; asm("v_add_f32 %0, %1, %2" : "=v"(d) : "v"(a), "v"(b)); return d; }
DI float sfma(float a, float b, float c) { float d; asm("v_fma_f32 %0, %1, %2, %3" : "=v"(d) : "v"(a), "v"(b), "v"(c)); return d; }
DI float sfms(float a, float b, float c) { float d; asm("v_fma_f32 %0, %1, %2, -%3" : "=v"(d) : "v"(a), "v"(b), "v"(c)); return d; }
DI void zero4(f32x4& v) {
  float a, b, c, d;
  asm volatile("v_mov_b32 %0, 0\n\tv_mov_b32 %1, 0\n\tv_mov_b32 %2, 0\n\tv_mov_b32 %3, 0" : "=v"(a), "=v"(b), "=v"(c), "=v"(d));
  v = (f32x4){a, b, c, d};
}
DI u32x2 swap32(float a, float b) { asm volatile("s_nop 1" : "+v"(a), "+v"(b)); return __builtin_amdgcn_permlane32_swap(f2u(a), f2u(b), false, false); }
DI u32x2 swap16(float a, float b) { asm volatile("s_nop 1" : "+v"(a), "+v"(b)); return __builtin_amdgcn_permlane16_swap(f2u(a), f2u(b), false, false); }
DI f32x2 pfma(f32x2 a, f32x2 b, f32x2 c) { return __builtin_elementwise_fma(a, b, c); }
DI f32x2 bc(float a) { return (f32x2){a, a}; }
DI f32x2 bco(float a) { f32x2 v = {a, a}; asm("" : "+v"(v)); return v; }
DI f32x2 cmul(f32x2 a, f32x2 b) { return (f32x2){sfms(a[0], b[0], smul(a[1], b[1])), sfma(a[0], b[1], smul(a[1], b[0]))}; }
DI f32x2 cfma(f32x2 a, f32x2 b, f32x2 c) { return (f32x2){sfms(a[0], b[0], sfms(a[1], b[1], c[0])), sfma(a[0], b[1], sfma(a[1], b[0], c[1]))}; }
DI f32x2 csel(bool p, f32x2 a, f32x2 b) { return (f32x2){p ? a[0] : b[0], p ? a[1] : b[1]}; }
DI void prog_set(unsigned a, unsigned v) {
  asm volatile("s_mov_b64 exec, 1\n\tds_write_b32 %0, %1\n\ts_mov_b64 exec, -1" ::"v"(a), "v"(v) : "memory");
}
DI void prog_wait(unsigned a, unsigned v) {
#pragma unroll 1
  for (int spin = 0; spin < SPIN_CAP * 16; ++spin) {
    if (__builtin_amdgcn_readfirstlane(*(volatile LAS unsigned*)(size_t)a) >= v) break;
    __builtin_amdgcn_s_sleep(1);
  }
  asm volatile("" ::: "memory");
}
DI void bar() { asm volatile("" ::: "memory"); __builtin_amdgcn_s_barrier(); asm volatile("" ::: "memory"); }
DI void dma1_dev(unsigned voff, const char* sbase, unsigned ldsaddr) {
  asm volatile("s_mov_b32 m0, %2\n\ts_nop 0\n\tglobal_load_lds_dwordx4 %0, %1 sc1" ::"v"(voff), "s"(sbase), "s"(ldsaddr) : "memory");
}
DI void dma2_dev(unsigned voff, const char* s0, const char* s1, unsigned ldsaddr) {
  asm volatile("s_mov_b32 m0, %3\n\ts_nop 0\n\tglobal_load_lds_dwordx4 %0, %1 sc1\n\tglobal_load_lds_dwordx4 %0, %2 offset:2048 sc1" ::"v"(voff), "s"(s0), "s"(s1), "s"(ldsaddr) : "memory");
}
DI void dma4_dev(unsigned voff, const char* s0, const char* s1, unsigned ldsaddr) {
  asm volatile("s_mov_b32 m0, %3\n\ts_nop 0\n\tglobal_load_lds_dwordx4 %0, %1 sc1\n\tglobal_load_lds_dwordx4 %0, %1 offset:1024 sc1\n\t"
               "global_load_lds_dwordx4 %0, %2 offset:2048 sc1\n\tglobal_load_lds_dwordx4 %0, %2 offset:3072 sc1" ::"v"(voff), "s"(s0), "s"(s1), "s"(ldsaddr) : "memory");
}
DI int kstep_of(int slot, int i) {
  const int q = slot + 4 * i + (slot >= 4 ? 28 : 0), cls = (q >= 14) + (q >= 28) + (q >= 42);
  return 4 * (q - 14 * cls) + ((0x9C >> (2 * cls)) & 3);
}
DI unsigned flag_of(int j, int c) {
  const int q = 14 * ((0x78 >> (2 * c)) & 3) + j;
  return q < 32 ? (q & 3) * 128 + (q >> 2) * 8 : 512 + (q & 3) * 128 + ((q - 32) >> 2) * 8;
}
DI void cnt_add(unsigned a) {
  asm volatile("s_mov_b64 exec, 1\n\tds_add_u32 %0, %1\n\ts_mov_b64 exec, -1" ::"v"(a), "v"(1u) : "memory");
}
DI f32x2 lo2(f32x4 v) { return __builtin_shufflevector(v, v, 0, 1); }
DI f32x2 hi2(f32x4 v) { return __builtin_shufflevector(v, v, 2, 3); }
DI void rows4(float v, float* o) {
  asm volatile("s_nop 1" : "+v"(v));
  const unsigned u = f2u(v);
  const u32x2 h = __builtin_amdgcn_permlane32_swap(u, u, false, false);
  const u32x2 a = __builtin_amdgcn_permlane16_swap(h.x, h.x, false, false), b = __builtin_amdgcn_permlane16_swap(h.y, h.y, false, false);
  o[0] = u2f(a.x); o[1] = u2f(a.y); o[2] = u2f(b.x); o[3] = u2f(b.y);
}
struct Col { f32x2 cs[4], bias[4]; };
DI void mkcol(Col& c, f32x4 cs0, f32x4 cs1, u32x2 b0, u32x2 b1) {
  c.cs[0] = lo2(cs0); c.cs[1] = hi2(cs0); c.cs[2] = lo2(cs1); c.cs[3] = hi2(cs1);
  c.bias[0] = (f32x2){bflo(b0.x), bfhi(b0.x)}; c.bias[1] = (f32x2){bflo(b0.y), bfhi(b0.y)};
  c.bias[2] = (f32x2){bflo(b1.x), bfhi(b1.x)}; c.bias[3] = (f32x2){bflo(b1.y), bfhi(b1.y)};
}
DI u32x4 gelu8(f32x4 a0, f32x4 a1, const Col& c, f32x2 rs, f32x2 nmrs) {
  f32x2 v[4], z[4];
  u32x4 o;
#define ST4(stmt) _Pragma("unroll") for (int h = 0; h < 4; ++h) { stmt; } SCHED();
  ST4(v[h] = pfma(c.cs[h], nmrs, c.bias[h]))
  ST4(v[h] = pfma(h == 0 ? lo2(a0) : h == 1 ? hi2(a0) : h == 2 ? lo2(a1) : hi2(a1), rs, v[h]))
  ST4(z[h] = v[h] * v[h])
  ST4(z[h] = pfma(z[h], bc(-0.10294324f), bc(-2.30220820f)))
  ST4(z[h] = z[h] * v[h])
  ST4((z[h] = (f32x2){__builtin_amdgcn_exp2f(z[h][0]), __builtin_amdgcn_exp2f(z[h][1])}))
  ST4(z[h] = z[h] + bc(1.f))
  ST4((z[h] = (f32x2){__builtin_amdgcn_rcpf(z[h][0]), __builtin_amdgcn_rcpf(z[h][1])}))
  ST4(v[h] = v[h] * z[h])
  ST4(o[h] = pk2(v[h][0], v[h][1]))
#undef ST4
  return o;
}

#define TRACE(i) do {} while (0)
#define TRACE2(i) do {} while (0)
#define TRACE3(i) do {} while (0)
#define STAMP(k) do {} while (0)
#define STAMPC(k) do {} while (0)
#define COUNT(k) do {} while (0)

__global__ void __launch_bounds__(512, 2) fused_kernel(Params p) {
  __shared__ __attribute__((aligned(16))) char lds_mem[LDS_BYTES];
  const unsigned long long t_start = __builtin_amdgcn_s_memrealtime();
  unsigned long long pc;
  asm volatile("s_getpc_b64 %0" : "=s"(pc));
  const unsigned LB = (unsigned)(size_t)(LAS char*)lds_mem;
  const int tid = threadIdx.x, lane = tid & 63;
  const int w = __builtin_amdgcn_readfirstlane(tid >> 6);
  if (w == 5) {
    const unsigned co = ((unsigned)(lane & 15) * 32u + (blockIdx.x >> 3)) * 128u;
    dma4w(co < CODE_BYTES ? co : 0u, (const char*)(pc & ~255ull), LB + L_DUMMY + 512);
  }
  const int b = blockIdx.x, idx = b >> 3, xcd = b & 7;
  if (tid == 448) __hip_atomic_store((unsigned*)(p.ws + WS_B2B) + b, (unsigned)t_start, __ATOMIC_RELAXED, __HIP_MEMORY_SCOPE_AGENT);
  const int r = 8 * (xcd >> 2) + (idx >> 2), j = 4 * (xcd & 3) + (idx & 3);
  const int rot = (2 * (xcd & 3) + 4 * (xcd >> 2)) & 7;
  const int mtile = 4 * xcd + (idx >> 3), ntile = (idx + NOFF) & 7;
  const int i16 = lane & 15, g = lane >> 4, qq = i16 >> 2, pp = i16 & 3;
  const int d = w & 1;
  const bool mlp = j < 14;
  u32x4 pre[4];
  unsigned v_lre, v_lim, v_ldt;
  if ((w >> 1) == 2) {
    const unsigned po = (unsigned)(32 * j + 16 * d + i16) * 2u;
    v_lre = *(const GAS unsigned short*)(p.lre + po); v_lim = *(const GAS unsigned short*)(p.lim + po); v_ldt = *(const GAS unsigned short*)(p.logdt + po);
    asm volatile("" ::: "memory");
  }
  u32x4 prb[2];
  if (w < 4) {
    const bool isb = w >= 2;
    const char* pb = isb ? p.w1 + (64 * (r & 7) + 32 * d) * 3584 + (mlp ? j : 13) * 256 + (r >> 3) * 128 : p.x + (128 * r + 8 * j + 4 * d) * 1024;
    const unsigned po = isb ? (lane >> 3) * 3584 + (lane & 7) * 16 : g * 1024 + i16 * 16, ps = isb ? 28672 : 256;
#pragma unroll
    for (int i = 0; i < 4; ++i) pre[i] = *(const GAS u32x4*)(pb + i * ps + po);
    if (isb) {
#pragma unroll
      for (int i = 0; i < 2; ++i) prb[i] = *(const GAS u32x4*)((r & 8 ? p.bim : p.bre) + (32 * j + 4 * (r & 7) + 2 * d) * 1024 + i * 512 + (lane >> 5) * 1024 + (lane & 31) * 16);
    }
    asm volatile("" ::: "memory");
  }
  const int kgo = 2 * (idx >> 3), kg = (w + KROT * kgo) & 7;
  auto slot_of = [&](int role) { return (((role + 1) & 2) << 1) + (((((role + 1) & 2) && !KROT ? 0 : kgo) + ((role & 1) ? 0 : 2) + d) & 3); };
  const int slot = slot_of(w >> 1);
  const u32x2 ep = *(const __attribute__((address_space(4))) u32x2*)(p.ws + WS_EPOCH);
  unsigned tagA, tagB;
  auto mktag = [&]() { tagA = ep.x; tagB = ep.y; asm volatile("" : "+s"(tagA), "+s"(tagB)); tagA += 1u; tagB += 1u; };
  const __amdgpu_buffer_rsrc_t wsr = __builtin_amdgcn_make_buffer_rsrc(p.ws, 0, WS_END, 0x00020000);
  const i32x4 wss = {(int)(size_t)p.ws, (int)((size_t)p.ws >> 32) & 0xffff, (int)WS_END, 0x00020000};
  auto fresh = [](int o) { asm volatile("" : "+s"(o) :: "memory"); return o; };
  const unsigned HB = LB + d * HALF;
  const unsigned PW = LB + L_FLG;
  s16x8 we[8][4];
  f32x4 acc2[4][4];
  unsigned home;
  const unsigned r2l = (4 * g + qq) * 128 + pp * 8, r2x = (2 * g + (qq >> 1)) & 3;
  auto absorb = [&](int i, unsigned page) {
#pragma unroll
    for (int n = 0; n < 4; ++n) { const unsigned a = page + r2l + ((n ^ r2x) * 32); we[i][n] = ldstr(a, a + 2048); }
  };
  auto mkvc = [&](unsigned* vc) {
#pragma unroll
    for (int i = 0; i < 8; ++i) vc[i] = (lane >> 3) * 1024 + (((lane & 7) ^ (4 * (i & 1) + g)) * 16) + 4096 + i * 7168;
  };
  auto mkv2 = [&](unsigned* v2) {
#pragma unroll
    for (int i = 0; i < 4; ++i) v2[i] = (lane >> 3) * 1024 + (((((lane & 7) >> 1) ^ g) << 1) | (lane & 1)) * 16 + i * 7168;
  };
  auto loadC = [&](const unsigned* vc, int kgc, unsigned dst) {
    const unsigned o = ntile * 65536 + kgc * 128;
    dma_x8(vc, p.cre + o, dst);
    dma_x8(vc, p.cim + o, dst + 8192);
  };
  const char* w2b = p.w2 + ntile * 128;
  auto adv = [&](unsigned& a, int dl) { a += dl; asm volatile("" : "+v"(a)); };
  const int tok = 32 * qq + 16 * (i16 & 1) + ((i16 >> 1) & 1);
  const unsigned kx = (g ^ (i16 >> 1)) * 16;
  const unsigned awb = LB + SL_W + (8 * g + qq) * 256 + pp * 8, awx = qq | ((g & 1) << 2);
  auto sum32 = [&](const u32x4* v) {
    float s0 = 0.f, s1 = 0.f;
#pragma unroll
    for (int i = 0; i < 4; ++i)
#pragma unroll
      for (int e = 0; e < 4; ++e) { s0 += bflo(v[i][e]); s1 += bfhi(v[i][e]); }
    return row16_sum(s0 + s1);
  };
  auto touch = [&]() {
    dma4w(d ? (lane & 15) * 128 : (lane < 56 ? lane : 0) * 128, d ? p.cre + b * 2048 : p.w2 + b * 7168, LB + L_DUMMY + d * 256);
    if (d) dma4w((lane & 15) * 128, p.cim + b * 2048, LB + L_DUMMY + d * 256);
  };
  auto zero2 = [&]() {
#pragma unroll
    for (int n = 0; n < 4; ++n) {
#pragma unroll
      for (int m = 0; m < 4; ++m) acc2[n][m] = (f32x4){0, 0, 0, 0};
      asm volatile("" : "+v"(acc2[n][0]), "+v"(acc2[n][1]), "+v"(acc2[n][2]), "+v"(acc2[n][3]));
    }
  };
  int ksl;
  unsigned hb, crd, pubo = 0xffff0000u;
  const int fm = WS_FLAG + mtile * 2048 + slot * 128, fs = WS_FLAG + mtile * 2048 + 1024 + kg * 32;
  u64x8 flm;
  u64x4 fls;
  auto snap = [&](bool ssm) { if (ssm) fls = __builtin_bit_cast(u64x4, sbl8(wss, fresh(fs), 1)); else flm = __builtin_bit_cast(u64x8, sbl16(wss, fresh(fm), 1)); };
  auto setup2 = [&](int nm) {
    ksl = (lane < nm ? kstep_of(slot, lane) : 56 + 4 * kg + ((lane - nm) & 3)) * 4096;
    ksl += w == 1 ? 2048 : 0;
    hb = WS_H + (unsigned)mtile * H_MT + lane * 16;
    crd = home + i16 * 128 + (g & 1) * 8 + (((g >> 1) ^ (i16 >> 1)) * 16);
    asm volatile("" : "+v"(ksl), "+v"(hb), "+v"(crd));
  };

  auto roleD = [&]() __attribute__((always_inline)) {
    __builtin_amdgcn_s_setprio(3);
    unsigned vx[8], vw[8], vb[4], v2[4];
#pragma unroll
    for (int i = 0; i < 8; ++i) vw[i] = g * 3584 + ((((i16 >> 1) ^ (g | (((i >> 1) & 1) << 2))) << 1) | (i16 & 1)) * 16 + 4096 + i * (14336 - 1024);
    const char* wb = p.w1 + j * 256 + d * (32 * 3584);
    auto part1 = [&](int c, unsigned slot) { if (mlp) dma_x8(vw, wb + ((c + rot) & 7) * (64 * 3584), LB + slot + SL_W + d * 8192); };
    if (TOUCH == 5) touch();
    part1(0, 0);
#pragma unroll
    for (int i = 0; i < 8; ++i)
      vx[i] = (32 * (g >> 1) + 16 * ((lane >> 3) & 1) + (g & 1)) * 1024 + (((lane & 7) ^ (4 * (i & 1) + g)) * 16) + 4096 + (i & 1) * 65536 + (i >> 1) * 2048 - i * 1024;
#pragma unroll
    for (int i = 0; i < 4; ++i) vb[i] = (lane >> 3) * 1024 + (((lane & 7) ^ (4 * (i & 1) + g)) * 16) + i * 7168;
    const char* xb = p.x + (r * 128 + d * 8) * 1024;
    const char* bb = (d ? p.bim : p.bre) + j * 32768;
    auto partx = [&](int c, unsigned slot) { dma_x8(vx, xb + ((c + rot) & 7) * 128, LB + slot + d * 8192); };
    auto partb = [&](int c, unsigned slot) { dma_x4(vb, bb + ((c + rot) & 7) * 128, LB + slot + SL_B + d * 4096); };
    auto part0 = [&](int c, unsigned slot) { partx(c, slot); partb(c, slot); };
    mkv2(v2);
    unsigned ra[4];
    home = HB + 3 * SLOT;
#pragma unroll
    for (int n = 0; n < 4; ++n) ra[n] = home + r2l + ((n ^ r2x) * 32);
    int kt = kstep_of(slot_of((lane >> 3) & 3), lane & 7) * 32768;
    asm volatile("" : "+v"(kt), "+v"(ra[0]), "+v"(ra[1]), "+v"(ra[2]), "+v"(ra[3]), "+v"(v2[0]), "+v"(v2[1]), "+v"(v2[2]), "+v"(v2[3]));
    bool low = false;
    auto page = [&](int role, int i, int grp, int pg) { dma_x4(v2, w2b + (unsigned)__builtin_amdgcn_readlane(kt, 8 * role + i), HB + grp * SLOT + pg * 4096, low); };
    auto c8 = [&](const char* src, unsigned dst) { dma_x4(vb, src, dst, true); dma_x4(vb, src + 32768, dst + 4096, true); };
    auto take = [&](int i) {
      SCHED();
#pragma unroll
      for (int n = 0; n < 4; ++n) we[i][n] = ldstr(ra[n] + PG(i) * 4096, ra[n] + PG(i) * 4096 + 2048);
      SCHED();
    };
    auto own = [&](int i) { page(3, i, 3, PG(i)); };
    unsigned l16 = lane * 16, la = home + 12288 + lane * 16, lb = LB + L_ST1 + d * 1024 + lane * 16, lc = LB + L_BS + d * 512 + (lane & 31) * 16;
    unsigned wa = LB + L_CS + d * 256 + (lane & 31) * 8 + (lane >> 5) * 4, wb2 = LB + L_LS + (2 * d + (lane >> 5)) * 288 + i16 * 16 + (g & 1) * 4;
    asm volatile("" : "+v"(l16), "+v"(la), "+v"(lb), "+v"(lc), "+v"(wa), "+v"(wb2));
    auto gather = [&](bool bs) {
      const char* cp = p.ws + WS_CS + j * 8192 + d * 4096;
      dma4_dev(l16, cp, cp, home + 12288);
      dma2_dev(l16, p.ws + WS_STAT + r * 2048 + d * 1024, p.ws + WS_STAT + (mtile >> 1) * 2048 + d * 1024 - 2048, LB + L_ST1 + d * 1024);
      if (bs) dma1_dev(l16, p.ws + WS_BS + j * 1024, LB + L_BS);
    };
    auto check = [&](auto between) -> bool {
      auto ld16 = [](unsigned a) { return *(volatile LAS u32x4*)(size_t)a; };
      auto bad = [&](unsigned a, u32x4 v) { return __builtin_amdgcn_bitop3_b32(v.z, tagB, __builtin_amdgcn_bitop3_b32(v.x, tagA, a, 0xBE), 0xBE); };
      const u32x4 c0 = ld16(la), c1 = ld16(la + 1024), c2 = ld16(la + 2048), c3 = ld16(la + 3072), s1 = ld16(lb), s2 = ld16(lb + 2048), s3 = ld16(lc);
      SCHED(); between(); SCHED();
      const u32x2 h = swap32(sadd(sadd(u2f(c0.y), u2f(c1.y)), sadd(u2f(c2.y), u2f(c3.y))), sadd(sadd(u2f(c0.w), u2f(c1.w)), sadd(u2f(c2.w), u2f(c3.w))));
      *(LAS float*)(size_t)wa = sadd(u2f(h.x), u2f(h.y));
      *(LAS unsigned*)(size_t)wb2 = s1.y;
      *(LAS unsigned*)(size_t)(wb2 + 8) = s1.w;
      const unsigned e = bad(bad(bad(bad(bad(bad(bad(0u, c0), c1), c2), c3) & (mlp ? ~0u : 0u), s1), s2), s3);
      return __builtin_amdgcn_ballot_w64(e != 0) == 0;
    };
    const unsigned me = PW + w * 4;
    const unsigned co = ntile * 65536 + kg * 128;
    zero2();
    setup2(8);
    auto landed = [&]() { if (mlp) wait_vm<20>(); else wait_vm<12>(); };
    SCHED();
    part0(0, 0);
    if (TOUCH == 4) touch();
    part1(1, SLOT); part0(1, SLOT);
    STAMP(1);
    mktag();
    if (mlp) wait_vm<20 + (TOUCH == 4)>(); else wait_vm<12 + (TOUCH == 4)>();
    STAMP(19);
    bar();
    STAMP(2);
    part1(2, 2 * SLOT); part0(2, 2 * SLOT); landed(); if (TOUCH == 1) touch();
    bar();
    if (d) dma4w(lane * 4, p.b1 + (mlp ? j : 13) * 256, LB + L_B1);
    part1(3, 0); part0(3, 0); landed();
    dma4w((lane < 28 ? lane : 0) * 1024, p.w2 + (2 * idx + d) * 28672 + ((3u - ((unsigned)(size_t)p.w2 >> 7)) & 7u) * 128, LB + L_DUMMY + d * 256);
    bar();
    STAMPC(21);
    dma_x4(vx, p.x + (128 * (mtile >> 1) + 8 * (mtile & 1) + 4 * d) * 1024 + ntile * 128 - 4096, home + 12288);
    part1(4, SLOT); part0(4, SLOT); landed();
    bar();
    own(0); part1(5, 2 * SLOT); part0(5, 2 * SLOT); landed();
    bar();
    gather(d); part1(6, 0); take(0); part0(6, 0); landed();
    bar();
    part1(7, SLOT);
    const bool ok = check([&]() { partx(7, SLOT); });
    SCHED();
    partb(7, SLOT);
    if (__builtin_expect(!ok, 0)) {
#pragma unroll 1
      for (int spin = 0; spin < SPIN_CAP; ++spin) {
        COUNT(20);
        __builtin_amdgcn_s_sleep(10);
        gather(true);
        wait_vm<0>();
        if (check([]() {})) break;
      }
    }
    landed();
    barrier();
    STAMP(3); STAMPC(22);
    page(0, 0, 2, 0); page(0, 1, 2, 1); page(0, 2, 2, 2); page(0, 3, 2, 3); page(0, 4, 2, 4); own(1);
    wait_vm<12>();
    bar();
    STAMP(4);
    own(2); page(2, 0, 0, 0); page(2, 1, 0, 1); page(2, 2, 0, 2); page(2, 3, 0, 3); page(2, 4, 0, 4);
    wait_vm<20>();
    STAMP(5);
    bar();
    low = true;
    STAMP(6);
    page(1, 0, 1, 0); page(1, 1, 1, 1); take(1); page(1, 2, 1, 2); take(2); page(1, 3, 1, 3); page(1, 4, 1, 4);
    own(3); own(4); wait_lgkm0(); own(5); own(6);
    wait_vm<16>(); prog_set(me, 1);
    page(0, 5, 2, 0); page(0, 6, 2, 1); page(0, 7, 2, 2);
    wait_vm<16>(); take(3); take(4); take(5); wait_lgkm0();
    own(7);
    c8(p.cre + co, home);
    wait_vm<8>(); prog_set(me, 2);
    take(6); take(7); wait_lgkm0();
    c8(p.cim + co, home + 8192);
    STAMP(7);
    prog_wait(PW + (2 + d) * 4, 1);
    snap(false);
    page(1, 5, 1, 4);
    { const unsigned o = ntile * 65536 + ((2 + d + KROT * kgo) & 7) * 128; c8(p.cre + o, HB + SLOT); c8(p.cim + o, HB + SLOT + 8192); }
    __builtin_amdgcn_s_setprio(0);
    STAMP(8);
  };
  auto roleL = [&]() __attribute__((always_inline)) {
    if (TOUCH == 0) touch();
    if (TLBW && d) { dma4w(0, p.cre + ntile * 65536, LB + L_DUMMY + 256); dma4w(0, p.cim + ntile * 65536, LB + L_DUMMY + 256); dma4w((lane & 1) * 0x1bf000, p.w2, LB + L_DUMMY + 256); }
    f32x4 acr[8], aci[8];
#pragma unroll
    for (int m = 0; m < 8; ++m) { acr[m] = (f32x4){0, 0, 0, 0}; aci[m] = (f32x4){0, 0, 0, 0}; asm volatile("" : "+v"(acr[m]), "+v"(aci[m])); }
    unsigned ax[2], ab[2];
    ax[0] = LB + i16 * 128 + kx; ax[1] = ax[0] ^ 64;
    ab[0] = LB + SL_B + (16 * d + i16) * 128 + kx; ab[1] = ab[0] ^ 64;
    unsigned sa = LB + L_LS + g * 288, csa = LB + L_BS + (16 * d + i16) * 16 + 4;
    asm volatile("" : "+v"(sa), "+v"(csa), "+v"(ax[0]), "+v"(ax[1]), "+v"(ab[0]), "+v"(ab[1]));
    s16x8 xf[2][8], bf[2][2];
    auto rd = [&](int set, int i) {
      if (i < 2) bf[set][i] = lds128(ab[set] + i * 4096);
      else xf[set][i - 2] = lds128(ax[set] + (i - 2) * 2048);
    };
    auto mm = [&](int set, int i) {
      if (i & 1) aci[i >> 1] = mfma(xf[set][i >> 1], bf[set][1], aci[i >> 1]);
      else acr[i >> 1] = mfma(xf[set][i >> 1], bf[set][0], acr[i >> 1]);
    };
    asm volatile("" : "+v"(v_lre), "+v"(v_lim), "+v"(v_ldt));
    const float lr = bflo(v_lre), li = bflo(v_lim);
    const float dt = bflo(pk2(expf(bflo(v_ldt)), 0.f));
    const float ang = smul(dt, li);
    const float rv = smul(ang, 0.15915494f);
    const float rev = (rv - __builtin_rintf(rv)) + sfma(ang, 6.4206382e-9f, sfms(ang, 0.15915494f, rv));
    float sn = __builtin_amdgcn_sinf(rev), cn = __builtin_amdgcn_cosf(rev);
    asm volatile("s_nop 0" : "+v"(sn), "+v"(cn));
    const float ex = expf(smul(dt, lr));
    f32x2 a1 = {smul(ex, cn), smul(ex, sn)};
    const float nr = a1[0] - 1.f, ni = a1[1], iden = 2.f / sfma(lr, lr, smul(li, li));
    f32x2 kk = {smul(sfma(nr, lr, smul(ni, li)), iden), smul(sfms(ni, lr, smul(nr, li)), iden)};
    f32x2 t = cmul(a1, a1); t = cmul(t, t); t = cmul(t, t);
    f32x2 a16 = cmul(t, t);
    f32x2 a32 = cmul(a16, a16);
    const f32x2 a64 = cmul(a32, a32), a128 = cmul(a64, a64), a256 = cmul(a128, a128);
    f32x2 a512 = cmul(a256, a256);
    const f32x2 one = {1.f, 0.f};
    f32x2 aseg = cmul(kk, cmul(csel(g & 1, a32, one), csel(g & 2, a64, one)));
    const int e4 = (r - 1 - g) & 3;
    f32x2 acar = cmul(csel(e4 & 1, a128, one), csel(e4 & 2, a256, one));
    const f32x2 ar = bco(a1[0]), ai = bco(a1[1]);
    asm volatile("" :: "v"(ar), "v"(ai));
    asm volatile("" : "+v"(kk), "+v"(a16), "+v"(a32), "+v"(a512), "+v"(aseg), "+v"(acar));
    mktag();
    __builtin_amdgcn_s_setprio(1);
    STAMP(1);
    bar();
    STAMP(2); TRACE3(1);
#pragma unroll
    for (int i = 0; i < 10; ++i) rd(0, i);
    wait_lgkm0();
    auto stage1 = [&](int dl) {
      SCHED();
#pragma unroll
      for (int i = 0; i < 16; ++i) {
        mm(0, i); SCHED();
        if (i < 10) rd(1, i);
        if (i == 11) { adv(ax[0], dl); adv(ab[0], dl); }
        SCHED();
      }
    };
    stage1(SLOT);
    barrier();
    TRACE3(2);
    int ph = 0;
#pragma unroll 1
    for (int c = 2; c <= 8; ++c) {
      const int dl = ph == 2 ? -2 * (int)SLOT : (int)SLOT;
      ph = ph == 2 ? 0 : ph + 1;
      SCHED();
#pragma unroll
      for (int i = 0; i < 16; ++i) {
        mm(1, i); SCHED();
        if (i < 10) rd(0, i);
        if (i == 11) { adv(ax[1], dl); adv(ab[1], dl); }
        SCHED();
      }
      wait_lgkm0();
      stage1(ph == 2 ? -2 * (int)SLOT : (int)SLOT);
      TRACE(2 * c);
      barrier();
      TRACE(2 * c + 1); TRACE3(c + 1);
    }
    __builtin_amdgcn_s_setprio(LPRIO);
    STAMP(4);
    f32x2 rs[16], mrs[16];
    float csr, csi;
#pragma unroll
    for (int i = 0; i < 16; ++i) {
      mm(1, i); SCHED();
      if (i == 0) { csr = ldsf1(csa); csi = ldsf1(csa + 512); }
      const f32x4 t = ldsf4(sa + i * 16);
      rs[i] = lo2(t); mrs[i] = hi2(t);
      if ((i & 3) == 3) TRACE2(20 + (i >> 2));
      SCHED();
    }
    const f32x2 ncr = bco(-csr), nci = bco(-csi);
    STAMP(5);
    f32x2 ur[16], ui[16], Er, Ei;
    SCHED();
#pragma unroll
    for (int s = 0; s < 16; ++s) {
      ur[s] = mrs[s] * ncr; SCHED();
      ui[s] = mrs[s] * nci; SCHED();
      ur[s] = pfma((s & 1) ? hi2(acr[s >> 1]) : lo2(acr[s >> 1]), rs[s], ur[s]); SCHED();
      ui[s] = pfma((s & 1) ? hi2(aci[s >> 1]) : lo2(aci[s >> 1]), rs[s], ui[s]); SCHED();
      if (s == 0) { Er = ur[0]; Ei = ui[0]; }
      else {
        const f32x2 t0 = pfma(Ei, -ai, ur[s]); SCHED();
        const f32x2 t1 = pfma(Er, ai, ui[s]); SCHED();
        Er = pfma(Er, ar, t0); SCHED();
        Ei = pfma(Ei, ar, t1); SCHED();
      }
    }
    TRACE2(1);
    const f32x2 E0 = {Er[0], Ei[0]};
    const f32x2 E = cfma(a16, E0, (f32x2){Er[1], Ei[1]});
    f32x2 P = {0.f, 0.f}, Tt = {0.f, 0.f};
    float er4[4], ei4[4];
    rows4(E[0], er4); rows4(E[1], ei4);
#pragma unroll
    for (int s = 0; s < 4; ++s) {
      const f32x2 e = {er4[s], ei4[s]};
      P = csel(s == g, Tt, P);
      Tt = s ? cfma(a32, Tt, e) : e;
    }
    const unsigned totb = WS_TOT + (unsigned)(j * 2 + d) * 4096;
    if (g == 0) __builtin_amdgcn_raw_buffer_store_b128((u32x4){tagA, f2u(Tt[0]), tagB, f2u(Tt[1])}, wsr, totb + r * 256 + i16 * 16, 0, 16);
    STAMP(6);
    home = HB;
    TRACE2(2);
    prog_wait(PW + (6 + d) * 4, 1);
#pragma unroll
    for (int i = 0; i < 5; ++i) absorb(i, home + i * 4096);
    unsigned vc[8], v2[4], vt[4];
    mkvc(vc); mkv2(v2);
#pragma unroll
    for (int i = 0; i < 4; ++i) { vt[i] = (4 * i + g < r ? i * 1024 + g * 256 : 0) + i16 * 16; asm volatile("" : "+v"(vt[i]), "+v"(v2[i]), "+v"(vc[i]), "+v"(vc[i + 4])); }
    wait_lgkm0();
    loadC(vc, kg, home);
    TRACE2(3);
    {
      const i32x4 tr = {wss[0], wss[1], (int)(totb + r * 256), wss[3]};
#pragma unroll 1
      for (int spin = 0; spin < 64; ++spin) {
        int n, t[15];
        asm volatile("s_buffer_load_dword %1, %16, %17 offset:0 glc\n\ts_buffer_load_dword %2, %16, %17 offset:256 glc\n\ts_buffer_load_dword %3, %16, %17 offset:512 glc\n\ts_buffer_load_dword %4, %16, %17 offset:768 glc\n\ts_buffer_load_dword %5, %16, %17 offset:1024 glc\n\ts_buffer_load_dword %6, %16, %17 offset:1280 glc\n\ts_buffer_load_dword %7, %16, %17 offset:1536 glc\n\ts_buffer_load_dword %8, %16, %17 offset:1792 glc\n\ts_buffer_load_dword %9, %16, %17 offset:2048 glc\n\ts_buffer_load_dword %10, %16, %17 offset:2304 glc\n\ts_buffer_load_dword %11, %16, %17 offset:2560 glc\n\ts_buffer_load_dword %12, %16, %17 offset:2816 glc\n\ts_buffer_load_dword %13, %16, %17 offset:3072 glc\n\ts_buffer_load_dword %14, %16, %17 offset:3328 glc\n\ts_buffer_load_dword %15, %16, %17 offset:3584 glc\n\t"
                     "s_mov_b32 %0, 0\n\ts_waitcnt lgkmcnt(0)\n\t"
                     "s_cmp_eq_u32 %1, %18\n\ts_addc_u32 %0, %0, 0\n\ts_cmp_eq_u32 %2, %18\n\ts_addc_u32 %0, %0, 0\n\ts_cmp_eq_u32 %3, %18\n\ts_addc_u32 %0, %0, 0\n\ts_cmp_eq_u32 %4, %18\n\ts_addc_u32 %0, %0, 0\n\ts_cmp_eq_u32 %5, %18\n\ts_addc_u32 %0, %0, 0\n\ts_cmp_eq_u32 %6, %18\n\ts_addc_u32 %0, %0, 0\n\ts_cmp_eq_u32 %7, %18\n\ts_addc_u32 %0, %0, 0\n\ts_cmp_eq_u32 %8, %18\n\ts_addc_u32 %0, %0, 0\n\ts_cmp_eq_u32 %9, %18\n\ts_addc_u32 %0, %0, 0\n\ts_cmp_eq_u32 %10, %18\n\ts_addc_u32 %0, %0, 0\n\ts_cmp_eq_u32 %11, %18\n\ts_addc_u32 %0, %0, 0\n\ts_cmp_eq_u32 %12, %18\n\ts_addc_u32 %0, %0, 0\n\ts_cmp_eq_u32 %13, %18\n\ts_addc_u32 %0, %0, 0\n\ts_cmp_eq_u32 %14, %18\n\ts_addc_u32 %0, %0, 0\n\ts_cmp_eq_u32 %15, %18\n\ts_addc_u32 %0, %0, 0"
                     : "=&s"(n), "=&s"(t[0]), "=&s"(t[1]), "=&s"(t[2]), "=&s"(t[3]), "=&s"(t[4]), "=&s"(t[5]), "=&s"(t[6]), "=&s"(t[7]), "=&s"(t[8]), "=&s"(t[9]), "=&s"(t[10]), "=&s"(t[11]), "=&s"(t[12]), "=&s"(t[13]), "=&s"(t[14]) : "s"(tr), "s"(totb), "s"(tagA) : "scc", "memory");
        if (n >= r) break;
        COUNT(20);
      }
    }
    STAMP(7);
    f32x2 car = {0.f, 0.f};
    auto fetch = [&]() {
#pragma unroll
      for (int i = 0; i < 4; ++i)
        if (4 * i < r) dma1_dev(vt[i], p.ws + totb, home + 16384 + i * 1024);
    };
    fetch();
    {
      const f32x2 kr = bco(kk[0]), nki = bco(-kk[1]);
#pragma unroll
      for (int s = 0; s < 16; ++s) {
        const f32x2 t0 = ur[s] * kr; SCHED();
        const f32x2 t1 = ur[s] * nki; SCHED();
        ur[s] = pfma(ui[s], nki, t0); SCHED();
        ui[s] = pfma(ui[s], -kr, t1); SCHED();
      }
    }
#pragma unroll
    for (int s = 0; s < 16; s += 4) asm volatile("" : "+v"(ur[s]), "+v"(ui[s]), "+v"(ur[s + 1]), "+v"(ui[s + 1]), "+v"(ur[s + 2]), "+v"(ui[s + 2]), "+v"(ur[s + 3]), "+v"(ui[s + 3]));
    f32x2 kP = cmul(kk, P), kE = cmul(kk, E0);
    asm volatile("" : "+v"(kP), "+v"(kE));
    setup2(6);
    wait_vm<0>();
    if (r > 0) {
      u32x4 tv[4];
#pragma unroll 1
      for (int spin = 0; spin < SPIN_CAP; ++spin) {
        bool ok = true;
#pragma unroll
        for (int i = 0; i < 4; ++i) {
          tv[i] = *(volatile LAS u32x4*)(size_t)(home + 16384 + i * 1024 + lane * 16);
          ok &= (4 * i + g >= r) | ((tv[i].x == tagA) & (tv[i].z == tagB));
        }
        if (__all(ok)) break;
        COUNT(20);
        __builtin_amdgcn_s_sleep(8);
        fetch();
        wait_vm<0>();
      }
#pragma unroll
      for (int i = 0; i < 4; ++i) car = csel(4 * i + g < r, cfma(a512, car, (f32x2){u2f(tv[i].y), u2f(tv[i].w)}), car);
      car = cmul(car, acar);
      float cr4[4], ci4[4];
      rows4(car[0], cr4); rows4(car[1], ci4);
      car = (f32x2){(cr4[0] + cr4[1]) + (cr4[2] + cr4[3]), (ci4[0] + ci4[1]) + (ci4[2] + ci4[3])};
    }
    dma_x4(v2, w2b + kstep_of(slot, 5) * 32768, home + 16384);
    STAMP(8);
    {
      const f32x2 h0 = cfma(aseg, car, kP), h1 = cfma(a16, h0, kE);
      f32x2 hr = {h0[0], h1[0]}, hi = {-h0[1], -h1[1]};
      const unsigned on = (qq == g) ? ((pp & 1) ? 0x3f800000u : 0x3f80u) : 0u;
      const u32x4 z0 = {(pp & 2) ? 0u : on, (pp & 2) ? on : 0u, 0u, 0u}, z1 = {0u, 0u, z0.x, z0.y};
      const s16x8 sel0 = __builtin_bit_cast(s16x8, z0), sel1 = __builtin_bit_cast(s16x8, z1);
      const unsigned ho = WS_H + (unsigned)(2 * r) * H_MT + (unsigned)(56 + 2 * j + d) * 4096 + lane * 16;
      u32x4 fr[4], fi[4];
      f32x4 tr[2], ti[2];
      const f32x4 zz = {0, 0, 0, 0};
      auto put = [&](int mp, int k) {
        const u32x4 o = {pk2(tr[k][0], tr[k][1]), pk2(tr[k][2], tr[k][3]), pk2(ti[k][0], ti[k][1]), pk2(ti[k][2], ti[k][3])};
        __builtin_amdgcn_raw_buffer_store_b128(o, wsr, ho + (mp >> 1) * H_MT + (2 * (mp & 1) + k) * 1024, 0, 16);
        SCHED();
      };
      SCHED();
#pragma unroll
      for (int mp = 0; mp < 5; ++mp) {
        if (mp > 0) {
#pragma unroll
          for (int k = 0; k < 2; ++k) {
            tr[k] = mfma(__builtin_bit_cast(s16x8, fr[mp - 1]), k ? sel1 : sel0, zz);
            ti[k] = mfma(__builtin_bit_cast(s16x8, fi[mp - 1]), k ? sel1 : sel0, zz);
          }
          SCHED();
        }
        if (mp < 4) {
#pragma unroll
          for (int e = 0; e < 4; ++e) {
            const int s = 4 * mp + e;
            const f32x2 t1 = pfma(hr, -ai, ui[s]); SCHED();
            const f32x2 t0 = pfma(hi, ai, ur[s]); SCHED();
            hi = pfma(hi, ar, t1); SCHED();
            hr = pfma(hr, ar, t0); SCHED();
            fi[mp][e] = pk2(hi[0], hi[1]); SCHED();
            fr[mp][e] = pk2(hr[0], hr[1]); SCHED();
          }
        }
        TRACE2(4 + 2 * mp);
        if (mp == 0) snap(false);
        if (mp > 0) { put(mp - 1, 0); put(mp - 1, 1); }
        TRACE2(5 + 2 * mp);
      }
      STAMP(19);
      if (lane < 2) pubo = WS_FLAG + (2 * r + lane) * 2048 + 1024 + (2 * j + d) * 8;
    }
    __builtin_amdgcn_s_setprio(0);
    STAMP(18);
  };
  auto roleB = [&]() __attribute__((always_inline)) {
    home = HB + SLOT;
    mktag();
    {
      float s0 = 0.f, s1 = 0.f;
#pragma unroll
      for (int e = 0; e < 8; ++e) { s0 = sadd(s0, bflo(prb[e >> 2][e & 3])); s1 = sadd(s1, bfhi(prb[e >> 2][e & 3])); }
      *(LAS float*)(size_t)(LB + L_LS + 512 + (4 * d + g) * 4) = row16_sum(s0 + s1);
    }
    if (mlp) {
      f32x4 acc[6][4];
#pragma unroll
      for (int s = 0; s < 6; ++s) {
#pragma unroll
        for (int m = 0; m < 4; ++m) acc[s][m] = (f32x4){0, 0, 0, 0};
        asm volatile("" : "+v"(acc[s][0]), "+v"(acc[s][1]), "+v"(acc[s][2]), "+v"(acc[s][3]));
      }
      unsigned ax[2], aw[6];
      ax[0] = LB + (64 * d + i16) * 128 + kx; ax[1] = ax[0] ^ 64;
#pragma unroll
      for (int s = 0; s < 6; ++s) aw[s] = awb + ((s ^ awx) * 32);
      unsigned sta = LB + L_LS + qq * 288 + (8 * d + ((i16 >> 1) & 1)) * 16 + (i16 & 1) * 4, csa = LB + L_CS + g * 16;
      asm volatile("" : "+v"(ax[0]), "+v"(ax[1]), "+v"(aw[0]), "+v"(aw[1]), "+v"(aw[2]), "+v"(aw[3]), "+v"(aw[4]), "+v"(aw[5]), "+v"(sta), "+v"(csa));
      s16x8 xf[2][4];
      s16x4 wf[2][12];
      auto rd = [&](int set, int i) {
        if (i < 4) xf[set][i] = lds128(ax[set] + i * 2048);
        else wf[set][i - 4] = ldstr4(aw[(i - 4) >> 1] + set * 8192 + (i & 1) * 1024);
      };
      auto mm = [&](int set, int i) { const int s = i >> 2, m = i & 3; acc[s][m] = mfma(cat(wf[set][2 * s], wf[set][2 * s + 1]), xf[set][m], acc[s][m]); };
      {
        float t[8];
#pragma unroll
        for (int e = 0; e < 8; ++e)
          t[e] = (e & 1) ? sadd(sadd(bfhi(pre[0][e >> 1]), bfhi(pre[1][e >> 1])), sadd(bfhi(pre[2][e >> 1]), bfhi(pre[3][e >> 1])))
                         : sadd(sadd(bflo(pre[0][e >> 1]), bflo(pre[1][e >> 1])), sadd(bflo(pre[2][e >> 1]), bflo(pre[3][e >> 1])));
#pragma unroll
        for (int e = 0; e < 4; ++e) { const u32x2 h = swap32(t[e], t[e + 4]); t[e] = sadd(u2f(h.x), u2f(h.y)); }
#pragma unroll
        for (int e = 0; e < 2; ++e) { const u32x2 h = swap16(t[e], t[e + 2]); t[e] = dpp_add<0x128>(sadd(u2f(h.x), u2f(h.y))); }
        *(LAS f32x2*)(size_t)(LB + L_LS + d * 256 + (4 * (lane & 7) + g) * 8) = (f32x2){t[0], t[1]};
      }
      __builtin_amdgcn_s_setprio(1);
      STAMP(1);
      barrier();
      STAMP(2); STAMPC(21);
#pragma unroll
      for (int i = 0; i < 16; ++i) rd(0, i);
      wait_lgkm0();
      auto stage1 = [&](int c, int dl) {
        SCHED();
#pragma unroll
        for (int i = 0; i < 24; ++i) {
          mm(0, i); SCHED();
          if (i < 16) rd(1, i);
          else if (i < 22) adv(aw[i - 16], dl);
          else if (i == 22) adv(ax[0], dl);
          SCHED();
        }
        TRACE(2 * c);
        barrier();
        TRACE(2 * c + 1);
        SCHED();
      };
      int ph = 0;
#pragma unroll 1
      for (int c = 1; c < 8; ++c) {
        const int dl = ph == 2 ? -2 * (int)SLOT : (int)SLOT;
        ph = ph == 2 ? 0 : ph + 1;
        stage1(c, dl);
#pragma unroll
        for (int i = 0; i < 24; ++i) {
          mm(1, i); SCHED();
          if (i < 16) rd(0, i);
          else if (i == 16) adv(ax[1], dl);
          else if (i == 23) wait_lgkm0();
          SCHED();
        }
      }
      stage1(8, 0);
      unsigned b1a = LB + L_B1 + g * 8;
      asm volatile("" : "+v"(b1a));
      STAMP(4); STAMPC(22);
      f32x2 rs[4], nm[4];
      f32x4 cs[6];
      u32x2 b1v[6];
      f32x2 st[4];
      auto colrd = [&](int s) { cs[s] = ldsf4(csa + 64 * s); b1v[s] = *(LAS u32x2*)(size_t)(b1a + 32 * s); };
#pragma unroll
      for (int i = 0; i < 4; ++i) st[i] = (f32x2){ldsf1(sta + i * 32), ldsf1(sta + i * 32 + 8)};
      colrd(0); colrd(1);
      SCHED();
#pragma unroll
      for (int i = 0; i < BSPLIT; ++i) mm(1, i);
      SCHED();
#pragma unroll
      for (int i = 0; i < 4; ++i) { rs[i] = bco(st[i][0]); nm[i] = bco(-st[i][1]); }
      STAMP(5);
      const unsigned fo = WS_FLAG + (2 * r + d) * 2048;
      const unsigned ho = WS_H + (unsigned)(2 * r + d) * H_MT + (unsigned)(4 * j) * 4096 + lane * 16;
#pragma unroll
      for (int pr = 0; pr < 3; ++pr) {
        Col col;
        mkcol(col, cs[2 * pr], cs[2 * pr + 1], b1v[2 * pr], b1v[2 * pr + 1]);
        if (pr < 2) { colrd(2 * pr + 2); colrd(2 * pr + 3); }
        SCHED();
        if (pr == 1) {
#pragma unroll
          for (int i = BSPLIT; i < 24; ++i) mm(1, i);
          SCHED();
          STAMP(7);
          prog_wait(PW + (6 + d) * 4, 1);
#pragma unroll
          for (int i = 0; i < 5; ++i) absorb(i, home + i * 4096);
          wait_lgkm0();
          prog_set(PW + w * 4, 1);
          if (BFLAG0 < 0) {
            wait_vm<0>();
            if (lane == 0) __builtin_amdgcn_raw_buffer_store_b64((u32x2){tagA, tagB}, wsr, fo + flag_of(j, 0), 0, 16);
          }
          STAMP(8);
        }
#pragma unroll
        for (int m = 0; m < 4; ++m) {
          __builtin_amdgcn_raw_buffer_store_b128(gelu8(acc[2 * pr][m], acc[2 * pr + 1][m], col, rs[m], nm[m]), wsr, ho + pr * 4096 + m * 1024, 0, 16);
          TRACE2(1 + 4 * pr + m);
          if (pr > 0 && m == (pr == 1 ? BFLAG0 : BFLAG)) {
            if (pr == 1) wait_vm<(BFLAG0 < 0 ? 0 : BFLAG0 + 1)>(); else wait_vm<BFLAG + 1>();
            if (lane == 0) __builtin_amdgcn_raw_buffer_store_b64((u32x2){tagA, tagB}, wsr, fo + flag_of(j, pr - 1), 0, 16);
          }
          if (pr == 2 && m == 0) snap(false);
          SCHED();
        }
      }
      STAMP(6);
      setup2(6);
      if (lane == 0) pubo = fo + flag_of(j, 2);
      __builtin_amdgcn_s_setprio(0);
    } else {
#pragma unroll 1
      for (int c = 0; c < 9; ++c) barrier();
      prog_wait(PW + (6 + d) * 4, 1);
#pragma unroll
      for (int i = 0; i < 5; ++i) absorb(i, home + i * 4096);
      wait_lgkm0();
      prog_set(PW + w * 4, 1);
      setup2(6);
      snap(false);
    }
  };
  auto roleA = [&]() __attribute__((always_inline)) {
    home = HB + 2 * SLOT;
    const int srow = 128 * r + 8 * j + 4 * d + g;
    u32x2 dv[4], bv[4];
#pragma unroll
    for (int n = 0; n < 4; ++n) { dv[n] = *(const GAS u32x2*)(p.dsk + ntile * 128 + n * 32 + g * 8); bv[n] = *(const GAS u32x2*)(p.b2 + ntile * 128 + n * 32 + g * 8); }
    if (tid < 16) *(LAS unsigned*)(size_t)(PW + tid * 4) = 0u;
    f32x4 acc[2][4];
#pragma unroll
    for (int m = 0; m < 4; ++m) { acc[0][m] = (f32x4){0, 0, 0, 0}; acc[1][m] = (f32x4){0, 0, 0, 0}; asm volatile("" : "+v"(acc[0][m]), "+v"(acc[1][m])); }
    unsigned ax0 = LB + (64 * d + i16) * 128 + kx, ax1 = ax0 ^ 64, aw0 = awb + ((6 ^ awx) * 32), aw1 = awb + ((7 ^ awx) * 32);
    unsigned sta = LB + L_LS + qq * 288 + (8 * d + ((i16 >> 1) & 1)) * 16 + (i16 & 1) * 4, csa = LB + L_CS + 384 + g * 16;
    asm volatile("" : "+v"(ax0), "+v"(ax1), "+v"(aw0), "+v"(aw1), "+v"(sta), "+v"(csa));
    int ph = 0;
    auto chunk = [&](auto side) {
      s16x8 xf[2][4], wf[2][2];
      if (ADELAY) __builtin_amdgcn_s_sleep(ADELAY);
#pragma unroll
      for (int k = 0; k < 2; ++k) {
        wf[k][0] = ldstr(aw0 + k * 8192, aw0 + k * 8192 + 1024);
        wf[k][1] = ldstr(aw1 + k * 8192, aw1 + k * 8192 + 1024);
#pragma unroll
        for (int m = 0; m < 4; ++m) xf[k][m] = lds128((k ? ax1 : ax0) + m * 2048);
      }
      SCHED();
      const int dl = ph == 2 ? -2 * (int)SLOT : (int)SLOT;
      ph = ph == 2 ? 0 : ph + 1;
      adv(ax0, dl); adv(ax1, dl); adv(aw0, dl); adv(aw1, dl);
      side(8);
      SCHED();
#pragma unroll
      for (int k = 0; k < 2; ++k)
#pragma unroll
        for (int m = 0; m < 4; ++m) {
          acc[0][m] = mfma(wf[k][0], xf[k][m], acc[0][m]); acc[1][m] = mfma(wf[k][1], xf[k][m], acc[1][m]);
          SCHED();
          side(4 * k + m);
          SCHED();
        }
    };
    auto none = [](int) {};
    mktag();
    asm volatile("" : "+v"(pre[3]));
    STAMP(19);
    {
      const float mean = sum32(pre) * (1.f / 512.f);
      float v0 = 0.f, v1 = 0.f;
#pragma unroll
      for (int i = 0; i < 4; ++i)
#pragma unroll
        for (int e = 0; e < 4; ++e) {
          const float d0 = bflo(pre[i][e]) - mean, d1 = bfhi(pre[i][e]) - mean;
          v0 = sfma(d0, d0, v0); v1 = sfma(d1, d1, v1);
        }
      const float rsd = __builtin_amdgcn_rsqf(row16_sum(v0 + v1) * (1.f / 512.f) + 1e-5f);
      if (i16 == 0) __builtin_amdgcn_raw_buffer_store_b128((u32x4){tagA, f2u(rsd), tagB, f2u(mean * rsd)}, wsr, WS_STAT + srow * 16, 0, 16);
    }
    __builtin_amdgcn_s_setprio(APRIO);
    STAMP(1);
    barrier();
    STAMP(2);
    chunk(none);
    if (d) {
      const f32x2 c0 = ldsf2(LB + L_LS + 512 + (lane & 3) * 8);
      if (lane < 4) __builtin_amdgcn_raw_buffer_store_b128((u32x4){tagA, f2u(sadd(c0[0], c0[1])), tagB, 0u}, wsr, WS_BS + j * 1024 + (4 * r + lane) * 16, 0, 16);
    } else if (mlp) {
      const f32x2 c0 = ldsf2(LB + L_LS + (lane & 31) * 8), c1 = ldsf2(LB + L_LS + 256 + (lane & 31) * 8);
      if (lane < 32) __builtin_amdgcn_raw_buffer_store_b128((u32x4){tagA, f2u(sadd(c0[0], c1[0])), tagB, f2u(sadd(c0[1], c1[1]))}, wsr, WS_CS + (j * 16 + r) * 512 + lane * 16, 0, 16);
    }
    barrier();
#pragma unroll 1
    for (int c = 1; c < 3; ++c) { chunk(none); barrier(); }
    chunk(none);
    asm volatile("" : "+v"(dv[0]), "+v"(dv[1]), "+v"(dv[2]), "+v"(dv[3]), "+v"(bv[0]), "+v"(bv[1]), "+v"(bv[2]), "+v"(bv[3]));
    u32x2 xv[2][4];
#pragma unroll
    for (int m = 0; m < 2; ++m)
#pragma unroll
      for (int n = 0; n < 4; ++n) xv[m][n] = *(LAS u32x2*)(size_t)(HB + 3 * SLOT + 12288 + (16 * m + i16) * 128 + (g & 1) * 8 + (((2 * n + (g >> 1)) ^ (i16 >> 1)) * 16));
    barrier();
#pragma unroll 1
    for (int c = 4; c < 6; ++c) { chunk(none); barrier(); }
    f32x2 st[4];
#pragma unroll
    for (int m = 0; m < 4; ++m) st[m] = (f32x2){ldsf1(sta + m * 32), ldsf1(sta + m * 32 + 8)};
    unsigned ra[4], vc[8], ho = mlp ? WS_H + (unsigned)(2 * r + d) * H_MT + (unsigned)(4 * j + 3) * 4096 + lane * 16 : 0xffff0000u;
    f32x2 rs[4], nm[4];
    chunk([&](int k) {
      if (k < 4) { rs[k] = bco(st[k][0]); nm[k] = bco(-st[k][1]); asm volatile("" : "+v"(rs[k]), "+v"(nm[k])); }
      if (k < 8) return;
#pragma unroll
      for (int n = 0; n < 4; ++n) ra[n] = home + r2l + ((n ^ r2x) * 32);
      asm volatile("" : "+v"(ra[0]), "+v"(ra[1]), "+v"(ra[2]), "+v"(ra[3]), "+v"(ho));
    });
    TRACE(14); barrier(); TRACE(15);
    STAMP(4);
    const f32x4 cs0 = ldsf4(csa), cs1 = ldsf4(csa + 64);
    const u32x2 b10 = *(LAS u32x2*)(size_t)(LB + L_B1 + 192 + g * 8), b11 = *(LAS u32x2*)(size_t)(LB + L_B1 + 224 + g * 8);
    Col col;
    auto take = [&](int i, int pg) {
#pragma unroll
      for (int n = 0; n < 4; ++n) we[i][n] = ldstr(ra[n] + pg * 4096, ra[n] + pg * 4096 + 2048);
    };
    chunk([&](int k) {
      if (k == 2) { mkcol(col, cs0, cs1, b10, b11); asm volatile("" : "+v"(col.bias[0]), "+v"(col.bias[1]), "+v"(col.bias[2]), "+v"(col.bias[3])); }
      if (k >= 3 && k < 6) take(k - 3, k - 3);
    });
    barrier();
    __builtin_amdgcn_s_setprio(2);
    STAMP(5);
#pragma unroll
    for (int m = 0; m < 4; ++m) {
      TRACE2(1 + 2 * m);
      const u32x4 o = gelu8(acc[0][m], acc[1][m], col, rs[m], nm[m]);
      TRACE2(2 + 2 * m);
      __builtin_amdgcn_raw_buffer_store_b128(o, wsr, ho + m * 1024, 0, 16);
      if (m == 0) { take(3, 3); take(4, 4); }
      SCHED();
    }
    STAMP(6);
#pragma unroll
    for (int m = 0; m < 2; ++m) {
      const u32x4 s2 = *(LAS u32x4*)(size_t)(LB + L_ST2 + (tok + 8 * (mtile & 1) + 4 * d + 2 * m) * 16);
      const f32x2 r2 = bco(u2f(s2.y)), n2 = bco(-u2f(s2.w));
#pragma unroll
      for (int n = 0; n < 4; ++n) {
        f32x2 o[2];
#pragma unroll
        for (int h = 0; h < 2; ++h) {
          const f32x2 xx = {bflo(xv[m][n][h]), bfhi(xv[m][n][h])};
          o[h] = pfma(pfma(xx, r2, n2), (f32x2){bflo(dv[n][h]), bfhi(dv[n][h])}, xx + (f32x2){bflo(bv[n][h]), bfhi(bv[n][h])});
        }
        acc2[n][m] = __builtin_shufflevector(o[0], o[1], 0, 1, 2, 3);
        acc2[n][2 + m] = (f32x4){0, 0, 0, 0};
        asm volatile("" : "+v"(acc2[n][m]), "+v"(acc2[n][2 + m]));
      }
    }
    STAMP(7);
    wait_vm<0>();
    if (mlp && lane == 0) __builtin_amdgcn_raw_buffer_store_b64((u32x2){tagA, tagB}, wsr, WS_FLAG + (2 * r + d) * 2048 + flag_of(j, 3), 0, 16);
    STAMP(18);
    mkvc(vc);
    setup2(8);
    prog_wait(PW + (6 + d) * 4, 2);
#pragma unroll
    for (int i = 5; i < 8; ++i) take(i, i - 5);
    wait_lgkm0();
    snap(false);
    loadC(vc, kg, home);
    STAMP(8);
    __builtin_amdgcn_s_setprio(0);
  };

  auto phase2 = [&](auto nm_, auto dep_) __attribute__((always_inline)) {
    STAMP(9);
    constexpr int NM = decltype(nm_)::value, N = NM + 4, DEP = decltype(dep_)::value;
    const u64 tag = ((u64)tagB << 32) | tagA;
    auto has = [&](int k) { return (k < NM ? flm[k < NM ? k : 0] : fls[k < NM ? 0 : k - NM]) == tag; };
    auto need = [&](int k, bool ok) {
      if (__builtin_expect(!ok, 0)) {
#pragma unroll 1
        for (int spin = 1; spin < SPIN_CAP; ++spin) {
          snap(k >= NM);
          if (has(k)) break;
          if (!(spin & 31)) {
            const u32x2 f = __builtin_amdgcn_raw_buffer_load_b64(wsr, 0, k < NM ? fm + 8 * k : fs + 8 * (k - NM), 16);
            if (__builtin_amdgcn_readfirstlane(f.x) == tagA && __builtin_amdgcn_readfirstlane(f.y) == tagB) break;
          }
          __builtin_amdgcn_s_sleep(PSLEEP);
        }
      }
      asm volatile("" ::: "memory");
    };
    auto publish = [&]() { __builtin_amdgcn_raw_buffer_store_b64((u32x2){tagA, tagB}, wsr, pubo, 0, 16); };
    auto loadH = [&](int i, int m) {
      if (m == 0) asm volatile("" : "+v"(hb));
      return __builtin_amdgcn_raw_buffer_load_b128(wsr, hb + (m & 1) * 1024, __builtin_amdgcn_readlane(ksl, i) ^ ((m & 2) * 1024), 16);
    };
    u32x4 hf[N][4];
    auto load4 = [&](int i) {
#pragma unroll
      for (int m = 0; m < 4; ++m) hf[i][m] = loadH(i, m);
    };
    STAMP(10);
    if (NM == 6 && __builtin_expect(!(has(0) && has(1)), 0)) { wait_vm<0>(); publish(); pubo = 0xffff0000u; }
    need(0, has(0)); load4(0);
    need(1, has(1)); load4(1);
    if (NM == 6) { wait_vm<8>(); publish(); }
    STAMP(11);
    s16x8 cfr[2][4];
    bool okn = false;
    SCHED();
#pragma unroll
    for (int i = 0; i < N; ++i) {
      const int k = i == 0 ? 2 : i + DEP;
      if (i == 1) STAMP(12);
      if (k == NM) STAMP(13);
      if (k < N) need(k, (i & 1) && k < NM ? okn : has(k));
      if (k == NM) STAMP(14);
      if (i > 0 || DEP == 2) {
        if (!(i & 1) && k + 1 < NM) {
          okn = has(k + 1);
          if (k + 2 < NM) snap(false);
        }
        if (k + 2 == NM) snap(true);
      }
      if (NM == 8 && i == 1 && w >= 6) prog_set(PW + w * 4, 3);
      if (NM == 6 && i == 3) { prog_wait(PW + (6 + d) * 4, 3); absorb(5, home + 16384); }
      if (i >= NM - 1 && i < N - 1) {
#pragma unroll
        for (int n = 0; n < 4; ++n) {
          const unsigned a = (crd ^ (32 * (i - NM + 1))) + n * 2048;
          cfr[(i + 1) & 1][n] = cat(*(LAS s16x4*)(size_t)a, *(LAS s16x4*)(size_t)(a + 8192));
        }
      }
      SCHED();
#pragma unroll
      for (int m = 0; m < 4; ++m) {
#pragma unroll
        for (int n = 0; n < 4; ++n) acc2[n][m] = mfma(i < NM ? we[i < NM ? i : 0][n] : cfr[i & 1][n], __builtin_bit_cast(s16x8, hf[i][m]), NM == 6 && i == 0 ? (f32x4){0, 0, 0, 0} : acc2[n][m]);
        SCHED();
        if (k < N) hf[k < N ? k : 0][m] = loadH(k, m);
        SCHED();
      }
      if (i == 0 && DEP == 3) {
        need(3, has(3)); load4(3);
        okn = has(4);
        snap(false);
        SCHED();
      }
    }
  };
  if (__builtin_expect(w >= 6 || w < 2, 1)) {
    if (__builtin_expect(w >= 6, 1)) roleD(); else roleA();
    phase2(std::integral_constant<int, 8>{}, std::integral_constant<int, DEPE>{});
  } else {
    if (w >= 4) roleL(); else roleB();
    phase2(std::integral_constant<int, 6>{}, std::integral_constant<int, DEPL>{});
  }
  STAMP(15);
  int tl = threadIdx.x;
  asm volatile("" : "+v"(tl));
  const int i16t = tl & 15, gt = (tl >> 4) & 3;
#pragma unroll
  for (int m = 0; m < 4; ++m)
#pragma unroll
    for (int n = 0; n < 4; ++n) *(LAS f32x4*)(size_t)(home + ((16 * m + i16t) ^ (w == 1 ? 32 : 0)) * 256 + (((4 * n + gt) ^ i16t) * 16)) = acc2[n][m];
  const int ck = tl & 15;
  const int orow = 32 * ((tl >> 6) & 3) + 16 * ((tl >> 4) & 1) + ((tl >> 5) & 1) + 2 * (tl >> 8) + 8 * (mtile & 1);
  char* op = p.out + (128 * (mtile >> 1) + orow) * 2048 + ntile * 256 + ck * 16;
  unsigned ra[4];
#pragma unroll
  for (int k = 0; k < 4; ++k) ra[k] = LB + (tl >> 4) * 256 + ((ck ^ ((tl >> 4) & 15)) * 16) + k * SLOT;
  asm volatile("" : "+v"(ra[0]), "+v"(ra[1]), "+v"(ra[2]), "+v"(ra[3]), "+v"(op));
  barrier();
  STAMP(16);
  {
    f32x4 pt[2][8];
#pragma unroll
    for (int h = 0; h < 2; ++h)
#pragma unroll
      for (int k = 0; k < 8; ++k) pt[h][k] = ldsf4(ra[k >> 1] + (k & 1) * HALF + h * 8192);
#pragma unroll
    for (int h = 0; h < 2; ++h) {
      const f32x4 s = ((pt[h][0] + pt[h][1]) + (pt[h][2] + pt[h][3])) + ((pt[h][4] + pt[h][5]) + (pt[h][6] + pt[h][7]));
      __builtin_nontemporal_store(s, (GAS f32x4*)(op + h * 8192));
    }
  }
  if (b == 255 && tl == 0) __builtin_amdgcn_raw_buffer_store_b64((u32x2){tagA, tagB}, wsr, WS_EPOCH, 0, 16);
  while (__builtin_amdgcn_s_memrealtime() - t_start < 2200) __builtin_amdgcn_s_sleep(2);
  { int mn = 0;
    for (int i = 0; i < 4; ++i) { int v = (int)(__hip_atomic_load((unsigned*)(p.ws + WS_B2B) + (tl & 63) + 64 * i, __ATOMIC_RELAXED, __HIP_MEMORY_SCOPE_AGENT) - (unsigned)t_start); mn = min(mn, v); }
    for (int m = 32; m >= 1; m >>= 1) mn = min(mn, __shfl_xor(mn, m));
    mn = __builtin_amdgcn_readfirstlane(mn);
    if (mn < -1500) mn = -1500;
    while ((long long)(__builtin_amdgcn_s_memrealtime() - t_start) < 3000 + mn) __builtin_amdgcn_s_sleep(2);
  }
}

extern "C" void kernel_launch(void* const* d_in, const int* in_sizes, int n_in, void* d_out, int out_size, void* d_ws,
                              size_t ws_size, hipStream_t stream) {
  Params p;
  p.x = (const char*)d_in[0]; p.lre = (const char*)d_in[1]; p.lim = (const char*)d_in[2];
  p.bre = (const char*)d_in[3]; p.bim = (const char*)d_in[4]; p.cre = (const char*)d_in[5]; p.cim = (const char*)d_in[6];
  p.logdt = (const char*)d_in[7]; p.dsk = (const char*)d_in[8];
  p.w1 = (const char*)d_in[10]; p.b1 = (const char*)d_in[11]; p.w2 = (const char*)d_in[12]; p.b2 = (const char*)d_in[13];
  p.out = (char*)d_out; p.ws = (char*)d_ws;
  fused_kernel<<<256, 512, 0, stream>>>(p);
}
```

```cpp
#include <hip/hip_runtime.h>
#include <stdint.h>
#include <type_traits>

typedef short s16x4 __attribute__((ext_vector_type(4)));
typedef short s16x8 __attribute__((ext_vector_type(8)));
typedef __bf16 bf16x8 __attribute__((ext_vector_type(8)));
typedef __bf16 bf16x2 __attribute__((ext_vector_type(2)));
typedef float f32x4 __attribute__((ext_vector_type(4)));
typedef float f32x2 __attribute__((ext_vector_type(2)));
typedef unsigned u32x4 __attribute__((ext_vector_type(4)));
typedef unsigned u32x2 __attribute__((ext_vector_type(2)));
typedef int i32x4 __attribute__((ext_vector_type(4)));
typedef unsigned long long u64;
typedef u64 u64x4 __attribute__((ext_vector_type(4)));
typedef u64 u64x8 __attribute__((ext_vector_type(8)));
typedef int i32x8 __attribute__((ext_vector_type(8)));
typedef int i32x16 __attribute__((ext_vector_type(16)));
__device__ i32x8 sbl8(i32x4, int, int) __asm("llvm.amdgcn.s.buffer.load.v8i32");
__device__ i32x16 sbl16(i32x4, int, int) __asm("llvm.amdgcn.s.buffer.load.v16i32");
#define LAS __attribute__((address_space(3)))
#define GAS __attribute__((address_space(1)))
#define DI __device__ __forceinline__

#define STAMPS 0
#define TRACING 0
#define GATHER_AT 4
#define ROT 1
#define TOUCH 1
#define TLBW 0
#define NOFF 0
#define PSLEEP 4
#define BFLAG 2
#define ABAR 0
#define KROT 0
#define LPRIO 3
#define CHK 0
#define BSPLIT 8
#define APRIO 0
#define ADELAY 0
#define BFLAG0 -1
#define DEPE 2
#define DEPL 2
#define LB_SLEEP 8
#define CHECK_AT 5
#define CPF 1
#define MAP 1

struct Params {
  const char *x, *lre, *lim, *bre, *bim, *cre, *cim, *logdt, *dsk, *w1, *b1, *w2, *b2;
  char *out, *ws;
};

constexpr unsigned WS_EPOCH = 0;
constexpr unsigned WS_STAT = 4096;
constexpr unsigned WS_TOT = WS_STAT + 2048 * 16;
constexpr unsigned WS_CS = WS_TOT + 16 * 2 * 16 * 256;
constexpr unsigned WS_BS = WS_CS + 16 * 16 * 512;
constexpr unsigned WS_FLAG = WS_BS + 16 * 1024;
constexpr unsigned WS_H = WS_FLAG + 32 * 2048;
constexpr unsigned H_MT = 88 * 4096;
constexpr unsigned WS_END = WS_H + 32 * H_MT;
constexpr unsigned WS_STAMP = 32u << 20;
constexpr unsigned WS_B2B = WS_STAMP + (4u << 20);
constexpr unsigned SLOT = 40960, HALF = 20480, SL_W = 16384, SL_B = 32768;
constexpr unsigned L_ST1 = 3 * SLOT + 16384, L_ST2 = L_ST1 + 2048;
constexpr unsigned L_CS = 3 * SLOT + HALF + 16384, L_FLG = L_CS + 512, L_B1 = L_CS + 768, L_BS = L_CS + 1024, L_LS = L_CS + 2048, L_DUMMY = L_CS + 3328;
constexpr unsigned LDS_BYTES = 163840;
constexpr int SPIN_CAP = 40000;
constexpr unsigned CODE_BYTES = 43520;

DI void dma4w(unsigned voff, const char* sbase, unsigned ldsaddr) {
  asm volatile("s_mov_b32 m0, %2\n\ts_nop 0\n\tglobal_load_lds_dword %0, %1" ::"v"(voff), "s"(sbase), "s"(ldsaddr));
}
DI void dma_x4(const unsigned* v, const char* sbase, unsigned ldsaddr, bool low = false) {
  if (low) {
    asm volatile("s_mov_b32 m0, %5\n\ts_setprio 0\n\t"
               "global_load_lds_dwordx4 %0, %4\n\tglobal_load_lds_dwordx4 %1, %4 offset:1024\n\t"
               "global_load_lds_dwordx4 %2, %4 offset:2048\n\tglobal_load_lds_dwordx4 %3, %4 offset:3072\n\ts_setprio 3"
               ::"v"(v[0]), "v"(v[1]), "v"(v[2]), "v"(v[3]), "s"(sbase), "s"(ldsaddr));
    return;
  }
  asm volatile("s_mov_b32 m0, %5\n\ts_nop 0\n\t"
               "global_load_lds_dwordx4 %0, %4\n\tglobal_load_lds_dwordx4 %1, %4 offset:1024\n\t"
               "global_load_lds_dwordx4 %2, %4 offset:2048\n\tglobal_load_lds_dwordx4 %3, %4 offset:3072"
               ::"v"(v[0]), "v"(v[1]), "v"(v[2]), "v"(v[3]), "s"(sbase), "s"(ldsaddr));
}
DI void dma_x8(const unsigned* v, const char* sbase, unsigned ldsaddr) {
  asm volatile("s_mov_b32 m0, %9\n\ts_nop 0\n\t"
               "global_load_lds_dwordx4 %0, %8 offset:-4096\n\tglobal_load_lds_dwordx4 %1, %8 offset:-3072\n\t"
               "global_load_lds_dwordx4 %2, %8 offset:-2048\n\tglobal_load_lds_dwordx4 %3, %8 offset:-1024\n\t"
               "global_load_lds_dwordx4 %4, %8\n\tglobal_load_lds_dwordx4 %5, %8 offset:1024\n\t"
               "global_load_lds_dwordx4 %6, %8 offset:2048\n\tglobal_load_lds_dwordx4 %7, %8 offset:3072"
               ::"v"(v[0]), "v"(v[1]), "v"(v[2]), "v"(v[3]), "v"(v[4]), "v"(v[5]), "v"(v[6]), "v"(v[7]), "s"(sbase), "s"(ldsaddr + 4096u));
}
constexpr int PG(int i) { return i & 3; }
template <int N> DI void wait_vm() { asm volatile("s_waitcnt vmcnt(%0)" ::"i"(N) : "memory"); }
DI void wait_lgkm0() { asm volatile("" ::: "memory"); __builtin_amdgcn_s_waitcnt(0xC07F); asm volatile("" ::: "memory"); }
DI void barrier() {
  asm volatile("" ::: "memory");
  __builtin_amdgcn_s_waitcnt(0xC07F);
  __builtin_amdgcn_s_barrier();
  asm volatile("" ::: "memory");
}
#define SCHED() __builtin_amdgcn_sched_barrier(0)
DI s16x8 lds128(unsigned a) { return *(LAS s16x8*)(size_t)a; }
DI f32x4 ldsf4(unsigned a) { return *(LAS f32x4*)(size_t)a; }
DI f32x2 ldsf2(unsigned a) { return *(LAS f32x2*)(size_t)a; }
DI float ldsf1(unsigned a) { return *(LAS float*)(size_t)a; }
DI s16x4 ldstr4(unsigned a) { return __builtin_amdgcn_ds_read_tr16_b64_v4i16((LAS s16x4*)(size_t)a); }
DI s16x8 cat(s16x4 lo, s16x4 hi) { return __builtin_shufflevector(lo, hi, 0, 1, 2, 3, 4, 5, 6, 7); }
DI s16x8 ldstr(unsigned a0, unsigned a1) { return cat(ldstr4(a0), ldstr4(a1)); }
DI f32x4 mfma(s16x8 a, s16x8 b, f32x4 c) {
  return __builtin_amdgcn_mfma_f32_16x16x32_bf16(__builtin_bit_cast(bf16x8, a), __builtin_bit_cast(bf16x8, b), c, 0, 0, 0);
}
DI unsigned pk2(float a, float b) { f32x2 v = {a, b}; return __builtin_bit_cast(unsigned, __builtin_convertvector(v, bf16x2)); }
DI float u2f(unsigned u) { return __builtin_bit_cast(float, u); }
DI unsigned f2u(float u) { return __builtin_bit_cast(unsigned, u); }
DI float bflo(unsigned u) { return __builtin_bit_cast(float, u << 16); }
DI float bfhi(unsigned u) { return __builtin_bit_cast(float, u & 0xffff0000u); }
template <int CTRL> DI float dpp_add(float v) {
  return v + __builtin_bit_cast(float, __builtin_amdgcn_update_dpp(0, __builtin_bit_cast(int, v), CTRL, 0xf, 0xf, true));
}
DI float row16_sum(float v) {
  v = dpp_add<0xB1>(v);
  v = dpp_add<0x4E>(v);
  v = dpp_add<0x141>(v);
  v = dpp_add<0x140>(v);
  return v;
}
DI float smul(float a, float b) { float d; asm("v_mul_f32 %0, %1, %2" : "=v"(d) : "v"(a), "v"(b)); return d; }
DI float sadd(float a, float b) { float d; asm("v_add_f32 %0, %1, %2" : "=v"(d) : "v"(a), "v"(b)); return d; }
DI float sfma(float a, float b, float c) { float d; asm("v_fma_f32 %0, %1, %2, %3" : "=v"(d) : "v"(a), "v"(b), "v"(c)); return d; }
DI float sfms(float a, float b, float c) { float d; asm("v_fma_f32 %0, %1, %2, -%3" : "=v"(d) : "v"(a), "v"(b), "v"(c)); return d; }
DI void zero4(f32x4& v) {
  float a, b, c, d;
  asm volatile("v_mov_b32 %0, 0\n\tv_mov_b32 %1, 0\n\tv_mov_b32 %2, 0\n\tv_mov_b32 %3, 0" : "=v"(a), "=v"(b), "=v"(c), "=v"(d));
  v = (f32x4){a, b, c, d};
}
DI u32x2 swap32(float a, float b) { asm volatile("s_nop 1" : "+v"(a), "+v"(b)); return __builtin_amdgcn_permlane32_swap(f2u(a), f2u(b), false, false); }
DI u32x2 swap16(float a, float b) { asm volatile("s_nop 1" : "+v"(a), "+v"(b)); return __builtin_amdgcn_permlane16_swap(f2u(a), f2u(b), false, false); }
DI f32x2 pfma(f32x2 a, f32x2 b, f32x2 c) { return __builtin_elementwise_fma(a, b, c); }
DI f32x2 bc(float a) { return (f32x2){a, a}; }
DI f32x2 bco(float a) { f32x2 v = {a, a}; asm("" : "+v"(v)); return v; }
DI f32x2 cmul(f32x2 a, f32x2 b) { return (f32x2){sfms(a[0], b[0], smul(a[1], b[1])), sfma(a[0], b[1], smul(a[1], b[0]))}; }
DI f32x2 cfma(f32x2 a, f32x2 b, f32x2 c) { return (f32x2){sfms(a[0], b[0], sfms(a[1], b[1], c[0])), sfma(a[0], b[1], sfma(a[1], b[0], c[1]))}; }
DI f32x2 csel(bool p, f32x2 a, f32x2 b) { return (f32x2){p ? a[0] : b[0], p ? a[1] : b[1]}; }
DI void prog_set(unsigned a, unsigned v) {
  asm volatile("s_mov_b64 exec, 1\n\tds_write_b32 %0, %1\n\ts_mov_b64 exec, -1" ::"v"(a), "v"(v) : "memory");
}
DI void prog_wait(unsigned a, unsigned v) {
#pragma unroll 1
  for (int spin = 0; spin < SPIN_CAP * 16; ++spin) {
    if (__builtin_amdgcn_readfirstlane(*(volatile LAS unsigned*)(size_t)a) >= v) break;
    __builtin_amdgcn_s_sleep(1);
  }
  asm volatile("" ::: "memory");
}
DI void bar() { asm volatile("" ::: "memory"); __builtin_amdgcn_s_barrier(); asm volatile("" ::: "memory"); }
DI void dma1_dev(unsigned voff, const char* sbase, unsigned ldsaddr) {
  asm volatile("s_mov_b32 m0, %2\n\ts_nop 0\n\tglobal_load_lds_dwordx4 %0, %1 sc1" ::"v"(voff), "s"(sbase), "s"(ldsaddr) : "memory");
}
DI void dma2_dev(unsigned voff, const char* s0, const char* s1, unsigned ldsaddr) {
  asm volatile("s_mov_b32 m0, %3\n\ts_nop 0\n\tglobal_load_lds_dwordx4 %0, %1 sc1\n\tglobal_load_lds_dwordx4 %0, %2 offset:2048 sc1" ::"v"(voff), "s"(s0), "s"(s1), "s"(ldsaddr) : "memory");
}
DI void dma4_dev(unsigned voff, const char* s0, const char* s1, unsigned ldsaddr) {
  asm volatile("s_mov_b32 m0, %3\n\ts_nop 0\n\tglobal_load_lds_dwordx4 %0, %1 sc1\n\tglobal_load_lds_dwordx4 %0, %1 offset:1024 sc1\n\t"
               "global_load_lds_dwordx4 %0, %2 offset:2048 sc1\n\tglobal_load_lds_dwordx4 %0, %2 offset:3072 sc1" ::"v"(voff), "s"(s0), "s"(s1), "s"(ldsaddr) : "memory");
}
DI int kstep_of(int slot, int i) {
  const int q = slot + 4 * i + (slot >= 4 ? 28 : 0), cls = (q >= 14) + (q >= 28) + (q >= 42);
  return 4 * (q - 14 * cls) + ((0x9C >> (2 * cls)) & 3);
}
DI unsigned flag_of(int j, int c) {
  const int q = 14 * ((0x78 >> (2 * c)) & 3) + j;
  return q < 32 ? (q & 3) * 128 + (q >> 2) * 8 : 512 + (q & 3) * 128 + ((q - 32) >> 2) * 8;
}
DI void cnt_add(unsigned a) {
  asm volatile("s_mov_b64 exec, 1\n\tds_add_u32 %0, %1\n\ts_mov_b64 exec, -1" ::"v"(a), "v"(1u) : "memory");
}
DI f32x2 lo2(f32x4 v) { return __builtin_shufflevector(v, v, 0, 1); }
DI f32x2 hi2(f32x4 v) { return __builtin_shufflevector(v, v, 2, 3); }
DI void rows4(float v, float* o) {
  asm volatile("s_nop 1" : "+v"(v));
  const unsigned u = f2u(v);
  const u32x2 h = __builtin_amdgcn_permlane32_swap(u, u, false, false);
  const u32x2 a = __builtin_amdgcn_permlane16_swap(h.x, h.x, false, false), b = __builtin_amdgcn_permlane16_swap(h.y, h.y, false, false);
  o[0] = u2f(a.x); o[1] = u2f(a.y); o[2] = u2f(b.x); o[3] = u2f(b.y);
}
struct Col { f32x2 cs[4], bias[4]; };
DI void mkcol(Col& c, f32x4 cs0, f32x4 cs1, u32x2 b0, u32x2 b1) {
  c.cs[0] = lo2(cs0); c.cs[1] = hi2(cs0); c.cs[2] = lo2(cs1); c.cs[3] = hi2(cs1);
  c.bias[0] = (f32x2){bflo(b0.x), bfhi(b0.x)}; c.bias[1] = (f32x2){bflo(b0.y), bfhi(b0.y)};
  c.bias[2] = (f32x2){bflo(b1.x), bfhi(b1.x)}; c.bias[3] = (f32x2){bflo(b1.y), bfhi(b1.y)};
}
DI u32x4 gelu8(f32x4 a0, f32x4 a1, const Col& c, f32x2 rs, f32x2 nmrs) {
  f32x2 v[4], z[4];
  u32x4 o;
#define ST4(stmt) _Pragma("unroll") for (int h = 0; h < 4; ++h) { stmt; } SCHED();
  ST4(v[h] = pfma(c.cs[h], nmrs, c.bias[h]))
  ST4(v[h] = pfma(h == 0 ? lo2(a0) : h == 1 ? hi2(a0) : h == 2 ? lo2(a1) : hi2(a1), rs, v[h]))
  ST4(z[h] = v[h] * v[h])
  ST4(z[h] = pfma(z[h], bc(-0.10294324f), bc(-2.30220820f)))
  ST4(z[h] = z[h] * v[h])
  ST4((z[h] = (f32x2){__builtin_amdgcn_exp2f(z[h][0]), __builtin_amdgcn_exp2f(z[h][1])}))
  ST4(z[h] = z[h] + bc(1.f))
  ST4((z[h] = (f32x2){__builtin_amdgcn_rcpf(z[h][0]), __builtin_amdgcn_rcpf(z[h][1])}))
  ST4(v[h] = v[h] * z[h])
  ST4(o[h] = pk2(v[h][0], v[h][1]))
#undef ST4
  return o;
}

#define TRACE(i) do {} while (0)
#define TRACE2(i) do {} while (0)
#define TRACE3(i) do {} while (0)
#define STAMP(k) do {} while (0)
#define STAMPC(k) do {} while (0)
#define COUNT(k) do {} while (0)

__global__ void __launch_bounds__(512, 2) fused_kernel(Params p) {
  __shared__ __attribute__((aligned(16))) char lds_mem[LDS_BYTES];
  const unsigned long long t_start = __builtin_amdgcn_s_memrealtime();
  unsigned long long pc;
  asm volatile("s_getpc_b64 %0" : "=s"(pc));
  const unsigned LB = (unsigned)(size_t)(LAS char*)lds_mem;
  const int tid = threadIdx.x, lane = tid & 63;
  const int w = __builtin_amdgcn_readfirstlane(tid >> 6);
  if (w == 5) {
    const unsigned co = ((unsigned)(lane & 15) * 32u + (blockIdx.x >> 3)) * 128u;
    dma4w(co < CODE_BYTES ? co : 0u, (const char*)(pc & ~255ull), LB + L_DUMMY + 512);
  }
  const int b = blockIdx.x, idx = b >> 3, xcd = b & 7;
  const int r = 8 * (xcd >> 2) + (idx >> 2), j = 4 * (xcd & 3) + (idx & 3);
  const int rot = (2 * (xcd & 3) + 4 * (xcd >> 2)) & 7;
  const int mtile = 4 * xcd + (idx >> 3), ntile = (idx + NOFF) & 7;
  const int i16 = lane & 15, g = lane >> 4, qq = i16 >> 2, pp = i16 & 3;
  const int d = w & 1;
  const bool mlp = j < 14;
  u32x4 pre[4];
  unsigned v_lre, v_lim, v_ldt;
  if ((w >> 1) == 2) {
    const unsigned po = (unsigned)(32 * j + 16 * d + i16) * 2u;
    v_lre = *(const GAS unsigned short*)(p.lre + po); v_lim = *(const GAS unsigned short*)(p.lim + po); v_ldt = *(const GAS unsigned short*)(p.logdt + po);
    asm volatile("" ::: "memory");
  }
  u32x4 prb[2];
  if (w < 4) {
    const bool isb = w >= 2;
    const char* pb = isb ? p.w1 + (64 * (r & 7) + 32 * d) * 3584 + (mlp ? j : 13) * 256 + (r >> 3) * 128 : p.x + (128 * r + 8 * j + 4 * d) * 1024;
    const unsigned po = isb ? (lane >> 3) * 3584 + (lane & 7) * 16 : g * 1024 + i16 * 16, ps = isb ? 28672 : 256;
#pragma unroll
    for (int i = 0; i < 4; ++i) pre[i] = *(const GAS u32x4*)(pb + i * ps + po);
    if (isb) {
#pragma unroll
      for (int i = 0; i < 2; ++i) prb[i] = *(const GAS u32x4*)((r & 8 ? p.bim : p.bre) + (32 * j + 4 * (r & 7) + 2 * d) * 1024 + i * 512 + (lane >> 5) * 1024 + (lane & 31) * 16);
    }
    asm volatile("" ::: "memory");
  }
  const int kgo = 2 * (idx >> 3), kg = (w + KROT * kgo) & 7;
  auto slot_of = [&](int role) { return (((role + 1) & 2) << 1) + (((((role + 1) & 2) && !KROT ? 0 : kgo) + ((role & 1) ? 0 : 2) + d) & 3); };
  const int slot = slot_of(w >> 1);
  const u32x2 ep = *(const __attribute__((address_space(4))) u32x2*)(p.ws + WS_EPOCH);
  unsigned tagA, tagB;
  auto mktag = [&]() { tagA = ep.x; tagB = ep.y; asm volatile("" : "+s"(tagA), "+s"(tagB)); tagA += 1u; tagB += 1u; };
  const __amdgpu_buffer_rsrc_t wsr = __builtin_amdgcn_make_buffer_rsrc(p.ws, 0, WS_END, 0x00020000);
  const i32x4 wss = {(int)(size_t)p.ws, (int)((size_t)p.ws >> 32) & 0xffff, (int)WS_END, 0x00020000};
  auto fresh = [](int o) { asm volatile("" : "+s"(o) :: "memory"); return o; };
  const unsigned HB = LB + d * HALF;
  const unsigned PW = LB + L_FLG;
  s16x8 we[8][4];
  f32x4 acc2[4][4];
  unsigned home;
  const unsigned r2l = (4 * g + qq) * 128 + pp * 8, r2x = (2 * g + (qq >> 1)) & 3;
  auto absorb = [&](int i, unsigned page) {
#pragma unroll
    for (int n = 0; n < 4; ++n) { const unsigned a = page + r2l + ((n ^ r2x) * 32); we[i][n] = ldstr(a, a + 2048); }
  };
  auto mkvc = [&](unsigned* vc) {
#pragma unroll
    for (int i = 0; i < 8; ++i) vc[i] = (lane >> 3) * 1024 + (((lane & 7) ^ (4 * (i & 1) + g)) * 16) + 4096 + i * 7168;
  };
  auto mkv2 = [&](unsigned* v2) {
#pragma unroll
    for (int i = 0; i < 4; ++i) v2[i] = (lane >> 3) * 1024 + (((((lane & 7) >> 1) ^ g) << 1) | (lane & 1)) * 16 + i * 7168;
  };
  auto loadC = [&](const unsigned* vc, int kgc, unsigned dst) {
    const unsigned o = ntile * 65536 + kgc * 128;
    dma_x8(vc, p.cre + o, dst);
    dma_x8(vc, p.cim + o, dst + 8192);
  };
  const char* w2b = p.w2 + ntile * 128;
  auto adv = [&](unsigned& a, int dl) { a += dl; asm volatile("" : "+v"(a)); };
  const int tok = 32 * qq + 16 * (i16 & 1) + ((i16 >> 1) & 1);
  const unsigned kx = (g ^ (i16 >> 1)) * 16;
  const unsigned awb = LB + SL_W + (8 * g + qq) * 256 + pp * 8, awx = qq | ((g & 1) << 2);
  auto sum32 = [&](const u32x4* v) {
    float s0 = 0.f, s1 = 0.f;
#pragma unroll
    for (int i = 0; i < 4; ++i)
#pragma unroll
      for (int e = 0; e < 4; ++e) { s0 += bflo(v[i][e]); s1 += bfhi(v[i][e]); }
    return row16_sum(s0 + s1);
  };
  auto touch = [&]() {
    dma4w(d ? (lane & 15) * 128 : (lane < 56 ? lane : 0) * 128, d ? p.cre + b * 2048 : p.w2 + b * 7168, LB + L_DUMMY + d * 256);
    if (d) dma4w((lane & 15) * 128, p.cim + b * 2048, LB + L_DUMMY + d * 256);
  };
  auto zero2 = [&]() {
#pragma unroll
    for (int n = 0; n < 4; ++n) {
#pragma unroll
      for (int m = 0; m < 4; ++m) acc2[n][m] = (f32x4){0, 0, 0, 0};
      asm volatile("" : "+v"(acc2[n][0]), "+v"(acc2[n][1]), "+v"(acc2[n][2]), "+v"(acc2[n][3]));
    }
  };
  int ksl;
  unsigned hb, crd, pubo = 0xffff0000u;
  const int fm = WS_FLAG + mtile * 2048 + slot * 128, fs = WS_FLAG + mtile * 2048 + 1024 + kg * 32;
  u64x8 flm;
  u64x4 fls;
  auto snap = [&](bool ssm) { if (ssm) fls = __builtin_bit_cast(u64x4, sbl8(wss, fresh(fs), 1)); else flm = __builtin_bit_cast(u64x8, sbl16(wss, fresh(fm), 1)); };
  auto setup2 = [&](int nm) {
    ksl = (lane < nm ? kstep_of(slot, lane) : 56 + 4 * kg + ((lane - nm) & 3)) * 4096;
    ksl += w == 1 ? 2048 : 0;
    hb = WS_H + (unsigned)mtile * H_MT + lane * 16;
    crd = home + i16 * 128 + (g & 1) * 8 + (((g >> 1) ^ (i16 >> 1)) * 16);
    asm volatile("" : "+v"(ksl), "+v"(hb), "+v"(crd));
  };

  auto roleD = [&]() __attribute__((always_inline)) {
    __builtin_amdgcn_s_setprio(3);
    unsigned vx[8], vw[8], vb[4], v2[4];
#pragma unroll
    for (int i = 0; i < 8; ++i) vw[i] = g * 3584 + ((((i16 >> 1) ^ (g | (((i >> 1) & 1) << 2))) << 1) | (i16 & 1)) * 16 + 4096 + i * (14336 - 1024);
    const char* wb = p.w1 + j * 256 + d * (32 * 3584);
    auto part1 = [&](int c, unsigned slot) { if (mlp) dma_x8(vw, wb + ((c + rot) & 7) * (64 * 3584), LB + slot + SL_W + d * 8192); };
    if (TOUCH == 5) touch();
    part1(0, 0);
#pragma unroll
    for (int i = 0; i < 8; ++i)
      vx[i] = (32 * (g >> 1) + 16 * ((lane >> 3) & 1) + (g & 1)) * 1024 + (((lane & 7) ^ (4 * (i & 1) + g)) * 16) + 4096 + (i & 1) * 65536 + (i >> 1) * 2048 - i * 1024;
#pragma unroll
    for (int i = 0; i < 4; ++i) vb[i] = (lane >> 3) * 1024 + (((lane & 7) ^ (4 * (i & 1) + g)) * 16) + i * 7168;
    const char* xb = p.x + (r * 128 + d * 8) * 1024;
    const char* bb = (d ? p.bim : p.bre) + j * 32768;
    auto partx = [&](int c, unsigned slot) { dma_x8(vx, xb + ((c + rot) & 7) * 128, LB + slot + d * 8192); };
    auto partb = [&](int c, unsigned slot) { dma_x4(vb, bb + ((c + rot) & 7) * 128, LB + slot + SL_B + d * 4096); };
    auto part0 = [&](int c, unsigned slot) { partx(c, slot); partb(c, slot); };
    mkv2(v2);
    unsigned ra[4];
    home = HB + 3 * SLOT;
#pragma unroll
    for (int n = 0; n < 4; ++n) ra[n] = home + r2l + ((n ^ r2x) * 32);
    int kt = kstep_of(slot_of((lane >> 3) & 3), lane & 7) * 32768;
    asm volatile("" : "+v"(kt), "+v"(ra[0]), "+v"(ra[1]), "+v"(ra[2]), "+v"(ra[3]), "+v"(v2[0]), "+v"(v2[1]), "+v"(v2[2]), "+v"(v2[3]));
    bool low = false;
    auto page = [&](int role, int i, int grp, int pg) { dma_x4(v2, w2b + (unsigned)__builtin_amdgcn_readlane(kt, 8 * role + i), HB + grp * SLOT + pg * 4096, low); };
    auto c8 = [&](const char* src, unsigned dst) { dma_x4(vb, src, dst, true); dma_x4(vb, src + 32768, dst + 4096, true); };
    auto take = [&](int i) {
      SCHED();
#pragma unroll
      for (int n = 0; n < 4; ++n) we[i][n] = ldstr(ra[n] + PG(i) * 4096, ra[n] + PG(i) * 4096 + 2048);
      SCHED();
    };
    auto own = [&](int i) { page(3, i, 3, PG(i)); };
    unsigned l16 = lane * 16, la = home + 12288 + lane * 16, lb = LB + L_ST1 + d * 1024 + lane * 16, lc = LB + L_BS + d * 512 + (lane & 31) * 16;
    unsigned wa = LB + L_CS + d * 256 + (lane & 31) * 8 + (lane >> 5) * 4, wb2 = LB + L_LS + (2 * d + (lane >> 5)) * 288 + i16 * 16 + (g & 1) * 4;
    asm volatile("" : "+v"(l16), "+v"(la), "+v"(lb), "+v"(lc), "+v"(wa), "+v"(wb2));
    auto gather = [&](bool bs) {
      const char* cp = p.ws + WS_CS + j * 8192 + d * 4096;
      dma4_dev(l16, cp, cp, home + 12288);
      dma2_dev(l16, p.ws + WS_STAT + r * 2048 + d * 1024, p.ws + WS_STAT + (mtile >> 1) * 2048 + d * 1024 - 2048, LB + L_ST1 + d * 1024);
      if (bs) dma1_dev(l16, p.ws + WS_BS + j * 1024, LB + L_BS);
    };
    auto check = [&](auto between) -> bool {
      auto ld16 = [](unsigned a) { return *(volatile LAS u32x4*)(size_t)a; };
      auto bad = [&](unsigned a, u32x4 v) { return __builtin_amdgcn_bitop3_b32(v.z, tagB, __builtin_amdgcn_bitop3_b32(v.x, tagA, a, 0xBE), 0xBE); };
      const u32x4 c0 = ld16(la), c1 = ld16(la + 1024), c2 = ld16(la + 2048), c3 = ld16(la + 3072), s1 = ld16(lb), s2 = ld16(lb + 2048), s3 = ld16(lc);
      SCHED(); between(); SCHED();
      const u32x2 h = swap32(sadd(sadd(u2f(c0.y), u2f(c1.y)), sadd(u2f(c2.y), u2f(c3.y))), sadd(sadd(u2f(c0.w), u2f(c1.w)), sadd(u2f(c2.w), u2f(c3.w))));
      *(LAS float*)(size_t)wa = sadd(u2f(h.x), u2f(h.y));
      *(LAS unsigned*)(size_t)wb2 = s1.y;
      *(LAS unsigned*)(size_t)(wb2 + 8) = s1.w;
      const unsigned e = bad(bad(bad(bad(bad(bad(bad(0u, c0), c1), c2), c3) & (mlp ? ~0u : 0u), s1), s2), s3);
      return __builtin_amdgcn_ballot_w64(e != 0) == 0;
    };
    const unsigned me = PW + w * 4;
    const unsigned co = ntile * 65536 + kg * 128;
    zero2();
    setup2(8);
    auto landed = [&]() { if (mlp) wait_vm<20>(); else wait_vm<12>(); };
    SCHED();
    part0(0, 0);
    if (TOUCH == 4) touch();
    part1(1, SLOT); part0(1, SLOT);
    STAMP(1);
    mktag();
    if (mlp) wait_vm<20 + (TOUCH == 4)>(); else wait_vm<12 + (TOUCH == 4)>();
    STAMP(19);
    bar();
    STAMP(2);
    part1(2, 2 * SLOT); part0(2, 2 * SLOT); landed(); if (TOUCH == 1) touch();
    bar();
    if (d) dma4w(lane * 4, p.b1 + (mlp ? j : 13) * 256, LB + L_B1);
    part1(3, 0); part0(3, 0); landed();
    dma4w((lane < 28 ? lane : 0) * 1024, p.w2 + (2 * idx + d) * 28672 + ((3u - ((unsigned)(size_t)p.w2 >> 7)) & 7u) * 128, LB + L_DUMMY + d * 256);
    bar();
    STAMPC(21);
    dma_x4(vx, p.x + (128 * (mtile >> 1) + 8 * (mtile & 1) + 4 * d) * 1024 + ntile * 128 - 4096, home + 12288);
    part1(4, SLOT); part0(4, SLOT); landed();
    bar();
    own(0); part1(5, 2 * SLOT); part0(5, 2 * SLOT); landed();
    bar();
    gather(d); part1(6, 0); take(0); part0(6, 0); landed();
    bar();
    part1(7, SLOT);
    const bool ok = check([&]() { partx(7, SLOT); });
    SCHED();
    partb(7, SLOT);
    if (__builtin_expect(!ok, 0)) {
#pragma unroll 1
      for (int spin = 0; spin < SPIN_CAP; ++spin) {
        COUNT(20);
        __builtin_amdgcn_s_sleep(10);
        gather(true);
        wait_vm<0>();
        if (check([]() {})) break;
      }
    }
    landed();
    barrier();
    STAMP(3); STAMPC(22);
    page(0, 0, 2, 0); page(0, 1, 2, 1); page(0, 2, 2, 2); page(0, 3, 2, 3); page(0, 4, 2, 4); own(1);
    wait_vm<12>();
    bar();
    STAMP(4);
    own(2); page(2, 0, 0, 0); page(2, 1, 0, 1); page(2, 2, 0, 2); page(2, 3, 0, 3); page(2, 4, 0, 4);
    wait_vm<20>();
    STAMP(5);
    bar();
    low = true;
    STAMP(6);
    page(1, 0, 1, 0); page(1, 1, 1, 1); take(1); page(1, 2, 1, 2); take(2); page(1, 3, 1, 3); page(1, 4, 1, 4);
    own(3); own(4); wait_lgkm0(); own(5); own(6);
    wait_vm<16>(); prog_set(me, 1);
    page(0, 5, 2, 0); page(0, 6, 2, 1); page(0, 7, 2, 2);
    wait_vm<16>(); take(3); take(4); take(5); wait_lgkm0();
    own(7);
    c8(p.cre + co, home);
    wait_vm<8>(); prog_set(me, 2);
    take(6); take(7); wait_lgkm0();
    c8(p.cim + co, home + 8192);
    STAMP(7);
    prog_wait(PW + (2 + d) * 4, 1);
    snap(false);
    page(1, 5, 1, 4);
    { const unsigned o = ntile * 65536 + ((2 + d + KROT * kgo) & 7) * 128; c8(p.cre + o, HB + SLOT); c8(p.cim + o, HB + SLOT + 8192); }
    __builtin_amdgcn_s_setprio(0);
    STAMP(8);
  };
  auto roleL = [&]() __attribute__((always_inline)) {
    if (TOUCH == 0) touch();
    if (TLBW && d) { dma4w(0, p.cre + ntile * 65536, LB + L_DUMMY + 256); dma4w(0, p.cim + ntile * 65536, LB + L_DUMMY + 256); dma4w((lane & 1) * 0x1bf000, p.w2, LB + L_DUMMY + 256); }
    f32x4 acr[8], aci[8];
#pragma unroll
    for (int m = 0; m < 8; ++m) { acr[m] = (f32x4){0, 0, 0, 0}; aci[m] = (f32x4){0, 0, 0, 0}; asm volatile("" : "+v"(acr[m]), "+v"(aci[m])); }
    unsigned ax[2], ab[2];
    ax[0] = LB + i16 * 128 + kx; ax[1] = ax[0] ^ 64;
    ab[0] = LB + SL_B + (16 * d + i16) * 128 + kx; ab[1] = ab[0] ^ 64;
    unsigned sa = LB + L_LS + g * 288, csa = LB + L_BS + (16 * d + i16) * 16 + 4;
    asm volatile("" : "+v"(sa), "+v"(csa), "+v"(ax[0]), "+v"(ax[1]), "+v"(ab[0]), "+v"(ab[1]));
    s16x8 xf[2][8], bf[2][2];
    auto rd = [&](int set, int i) {
      if (i < 2) bf[set][i] = lds128(ab[set] + i * 4096);
      else xf[set][i - 2] = lds128(ax[set] + (i - 2) * 2048);
    };
    auto mm = [&](int set, int i) {
      if (i & 1) aci[i >> 1] = mfma(xf[set][i >> 1], bf[set][1], aci[i >> 1]);
      else acr[i >> 1] = mfma(xf[set][i >> 1], bf[set][0], acr[i >> 1]);
    };
    asm volatile("" : "+v"(v_lre), "+v"(v_lim), "+v"(v_ldt));
    const float lr = bflo(v_lre), li = bflo(v_lim);
    const float dt = bflo(pk2(expf(bflo(v_ldt)), 0.f));
    const float ang = smul(dt, li);
    const float rv = smul(ang, 0.15915494f);
    const float rev = (rv - __builtin_rintf(rv)) + sfma(ang, 6.4206382e-9f, sfms(ang, 0.15915494f, rv));
    float sn = __builtin_amdgcn_sinf(rev), cn = __builtin_amdgcn_cosf(rev);
    asm volatile("s_nop 0" : "+v"(sn), "+v"(cn));
    const float ex = expf(smul(dt, lr));
    f32x2 a1 = {smul(ex, cn), smul(ex, sn)};
    const float nr = a1[0] - 1.f, ni = a1[1], iden = 2.f / sfma(lr, lr, smul(li, li));
    f32x2 kk = {smul(sfma(nr, lr, smul(ni, li)), iden), smul(sfms(ni, lr, smul(nr, li)), iden)};
    f32x2 t = cmul(a1, a1); t = cmul(t, t); t = cmul(t, t);
    f32x2 a16 = cmul(t, t);
    f32x2 a32 = cmul(a16, a16);
    const f32x2 a64 = cmul(a32, a32), a128 = cmul(a64, a64), a256 = cmul(a128, a128);
    f32x2 a512 = cmul(a256, a256);
    const f32x2 one = {1.f, 0.f};
    f32x2 aseg = cmul(kk, cmul(csel(g & 1, a32, one), csel(g & 2, a64, one)));
    const int e4 = (r - 1 - g) & 3;
    f32x2 acar = cmul(csel(e4 & 1, a128, one), csel(e4 & 2, a256, one));
    const f32x2 ar = bco(a1[0]), ai = bco(a1[1]);
    asm volatile("" :: "v"(ar), "v"(ai));
    asm volatile("" : "+v"(kk), "+v"(a16), "+v"(a32), "+v"(a512), "+v"(aseg), "+v"(acar));
    mktag();
    __builtin_amdgcn_s_setprio(1);
    STAMP(1);
    bar();
    STAMP(2); TRACE3(1);
#pragma unroll
    for (int i = 0; i < 10; ++i) rd(0, i);
    wait_lgkm0();
    auto stage1 = [&](int dl) {
      SCHED();
#pragma unroll
      for (int i = 0; i < 16; ++i) {
        mm(0, i); SCHED();
        if (i < 10) rd(1, i);
        if (i == 11) { adv(ax[0], dl); adv(ab[0], dl); }
        SCHED();
      }
    };
    stage1(SLOT);
    barrier();
    TRACE3(2);
    int ph = 0;
#pragma unroll 1
    for (int c = 2; c <= 8; ++c) {
      const int dl = ph == 2 ? -2 * (int)SLOT : (int)SLOT;
      ph = ph == 2 ? 0 : ph + 1;
      SCHED();
#pragma unroll
      for (int i = 0; i < 16; ++i) {
        mm(1, i); SCHED();
        if (i < 10) rd(0, i);
        if (i == 11) { adv(ax[1], dl); adv(ab[1], dl); }
        SCHED();
      }
      wait_lgkm0();
      stage1(ph == 2 ? -2 * (int)SLOT : (int)SLOT);
      TRACE(2 * c);
      barrier();
      TRACE(2 * c + 1); TRACE3(c + 1);
    }
    __builtin_amdgcn_s_setprio(LPRIO);
    STAMP(4);
    f32x2 rs[16], mrs[16];
    float csr, csi;
#pragma unroll
    for (int i = 0; i < 16; ++i) {
      mm(1, i); SCHED();
      if (i == 0) { csr = ldsf1(csa); csi = ldsf1(csa + 512); }
      const f32x4 t = ldsf4(sa + i * 16);
      rs[i] = lo2(t); mrs[i] = hi2(t);
      if ((i & 3) == 3) TRACE2(20 + (i >> 2));
      SCHED();
    }
    const f32x2 ncr = bco(-csr), nci = bco(-csi);
    STAMP(5);
    f32x2 ur[16], ui[16], Er, Ei;
    SCHED();
#pragma unroll
    for (int s = 0; s < 16; ++s) {
      ur[s] = mrs[s] * ncr; SCHED();
      ui[s] = mrs[s] * nci; SCHED();
      ur[s] = pfma((s & 1) ? hi2(acr[s >> 1]) : lo2(acr[s >> 1]), rs[s], ur[s]); SCHED();
      ui[s] = pfma((s & 1) ? hi2(aci[s >> 1]) : lo2(aci[s >> 1]), rs[s], ui[s]); SCHED();
      if (s == 0) { Er = ur[0]; Ei = ui[0]; }
      else {
        const f32x2 t0 = pfma(Ei, -ai, ur[s]); SCHED();
        const f32x2 t1 = pfma(Er, ai, ui[s]); SCHED();
        Er = pfma(Er, ar, t0); SCHED();
        Ei = pfma(Ei, ar, t1); SCHED();
      }
    }
    TRACE2(1);
    const f32x2 E0 = {Er[0], Ei[0]};
    const f32x2 E = cfma(a16, E0, (f32x2){Er[1], Ei[1]});
    f32x2 P = {0.f, 0.f}, Tt = {0.f, 0.f};
    float er4[4], ei4[4];
    rows4(E[0], er4); rows4(E[1], ei4);
#pragma unroll
    for (int s = 0; s < 4; ++s) {
      const f32x2 e = {er4[s], ei4[s]};
      P = csel(s == g, Tt, P);
      Tt = s ? cfma(a32, Tt, e) : e;
    }
    const unsigned totb = WS_TOT + (unsigned)(j * 2 + d) * 4096;
    if (g == 0) __builtin_amdgcn_raw_buffer_store_b128((u32x4){tagA, f2u(Tt[0]), tagB, f2u(Tt[1])}, wsr, totb + r * 256 + i16 * 16, 0, 16);
    STAMP(6);
    home = HB;
    TRACE2(2);
    prog_wait(PW + (6 + d) * 4, 1);
#pragma unroll
    for (int i = 0; i < 5; ++i) absorb(i, home + i * 4096);
    unsigned vc[8], v2[4], vt[4];
    mkvc(vc); mkv2(v2);
#pragma unroll
    for (int i = 0; i < 4; ++i) { vt[i] = (4 * i + g < r ? i * 1024 + g * 256 : 0) + i16 * 16; asm volatile("" : "+v"(vt[i]), "+v"(v2[i]), "+v"(vc[i]), "+v"(vc[i + 4])); }
    wait_lgkm0();
    loadC(vc, kg, home);
    TRACE2(3);
    {
      const i32x4 tr = {wss[0], wss[1], (int)(totb + r * 256), wss[3]};
#pragma unroll 1
      for (int spin = 0; spin < 64; ++spin) {
        int n, t[15];
        asm volatile("s_buffer_load_dword %1, %16, %17 offset:0 glc\n\ts_buffer_load_dword %2, %16, %17 offset:256 glc\n\ts_buffer_load_dword %3, %16, %17 offset:512 glc\n\ts_buffer_load_dword %4, %16, %17 offset:768 glc\n\ts_buffer_load_dword %5, %16, %17 offset:1024 glc\n\ts_buffer_load_dword %6, %16, %17 offset:1280 glc\n\ts_buffer_load_dword %7, %16, %17 offset:1536 glc\n\ts_buffer_load_dword %8, %16, %17 offset:1792 glc\n\ts_buffer_load_dword %9, %16, %17 offset:2048 glc\n\ts_buffer_load_dword %10, %16, %17 offset:2304 glc\n\ts_buffer_load_dword %11, %16, %17 offset:2560 glc\n\ts_buffer_load_dword %12, %16, %17 offset:2816 glc\n\ts_buffer_load_dword %13, %16, %17 offset:3072 glc\n\ts_buffer_load_dword %14, %16, %17 offset:3328 glc\n\ts_buffer_load_dword %15, %16, %17 offset:3584 glc\n\t"
                     "s_mov_b32 %0, 0\n\ts_waitcnt lgkmcnt(0)\n\t"
                     "s_cmp_eq_u32 %1, %18\n\ts_addc_u32 %0, %0, 0\n\ts_cmp_eq_u32 %2, %18\n\ts_addc_u32 %0, %0, 0\n\ts_cmp_eq_u32 %3, %18\n\ts_addc_u32 %0, %0, 0\n\ts_cmp_eq_u32 %4, %18\n\ts_addc_u32 %0, %0, 0\n\ts_cmp_eq_u32 %5, %18\n\ts_addc_u32 %0, %0, 0\n\ts_cmp_eq_u32 %6, %18\n\ts_addc_u32 %0, %0, 0\n\ts_cmp_eq_u32 %7, %18\n\ts_addc_u32 %0, %0, 0\n\ts_cmp_eq_u32 %8, %18\n\ts_addc_u32 %0, %0, 0\n\ts_cmp_eq_u32 %9, %18\n\ts_addc_u32 %0, %0, 0\n\ts_cmp_eq_u32 %10, %18\n\ts_addc_u32 %0, %0, 0\n\ts_cmp_eq_u32 %11, %18\n\ts_addc_u32 %0, %0, 0\n\ts_cmp_eq_u32 %12, %18\n\ts_addc_u32 %0, %0, 0\n\ts_cmp_eq_u32 %13, %18\n\ts_addc_u32 %0, %0, 0\n\ts_cmp_eq_u32 %14, %18\n\ts_addc_u32 %0, %0, 0\n\ts_cmp_eq_u32 %15, %18\n\ts_addc_u32 %0, %0, 0"
                     : "=&s"(n), "=&s"(t[0]), "=&s"(t[1]), "=&s"(t[2]), "=&s"(t[3]), "=&s"(t[4]), "=&s"(t[5]), "=&s"(t[6]), "=&s"(t[7]), "=&s"(t[8]), "=&s"(t[9]), "=&s"(t[10]), "=&s"(t[11]), "=&s"(t[12]), "=&s"(t[13]), "=&s"(t[14]) : "s"(tr), "s"(totb), "s"(tagA) : "scc", "memory");
        if (n >= r) break;
        COUNT(20);
      }
    }
    STAMP(7);
    f32x2 car = {0.f, 0.f};
    auto fetch = [&]() {
#pragma unroll
      for (int i = 0; i < 4; ++i)
        if (4 * i < r) dma1_dev(vt[i], p.ws + totb, home + 16384 + i * 1024);
    };
    fetch();
    {
      const f32x2 kr = bco(kk[0]), nki = bco(-kk[1]);
#pragma unroll
      for (int s = 0; s < 16; ++s) {
        const f32x2 t0 = ur[s] * kr; SCHED();
        const f32x2 t1 = ur[s] * nki; SCHED();
        ur[s] = pfma(ui[s], nki, t0); SCHED();
        ui[s] = pfma(ui[s], -kr, t1); SCHED();
      }
    }
#pragma unroll
    for (int s = 0; s < 16; s += 4) asm volatile("" : "+v"(ur[s]), "+v"(ui[s]), "+v"(ur[s + 1]), "+v"(ui[s + 1]), "+v"(ur[s + 2]), "+v"(ui[s + 2]), "+v"(ur[s + 3]), "+v"(ui[s + 3]));
    f32x2 kP = cmul(kk, P), kE = cmul(kk, E0);
    asm volatile("" : "+v"(kP), "+v"(kE));
    setup2(6);
    wait_vm<0>();
    if (r > 0) {
      u32x4 tv[4];
#pragma unroll 1
      for (int spin = 0; spin < SPIN_CAP; ++spin) {
        bool ok = true;
#pragma unroll
        for (int i = 0; i < 4; ++i) {
          tv[i] = *(volatile LAS u32x4*)(size_t)(home + 16384 + i * 1024 + lane * 16);
          ok &= (4 * i + g >= r) | ((tv[i].x == tagA) & (tv[i].z == tagB));
        }
        if (__all(ok)) break;
        COUNT(20);
        __builtin_amdgcn_s_sleep(8);
        fetch();
        wait_vm<0>();
      }
#pragma unroll
      for (int i = 0; i < 4; ++i) car = csel(4 * i + g < r, cfma(a512, car, (f32x2){u2f(tv[i].y), u2f(tv[i].w)}), car);
      car = cmul(car, acar);
      float cr4[4], ci4[4];
      rows4(car[0], cr4); rows4(car[1], ci4);
      car = (f32x2){(cr4[0] + cr4[1]) + (cr4[2] + cr4[3]), (ci4[0] + ci4[1]) + (ci4[2] + ci4[3])};
    }
    dma_x4(v2, w2b + kstep_of(slot, 5) * 32768, home + 16384);
    STAMP(8);
    {
      const f32x2 h0 = cfma(aseg, car, kP), h1 = cfma(a16, h0, kE);
      f32x2 hr = {h0[0], h1[0]}, hi = {-h0[1], -h1[1]};
      const unsigned on = (qq == g) ? ((pp & 1) ? 0x3f800000u : 0x3f80u) : 0u;
      const u32x4 z0 = {(pp & 2) ? 0u : on, (pp & 2) ? on : 0u, 0u, 0u}, z1 = {0u, 0u, z0.x, z0.y};
      const s16x8 sel0 = __builtin_bit_cast(s16x8, z0), sel1 = __builtin_bit_cast(s16x8, z1);
      const unsigned ho = WS_H + (unsigned)(2 * r) * H_MT + (unsigned)(56 + 2 * j + d) * 4096 + lane * 16;
      u32x4 fr[4], fi[4];
      f32x4 tr[2], ti[2];
      const f32x4 zz = {0, 0, 0, 0};
      auto put = [&](int mp, int k) {
        const u32x4 o = {pk2(tr[k][0], tr[k][1]), pk2(tr[k][2], tr[k][3]), pk2(ti[k][0], ti[k][1]), pk2(ti[k][2], ti[k][3])};
        __builtin_amdgcn_raw_buffer_store_b128(o, wsr, ho + (mp >> 1) * H_MT + (2 * (mp & 1) + k) * 1024, 0, 16);
        SCHED();
      };
      SCHED();
#pragma unroll
      for (int mp = 0; mp < 5; ++mp) {
        if (mp > 0) {
#pragma unroll
          for (int k = 0; k < 2; ++k) {
            tr[k] = mfma(__builtin_bit_cast(s16x8, fr[mp - 1]), k ? sel1 : sel0, zz);
            ti[k] = mfma(__builtin_bit_cast(s16x8, fi[mp - 1]), k ? sel1 : sel0, zz);
          }
          SCHED();
        }
        if (mp < 4) {
#pragma unroll
          for (int e = 0; e < 4; ++e) {
            const int s = 4 * mp + e;
            const f32x2 t1 = pfma(hr, -ai, ui[s]); SCHED();
            const f32x2 t0 = pfma(hi, ai, ur[s]); SCHED();
            hi = pfma(hi, ar, t1); SCHED();
            hr = pfma(hr, ar, t0); SCHED();
            fi[mp][e] = pk2(hi[0], hi[1]); SCHED();
            fr[mp][e] = pk2(hr[0], hr[1]); SCHED();
          }
        }
        TRACE2(4 + 2 * mp);
        if (mp == 0) snap(false);
        if (mp > 0) { put(mp - 1, 0); put(mp - 1, 1); }
        TRACE2(5 + 2 * mp);
      }
      STAMP(19);
      if (lane < 2) pubo = WS_FLAG + (2 * r + lane) * 2048 + 1024 + (2 * j + d) * 8;
    }
    __builtin_amdgcn_s_setprio(0);
    STAMP(18);
  };
  auto roleB = [&]() __attribute__((always_inline)) {
    home = HB + SLOT;
    mktag();
    {
      float s0 = 0.f, s1 = 0.f;
#pragma unroll
      for (int e = 0; e < 8; ++e) { s0 = sadd(s0, bflo(prb[e >> 2][e & 3])); s1 = sadd(s1, bfhi(prb[e >> 2][e & 3])); }
      *(LAS float*)(size_t)(LB + L_LS + 512 + (4 * d + g) * 4) = row16_sum(s0 + s1);
    }
    if (mlp) {
      f32x4 acc[6][4];
#pragma unroll
      for (int s = 0; s < 6; ++s) {
#pragma unroll
        for (int m = 0; m < 4; ++m) acc[s][m] = (f32x4){0, 0, 0, 0};
        asm volatile("" : "+v"(acc[s][0]), "+v"(acc[s][1]), "+v"(acc[s][2]), "+v"(acc[s][3]));
      }
      unsigned ax[2], aw[6];
      ax[0] = LB + (64 * d + i16) * 128 + kx; ax[1] = ax[0] ^ 64;
#pragma unroll
      for (int s = 0; s < 6; ++s) aw[s] = awb + ((s ^ awx) * 32);
      unsigned sta = LB + L_LS + qq * 288 + (8 * d + ((i16 >> 1) & 1)) * 16 + (i16 & 1) * 4, csa = LB + L_CS + g * 16;
      asm volatile("" : "+v"(ax[0]), "+v"(ax[1]), "+v"(aw[0]), "+v"(aw[1]), "+v"(aw[2]), "+v"(aw[3]), "+v"(aw[4]), "+v"(aw[5]), "+v"(sta), "+v"(csa));
      s16x8 xf[2][4];
      s16x4 wf[2][12];
      auto rd = [&](int set, int i) {
        if (i < 4) xf[set][i] = lds128(ax[set] + i * 2048);
        else wf[set][i - 4] = ldstr4(aw[(i - 4) >> 1] + set * 8192 + (i & 1) * 1024);
      };
      auto mm = [&](int set, int i) { const int s = i >> 2, m = i & 3; acc[s][m] = mfma(cat(wf[set][2 * s], wf[set][2 * s + 1]), xf[set][m], acc[s][m]); };
      {
        float t[8];
#pragma unroll
        for (int e = 0; e < 8; ++e)
          t[e] = (e & 1) ? sadd(sadd(bfhi(pre[0][e >> 1]), bfhi(pre[1][e >> 1])), sadd(bfhi(pre[2][e >> 1]), bfhi(pre[3][e >> 1])))
                         : sadd(sadd(bflo(pre[0][e >> 1]), bflo(pre[1][e >> 1])), sadd(bflo(pre[2][e >> 1]), bflo(pre[3][e >> 1])));
#pragma unroll
        for (int e = 0; e < 4; ++e) { const u32x2 h = swap32(t[e], t[e + 4]); t[e] = sadd(u2f(h.x), u2f(h.y)); }
#pragma unroll
        for (int e = 0; e < 2; ++e) { const u32x2 h = swap16(t[e], t[e + 2]); t[e] = dpp_add<0x128>(sadd(u2f(h.x), u2f(h.y))); }
        *(LAS f32x2*)(size_t)(LB + L_LS + d * 256 + (4 * (lane & 7) + g) * 8) = (f32x2){t[0], t[1]};
      }
      __builtin_amdgcn_s_setprio(1);
      STAMP(1);
      barrier();
      STAMP(2); STAMPC(21);
#pragma unroll
      for (int i = 0; i < 16; ++i) rd(0, i);
      wait_lgkm0();
      auto stage1 = [&](int c, int dl) {
        SCHED();
#pragma unroll
        for (int i = 0; i < 24; ++i) {
          mm(0, i); SCHED();
          if (i < 16) rd(1, i);
          else if (i < 22) adv(aw[i - 16], dl);
          else if (i == 22) adv(ax[0], dl);
          SCHED();
        }
        TRACE(2 * c);
        barrier();
        TRACE(2 * c + 1);
        SCHED();
      };
      int ph = 0;
#pragma unroll 1
      for (int c = 1; c < 8; ++c) {
        const int dl = ph == 2 ? -2 * (int)SLOT : (int)SLOT;
        ph = ph == 2 ? 0 : ph + 1;
        stage1(c, dl);
#pragma unroll
        for (int i = 0; i < 24; ++i) {
          mm(1, i); SCHED();
          if (i < 16) rd(0, i);
          else if (i == 16) adv(ax[1], dl);
          else if (i == 23) wait_lgkm0();
          SCHED();
        }
      }
      stage1(8, 0);
      unsigned b1a = LB + L_B1 + g * 8;
      asm volatile("" : "+v"(b1a));
      STAMP(4); STAMPC(22);
      f32x2 rs[4], nm[4];
      f32x4 cs[6];
      u32x2 b1v[6];
      f32x2 st[4];
      auto colrd = [&](int s) { cs[s] = ldsf4(csa + 64 * s); b1v[s] = *(LAS u32x2*)(size_t)(b1a + 32 * s); };
#pragma unroll
      for (int i = 0; i < 4; ++i) st[i] = (f32x2){ldsf1(sta + i * 32), ldsf1(sta + i * 32 + 8)};
      colrd(0); colrd(1);
      SCHED();
#pragma unroll
      for (int i = 0; i < BSPLIT; ++i) mm(1, i);
      SCHED();
#pragma unroll
      for (int i = 0; i < 4; ++i) { rs[i] = bco(st[i][0]); nm[i] = bco(-st[i][1]); }
      STAMP(5);
      const unsigned fo = WS_FLAG + (2 * r + d) * 2048;
      const unsigned ho = WS_H + (unsigned)(2 * r + d) * H_MT + (unsigned)(4 * j) * 4096 + lane * 16;
#pragma unroll
      for (int pr = 0; pr < 3; ++pr) {
        Col col;
        mkcol(col, cs[2 * pr], cs[2 * pr + 1], b1v[2 * pr], b1v[2 * pr + 1]);
        if (pr < 2) { colrd(2 * pr + 2); colrd(2 * pr + 3); }
        SCHED();
        if (pr == 1) {
#pragma unroll
          for (int i = BSPLIT; i < 24; ++i) mm(1, i);
          SCHED();
          STAMP(7);
          prog_wait(PW + (6 + d) * 4, 1);
#pragma unroll
          for (int i = 0; i < 5; ++i) absorb(i, home + i * 4096);
          wait_lgkm0();
          prog_set(PW + w * 4, 1);
          if (BFLAG0 < 0) {
            wait_vm<0>();
            if (lane == 0) __builtin_amdgcn_raw_buffer_store_b64((u32x2){tagA, tagB}, wsr, fo + flag_of(j, 0), 0, 16);
          }
          STAMP(8);
        }
#pragma unroll
        for (int m = 0; m < 4; ++m) {
          __builtin_amdgcn_raw_buffer_store_b128(gelu8(acc[2 * pr][m], acc[2 * pr + 1][m], col, rs[m], nm[m]), wsr, ho + pr * 4096 + m * 1024, 0, 16);
          TRACE2(1 + 4 * pr + m);
          if (pr > 0 && m == (pr == 1 ? BFLAG0 : BFLAG)) {
            if (pr == 1) wait_vm<(BFLAG0 < 0 ? 0 : BFLAG0 + 1)>(); else wait_vm<BFLAG + 1>();
            if (lane == 0) __builtin_amdgcn_raw_buffer_store_b64((u32x2){tagA, tagB}, wsr, fo + flag_of(j, pr - 1), 0, 16);
          }
          if (pr == 2 && m == 0) snap(false);
          SCHED();
        }
      }
      STAMP(6);
      setup2(6);
      if (lane == 0) pubo = fo + flag_of(j, 2);
      __builtin_amdgcn_s_setprio(0);
    } else {
#pragma unroll 1
      for (int c = 0; c < 9; ++c) barrier();
      prog_wait(PW + (6 + d) * 4, 1);
#pragma unroll
      for (int i = 0; i < 5; ++i) absorb(i, home + i * 4096);
      wait_lgkm0();
      prog_set(PW + w * 4, 1);
      setup2(6);
      snap(false);
    }
  };
  auto roleA = [&]() __attribute__((always_inline)) {
    home = HB + 2 * SLOT;
    const int srow = 128 * r + 8 * j + 4 * d + g;
    u32x2 dv[4], bv[4];
#pragma unroll
    for (int n = 0; n < 4; ++n) { dv[n] = *(const GAS u32x2*)(p.dsk + ntile * 128 + n * 32 + g * 8); bv[n] = *(const GAS u32x2*)(p.b2 + ntile * 128 + n * 32 + g * 8); }
    if (tid < 16) *(LAS unsigned*)(size_t)(PW + tid * 4) = 0u;
    f32x4 acc[2][4];
#pragma unroll
    for (int m = 0; m < 4; ++m) { acc[0][m] = (f32x4){0, 0, 0, 0}; acc[1][m] = (f32x4){0, 0, 0, 0}; asm volatile("" : "+v"(acc[0][m]), "+v"(acc[1][m])); }
    unsigned ax0 = LB + (64 * d + i16) * 128 + kx, ax1 = ax0 ^ 64, aw0 = awb + ((6 ^ awx) * 32), aw1 = awb + ((7 ^ awx) * 32);
    unsigned sta = LB + L_LS + qq * 288 + (8 * d + ((i16 >> 1) & 1)) * 16 + (i16 & 1) * 4, csa = LB + L_CS + 384 + g * 16;
    asm volatile("" : "+v"(ax0), "+v"(ax1), "+v"(aw0), "+v"(aw1), "+v"(sta), "+v"(csa));
    int ph = 0;
    auto chunk = [&](auto side) {
      s16x8 xf[2][4], wf[2][2];
      if (ADELAY) __builtin_amdgcn_s_sleep(ADELAY);
#pragma unroll
      for (int k = 0; k < 2; ++k) {
        wf[k][0] = ldstr(aw0 + k * 8192, aw0 + k * 8192 + 1024);
        wf[k][1] = ldstr(aw1 + k * 8192, aw1 + k * 8192 + 1024);
#pragma unroll
        for (int m = 0; m < 4; ++m) xf[k][m] = lds128((k ? ax1 : ax0) + m * 2048);
      }
      SCHED();
      const int dl = ph == 2 ? -2 * (int)SLOT : (int)SLOT;
      ph = ph == 2 ? 0 : ph + 1;
      adv(ax0, dl); adv(ax1, dl); adv(aw0, dl); adv(aw1, dl);
      side(8);
      SCHED();
#pragma unroll
      for (int k = 0; k < 2; ++k)
#pragma unroll
        for (int m = 0; m < 4; ++m) {
          acc[0][m] = mfma(wf[k][0], xf[k][m], acc[0][m]); acc[1][m] = mfma(wf[k][1], xf[k][m], acc[1][m]);
          SCHED();
          side(4 * k + m);
          SCHED();
        }
    };
    auto none = [](int) {};
    mktag();
    asm volatile("" : "+v"(pre[3]));
    STAMP(19);
    {
      const float mean = sum32(pre) * (1.f / 512.f);
      float v0 = 0.f, v1 = 0.f;
#pragma unroll
      for (int i = 0; i < 4; ++i)
#pragma unroll
        for (int e = 0; e < 4; ++e) {
          const float d0 = bflo(pre[i][e]) - mean, d1 = bfhi(pre[i][e]) - mean;
          v0 = sfma(d0, d0, v0); v1 = sfma(d1, d1, v1);
        }
      const float rsd = __builtin_amdgcn_rsqf(row16_sum(v0 + v1) * (1.f / 512.f) + 1e-5f);
      if (i16 == 0) __builtin_amdgcn_raw_buffer_store_b128((u32x4){tagA, f2u(rsd), tagB, f2u(mean * rsd)}, wsr, WS_STAT + srow * 16, 0, 16);
    }
    __builtin_amdgcn_s_setprio(APRIO);
    STAMP(1);
    barrier();
    STAMP(2);
    chunk(none);
    if (d) {
      const f32x2 c0 = ldsf2(LB + L_LS + 512 + (lane & 3) * 8);
      if (lane < 4) __builtin_amdgcn_raw_buffer_store_b128((u32x4){tagA, f2u(sadd(c0[0], c0[1])), tagB, 0u}, wsr, WS_BS + j * 1024 + (4 * r + lane) * 16, 0, 16);
    } else if (mlp) {
      const f32x2 c0 = ldsf2(LB + L_LS + (lane & 31) * 8), c1 = ldsf2(LB + L_LS + 256 + (lane & 31) * 8);
      if (lane < 32) __builtin_amdgcn_raw_buffer_store_b128((u32x4){tagA, f2u(sadd(c0[0], c1[0])), tagB, f2u(sadd(c0[1], c1[1]))}, wsr, WS_CS + (j * 16 + r) * 512 + lane * 16, 0, 16);
    }
    barrier();
#pragma unroll 1
    for (int c = 1; c < 3; ++c) { chunk(none); barrier(); }
    chunk(none);
    asm volatile("" : "+v"(dv[0]), "+v"(dv[1]), "+v"(dv[2]), "+v"(dv[3]), "+v"(bv[0]), "+v"(bv[1]), "+v"(bv[2]), "+v"(bv[3]));
    u32x2 xv[2][4];
#pragma unroll
    for (int m = 0; m < 2; ++m)
#pragma unroll
      for (int n = 0; n < 4; ++n) xv[m][n] = *(LAS u32x2*)(size_t)(HB + 3 * SLOT + 12288 + (16 * m + i16) * 128 + (g & 1) * 8 + (((2 * n + (g >> 1)) ^ (i16 >> 1)) * 16));
    barrier();
#pragma unroll 1
    for (int c = 4; c < 6; ++c) { chunk(none); barrier(); }
    f32x2 st[4];
#pragma unroll
    for (int m = 0; m < 4; ++m) st[m] = (f32x2){ldsf1(sta + m * 32), ldsf1(sta + m * 32 + 8)};
    unsigned ra[4], vc[8], ho = mlp ? WS_H + (unsigned)(2 * r + d) * H_MT + (unsigned)(4 * j + 3) * 4096 + lane * 16 : 0xffff0000u;
    f32x2 rs[4], nm[4];
    chunk([&](int k) {
      if (k < 4) { rs[k] = bco(st[k][0]); nm[k] = bco(-st[k][1]); asm volatile("" : "+v"(rs[k]), "+v"(nm[k])); }
      if (k < 8) return;
#pragma unroll
      for (int n = 0; n < 4; ++n) ra[n] = home + r2l + ((n ^ r2x) * 32);
      asm volatile("" : "+v"(ra[0]), "+v"(ra[1]), "+v"(ra[2]), "+v"(ra[3]), "+v"(ho));
    });
    TRACE(14); barrier(); TRACE(15);
    STAMP(4);
    const f32x4 cs0 = ldsf4(csa), cs1 = ldsf4(csa + 64);
    const u32x2 b10 = *(LAS u32x2*)(size_t)(LB + L_B1 + 192 + g * 8), b11 = *(LAS u32x2*)(size_t)(LB + L_B1 + 224 + g * 8);
    Col col;
    auto take = [&](int i, int pg) {
#pragma unroll
      for (int n = 0; n < 4; ++n) we[i][n] = ldstr(ra[n] + pg * 4096, ra[n] + pg * 4096 + 2048);
    };
    chunk([&](int k) {
      if (k == 2) { mkcol(col, cs0, cs1, b10, b11); asm volatile("" : "+v"(col.bias[0]), "+v"(col.bias[1]), "+v"(col.bias[2]), "+v"(col.bias[3])); }
      if (k >= 3 && k < 6) take(k - 3, k - 3);
    });
    barrier();
    __builtin_amdgcn_s_setprio(2);
    STAMP(5);
#pragma unroll
    for (int m = 0; m < 4; ++m) {
      TRACE2(1 + 2 * m);
      const u32x4 o = gelu8(acc[0][m], acc[1][m], col, rs[m], nm[m]);
      TRACE2(2 + 2 * m);
      __builtin_amdgcn_raw_buffer_store_b128(o, wsr, ho + m * 1024, 0, 16);
      if (m == 0) { take(3, 3); take(4, 4); }
      SCHED();
    }
    STAMP(6);
#pragma unroll
    for (int m = 0; m < 2; ++m) {
      const u32x4 s2 = *(LAS u32x4*)(size_t)(LB + L_ST2 + (tok + 8 * (mtile & 1) + 4 * d + 2 * m) * 16);
      const f32x2 r2 = bco(u2f(s2.y)), n2 = bco(-u2f(s2.w));
#pragma unroll
      for (int n = 0; n < 4; ++n) {
        f32x2 o[2];
#pragma unroll
        for (int h = 0; h < 2; ++h) {
          const f32x2 xx = {bflo(xv[m][n][h]), bfhi(xv[m][n][h])};
          o[h] = pfma(pfma(xx, r2, n2), (f32x2){bflo(dv[n][h]), bfhi(dv[n][h])}, xx + (f32x2){bflo(bv[n][h]), bfhi(bv[n][h])});
        }
        acc2[n][m] = __builtin_shufflevector(o[0], o[1], 0, 1, 2, 3);
        acc2[n][2 + m] = (f32x4){0, 0, 0, 0};
        asm volatile("" : "+v"(acc2[n][m]), "+v"(acc2[n][2 + m]));
      }
    }
    STAMP(7);
    wait_vm<0>();
    if (mlp && lane == 0) __builtin_amdgcn_raw_buffer_store_b64((u32x2){tagA, tagB}, wsr, WS_FLAG + (2 * r + d) * 2048 + flag_of(j, 3), 0, 16);
    STAMP(18);
    mkvc(vc);
    setup2(8);
    prog_wait(PW + (6 + d) * 4, 2);
#pragma unroll
    for (int i = 5; i < 8; ++i) take(i, i - 5);
    wait_lgkm0();
    snap(false);
    loadC(vc, kg, home);
    STAMP(8);
    __builtin_amdgcn_s_setprio(0);
  };

  auto phase2 = [&](auto nm_, auto dep_) __attribute__((always_inline)) {
    STAMP(9);
    constexpr int NM = decltype(nm_)::value, N = NM + 4, DEP = decltype(dep_)::value;
    const u64 tag = ((u64)tagB << 32) | tagA;
    auto has = [&](int k) { return (k < NM ? flm[k < NM ? k : 0] : fls[k < NM ? 0 : k - NM]) == tag; };
    auto need = [&](int k, bool ok) {
      if (__builtin_expect(!ok, 0)) {
#pragma unroll 1
        for (int spin = 1; spin < SPIN_CAP; ++spin) {
          snap(k >= NM);
          if (has(k)) break;
          if (!(spin & 31)) {
            const u32x2 f = __builtin_amdgcn_raw_buffer_load_b64(wsr, 0, k < NM ? fm + 8 * k : fs + 8 * (k - NM), 16);
            if (__builtin_amdgcn_readfirstlane(f.x) == tagA && __builtin_amdgcn_readfirstlane(f.y) == tagB) break;
          }
          __builtin_amdgcn_s_sleep(PSLEEP);
        }
      }
      asm volatile("" ::: "memory");
    };
    auto publish = [&]() { __builtin_amdgcn_raw_buffer_store_b64((u32x2){tagA, tagB}, wsr, pubo, 0, 16); };
    auto loadH = [&](int i, int m) {
      if (m == 0) asm volatile("" : "+v"(hb));
      return __builtin_amdgcn_raw_buffer_load_b128(wsr, hb + (m & 1) * 1024, __builtin_amdgcn_readlane(ksl, i) ^ ((m & 2) * 1024), 16);
    };
    u32x4 hf[N][4];
    auto load4 = [&](int i) {
#pragma unroll
      for (int m = 0; m < 4; ++m) hf[i][m] = loadH(i, m);
    };
    STAMP(10);
    if (NM == 6 && __builtin_expect(!(has(0) && has(1)), 0)) { wait_vm<0>(); publish(); pubo = 0xffff0000u; }
    need(0, has(0)); load4(0);
    need(1, has(1)); load4(1);
    if (NM == 6) { wait_vm<8>(); publish(); }
    STAMP(11);
    s16x8 cfr[2][4];
    bool okn = false;
    SCHED();
#pragma unroll
    for (int i = 0; i < N; ++i) {
      const int k = i == 0 ? 2 : i + DEP;
      if (i == 1) STAMP(12);
      if (k == NM) STAMP(13);
      if (k < N) need(k, (i & 1) && k < NM ? okn : has(k));
      if (k == NM) STAMP(14);
      if (i > 0 || DEP == 2) {
        if (!(i & 1) && k + 1 < NM) {
          okn = has(k + 1);
          if (k + 2 < NM) snap(false);
        }
        if (k + 2 == NM) snap(true);
      }
      if (NM == 8 && i == 1 && w >= 6) prog_set(PW + w * 4, 3);
      if (NM == 6 && i == 3) { prog_wait(PW + (6 + d) * 4, 3); absorb(5, home + 16384); }
      if (i >= NM - 1 && i < N - 1) {
#pragma unroll
        for (int n = 0; n < 4; ++n) {
          const unsigned a = (crd ^ (32 * (i - NM + 1))) + n * 2048;
          cfr[(i + 1) & 1][n] = cat(*(LAS s16x4*)(size_t)a, *(LAS s16x4*)(size_t)(a + 8192));
        }
      }
      SCHED();
#pragma unroll
      for (int m = 0; m < 4; ++m) {
#pragma unroll
        for (int n = 0; n < 4; ++n) acc2[n][m] = mfma(i < NM ? we[i < NM ? i : 0][n] : cfr[i & 1][n], __builtin_bit_cast(s16x8, hf[i][m]), NM == 6 && i == 0 ? (f32x4){0, 0, 0, 0} : acc2[n][m]);
        SCHED();
        if (k < N) hf[k < N ? k : 0][m] = loadH(k, m);
        SCHED();
      }
      if (i == 0 && DEP == 3) {
        need(3, has(3)); load4(3);
        okn = has(4);
        snap(false);
        SCHED();
      }
    }
  };
  if (__builtin_expect(w >= 6 || w < 2, 1)) {
    if (__builtin_expect(w >= 6, 1)) roleD(); else roleA();
    phase2(std::integral_constant<int, 8>{}, std::integral_constant<int, DEPE>{});
  } else {
    if (w >= 4) roleL(); else roleB();
    phase2(std::integral_constant<int, 6>{}, std::integral_constant<int, DEPL>{});
  }
  STAMP(15);
  int tl = threadIdx.x;
  asm volatile("" : "+v"(tl));
  const int i16t = tl & 15, gt = (tl >> 4) & 3;
#pragma unroll
  for (int m = 0; m < 4; ++m)
#pragma unroll
    for (int n = 0; n < 4; ++n) *(LAS f32x4*)(size_t)(home + ((16 * m + i16t) ^ (w == 1 ? 32 : 0)) * 256 + (((4 * n + gt) ^ i16t) * 16)) = acc2[n][m];
  const int ck = tl & 15;
  const int orow = 32 * ((tl >> 6) & 3) + 16 * ((tl >> 4) & 1) + ((tl >> 5) & 1) + 2 * (tl >> 8) + 8 * (mtile & 1);
  char* op = p.out + (128 * (mtile >> 1) + orow) * 2048 + ntile * 256 + ck * 16;
  unsigned ra[4];
#pragma unroll
  for (int k = 0; k < 4; ++k) ra[k] = LB + (tl >> 4) * 256 + ((ck ^ ((tl >> 4) & 15)) * 16) + k * SLOT;
  asm volatile("" : "+v"(ra[0]), "+v"(ra[1]), "+v"(ra[2]), "+v"(ra[3]), "+v"(op));
  barrier();
  STAMP(16);
  {
    f32x4 pt[2][8];
#pragma unroll
    for (int h = 0; h < 2; ++h)
#pragma unroll
      for (int k = 0; k < 8; ++k) pt[h][k] = ldsf4(ra[k >> 1] + (k & 1) * HALF + h * 8192);
#pragma unroll
    for (int h = 0; h < 2; ++h) {
      const f32x4 s = ((pt[h][0] + pt[h][1]) + (pt[h][2] + pt[h][3])) + ((pt[h][4] + pt[h][5]) + (pt[h][6] + pt[h][7]));
      __builtin_nontemporal_store(s, (GAS f32x4*)(op + h * 8192));
    }
  }
  if (b == 255 && tl == 0) __builtin_amdgcn_raw_buffer_store_b64((u32x2){tagA, tagB}, wsr, WS_EPOCH, 0, 16);
  if (b < 16 && w == 0) { while (__builtin_amdgcn_s_memrealtime() - t_start < 2400) __builtin_amdgcn_s_sleep(2);
 asm volatile("s_waitcnt vmcnt(0)\n\tbuffer_wbl2 sc0 sc1\n\ts_waitcnt vmcnt(0)\n\tbuffer_inv sc0 sc1\n\ts_waitcnt vmcnt(0)" ::: "memory"); }
  while (__builtin_amdgcn_s_memrealtime() - t_start < 3000) __builtin_amdgcn_s_sleep(2);
}

extern "C" void kernel_launch(void* const* d_in, const int* in_sizes, int n_in, void* d_out, int out_size, void* d_ws,
                              size_t ws_size, hipStream_t stream) {
  Params p;
  p.x = (const char*)d_in[0]; p.lre = (const char*)d_in[1]; p.lim = (const char*)d_in[2];
  p.bre = (const char*)d_in[3]; p.bim = (const char*)d_in[4]; p.cre = (const char*)d_in[5]; p.cim = (const char*)d_in[6];
  p.logdt = (const char*)d_in[7]; p.dsk = (const char*)d_in[8];
  p.w1 = (const char*)d_in[10]; p.b1 = (const char*)d_in[11]; p.w2 = (const char*)d_in[12]; p.b2 = (const char*)d_in[13];
  p.out = (char*)d_out; p.ws = (char*)d_ws;
  fused_kernel<<<256, 512, 0, stream>>>(p);
}
```

```cpp
#include <hip/hip_runtime.h>
#include <stdint.h>
#include <type_traits>

typedef short s16x4 __attribute__((ext_vector_type(4)));
typedef short s16x8 __attribute__((ext_vector_type(8)));
typedef __bf16 bf16x8 __attribute__((ext_vector_type(8)));
typedef __bf16 bf16x2 __attribute__((ext_vector_type(2)));
typedef float f32x4 __attribute__((ext_vector_type(4)));
typedef float f32x2 __attribute__((ext_vector_type(2)));
typedef unsigned u32x4 __attribute__((ext_vector_type(4)));
typedef unsigned u32x2 __attribute__((ext_vector_type(2)));
typedef int i32x4 __attribute__((ext_vector_type(4)));
typedef unsigned long long u64;
typedef u64 u64x4 __attribute__((ext_vector_type(4)));
typedef u64 u64x8 __attribute__((ext_vector_type(8)));
typedef int i32x8 __attribute__((ext_vector_type(8)));
typedef int i32x16 __attribute__((ext_vector_type(16)));
__device__ i32x8 sbl8(i32x4, int, int) __asm("llvm.amdgcn.s.buffer.load.v8i32");
__device__ i32x16 sbl16(i32x4, int, int) __asm("llvm.amdgcn.s.buffer.load.v16i32");
#define LAS __attribute__((address_space(3)))
#define GAS __attribute__((address_space(1)))
#define DI __device__ __forceinline__

#define STAMPS 0
#define TRACING 0
#define GATHER_AT 4
#define ROT 1
#define TOUCH 1
#define TLBW 0
#define NOFF 0
#define PSLEEP 4
#define BFLAG 2
#define ABAR 0
#define KROT 0
#define LPRIO 3
#define CHK 0
#define BSPLIT 8
#define APRIO 0
#define ADELAY 0
#define BFLAG0 1
#define DEPE 2
#define DEPL 2
#define LB_SLEEP 8
#define CHECK_AT 5
#define CPF 1
#define HAUX 16
#define LSNAP 1
#define BSNAP 1
#define L2T 0
#define L2C 0
#define SDEP 2
#define YPRE 1
#define KEEP 2
#define LSEP 0
#define PSTEP 9
#define NOC 0
#define ESEP 0
#define HST 16
#define EPRI 1
#define EOFF 6
#define WOFF 6
#define CPW 5
#define MAP 1

struct Params {
  const char *x, *lre, *lim, *bre, *bim, *cre, *cim, *logdt, *dsk, *w1, *b1, *w2, *b2;
  char *out, *ws;
};

constexpr unsigned WS_EPOCH = 0;
constexpr unsigned WS_STAT = 4096;
constexpr unsigned WS_TOT = WS_STAT + 2048 * 16;
constexpr unsigned WS_CS = WS_TOT + 16 * 2 * 16 * 256;
constexpr unsigned WS_BS = WS_CS + 16 * 16 * 512;
constexpr unsigned WS_FLAG = WS_BS + 16 * 1024;
constexpr unsigned WS_H = WS_FLAG + 32 * 2048;
constexpr unsigned H_MT = 88 * 4096;
constexpr unsigned WS_END = WS_H + 32 * H_MT;
constexpr unsigned WS_STAMP = 32u << 20;
constexpr unsigned WS_B2B = WS_STAMP + (4u << 20);
constexpr unsigned SLOT = 40960, HALF = 20480, SL_W = 16384, SL_B = 32768;
constexpr unsigned L_ST1 = 3 * SLOT + 16384, L_ST2 = L_ST1 + 2048;
constexpr unsigned L_CS = 3 * SLOT + HALF + 16384, L_FLG = L_CS + 512, L_B1 = L_CS + 768, L_BS = L_CS + 1024, L_LS = L_CS + 2048, L_DUMMY = L_CS + 3328;
constexpr unsigned LDS_BYTES = 163840;
constexpr int SPIN_CAP = 40000;
constexpr unsigned CODE_BYTES = 43776;

DI void dma4w(unsigned voff, const char* sbase, unsigned ldsaddr) {
  asm volatile("s_mov_b32 m0, %2\n\ts_nop 0\n\tglobal_load_lds_dword %0, %1" ::"v"(voff), "s"(sbase), "s"(ldsaddr));
}
DI void dma_x4(const unsigned* v, const char* sbase, unsigned ldsaddr, bool low = false) {
  if (low) {
    asm volatile("s_mov_b32 m0, %5\n\ts_setprio 0\n\t"
               "global_load_lds_dwordx4 %0, %4\n\tglobal_load_lds_dwordx4 %1, %4 offset:1024\n\t"
               "global_load_lds_dwordx4 %2, %4 offset:2048\n\tglobal_load_lds_dwordx4 %3, %4 offset:3072\n\ts_setprio 3"
               ::"v"(v[0]), "v"(v[1]), "v"(v[2]), "v"(v[3]), "s"(sbase), "s"(ldsaddr));
    return;
  }
  asm volatile("s_mov_b32 m0, %5\n\ts_nop 0\n\t"
               "global_load_lds_dwordx4 %0, %4\n\tglobal_load_lds_dwordx4 %1, %4 offset:1024\n\t"
               "global_load_lds_dwordx4 %2, %4 offset:2048\n\tglobal_load_lds_dwordx4 %3, %4 offset:3072"
               ::"v"(v[0]), "v"(v[1]), "v"(v[2]), "v"(v[3]), "s"(sbase), "s"(ldsaddr));
}
DI void dma_x8(const unsigned* v, const char* sbase, unsigned ldsaddr) {
  asm volatile("s_mov_b32 m0, %9\n\ts_nop 0\n\t"
               "global_load_lds_dwordx4 %0, %8 offset:-4096\n\tglobal_load_lds_dwordx4 %1, %8 offset:-3072\n\t"
               "global_load_lds_dwordx4 %2, %8 offset:-2048\n\tglobal_load_lds_dwordx4 %3, %8 offset:-1024\n\t"
               "global_load_lds_dwordx4 %4, %8\n\tglobal_load_lds_dwordx4 %5, %8 offset:1024\n\t"
               "global_load_lds_dwordx4 %6, %8 offset:2048\n\tglobal_load_lds_dwordx4 %7, %8 offset:3072"
               ::"v"(v[0]), "v"(v[1]), "v"(v[2]), "v"(v[3]), "v"(v[4]), "v"(v[5]), "v"(v[6]), "v"(v[7]), "s"(sbase), "s"(ldsaddr + 4096u));
}
constexpr int PG(int i) { return i & 3; }
template <int N> DI void wait_vm() { asm volatile("s_waitcnt vmcnt(%0)" ::"i"(N) : "memory"); }
DI void wait_lgkm0() { asm volatile("" ::: "memory"); __builtin_amdgcn_s_waitcnt(0xC07F); asm volatile("" ::: "memory"); }
DI void barrier() {
  asm volatile("" ::: "memory");
  __builtin_amdgcn_s_waitcnt(0xC07F);
  __builtin_amdgcn_s_barrier();
  asm volatile("" ::: "memory");
}
#define SCHED() __builtin_amdgcn_sched_barrier(0)
DI s16x8 lds128(unsigned a) { return *(LAS s16x8*)(size_t)a; }
DI f32x4 ldsf4(unsigned a) { return *(LAS f32x4*)(size_t)a; }
DI f32x2 ldsf2(unsigned a) { return *(LAS f32x2*)(size_t)a; }
DI float ldsf1(unsigned a) { return *(LAS float*)(size_t)a; }
DI s16x4 ldstr4(unsigned a) { return __builtin_amdgcn_ds_read_tr16_b64_v4i16((LAS s16x4*)(size_t)a); }
DI s16x8 cat(s16x4 lo, s16x4 hi) { return __builtin_shufflevector(lo, hi, 0, 1, 2, 3, 4, 5, 6, 7); }
DI s16x8 ldstr(unsigned a0, unsigned a1) { return cat(ldstr4(a0), ldstr4(a1)); }
DI f32x4 mfma(s16x8 a, s16x8 b, f32x4 c) {
  return __builtin_amdgcn_mfma_f32_16x16x32_bf16(__builtin_bit_cast(bf16x8, a), __builtin_bit_cast(bf16x8, b), c, 0, 0, 0);
}
DI unsigned pk2(float a, float b) { f32x2 v = {a, b}; return __builtin_bit_cast(unsigned, __builtin_convertvector(v, bf16x2)); }
DI float u2f(unsigned u) { return __builtin_bit_cast(float, u); }
DI unsigned f2u(float u) { return __builtin_bit_cast(unsigned, u); }
DI float bflo(unsigned u) { return __builtin_bit_cast(float, u << 16); }
DI float bfhi(unsigned u) { return __builtin_bit_cast(float, u & 0xffff0000u); }
template <int CTRL> DI float dpp_add(float v) {
  return v + __builtin_bit_cast(float, __builtin_amdgcn_update_dpp(0, __builtin_bit_cast(int, v), CTRL, 0xf, 0xf, true));
}
DI float row16_sum(float v) {
  v = dpp_add<0xB1>(v);
  v = dpp_add<0x4E>(v);
  v = dpp_add<0x141>(v);
  v = dpp_add<0x140>(v);
  return v;
}
DI float smul(float a, float b) { float d; asm("v_mul_f32 %0, %1, %2" : "=v"(d) : "v"(a), "v"(b)); return d; }
DI float sadd(float a, float b) { float d; asm("v_add_f32 %0, %1, %2" : "=v"(d) : "v"(a), "v"(b)); return d; }
DI float sfma(float a, float b, float c) { float d; asm("v_fma_f32 %0, %1, %2, %3" : "=v"(d) : "v"(a), "v"(b), "v"(c)); return d; }
DI float sfms(float a, float b, float c) { float d; asm("v_fma_f32 %0, %1, %2, -%3" : "=v"(d) : "v"(a), "v"(b), "v"(c)); return d; }
DI void zero4(f32x4& v) {
  float a, b, c, d;
  asm volatile("v_mov_b32 %0, 0\n\tv_mov_b32 %1, 0\n\tv_mov_b32 %2, 0\n\tv_mov_b32 %3, 0" : "=v"(a), "=v"(b), "=v"(c), "=v"(d));
  v = (f32x4){a, b, c, d};
}
DI u32x2 swap32(float a, float b) { asm volatile("s_nop 1" : "+v"(a), "+v"(b)); return __builtin_amdgcn_permlane32_swap(f2u(a), f2u(b), false, false); }
DI u32x2 swap16(float a, float b) { asm volatile("s_nop 1" : "+v"(a), "+v"(b)); return __builtin_amdgcn_permlane16_swap(f2u(a), f2u(b), false, false); }
DI f32x2 pfma(f32x2 a, f32x2 b, f32x2 c) { return __builtin_elementwise_fma(a, b, c); }
DI f32x2 bc(float a) { return (f32x2){a, a}; }
DI f32x2 bco(float a) { f32x2 v = {a, a}; asm("" : "+v"(v)); return v; }
DI f32x2 cmul(f32x2 a, f32x2 b) { return (f32x2){sfms(a[0], b[0], smul(a[1], b[1])), sfma(a[0], b[1], smul(a[1], b[0]))}; }
DI f32x2 cfma(f32x2 a, f32x2 b, f32x2 c) { return (f32x2){sfms(a[0], b[0], sfms(a[1], b[1], c[0])), sfma(a[0], b[1], sfma(a[1], b[0], c[1]))}; }
DI f32x2 csel(bool p, f32x2 a, f32x2 b) { return (f32x2){p ? a[0] : b[0], p ? a[1] : b[1]}; }
DI void prog_set(unsigned a, unsigned v) {
  asm volatile("s_mov_b64 exec, 1\n\tds_write_b32 %0, %1\n\ts_mov_b64 exec, -1" ::"v"(a), "v"(v) : "memory");
}
DI void prog_wait(unsigned a, unsigned v) {
#pragma unroll 1
  for (int spin = 0; spin < SPIN_CAP * 16; ++spin) {
    if (__builtin_amdgcn_readfirstlane(*(volatile LAS unsigned*)(size_t)a) >= v) break;
    __builtin_amdgcn_s_sleep(1);
  }
  asm volatile("" ::: "memory");
}
DI void bar() { asm volatile("" ::: "memory"); __builtin_amdgcn_s_barrier(); asm volatile("" ::: "memory"); }
DI void dma1_dev(unsigned voff, const char* sbase, unsigned ldsaddr) {
  asm volatile("s_mov_b32 m0, %2\n\ts_nop 0\n\tglobal_load_lds_dwordx4 %0, %1 sc1" ::"v"(voff), "s"(sbase), "s"(ldsaddr) : "memory");
}
DI void dma2_dev(unsigned voff, const char* s0, const char* s1, unsigned ldsaddr) {
  asm volatile("s_mov_b32 m0, %3\n\ts_nop 0\n\tglobal_load_lds_dwordx4 %0, %1 sc1\n\tglobal_load_lds_dwordx4 %0, %2 offset:2048 sc1" ::"v"(voff), "s"(s0), "s"(s1), "s"(ldsaddr) : "memory");
}
DI void dma4_dev(unsigned voff, const char* s0, const char* s1, unsigned ldsaddr) {
  asm volatile("s_mov_b32 m0, %3\n\ts_nop 0\n\tglobal_load_lds_dwordx4 %0, %1 sc1\n\tglobal_load_lds_dwordx4 %0, %1 offset:1024 sc1\n\t"
               "global_load_lds_dwordx4 %0, %2 offset:2048 sc1\n\tglobal_load_lds_dwordx4 %0, %2 offset:3072 sc1" ::"v"(voff), "s"(s0), "s"(s1), "s"(ldsaddr) : "memory");
}
DI int kstep_of(int slot, int i) {
  const int q = slot + 4 * i + (slot >= 4 ? 28 : 0), cls = (q >= 14) + (q >= 28) + (q >= 42);
  return 4 * (q - 14 * cls) + ((0x9C >> (2 * cls)) & 3);
}
DI unsigned flag_of(int j, int c) {
  const int q = 14 * ((0x78 >> (2 * c)) & 3) + j;
  return q < 32 ? (q & 3) * 128 + (q >> 2) * 8 : 512 + (q & 3) * 128 + ((q - 32) >> 2) * 8;
}
DI void cnt_add(unsigned a) {
  asm volatile("s_mov_b64 exec, 1\n\tds_add_u32 %0, %1\n\ts_mov_b64 exec, -1" ::"v"(a), "v"(1u) : "memory");
}
DI f32x2 lo2(f32x4 v) { return __builtin_shufflevector(v, v, 0, 1); }
DI f32x2 hi2(f32x4 v) { return __builtin_shufflevector(v, v, 2, 3); }
DI void rows4(float v, float* o) {
  asm volatile("s_nop 1" : "+v"(v));
  const unsigned u = f2u(v);
  const u32x2 h = __builtin_amdgcn_permlane32_swap(u, u, false, false);
  const u32x2 a = __builtin_amdgcn_permlane16_swap(h.x, h.x, false, false), b = __builtin_amdgcn_permlane16_swap(h.y, h.y, false, false);
  o[0] = u2f(a.x); o[1] = u2f(a.y); o[2] = u2f(b.x); o[3] = u2f(b.y);
}
struct Col { f32x2 cs[4], bias[4]; };
DI void mkcol(Col& c, f32x4 cs0, f32x4 cs1, u32x2 b0, u32x2 b1) {
  c.cs[0] = lo2(cs0); c.cs[1] = hi2(cs0); c.cs[2] = lo2(cs1); c.cs[3] = hi2(cs1);
  c.bias[0] = (f32x2){bflo(b0.x), bfhi(b0.x)}; c.bias[1] = (f32x2){bflo(b0.y), bfhi(b0.y)};
  c.bias[2] = (f32x2){bflo(b1.x), bfhi(b1.x)}; c.bias[3] = (f32x2){bflo(b1.y), bfhi(b1.y)};
}
DI u32x4 gelu8(f32x4 a0, f32x4 a1, const Col& c, f32x2 rs, f32x2 nmrs) {
  f32x2 v[4], z[4];
  u32x4 o;
#define ST4(stmt) _Pragma("unroll") for (int h = 0; h < 4; ++h) { stmt; } SCHED();
  ST4(v[h] = pfma(c.cs[h], nmrs, c.bias[h]))
  ST4(v[h] = pfma(h == 0 ? lo2(a0) : h == 1 ? hi2(a0) : h == 2 ? lo2(a1) : hi2(a1), rs, v[h]))
  ST4(z[h] = v[h] * v[h])
  ST4(z[h] = pfma(z[h], bc(-0.10294324f), bc(-2.30220820f)))
  ST4(z[h] = z[h] * v[h])
  ST4((z[h] = (f32x2){__builtin_amdgcn_exp2f(z[h][0]), __builtin_amdgcn_exp2f(z[h][1])}))
  ST4(z[h] = z[h] + bc(1.f))
  ST4((z[h] = (f32x2){__builtin_amdgcn_rcpf(z[h][0]), __builtin_amdgcn_rcpf(z[h][1])}))
  ST4(v[h] = v[h] * z[h])
  ST4(o[h] = pk2(v[h][0], v[h][1]))
#undef ST4
  return o;
}

#define TRACE(i) do {} while (0)
#define TRACE2(i) do {} while (0)
#define TRACE3(i) do {} while (0)
#define TRACE4(i) do {} while (0)
#define STAMP(k) do {} while (0)
#define STAMPC(k) do {} while (0)
#define COUNT(k) do {} while (0)

__global__ void __launch_bounds__(512, 2) fused_kernel(Params p) {
  __shared__ __attribute__((aligned(16))) char lds_mem[LDS_BYTES];
  unsigned long long pc;
  asm volatile("s_getpc_b64 %0" : "=s"(pc));
  const unsigned LB = (unsigned)(size_t)(LAS char*)lds_mem;
  const int tid = threadIdx.x, lane = tid & 63;
  const int w = (__builtin_amdgcn_readfirstlane(tid >> 6) + WOFF) & 7;
  if (w == CPW) {
    const unsigned co = ((unsigned)(lane & 15) * 32u + (blockIdx.x >> 3)) * 128u;
    dma4w(co < CODE_BYTES ? co : 0u, (const char*)(pc & ~255ull), LB + L_DUMMY + 512);
  }
  const int b = blockIdx.x, idx = b >> 3, xcd = b & 7;
  const int r = 8 * (xcd >> 2) + (idx >> 2), j = 4 * (xcd & 3) + (idx & 3);
  const int rot = (2 * (xcd & 3) + 4 * (xcd >> 2)) & 7;
  const int mtile = 4 * xcd + (idx >> 3), ntile = (idx + NOFF) & 7;
  const int i16 = lane & 15, g = lane >> 4, qq = i16 >> 2, pp = i16 & 3;
  const int d = w & 1;
  const bool mlp = j < 14;
  u32x4 pre[4];
  unsigned v_lre, v_lim, v_ldt;
  if ((w >> 1) == 2) {
    const unsigned po = (unsigned)(32 * j + 16 * d + i16) * 2u;
    v_lre = *(const GAS unsigned short*)(p.lre + po); v_lim = *(const GAS unsigned short*)(p.lim + po); v_ldt = *(const GAS unsigned short*)(p.logdt + po);
    asm volatile("" ::: "memory");
  }
  u32x4 prb[2];
  if (w < 4) {
    const bool isb = w >= 2;
    const char* pb = isb ? p.w1 + (64 * (r & 7) + 32 * d) * 3584 + (mlp ? j : 13) * 256 + (r >> 3) * 128 : p.x + (128 * r + 8 * j + 4 * d) * 1024;
    const unsigned po = isb ? (lane >> 3) * 3584 + (lane & 7) * 16 : g * 1024 + i16 * 16, ps = isb ? 28672 : 256;
#pragma unroll
    for (int i = 0; i < 4; ++i) pre[i] = *(const GAS u32x4*)(pb + i * ps + po);
    if (isb) {
#pragma unroll
      for (int i = 0; i < 2; ++i) prb[i] = *(const GAS u32x4*)((r & 8 ? p.bim : p.bre) + (32 * j + 4 * (r & 7) + 2 * d) * 1024 + i * 512 + (lane >> 5) * 1024 + (lane & 31) * 16);
    }
    asm volatile("" ::: "memory");
  }
  const int kgo = 2 * (idx >> 3), late = ((w + 2) >> 2) & 1, kg = 2 * (((w & 2) + d + KROT * ntile) & 3) + late;
  auto slot_of = [&](int role) { return (((role + 1) & 2) << 1) + (((((role + 1) & 2) ? 0 : kgo) + ((role & 1) ? 0 : 2) + d) & 3); };
  const int slot = slot_of(w >> 1);
  const u32x2 ep = *(const __attribute__((address_space(4))) u32x2*)(p.ws + WS_EPOCH);
  unsigned tagA, tagB;
  auto mktag = [&]() { tagA = ep.x; tagB = ep.y; asm volatile("" : "+s"(tagA), "+s"(tagB)); tagA += 1u; tagB += 1u; };
  const __amdgpu_buffer_rsrc_t wsr = __builtin_amdgcn_make_buffer_rsrc(p.ws, 0, WS_END, 0x00020000);
  const i32x4 wss = {(int)(size_t)p.ws, (int)((size_t)p.ws >> 32) & 0xffff, (int)WS_END, 0x00020000};
  auto fresh = [](int o) { asm volatile("" : "+s"(o) :: "memory"); return o; };
  const unsigned HB = LB + d * HALF;
  const unsigned PW = LB + L_FLG;
  s16x8 we[8][4];
  f32x4 acc2[4][4];
  const unsigned home = HB + ((2 - (w >> 1)) & 3) * SLOT;
  const unsigned r2l = (4 * g + qq) * 128 + pp * 8, r2x = (2 * g + (qq >> 1)) & 3;
  auto absorb = [&](int i, unsigned page) {
#pragma unroll
    for (int n = 0; n < 4; ++n) { const unsigned a = page + r2l + ((n ^ r2x) * 32); we[i][n] = ldstr(a, a + 2048); }
  };
  auto mkvc = [&](unsigned* vc) {
#pragma unroll
    for (int i = 0; i < 8; ++i) vc[i] = (lane >> 3) * 1024 + (((lane & 7) ^ (4 * (i & 1) + g)) * 16) + 4096 + i * 7168;
  };
  auto mkv2 = [&](unsigned* v2) {
#pragma unroll
    for (int i = 0; i < 4; ++i) v2[i] = (lane >> 3) * 1024 + (((((lane & 7) >> 1) ^ g) << 1) | (lane & 1)) * 16 + i * 7168;
  };
  auto loadC = [&](const unsigned* vc, int kgc, unsigned dst) {
    const unsigned o = ntile * 65536 + kgc * 128;
    dma_x8(vc, p.cre + o, dst);
    dma_x8(vc, p.cim + o, dst + 8192);
  };
  const char* w2b = p.w2 + ntile * 128;
  auto adv = [&](unsigned& a, int dl) { a += dl; asm volatile("" : "+v"(a)); };
  const int tok = 32 * qq + 16 * (i16 & 1) + ((i16 >> 1) & 1);
  const unsigned kx = (g ^ (i16 >> 1)) * 16;
  const unsigned awb = LB + SL_W + (8 * g + qq) * 256 + pp * 8, awx = qq | ((g & 1) << 2);
  auto sum32 = [&](const u32x4* v) {
    float s0 = 0.f, s1 = 0.f;
#pragma unroll
    for (int i = 0; i < 4; ++i)
#pragma unroll
      for (int e = 0; e < 4; ++e) { s0 += bflo(v[i][e]); s1 += bfhi(v[i][e]); }
    return row16_sum(s0 + s1);
  };
  auto touch = [&]() {
    dma4w(d ? (lane & 15) * 128 : (lane < 56 ? lane : 0) * 128, d ? p.cre + b * 2048 : p.w2 + b * 7168, LB + L_DUMMY + d * 256);
    if (d) dma4w((lane & 15) * 128, p.cim + b * 2048, LB + L_DUMMY + d * 256);
  };
  auto zero2 = [&]() {
#pragma unroll
    for (int n = 0; n < 4; ++n) {
#pragma unroll
      for (int m = 0; m < 4; ++m) acc2[n][m] = (f32x4){0, 0, 0, 0};
      asm volatile("" : "+v"(acc2[n][0]), "+v"(acc2[n][1]), "+v"(acc2[n][2]), "+v"(acc2[n][3]));
    }
  };
  int ksl;
  unsigned hb, crd, pubo = 0xffff0000u;
  const int fm = WS_FLAG + mtile * 2048 + slot * 128, fs = WS_FLAG + mtile * 2048 + 1024 + (kg >> 1) * 64;
  u64x8 fl;
  auto snap = [&](bool ssm) { fl = __builtin_bit_cast(u64x8, sbl16(wss, fresh(ssm ? fs : fm), 1)); SCHED(); };
  auto seen = [&]() { SCHED(); asm volatile("" : "+s"(fl)); };
  auto setup2 = [&](int nm) {
    ksl = (lane < nm ? kstep_of(slot, lane) : 56 + 4 * kg + late + ((lane - nm) & 7)) * 4096;
    if (NOC & 16) ksl = (w * 11 + (lane & 1)) * 4096;
    ksl += w == 1 ? 2048 : 0;
    hb = WS_H + (unsigned)mtile * H_MT + lane * 16;
    crd = home + i16 * 128 + (g & 1) * 8 + (((g >> 1) ^ (i16 >> 1)) * 16);
    asm volatile("" : "+v"(ksl), "+v"(hb), "+v"(crd));
  };

  auto roleD = [&]() __attribute__((always_inline)) {
    __builtin_amdgcn_s_waitcnt(0x0F70);
    __builtin_amdgcn_s_setprio(3);
    unsigned vx[8], vw[8], vb[4], v2[4];
#pragma unroll
    for (int i = 0; i < 8; ++i) vw[i] = g * 3584 + ((((i16 >> 1) ^ (g | (((i >> 1) & 1) << 2))) << 1) | (i16 & 1)) * 16 + 4096 + i * (14336 - 1024);
    const char* wb = p.w1 + j * 256 + d * (32 * 3584);
    auto part1 = [&](int c, unsigned slot) { if (mlp) dma_x8(vw, wb + ((c + rot) & 7) * (64 * 3584), LB + slot + SL_W + d * 8192); };
    asm volatile("" :: "s"(wb));
    STAMP(18);
    part1(0, 0);
#pragma unroll
    for (int i = 0; i < 8; ++i)
      vx[i] = (32 * (g >> 1) + 16 * ((lane >> 3) & 1) + (g & 1)) * 1024 + (((lane & 7) ^ (4 * (i & 1) + g)) * 16) + 4096 + (i & 1) * 65536 + (i >> 1) * 2048 - i * 1024;
#pragma unroll
    for (int i = 0; i < 4; ++i) vb[i] = (lane >> 3) * 1024 + (((lane & 7) ^ (4 * (i & 1) + g)) * 16) + i * 7168;
    const char* xb = p.x + (r * 128 + d * 8) * 1024;
    const char* bb = (d ? p.bim : p.bre) + j * 32768;
    auto partx = [&](int c, unsigned slot) { dma_x8(vx, xb + ((c + rot) & 7) * 128, LB + slot + d * 8192); };
    auto partb = [&](int c, unsigned slot) { dma_x4(vb, bb + ((c + rot) & 7) * 128, LB + slot + SL_B + d * 4096); };
    auto part0 = [&](int c, unsigned slot) { partx(c, slot); partb(c, slot); };
    mkv2(v2);
    unsigned ra[4];
#pragma unroll
    for (int n = 0; n < 4; ++n) ra[n] = home + r2l + ((n ^ r2x) * 32);
    int kt = kstep_of(slot_of((lane >> 3) & 3), lane & 7) * 32768;
    asm volatile("" : "+v"(kt), "+v"(ra[0]), "+v"(ra[1]), "+v"(ra[2]), "+v"(ra[3]), "+v"(v2[0]), "+v"(v2[1]), "+v"(v2[2]), "+v"(v2[3]));
    bool low = false;
    auto page = [&](int role, int i, int grp, int pg) { if ((NOC & 4) && low) return; dma_x4(v2, w2b + (unsigned)__builtin_amdgcn_readlane(kt, 8 * role + i), HB + grp * SLOT + pg * 4096, low); };
    auto take = [&](int i) {
      SCHED();
#pragma unroll
      for (int n = 0; n < 4; ++n) we[i][n] = ldstr(ra[n] + PG(i) * 4096, ra[n] + PG(i) * 4096 + 2048);
      SCHED();
    };
    auto own = [&](int i) { page(3, i, 3, PG(i)); };
    unsigned l16 = lane * 16, la = home + 12288 + lane * 16, lb = LB + L_ST1 + d * 1024 + lane * 16, lc = LB + L_BS + d * 512 + (lane & 31) * 16;
    unsigned wa = LB + L_CS + d * 256 + (lane & 31) * 8 + (lane >> 5) * 4, wb2 = LB + L_LS + (2 * d + (lane >> 5)) * 288 + i16 * 16 + (g & 1) * 4;
    asm volatile("" : "+v"(l16), "+v"(la), "+v"(lb), "+v"(lc), "+v"(wa), "+v"(wb2));
    auto gather = [&](bool bs) {
      const char* cp = p.ws + WS_CS + j * 8192 + d * 4096;
      dma4_dev(l16, cp, cp, home + 12288);
      dma2_dev(l16, p.ws + WS_STAT + r * 2048 + d * 1024, p.ws + WS_STAT + (mtile >> 1) * 2048 + d * 1024 - 2048, LB + L_ST1 + d * 1024);
      if (bs) dma1_dev(l16, p.ws + WS_BS + j * 1024, LB + L_BS);
    };
    auto check = [&](auto between) -> bool {
      auto ld16 = [](unsigned a) { return *(volatile LAS u32x4*)(size_t)a; };
      auto bad = [&](unsigned a, u32x4 v) { return __builtin_amdgcn_bitop3_b32(v.z, tagB, __builtin_amdgcn_bitop3_b32(v.x, tagA, a, 0xBE), 0xBE); };
      const u32x4 c0 = ld16(la), c1 = ld16(la + 1024), c2 = ld16(la + 2048), c3 = ld16(la + 3072), s1 = ld16(lb), s2 = ld16(lb + 2048), s3 = ld16(lc);
      SCHED(); between(); SCHED();
      const u32x2 h = swap32(sadd(sadd(u2f(c0.y), u2f(c1.y)), sadd(u2f(c2.y), u2f(c3.y))), sadd(sadd(u2f(c0.w), u2f(c1.w)), sadd(u2f(c2.w), u2f(c3.w))));
      *(LAS float*)(size_t)wa = sadd(u2f(h.x), u2f(h.y));
      *(LAS unsigned*)(size_t)wb2 = s1.y;
      *(LAS unsigned*)(size_t)(wb2 + 8) = s1.w;
      const unsigned e = bad(bad(bad(bad(bad(bad(bad(0u, c0), c1), c2), c3) & (mlp ? ~0u : 0u), s1), s2), s3);
      return __builtin_amdgcn_ballot_w64(e != 0) == 0;
    };
    const unsigned me = PW + w * 4;
    zero2();
    setup2(8);
    auto landed = [&]() { if (mlp) wait_vm<20>(); else wait_vm<12>(); };
    SCHED();
    part0(0, 0);
    if (TOUCH == 4) touch();
    part1(1, SLOT); part0(1, SLOT);
    STAMP(1);
    mktag();
    if (mlp) wait_vm<20 + (TOUCH == 4)>(); else wait_vm<12 + (TOUCH == 4)>();
    STAMP(19);
    bar();
    STAMP(2);
    part1(2, 2 * SLOT); part0(2, 2 * SLOT); landed(); if (TOUCH == 1) touch();
    bar();
    if (d) dma4w(lane * 4, p.b1 + (mlp ? j : 13) * 256, LB + L_B1);
    part1(3, 0); part0(3, 0); landed();
    unsigned vt = (lane < 56 ? lane : 0) * 128;
    asm volatile("" : "+v"(vt));
    auto l2t = [&](int t) { if (t < L2T) dma4w(vt, p.w2 + (t * 64 + 2 * idx + d) * 7168, LB + L_DUMMY + d * 256); };
    auto l2c = [&](int t) { if (t < L2C) dma4w(l16 * 8, (t ? p.cim : p.cre) + (2 * idx + d) * 8192, LB + L_DUMMY + d * 256); };
    if (L2T < 4) dma4w((lane < 28 ? lane : 0) * 1024, p.w2 + (2 * idx + d) * 28672 + ((3u - ((unsigned)(size_t)p.w2 >> 7)) & 7u) * 128, LB + L_DUMMY + d * 256);
    l2t(0);
    bar();
    STAMPC(21);
    dma_x4(vx, p.x + (128 * (mtile >> 1) + 8 * (mtile & 1) + 4 * d) * 1024 + ntile * 128 - 4096, home + 12288);
    part1(4, SLOT); part0(4, SLOT); landed(); l2t(1); l2c(0);
    bar();
    own(0); part1(5, 2 * SLOT); part0(5, 2 * SLOT); landed(); l2t(2); l2c(1);
    bar();
    gather(d); part1(6, 0); take(0); part0(6, 0); landed(); l2t(3);
    bar();
    part1(7, SLOT);
    const bool ok = check([&]() { partx(7, SLOT); });
    SCHED();
    partb(7, SLOT);
    if (__builtin_expect(!ok, 0)) {
#pragma unroll 1
      for (int spin = 0; spin < SPIN_CAP; ++spin) {
        COUNT(20);
        __builtin_amdgcn_s_sleep(10);
        gather(true);
        wait_vm<0>();
        if (check([]() {})) break;
      }
    }
    landed();
    barrier();
    STAMP(3); STAMPC(22);
    page(0, 0, 2, 0); page(0, 1, 2, 1); page(0, 2, 2, 2); page(0, 3, 2, 3); page(0, 4, 2, 4); own(1);
    wait_vm<12>();
    bar();
    STAMP(4);
    own(2); page(2, 0, 0, 0); page(2, 1, 0, 1); page(2, 2, 0, 2); page(2, 3, 0, 3); page(2, 4, 0, 4);
    wait_vm<20>();
    STAMP(5);
    bar();
    TRACE4(31);
    low = true;
    STAMP(6);
    take(1); take(2);
    page(0, 5, 2, 0); page(0, 6, 2, 1); page(0, 7, 2, 2);
    wait_vm<12>(); prog_set(me, 1);
    own(3); own(4); wait_lgkm0(); own(5); own(6);
    wait_vm<16>(); prog_set(me, 2);
    page(1, 0, 1, 0); page(1, 1, 1, 1); page(1, 2, 1, 2);
    wait_vm<16>(); take(3); take(4); take(5); wait_lgkm0();
    own(7);
    page(1, 3, 1, 3); page(1, 4, 1, 4);
    auto c8 = [&](const char* src, unsigned dst) { if (NOC & 1) return; dma_x4(vb, src, dst, true); dma_x4(vb, src + 32768, dst + 4096, true); };
    const unsigned co = ntile * 65536 + kg * 128;
    c8(p.cre + co, home);
    wait_vm<8>();
    prog_set(me, 3);
    take(6); take(7);
    prog_wait(PW + d * 4, 1);
    c8(p.cim + co, home + 8192);
    STAMP(7);
    snap(false);
    c8(p.cre + (co ^ 512), HB + 2 * SLOT); c8(p.cim + (co ^ 512), HB + 2 * SLOT + 8192);
    seen();
    STAMP(8);
  };
  auto roleL = [&]() __attribute__((always_inline)) {
    if (TOUCH == 0) touch();
    if (TLBW && d) { dma4w(0, p.cre + ntile * 65536, LB + L_DUMMY + 256); dma4w(0, p.cim + ntile * 65536, LB + L_DUMMY + 256); dma4w((lane & 1) * 0x1bf000, p.w2, LB + L_DUMMY + 256); }
    f32x4 acr[8], aci[8];
#pragma unroll
    for (int m = 0; m < 8; ++m) { acr[m] = (f32x4){0, 0, 0, 0}; aci[m] = (f32x4){0, 0, 0, 0}; asm volatile("" : "+v"(acr[m]), "+v"(aci[m])); }
    unsigned ax[2], ab[2];
    ax[0] = LB + i16 * 128 + kx; ax[1] = ax[0] ^ 64;
    ab[0] = LB + SL_B + (16 * d + i16) * 128 + kx; ab[1] = ab[0] ^ 64;
    unsigned sa = LB + L_LS + g * 288, csa = LB + L_BS + (16 * d + i16) * 16 + 4;
    asm volatile("" : "+v"(sa), "+v"(csa), "+v"(ax[0]), "+v"(ax[1]), "+v"(ab[0]), "+v"(ab[1]));
    s16x8 xf[2][8], bf[2][2];
    auto rd = [&](int set, int i) {
      if (i < 2) bf[set][i] = lds128(ab[set] + i * 4096);
      else xf[set][i - 2] = lds128(ax[set] + (i - 2) * 2048);
    };
    auto mm = [&](int set, int i) {
      if (i & 1) aci[i >> 1] = mfma(xf[set][i >> 1], bf[set][1], aci[i >> 1]);
      else acr[i >> 1] = mfma(xf[set][i >> 1], bf[set][0], acr[i >> 1]);
    };
    asm volatile("" : "+v"(v_lre), "+v"(v_lim), "+v"(v_ldt));
    const float lr = bflo(v_lre), li = bflo(v_lim);
    const float dt = bflo(pk2(expf(bflo(v_ldt)), 0.f));
    const float ang = smul(dt, li);
    const float rv = smul(ang, 0.15915494f);
    const float rev = (rv - __builtin_rintf(rv)) + sfma(ang, 6.4206382e-9f, sfms(ang, 0.15915494f, rv));
    float sn = __builtin_amdgcn_sinf(rev), cn = __builtin_amdgcn_cosf(rev);
    asm volatile("s_nop 0" : "+v"(sn), "+v"(cn));
    const float ex = expf(smul(dt, lr));
    f32x2 a1 = {smul(ex, cn), smul(ex, sn)};
    const float nr = a1[0] - 1.f, ni = a1[1], iden = 2.f / sfma(lr, lr, smul(li, li));
    f32x2 kk = {smul(sfma(nr, lr, smul(ni, li)), iden), smul(sfms(ni, lr, smul(nr, li)), iden)};
    f32x2 t = cmul(a1, a1); t = cmul(t, t); t = cmul(t, t);
    f32x2 a16 = cmul(t, t);
    f32x2 a32 = cmul(a16, a16);
    const f32x2 a64 = cmul(a32, a32), a128 = cmul(a64, a64), a256 = cmul(a128, a128);
    f32x2 a512 = cmul(a256, a256);
    const f32x2 one = {1.f, 0.f};
    f32x2 aseg = cmul(kk, cmul(csel(g & 1, a32, one), csel(g & 2, a64, one)));
    const int e4 = (r - 1 - g) & 3;
    f32x2 acar = cmul(csel(e4 & 1, a128, one), csel(e4 & 2, a256, one));
    const f32x2 ar = bco(a1[0]), ai = bco(a1[1]);
    asm volatile("" :: "v"(ar), "v"(ai));
    asm volatile("" : "+v"(kk), "+v"(a16), "+v"(a32), "+v"(a512), "+v"(aseg), "+v"(acar));
    const unsigned on = (qq == g) ? ((pp & 1) ? 0xffff0000u : 0xffffu) : 0u;
    unsigned m0 = (pp & 2) ? 0u : on, m1 = (pp & 2) ? on : 0u;
    auto scan = [&](f32x2 h0, f32x2 h1, const f32x2* ur, const f32x2* ui, auto out) {
      const u32x4 z0 = {m0 & 0x3f803f80u, m1 & 0x3f803f80u, 0u, 0u}, z1 = {0u, 0u, z0.x, z0.y};
      const f32x4 zz = {0, 0, 0, 0};
      f32x2 hr = {h0[0], h1[0]}, hi = {-h0[1], -h1[1]};
      u32x4 fr[4], fi[4];
      f32x4 tr[2], ti[2];
      SCHED();
#pragma unroll
      for (int mp = 0; mp < 5; ++mp) {
        if (mp > 0) {
#pragma unroll
          for (int k = 0; k < 2; ++k) {
            tr[k] = mfma(__builtin_bit_cast(s16x8, fr[mp - 1]), __builtin_bit_cast(s16x8, k ? z1 : z0), zz);
            ti[k] = mfma(__builtin_bit_cast(s16x8, fi[mp - 1]), __builtin_bit_cast(s16x8, k ? z1 : z0), zz);
          }
          SCHED();
        }
        if (mp < 4) {
#pragma unroll
          for (int e = 0; e < 4; ++e) {
            const int s = 4 * mp + e;
            const f32x2 t1 = ur ? pfma(hr, -ai, ui[s]) : hr * -ai; SCHED();
            const f32x2 t0 = ur ? pfma(hi, ai, ur[s]) : hi * ai; SCHED();
            hi = pfma(hi, ar, t1); SCHED();
            hr = pfma(hr, ar, t0); SCHED();
            fi[mp][e] = pk2(hi[0], hi[1]); SCHED();
            fr[mp][e] = pk2(hr[0], hr[1]); SCHED();
          }
        }
        if (mp > 0) { out(2 * mp - 2, tr[0], ti[0]); out(2 * mp - 1, tr[1], ti[1]); SCHED(); }
      }
    };
    auto pack = [](f32x4 a, f32x4 b) { return (u32x4){pk2(a[0], a[1]), pk2(a[2], a[3]), pk2(b[0], b[1]), pk2(b[2], b[3])}; };
    u32x4 yb[8];
    auto resp = [&]() { scan(aseg, cmul(a16, aseg), nullptr, nullptr, [&](int q, f32x4 a, f32x4 b) { yb[q] = pack(a, b); asm volatile("" : "+v"(yb[q])); }); };
    if (YPRE) resp();
    mktag();
    __builtin_amdgcn_s_waitcnt(0x0F70);
    __builtin_amdgcn_s_setprio(1);
    STAMP(1);
    bar();
    STAMP(2); TRACE3(1);
#pragma unroll
    for (int i = 0; i < 10; ++i) rd(0, i);
    wait_lgkm0();
    auto stage1 = [&](int dl) {
      SCHED();
#pragma unroll
      for (int i = 0; i < 16; ++i) {
        mm(0, i); SCHED();
        if (i < 10) rd(1, i);
        if (i == 11) { adv(ax[0], dl); adv(ab[0], dl); }
        SCHED();
      }
    };
    stage1(SLOT);
    barrier();
    TRACE3(2);
    int ph = 0;
#pragma unroll 1
    for (int c = 2; c <= 8; ++c) {
      const int dl = ph == 2 ? -2 * (int)SLOT : (int)SLOT;
      ph = ph == 2 ? 0 : ph + 1;
      SCHED();
#pragma unroll
      for (int i = 0; i < 16; ++i) {
        mm(1, i); SCHED();
        if (i < 10) rd(0, i);
        if (i == 11) { adv(ax[1], dl); adv(ab[1], dl); }
        SCHED();
      }
      wait_lgkm0();
      stage1(ph == 2 ? -2 * (int)SLOT : (int)SLOT);
      TRACE(2 * c);
      barrier();
      TRACE(2 * c + 1); TRACE3(c + 1);
    }
    __builtin_amdgcn_s_setprio(LPRIO);
    STAMP(4);
    f32x2 rs[16], mrs[16];
    float csr, csi;
#pragma unroll
    for (int i = 0; i < 16; ++i) {
      mm(1, i); SCHED();
      if (i == 0) { csr = ldsf1(csa); csi = ldsf1(csa + 512); }
      if (i < 8) { const f32x4 t = ldsf4(sa + i * 16); rs[i] = lo2(t); mrs[i] = hi2(t); }
      SCHED();
    }
    const f32x2 ncr = bco(-csr), nci = bco(-csi);
    STAMP(5);
    f32x2 ur[16], ui[16], Er, Ei;
    SCHED();
#pragma unroll
    for (int s = 0; s < 16; ++s) {
      if (s < 8) { const f32x4 t = ldsf4(sa + (s + 8) * 16); rs[s + 8] = lo2(t); mrs[s + 8] = hi2(t); SCHED(); }
      ur[s] = mrs[s] * ncr; SCHED();
      ui[s] = mrs[s] * nci; SCHED();
      ur[s] = pfma((s & 1) ? hi2(acr[s >> 1]) : lo2(acr[s >> 1]), rs[s], ur[s]); SCHED();
      ui[s] = pfma((s & 1) ? hi2(aci[s >> 1]) : lo2(aci[s >> 1]), rs[s], ui[s]); SCHED();
      if (s == 0) { Er = ur[0]; Ei = ui[0]; }
      else {
        const f32x2 t0 = pfma(Ei, -ai, ur[s]); SCHED();
        const f32x2 t1 = pfma(Er, ai, ui[s]); SCHED();
        Er = pfma(Er, ar, t0); SCHED();
        Ei = pfma(Ei, ar, t1); SCHED();
      }
    }
    TRACE2(1);
    const f32x2 E0 = {Er[0], Ei[0]};
    const f32x2 E = cfma(a16, E0, (f32x2){Er[1], Ei[1]});
    f32x2 P = {0.f, 0.f}, Tt = {0.f, 0.f};
    float er4[4], ei4[4];
    rows4(E[0], er4); rows4(E[1], ei4);
#pragma unroll
    for (int s = 0; s < 4; ++s) {
      const f32x2 e = {er4[s], ei4[s]};
      P = csel(s == g, Tt, P);
      Tt = s ? cfma(a32, Tt, e) : e;
    }
    const unsigned totb = WS_TOT + (unsigned)(j * 2 + d) * 4096;
    if (g == 0) __builtin_amdgcn_raw_buffer_store_b128((u32x4){tagA, f2u(Tt[0]), tagB, f2u(Tt[1])}, wsr, totb + r * 256 + i16 * 16, 0, 16);
    STAMP(6);
    TRACE2(2);
    {
      const f32x2 kr = bco(kk[0]), nki = bco(-kk[1]);
#pragma unroll
      for (int s = 0; s < 16; ++s) {
        const f32x2 t0 = ur[s] * kr; SCHED();
        const f32x2 t1 = ur[s] * nki; SCHED();
        ur[s] = pfma(ui[s], nki, t0); SCHED();
        ui[s] = pfma(ui[s], -kr, t1); SCHED();
      }
    }
#pragma unroll
    for (int s = 0; s < 16; s += 4) asm volatile("" : "+v"(ur[s]), "+v"(ui[s]), "+v"(ur[s + 1]), "+v"(ui[s + 1]), "+v"(ur[s + 2]), "+v"(ui[s + 2]), "+v"(ur[s + 3]), "+v"(ui[s + 3]));
    f32x4 hr4[8], hi4[8];
    {
      const f32x2 kP = cmul(kk, P);
      scan(kP, cfma(a16, kP, cmul(kk, E0)), ur, ui, [&](int q, f32x4 a, f32x4 b) { hr4[q] = a; hi4[q] = b; asm volatile("" : "+v"(hr4[q]), "+v"(hi4[q])); });
    }
    TRACE2(3);
    prog_wait(PW + (6 + d) * 4, 1);
#pragma unroll
    for (int i = 0; i < 5; ++i) absorb(i, home + i * 4096);
    unsigned v2[4], vt[4];
    mkv2(v2);
#pragma unroll
    for (int i = 0; i < 4; ++i) { vt[i] = (4 * i + g < r ? g * 256 : -i * 1024) + i16 * 16 + 4096; asm volatile("" : "+v"(vt[i]), "+v"(v2[i])); }
    {
      const i32x4 tr = {wss[0], wss[1], (int)(totb + r * 256), wss[3]};
#pragma unroll 1
      for (int spin = 0; spin < 64; ++spin) {
        int n, t[15];
        asm volatile("s_buffer_load_dword %1, %16, %17 offset:0 glc\n\ts_buffer_load_dword %2, %16, %17 offset:256 glc\n\ts_buffer_load_dword %3, %16, %17 offset:512 glc\n\ts_buffer_load_dword %4, %16, %17 offset:768 glc\n\ts_buffer_load_dword %5, %16, %17 offset:1024 glc\n\ts_buffer_load_dword %6, %16, %17 offset:1280 glc\n\ts_buffer_load_dword %7, %16, %17 offset:1536 glc\n\ts_buffer_load_dword %8, %16, %17 offset:1792 glc\n\ts_buffer_load_dword %9, %16, %17 offset:2048 glc\n\ts_buffer_load_dword %10, %16, %17 offset:2304 glc\n\ts_buffer_load_dword %11, %16, %17 offset:2560 glc\n\ts_buffer_load_dword %12, %16, %17 offset:2816 glc\n\ts_buffer_load_dword %13, %16, %17 offset:3072 glc\n\ts_buffer_load_dword %14, %16, %17 offset:3328 glc\n\ts_buffer_load_dword %15, %16, %17 offset:3584 glc\n\t"
                     "s_mov_b32 %0, 0\n\ts_waitcnt lgkmcnt(0)\n\t"
                     "s_cmp_eq_u32 %1, %18\n\ts_addc_u32 %0, %0, 0\n\ts_cmp_eq_u32 %2, %18\n\ts_addc_u32 %0, %0, 0\n\ts_cmp_eq_u32 %3, %18\n\ts_addc_u32 %0, %0, 0\n\ts_cmp_eq_u32 %4, %18\n\ts_addc_u32 %0, %0, 0\n\ts_cmp_eq_u32 %5, %18\n\ts_addc_u32 %0, %0, 0\n\ts_cmp_eq_u32 %6, %18\n\ts_addc_u32 %0, %0, 0\n\ts_cmp_eq_u32 %7, %18\n\ts_addc_u32 %0, %0, 0\n\ts_cmp_eq_u32 %8, %18\n\ts_addc_u32 %0, %0, 0\n\ts_cmp_eq_u32 %9, %18\n\ts_addc_u32 %0, %0, 0\n\ts_cmp_eq_u32 %10, %18\n\ts_addc_u32 %0, %0, 0\n\ts_cmp_eq_u32 %11, %18\n\ts_addc_u32 %0, %0, 0\n\ts_cmp_eq_u32 %12, %18\n\ts_addc_u32 %0, %0, 0\n\ts_cmp_eq_u32 %13, %18\n\ts_addc_u32 %0, %0, 0\n\ts_cmp_eq_u32 %14, %18\n\ts_addc_u32 %0, %0, 0\n\ts_cmp_eq_u32 %15, %18\n\ts_addc_u32 %0, %0, 0"
                     : "=&s"(n), "=&s"(t[0]), "=&s"(t[1]), "=&s"(t[2]), "=&s"(t[3]), "=&s"(t[4]), "=&s"(t[5]), "=&s"(t[6]), "=&s"(t[7]), "=&s"(t[8]), "=&s"(t[9]), "=&s"(t[10]), "=&s"(t[11]), "=&s"(t[12]), "=&s"(t[13]), "=&s"(t[14]) : "s"(tr), "s"(totb), "s"(tagA) : "scc", "memory");
        if (n >= r) break;
        COUNT(20);
      }
    }
    STAMP(7);
    auto fetch = [&]() {
      asm volatile("s_mov_b32 m0, %5\n\ts_nop 0\n\tglobal_load_lds_dwordx4 %0, %4 sc1\n\tglobal_load_lds_dwordx4 %1, %4 offset:1024 sc1\n\t"
                   "global_load_lds_dwordx4 %2, %4 offset:2048 sc1\n\tglobal_load_lds_dwordx4 %3, %4 offset:3072 sc1"
                   ::"v"(vt[0]), "v"(vt[1]), "v"(vt[2]), "v"(vt[3]), "s"(p.ws + totb - 4096), "s"(home + 16384) : "memory");
    };
    wait_lgkm0();
    if (r > 0) fetch();
    TRACE2(14);
    if (!YPRE) resp();
    f32x2 wt[4];
#pragma unroll
    for (int i = 3; i >= 0; --i) {
      const bool v = 4 * i + g < r;
      wt[i] = csel(v, acar, (f32x2){0.f, 0.f});
      if (i) acar = csel(v, cmul(acar, a512), acar);
      asm volatile("" : "+v"(wt[i]));
    }
    setup2(6);
    unsigned ho = WS_H + (unsigned)(2 * r) * H_MT + (unsigned)(56 + 2 * j + d) * 4096 + lane * 16, ta = home + 16384 + lane * 16;
    asm volatile("" : "+v"(ho), "+v"(ta), "+v"(m0), "+v"(m1));
    TRACE2(4);
    wait_vm<0>();
    TRACE2(15);
    float cr = 0.f, ci = 0.f;
    if (r > 0) {
#pragma unroll 1
      for (int spin = 0; spin < SPIN_CAP; ++spin) {
        u32x4 tv[4];
        unsigned e = 0;
#pragma unroll
        for (int i = 0; i < 4; ++i) tv[i] = *(volatile LAS u32x4*)(size_t)(ta + i * 1024);
        f32x2 c[2];
#pragma unroll
        for (int i = 0; i < 4; ++i) {
          const f32x2 t = {u2f(tv[i].y), u2f(tv[i].w)};
          c[i & 1] = i < 2 ? cmul(wt[i], t) : cfma(wt[i], t, c[i & 1]);
          e = __builtin_amdgcn_bitop3_b32(tv[i].z, tagB, __builtin_amdgcn_bitop3_b32(tv[i].x, tagA, e, 0xBE), 0xBE);
        }
        const u32x2 h = swap32(sadd(c[0][0], c[1][0]), sadd(c[0][1], c[1][1]));
        const float x = sadd(u2f(h.x), u2f(h.y));
        const u32x2 h2 = swap16(x, x);
        const float y = sadd(u2f(h2.x), u2f(h2.y));
        const u32x2 h3 = swap32(y, y);
        cr = u2f(h3.x); ci = u2f(h3.y);
        if (__builtin_expect(__builtin_amdgcn_ballot_w64(e != 0) == 0, 1)) break;
        COUNT(20);
        __builtin_amdgcn_s_sleep(8);
        fetch();
        wait_vm<0>();
      }
    }
    STAMP(8);
    {
      const unsigned c0 = pk2(cr, cr), c1 = pk2(ci, ci), c2 = pk2(-ci, -ci);
      const s16x8 are = __builtin_bit_cast(s16x8, (u32x4){m0 & c0, m1 & c0, m0 & c1, m1 & c1}), aim = __builtin_bit_cast(s16x8, (u32x4){m0 & c2, m1 & c2, m0 & c0, m1 & c0});
      u32x4 o[8];
      SCHED();
#pragma unroll
      for (int q = 0; q < 10; ++q) {
        if (q < 8) {
          hr4[q] = mfma(are, __builtin_bit_cast(s16x8, yb[q]), hr4[q]);
          hi4[q] = mfma(aim, __builtin_bit_cast(s16x8, yb[q]), hi4[q]);
          SCHED();
        }
        if (q >= 2) { o[q - 2] = pack(hr4[q - 2], hi4[q - 2]); __builtin_amdgcn_raw_buffer_store_b128(o[q - 2], wsr, ho + ((q - 2) & 3) * 1024, ((q - 2) >> 2) * H_MT, HST); }
        SCHED();
      }
      TRACE2(5);
      dma_x4(v2, w2b + kstep_of(slot, 5) * 32768, home + 16384);
      asm volatile("" :: "v"(o[0]), "v"(o[1]), "v"(o[2]), "v"(o[3]), "v"(o[4]), "v"(o[5]), "v"(o[6]), "v"(o[7]), "v"(ho));
      STAMP(19);
      pubo = lane < 2 ? WS_FLAG + (2 * r + lane) * 2048 + 1024 + (2 * j + d) * 8 : pubo;
    }
    STAMP(18);
  };
  auto roleB = [&]() __attribute__((always_inline)) {
    mktag();
    {
      float s0 = 0.f, s1 = 0.f;
#pragma unroll
      for (int e = 0; e < 8; ++e) { s0 = sadd(s0, bflo(prb[e >> 2][e & 3])); s1 = sadd(s1, bfhi(prb[e >> 2][e & 3])); }
      *(LAS float*)(size_t)(LB + L_LS + 512 + (4 * d + g) * 4) = row16_sum(s0 + s1);
      __builtin_amdgcn_s_waitcnt(0x0F70);
    }
    if (mlp) {
      f32x4 acc[6][4];
#pragma unroll
      for (int s = 0; s < 6; ++s) {
#pragma unroll
        for (int m = 0; m < 4; ++m) acc[s][m] = (f32x4){0, 0, 0, 0};
        asm volatile("" : "+v"(acc[s][0]), "+v"(acc[s][1]), "+v"(acc[s][2]), "+v"(acc[s][3]));
      }
      unsigned ax[2], aw[6];
      ax[0] = LB + (64 * d + i16) * 128 + kx; ax[1] = ax[0] ^ 64;
#pragma unroll
      for (int s = 0; s < 6; ++s) aw[s] = awb + ((s ^ awx) * 32);
      unsigned sta = LB + L_LS + qq * 288 + (8 * d + ((i16 >> 1) & 1)) * 16 + (i16 & 1) * 4, csa = LB + L_CS + g * 16;
      asm volatile("" : "+v"(ax[0]), "+v"(ax[1]), "+v"(aw[0]), "+v"(aw[1]), "+v"(aw[2]), "+v"(aw[3]), "+v"(aw[4]), "+v"(aw[5]), "+v"(sta), "+v"(csa));
      s16x8 xf[2][4];
      s16x4 wf[2][12];
      auto rd = [&](int set, int i) {
        if (i < 4) xf[set][i] = lds128(ax[set] + i * 2048);
        else wf[set][i - 4] = ldstr4(aw[(i - 4) >> 1] + set * 8192 + (i & 1) * 1024);
      };
      auto mm = [&](int set, int i) { const int s = i >> 2, m = i & 3; acc[s][m] = mfma(cat(wf[set][2 * s], wf[set][2 * s + 1]), xf[set][m], acc[s][m]); };
      {
        float t[8];
#pragma unroll
        for (int e = 0; e < 8; ++e)
          t[e] = (e & 1) ? sadd(sadd(bfhi(pre[0][e >> 1]), bfhi(pre[1][e >> 1])), sadd(bfhi(pre[2][e >> 1]), bfhi(pre[3][e >> 1])))
                         : sadd(sadd(bflo(pre[0][e >> 1]), bflo(pre[1][e >> 1])), sadd(bflo(pre[2][e >> 1]), bflo(pre[3][e >> 1])));
#pragma unroll
        for (int e = 0; e < 4; ++e) { const u32x2 h = swap32(t[e], t[e + 4]); t[e] = sadd(u2f(h.x), u2f(h.y)); }
#pragma unroll
        for (int e = 0; e < 2; ++e) { const u32x2 h = swap16(t[e], t[e + 2]); t[e] = dpp_add<0x128>(sadd(u2f(h.x), u2f(h.y))); }
        *(LAS f32x2*)(size_t)(LB + L_LS + d * 256 + (4 * (lane & 7) + g) * 8) = (f32x2){t[0], t[1]};
      }
      __builtin_amdgcn_s_setprio(1);
      STAMP(1);
      barrier();
      STAMP(2); STAMPC(21);
#pragma unroll
      for (int i = 0; i < 16; ++i) rd(0, i);
      wait_lgkm0();
      auto stage1 = [&](int c, int dl) {
        SCHED();
#pragma unroll
        for (int i = 0; i < 24; ++i) {
          mm(0, i); SCHED();
          if (i < 16) rd(1, i);
          else if (i < 22) adv(aw[i - 16], dl);
          else if (i == 22) adv(ax[0], dl);
          SCHED();
        }
        TRACE(2 * c);
        barrier();
        TRACE(2 * c + 1);
        SCHED();
      };
      int ph = 0;
#pragma unroll 1
      for (int c = 1; c < 8; ++c) {
        const int dl = ph == 2 ? -2 * (int)SLOT : (int)SLOT;
        ph = ph == 2 ? 0 : ph + 1;
        stage1(c, dl);
#pragma unroll
        for (int i = 0; i < 24; ++i) {
          mm(1, i); SCHED();
          if (i < 16) rd(0, i);
          else if (i == 16) adv(ax[1], dl);
          else if (i == 23) wait_lgkm0();
          SCHED();
        }
      }
      stage1(8, 0);
      unsigned b1a = LB + L_B1 + g * 8;
      asm volatile("" : "+v"(b1a));
      STAMP(4); STAMPC(22);
      f32x2 rs[4], nm[4];
      f32x4 cs[6];
      u32x2 b1v[6];
      f32x2 st[4];
      auto colrd = [&](int s) { cs[s] = ldsf4(csa + 64 * s); b1v[s] = *(LAS u32x2*)(size_t)(b1a + 32 * s); };
#pragma unroll
      for (int i = 0; i < 4; ++i) st[i] = (f32x2){ldsf1(sta + i * 32), ldsf1(sta + i * 32 + 8)};
      colrd(0); colrd(1);
      SCHED();
#pragma unroll
      for (int i = 0; i < BSPLIT; ++i) mm(1, i);
      SCHED();
#pragma unroll
      for (int i = 0; i < 4; ++i) { rs[i] = bco(st[i][0]); nm[i] = bco(-st[i][1]); }
      STAMP(5);
      const unsigned fo = WS_FLAG + (2 * r + d) * 2048;
      pubo = lane ? pubo : 0u;
      u32x4 ob[12];
      const unsigned ho = WS_H + (unsigned)(2 * r + d) * H_MT + (unsigned)(4 * j) * 4096 + lane * 16;
#pragma unroll
      for (int pr = 0; pr < 3; ++pr) {
        Col col;
        mkcol(col, cs[2 * pr], cs[2 * pr + 1], b1v[2 * pr], b1v[2 * pr + 1]);
        if (pr < 2) { colrd(2 * pr + 2); colrd(2 * pr + 3); }
        SCHED();
        if (pr == 1) {
#pragma unroll
          for (int i = BSPLIT; i < 24; ++i) mm(1, i);
          SCHED();
          STAMP(7);
        }
#pragma unroll
        for (int m = 0; m < 4; ++m) {
          ob[4 * pr + m] = gelu8(acc[2 * pr][m], acc[2 * pr + 1][m], col, rs[m], nm[m]);
          __builtin_amdgcn_raw_buffer_store_b128(ob[4 * pr + m], wsr, ho + m * 1024, pr * 4096, HST);
          if (4 * pr + m >= KEEP) asm volatile("" :: "v"(ob[4 * pr + m - KEEP]));
          TRACE2(1 + 4 * pr + m);
          if (pr > 0 && m == (pr == 1 ? BFLAG0 : BFLAG)) {
            if (pr == 1) wait_vm<BFLAG0 + 1>(); else wait_vm<BFLAG + 1>();
            __builtin_amdgcn_raw_buffer_store_b64((u32x2){tagA, tagB}, wsr, pubo, fo + flag_of(j, pr - 1), 16);
          }
          SCHED();
        }
      }
      pubo |= fo + flag_of(j, 2);
      STAMP(6);
    } else {
#pragma unroll 1
      for (int c = 0; c < 9; ++c) barrier();
    }
    prog_wait(PW + (6 + d) * 4, 3);
#pragma unroll
    for (int i = 0; i < 5; ++i) absorb(i, home + i * 4096);
    unsigned v2[4];
    mkv2(v2);
    setup2(6);
#pragma unroll
    for (int i = 0; i < 4; ++i) asm volatile("" : "+v"(v2[i]));
    wait_lgkm0();
    STAMP(8);
    dma_x4(v2, w2b + kstep_of(slot, 5) * 32768, home + 16384);
  };
  auto roleA = [&]() __attribute__((always_inline)) {
    const int srow = 128 * r + 8 * j + 4 * d + g;
    u32x2 dv[4], bv[4];
#pragma unroll
    for (int n = 0; n < 4; ++n) { dv[n] = *(const GAS u32x2*)(p.dsk + ntile * 128 + n * 32 + g * 8); bv[n] = *(const GAS u32x2*)(p.b2 + ntile * 128 + n * 32 + g * 8); }
    if (lane < 8) *(LAS unsigned*)(size_t)(PW + lane * 4) = 0u;
    f32x4 acc[2][4];
#pragma unroll
    for (int m = 0; m < 4; ++m) { acc[0][m] = (f32x4){0, 0, 0, 0}; acc[1][m] = (f32x4){0, 0, 0, 0}; asm volatile("" : "+v"(acc[0][m]), "+v"(acc[1][m])); }
    unsigned ax0 = LB + (64 * d + i16) * 128 + kx, ax1 = ax0 ^ 64, aw0 = awb + ((6 ^ awx) * 32), aw1 = awb + ((7 ^ awx) * 32);
    unsigned sta = LB + L_LS + qq * 288 + (8 * d + ((i16 >> 1) & 1)) * 16 + (i16 & 1) * 4, csa = LB + L_CS + 384 + g * 16;
    asm volatile("" : "+v"(ax0), "+v"(ax1), "+v"(aw0), "+v"(aw1), "+v"(sta), "+v"(csa));
    int ph = 0;
    auto chunk = [&](auto side) {
      s16x8 xf[2][4], wf[2][2];
      if (ADELAY) __builtin_amdgcn_s_sleep(ADELAY);
#pragma unroll
      for (int k = 0; k < 2; ++k) {
        wf[k][0] = ldstr(aw0 + k * 8192, aw0 + k * 8192 + 1024);
        wf[k][1] = ldstr(aw1 + k * 8192, aw1 + k * 8192 + 1024);
#pragma unroll
        for (int m = 0; m < 4; ++m) xf[k][m] = lds128((k ? ax1 : ax0) + m * 2048);
      }
      SCHED();
      const int dl = ph == 2 ? -2 * (int)SLOT : (int)SLOT;
      ph = ph == 2 ? 0 : ph + 1;
      adv(ax0, dl); adv(ax1, dl); adv(aw0, dl); adv(aw1, dl);
      side(8);
      SCHED();
#pragma unroll
      for (int k = 0; k < 2; ++k)
#pragma unroll
        for (int m = 0; m < 4; ++m) {
          acc[0][m] = mfma(wf[k][0], xf[k][m], acc[0][m]); acc[1][m] = mfma(wf[k][1], xf[k][m], acc[1][m]);
          SCHED();
          side(4 * k + m);
          SCHED();
        }
    };
    auto none = [](int) {};
    mktag();
    asm volatile("" : "+v"(pre[3]));
    STAMP(19);
    {
      const float mean = sum32(pre) * (1.f / 512.f);
      float v0 = 0.f, v1 = 0.f;
#pragma unroll
      for (int i = 0; i < 4; ++i)
#pragma unroll
        for (int e = 0; e < 4; ++e) {
          const float d0 = bflo(pre[i][e]) - mean, d1 = bfhi(pre[i][e]) - mean;
          v0 = sfma(d0, d0, v0); v1 = sfma(d1, d1, v1);
        }
      const float rsd = __builtin_amdgcn_rsqf(row16_sum(v0 + v1) * (1.f / 512.f) + 1e-5f);
      if (i16 == 0) __builtin_amdgcn_raw_buffer_store_b128((u32x4){tagA, f2u(rsd), tagB, f2u(mean * rsd)}, wsr, WS_STAT + srow * 16, 0, 16);
    }
    __builtin_amdgcn_s_setprio(APRIO);
    STAMP(1);
    barrier();
    STAMP(2);
    chunk(none);
    if (d) {
      const f32x2 c0 = ldsf2(LB + L_LS + 512 + (lane & 3) * 8);
      if (lane < 4) __builtin_amdgcn_raw_buffer_store_b128((u32x4){tagA, f2u(sadd(c0[0], c0[1])), tagB, 0u}, wsr, WS_BS + j * 1024 + (4 * r + lane) * 16, 0, 16);
    } else if (mlp) {
      const f32x2 c0 = ldsf2(LB + L_LS + (lane & 31) * 8), c1 = ldsf2(LB + L_LS + 256 + (lane & 31) * 8);
      if (lane < 32) __builtin_amdgcn_raw_buffer_store_b128((u32x4){tagA, f2u(sadd(c0[0], c1[0])), tagB, f2u(sadd(c0[1], c1[1]))}, wsr, WS_CS + (j * 16 + r) * 512 + lane * 16, 0, 16);
    }
    barrier();
#pragma unroll 1
    for (int c = 1; c < 3; ++c) { chunk(none); barrier(); }
    chunk(none);
    asm volatile("" : "+v"(dv[0]), "+v"(dv[1]), "+v"(dv[2]), "+v"(dv[3]), "+v"(bv[0]), "+v"(bv[1]), "+v"(bv[2]), "+v"(bv[3]));
    __builtin_amdgcn_s_waitcnt(0x0F70);
    u32x2 xv[2][4];
#pragma unroll
    for (int m = 0; m < 2; ++m)
#pragma unroll
      for (int n = 0; n < 4; ++n) xv[m][n] = *(LAS u32x2*)(size_t)(HB + 3 * SLOT + 12288 + (16 * m + i16) * 128 + (g & 1) * 8 + (((2 * n + (g >> 1)) ^ (i16 >> 1)) * 16));
    barrier();
#pragma unroll 1
    for (int c = 4; c < 6; ++c) { chunk(none); barrier(); }
    f32x2 st[4];
#pragma unroll
    for (int m = 0; m < 4; ++m) st[m] = (f32x2){ldsf1(sta + m * 32), ldsf1(sta + m * 32 + 8)};
    unsigned ra[4], ho = mlp ? WS_H + (unsigned)(2 * r + d) * H_MT + (unsigned)(4 * j + 3) * 4096 + lane * 16 : 0xffff0000u;
    f32x2 rs[4], nm[4];
    chunk([&](int k) {
      if (k < 4) { rs[k] = bco(st[k][0]); nm[k] = bco(-st[k][1]); asm volatile("" : "+v"(rs[k]), "+v"(nm[k])); }
      if (k < 8) return;
#pragma unroll
      for (int n = 0; n < 4; ++n) ra[n] = home + r2l + ((n ^ r2x) * 32);
      asm volatile("" : "+v"(ra[0]), "+v"(ra[1]), "+v"(ra[2]), "+v"(ra[3]), "+v"(ho));
    });
    TRACE(14); barrier(); TRACE(15);
    STAMP(4);
    const f32x4 cs0 = ldsf4(csa), cs1 = ldsf4(csa + 64);
    const u32x2 b10 = *(LAS u32x2*)(size_t)(LB + L_B1 + 192 + g * 8), b11 = *(LAS u32x2*)(size_t)(LB + L_B1 + 224 + g * 8);
    Col col;
    auto take = [&](int i, int pg) {
#pragma unroll
      for (int n = 0; n < 4; ++n) we[i][n] = ldstr(ra[n] + pg * 4096, ra[n] + pg * 4096 + 2048);
    };
    chunk([&](int k) {
      if (k == 2) { mkcol(col, cs0, cs1, b10, b11); asm volatile("" : "+v"(col.bias[0]), "+v"(col.bias[1]), "+v"(col.bias[2]), "+v"(col.bias[3])); }
      if (k >= 3 && k < 6) take(k - 3, k - 3);
    });
    barrier();
    __builtin_amdgcn_s_setprio(2);
    STAMP(5);
    u32x4 oa[4];
#pragma unroll
    for (int m = 0; m < 4; ++m) {
      TRACE2(1 + 2 * m);
      oa[m] = gelu8(acc[0][m], acc[1][m], col, rs[m], nm[m]);
      TRACE2(2 + 2 * m);
      __builtin_amdgcn_raw_buffer_store_b128(oa[m], wsr, ho + m * 1024, 0, HST);
      if (m >= 1) asm volatile("" :: "v"(oa[m - 1]));
      if (m == 0) { take(3, 3); take(4, 4); }
      SCHED();
    }
    STAMP(6);
    prog_wait(PW + (6 + d) * 4, 2);
#pragma unroll
    for (int i = 5; i < 8; ++i) take(i, i - 5);
    u32x4 s2v[2];
#pragma unroll
    for (int m = 0; m < 2; ++m) s2v[m] = *(LAS u32x4*)(size_t)(LB + L_ST2 + (tok + 8 * (mtile & 1) + 4 * d + 2 * m) * 16);
    setup2(8);
    wait_lgkm0();
    asm volatile("" :: "v"(oa[3]));
    prog_set(PW + w * 4, 1);
    snap(false);
#pragma unroll
    for (int m = 0; m < 2; ++m) {
      const u32x4 s2 = s2v[m];
      const f32x2 r2 = bco(u2f(s2.y)), n2 = bco(-u2f(s2.w));
#pragma unroll
      for (int n = 0; n < 4; ++n) {
        f32x2 o[2];
#pragma unroll
        for (int h = 0; h < 2; ++h) {
          const f32x2 xx = {bflo(xv[m][n][h]), bfhi(xv[m][n][h])};
          o[h] = pfma(pfma(xx, r2, n2), (f32x2){bflo(dv[n][h]), bfhi(dv[n][h])}, xx + (f32x2){bflo(bv[n][h]), bfhi(bv[n][h])});
        }
        acc2[n][m] = __builtin_shufflevector(o[0], o[1], 0, 1, 2, 3);
        acc2[n][2 + m] = (f32x4){0, 0, 0, 0};
        asm volatile("" : "+v"(acc2[n][m]), "+v"(acc2[n][2 + m]));
      }
    }
    pubo = mlp && lane == 0 ? WS_FLAG + (2 * r + d) * 2048 + flag_of(j, 3) : pubo;
    seen();
    STAMP(8);
  };

  auto phase2 = [&](auto nm_) __attribute__((always_inline)) {
    STAMP(9);
    constexpr int NM = decltype(nm_)::value, S0 = NM == 8 ? 0 : 5, N = NM == 8 ? 13 : 9;
    const u64 tag = ((u64)tagB << 32) | tagA;
    auto has = [&](int k) { return fl[k < NM ? k : k - NM + S0] == tag; };
    auto need = [&](int k) {
      if (__builtin_expect(!has(k), 0)) {
#pragma unroll 1
        for (int spin = 1; spin < SPIN_CAP; ++spin) {
          COUNT(20);
          snap(k >= NM);
          if (has(k)) break;
          if (!(spin & 31)) {
            const u32x2 f = __builtin_amdgcn_raw_buffer_load_b64(wsr, 0, k < NM ? fm + 8 * k : fs + 8 * (k - NM + S0), 16);
            if (__builtin_amdgcn_readfirstlane(f.x) == tagA && __builtin_amdgcn_readfirstlane(f.y) == tagB) break;
          }
          __builtin_amdgcn_s_sleep(PSLEEP);
        }
      }
      asm volatile("" ::: "memory");
    };
    auto publish = [&]() { __builtin_amdgcn_raw_buffer_store_b64((u32x2){tagA, tagB}, wsr, pubo, 0, 16); };
    auto loadH = [&](int i, int m) -> u32x4 {
      if (m == 0) asm volatile("" : "+v"(hb));
      if ((NOC & 8) && (m & 1)) { u32x4 z; asm volatile("" : "=v"(z)); return z; }
      return __builtin_amdgcn_raw_buffer_load_b128(wsr, hb + (m & 1) * 1024, __builtin_amdgcn_readlane(ksl, i) ^ ((m & 2) * 1024), HAUX);
    };
    u32x4 hf[N][4];
    auto load4 = [&](int i) {
#pragma unroll
      for (int m = 0; m < 4; ++m) hf[i][m] = loadH(i, m);
    };
    STAMP(10);
    if (NM == 8) {
      if (__builtin_expect(!(has(0) && has(1)), 0)) { wait_vm<0>(); publish(); pubo = 0xffff0000u; }
      need(0); load4(0);
      need(1); load4(1);
      wait_vm<8>(); publish();
      __builtin_amdgcn_s_setprio(EPRI);
    } else {
      unsigned vc[8];
      mkvc(vc);
      asm volatile("" : "+v"(vc[0]), "+v"(vc[1]), "+v"(vc[2]), "+v"(vc[3]), "+v"(vc[4]), "+v"(vc[5]), "+v"(vc[6]), "+v"(vc[7]));
      const unsigned o = ntile * 65536 + kg * 128, hs = fresh(HB + (w < 4 ? SLOT : 0));
      __builtin_amdgcn_s_setprio(0);
      dma_x8(vc, p.cre + o, hs);
      wait_vm<8>(); publish();
      snap(false);
      dma_x8(vc, p.cim + o, hs + 8192);
      seen();
      need(0); load4(0);
      need(1); load4(1);
    }
    STAMP(11);
    s16x8 cfr[N - NM][4];
    auto rdc = [&](int q, unsigned base) {
#pragma unroll
      for (int n = 0; n < 4; ++n) {
        unsigned a = base + n * 2048;
        asm volatile("" : "+v"(a));
        cfr[q][n] = cat(*(LAS s16x4*)(size_t)a, *(LAS s16x4*)(size_t)(a + 8192));
      }
    };
    SCHED();
#pragma unroll
    for (int i = 0; i < N; ++i) {
      const int k = i + (i < NM - 2 ? 2 : SDEP);
      if (i == 2) STAMP(12);
      if (!(i & 1) || k >= NM) TRACE4(2 * i + 1);
      if (i == NM - 2) {
        STAMP(13);
#pragma unroll
        for (int e = NM; e < NM + SDEP - 2; ++e) { need(e); load4(e); }
      }
      if (k < NM) {
        if (NM == 6 && i == 0) snap(false);
        else if (NM == 6 && i == 2) { need(2); load4(2); need(3); load4(3); }
        if (!(i & 1) && (NM == 8 || i)) { need(k); need(k + 1); TRACE4(2 * i + 2); if (NM == 6 && i == 2) absorb(5, home + 16384); if (!(ESEP && i == 0)) snap(k + 2 == NM); }
      } else if (k < N) { need(k); TRACE4(2 * i + 2); }
      if (i == NM - 2) STAMP(14);
      if (NM == 8 && i == 1) prog_set(PW + w * 4, 4);
      if (NM == 8 && i == 6) prog_wait(PW + (w | 6) * 4, 4);
      if (NM == 6 && i == 1) prog_set(PW + w * 4, 1);
      if (NM == 8 && i == EOFF) __builtin_amdgcn_s_setprio(0);
      if (NM == 8 && i == PSTEP) { prog_wait(PW + (w ^ 4) * 4, 1); rdc(4, crd - 2 * SLOT); }
      if (NM == 8 && i == PSTEP + 1) { wait_lgkm0(); prog_set(PW + w * 4, 5); }
      if (i >= NM - 1 && i < NM + (NM == 8 ? 3 : 2)) rdc(i + 1 - NM, crd ^ (32 * (i + 1 - NM + (NM == 8 ? 0 : 1))));
      SCHED();
#pragma unroll
      for (int m = 0; m < 4; ++m) {
#pragma unroll
        for (int n = 0; n < 4; ++n)
          if (NM == 8 && i == 0) asm volatile("v_mfma_f32_16x16x32_bf16 %0, %1, %2, %0" : "+v"(acc2[n][m]) : "v"(we[0][n]), "v"(hf[0][m]));
          else acc2[n][m] = mfma(i < NM ? we[i < NM ? i : 0][n] : cfr[i < NM ? 0 : i - NM][n], __builtin_bit_cast(s16x8, hf[i][m]), NM == 6 && i == 0 ? (f32x4){0, 0, 0, 0} : acc2[n][m]);
        SCHED();
        if (k < N && (NM == 8 || i > 1)) hf[k < N ? k : 0][m] = loadH(k, m);
        SCHED();
      }
      if (ESEP && NM == 8 && i < 2) TRACE4(3 + i);
      if (ESEP && NM == 8 && i == 1) snap(false);
      if ((i & 1) && k < NM) seen();
    }
    TRACE4(30);
    if (NM == 6) prog_wait(PW + (w ^ 4) * 4, 5);
  };
  if (__builtin_expect(w >= 6 || w < 2, 1)) {
    if (__builtin_expect(w >= 6, 1)) roleD(); else roleA();
    phase2(std::integral_constant<int, 8>{});
  } else {
    if (w >= 4) roleL(); else roleB();
    phase2(std::integral_constant<int, 6>{});
  }
  STAMP(15);
  int tl = threadIdx.x;
  asm volatile("" : "+v"(tl));
  const int i16t = tl & 15, gt = (tl >> 4) & 3;
#pragma unroll
  for (int m = 0; m < 4; ++m)
#pragma unroll
    for (int n = 0; n < 4; ++n) *(LAS f32x4*)(size_t)(home + ((16 * m + i16t) ^ (w == 1 ? 32 : 0)) * 256 + (((4 * n + gt) ^ i16t) * 16)) = acc2[n][m];
  const int ck = tl & 15;
  const int orow = 32 * ((tl >> 6) & 3) + 16 * ((tl >> 4) & 1) + ((tl >> 5) & 1) + 2 * (tl >> 8) + 8 * (mtile & 1);
  char* op = p.out + (128 * (mtile >> 1) + orow) * 2048 + ntile * 256 + ck * 16;
  unsigned ra[4];
#pragma unroll
  for (int k = 0; k < 4; ++k) ra[k] = LB + (tl >> 4) * 256 + ((ck ^ ((tl >> 4) & 15)) * 16) + k * SLOT;
  asm volatile("" : "+v"(ra[0]), "+v"(ra[1]), "+v"(ra[2]), "+v"(ra[3]), "+v"(op));
  barrier();
  STAMP(16);
  {
    f32x4 pt[2][8];
#pragma unroll
    for (int h = 0; h < 2; ++h)
#pragma unroll
      for (int k = 0; k < 8; ++k) pt[h][k] = ldsf4(ra[k >> 1] + (k & 1) * HALF + h * 8192);
#pragma unroll
    for (int h = 0; h < 2; ++h) {
      const f32x4 s = ((pt[h][0] + pt[h][1]) + (pt[h][2] + pt[h][3])) + ((pt[h][4] + pt[h][5]) + (pt[h][6] + pt[h][7]));
      __builtin_nontemporal_store(s, (GAS f32x4*)(op + h * 8192));
    }
  }
  if (b == 255 && tl == 0) __builtin_amdgcn_raw_buffer_store_b64((u32x2){tagA, tagB}, wsr, WS_EPOCH, 0, 16);
}

extern "C" void kernel_launch(void* const* d_in, const int* in_sizes, int n_in, void* d_out, int out_size, void* d_ws,
                              size_t ws_size, hipStream_t stream) {
  Params p;
  p.x = (const char*)d_in[0]; p.lre = (const char*)d_in[1]; p.lim = (const char*)d_in[2];
  p.bre = (const char*)d_in[3]; p.bim = (const char*)d_in[4]; p.cre = (const char*)d_in[5]; p.cim = (const char*)d_in[6];
  p.logdt = (const char*)d_in[7]; p.dsk = (const char*)d_in[8];
  p.w1 = (const char*)d_in[10]; p.b1 = (const char*)d_in[11]; p.w2 = (const char*)d_in[12]; p.b2 = (const char*)d_in[13];
  p.out = (char*)d_out; p.ws = (char*)d_ws;
  fused_kernel<<<256, 512, 0, stream>>>(p);
}
```

```cpp
#include <hip/hip_runtime.h>
#include <stdint.h>
#include <type_traits>

typedef short s16x4 __attribute__((ext_vector_type(4)));
typedef short s16x8 __attribute__((ext_vector_type(8)));
typedef __bf16 bf16x8 __attribute__((ext_vector_type(8)));
typedef __bf16 bf16x2 __attribute__((ext_vector_type(2)));
typedef float f32x4 __attribute__((ext_vector_type(4)));
typedef float f32x2 __attribute__((ext_vector_type(2)));
typedef unsigned u32x4 __attribute__((ext_vector_type(4)));
typedef unsigned u32x2 __attribute__((ext_vector_type(2)));
typedef int i32x4 __attribute__((ext_vector_type(4)));
typedef unsigned long long u64;
typedef u64 u64x4 __attribute__((ext_vector_type(4)));
typedef u64 u64x8 __attribute__((ext_vector_type(8)));
typedef int i32x8 __attribute__((ext_vector_type(8)));
typedef int i32x16 __attribute__((ext_vector_type(16)));
__device__ i32x8 sbl8(i32x4, int, int) __asm("llvm.amdgcn.s.buffer.load.v8i32");
__device__ i32x16 sbl16(i32x4, int, int) __asm("llvm.amdgcn.s.buffer.load.v16i32");
#define LAS __attribute__((address_space(3)))
#define GAS __attribute__((address_space(1)))
#define DI __device__ __forceinline__

#define STAMPS 0
#define TRACING 0
#define GATHER_AT 4
#define ROT 1
#define TOUCH 7
#define TLBW 0
#define NOFF 0
#define PSLEEP 4
#define BFLAG 2
#define ABAR 0
#define KROT 0
#define LPRIO 3
#define CHK 0
#define BSPLIT 8
#define APRIO 0
#define ADELAY 0
#define BFLAG0 1
#define DEPE 2
#define DEPL 2
#define LB_SLEEP 8
#define CHECK_AT 5
#define CPF 1
#define HAUX 16
#define LSNAP 1
#define BSNAP 1
#define L2T 0
#define L2C 0
#define SDEP 2
#define YPRE 1
#define KEEP 2
#define LSEP 0
#define PSTEP 9
#define NOC 0
#define ESEP 0
#define HST 16
#define EPRI 1
#define EOFF 6
#define WOFF 4
#define CPW 5
#define TST 1
#define TPP 3
#define S3AT 3
#define JM 0
#define ASKIP 0
#define MAP 1

struct Params {
  const char *x, *lre, *lim, *bre, *bim, *cre, *cim, *logdt, *dsk, *w1, *b1, *w2, *b2;
  char *out, *ws;
};

constexpr unsigned WS_EPOCH = 0;
constexpr unsigned WS_STAT = 4096;
constexpr unsigned WS_TOT = WS_STAT + 2048 * 16;
constexpr unsigned WS_CS = WS_TOT + 16 * 2 * 16 * 256;
constexpr unsigned WS_BS = WS_CS + 16 * 16 * 512;
constexpr unsigned WS_FLAG = WS_BS + 16 * 1024;
constexpr unsigned WS_H = WS_FLAG + 32 * 2048;
constexpr unsigned H_MT = 88 * 4096;
constexpr unsigned WS_END = WS_H + 32 * H_MT;
constexpr unsigned WS_STAMP = 32u << 20;
constexpr unsigned WS_B2B = WS_STAMP + (4u << 20);
constexpr unsigned SLOT = 40960, HALF = 20480, SL_W = 16384, SL_B = 32768;
constexpr unsigned L_ST1 = 3 * SLOT + 16384, L_ST2 = L_ST1 + 2048;
constexpr unsigned L_CS = 3 * SLOT + HALF + 16384, L_FLG = L_CS + 512, L_B1 = L_CS + 768, L_BS = L_CS + 1024, L_LS = L_CS + 2048, L_DUMMY = L_CS + 3328;
constexpr unsigned LDS_BYTES = 163840;
constexpr int SPIN_CAP = 40000;
constexpr unsigned CODE_BYTES = 44032;

DI void dma4w(unsigned voff, const char* sbase, unsigned ldsaddr) {
  asm volatile("s_mov_b32 m0, %2\n\ts_nop 0\n\tglobal_load_lds_dword %0, %1" ::"v"(voff), "s"(sbase), "s"(ldsaddr));
}
DI void dma_x4(const unsigned* v, const char* sbase, unsigned ldsaddr, bool low = false) {
  if (low) {
    asm volatile("s_mov_b32 m0, %5\n\ts_setprio 0\n\t"
               "global_load_lds_dwordx4 %0, %4\n\tglobal_load_lds_dwordx4 %1, %4 offset:1024\n\t"
               "global_load_lds_dwordx4 %2, %4 offset:2048\n\tglobal_load_lds_dwordx4 %3, %4 offset:3072\n\ts_setprio 3"
               ::"v"(v[0]), "v"(v[1]), "v"(v[2]), "v"(v[3]), "s"(sbase), "s"(ldsaddr));
    return;
  }
  asm volatile("s_mov_b32 m0, %5\n\ts_nop 0\n\t"
               "global_load_lds_dwordx4 %0, %4\n\tglobal_load_lds_dwordx4 %1, %4 offset:1024\n\t"
               "global_load_lds_dwordx4 %2, %4 offset:2048\n\tglobal_load_lds_dwordx4 %3, %4 offset:3072"
               ::"v"(v[0]), "v"(v[1]), "v"(v[2]), "v"(v[3]), "s"(sbase), "s"(ldsaddr));
}
DI void dma_x8(const unsigned* v, const char* sbase, unsigned ldsaddr) {
  asm volatile("s_mov_b32 m0, %9\n\ts_nop 0\n\t"
               "global_load_lds_dwordx4 %0, %8 offset:-4096\n\tglobal_load_lds_dwordx4 %1, %8 offset:-3072\n\t"
               "global_load_lds_dwordx4 %2, %8 offset:-2048\n\tglobal_load_lds_dwordx4 %3, %8 offset:-1024\n\t"
               "global_load_lds_dwordx4 %4, %8\n\tglobal_load_lds_dwordx4 %5, %8 offset:1024\n\t"
               "global_load_lds_dwordx4 %6, %8 offset:2048\n\tglobal_load_lds_dwordx4 %7, %8 offset:3072"
               ::"v"(v[0]), "v"(v[1]), "v"(v[2]), "v"(v[3]), "v"(v[4]), "v"(v[5]), "v"(v[6]), "v"(v[7]), "s"(sbase), "s"(ldsaddr + 4096u));
}
constexpr int PG(int i) { return i & 3; }
template <int N> DI void wait_vm() { asm volatile("s_waitcnt vmcnt(%0)" ::"i"(N) : "memory"); }
DI void wait_lgkm0() { asm volatile("" ::: "memory"); __builtin_amdgcn_s_waitcnt(0xC07F); asm volatile("" ::: "memory"); }
DI void barrier() {
  asm volatile("" ::: "memory");
  __builtin_amdgcn_s_waitcnt(0xC07F);
  __builtin_amdgcn_s_barrier();
  asm volatile("" ::: "memory");
}
#define SCHED() __builtin_amdgcn_sched_barrier(0)
DI s16x8 lds128(unsigned a) { return *(LAS s16x8*)(size_t)a; }
DI f32x4 ldsf4(unsigned a) { return *(LAS f32x4*)(size_t)a; }
DI f32x2 ldsf2(unsigned a) { return *(LAS f32x2*)(size_t)a; }
DI float ldsf1(unsigned a) { return *(LAS float*)(size_t)a; }
DI s16x4 ldstr4(unsigned a) { return __builtin_amdgcn_ds_read_tr16_b64_v4i16((LAS s16x4*)(size_t)a); }
DI s16x8 cat(s16x4 lo, s16x4 hi) { return __builtin_shufflevector(lo, hi, 0, 1, 2, 3, 4, 5, 6, 7); }
DI s16x8 ldstr(unsigned a0, unsigned a1) { return cat(ldstr4(a0), ldstr4(a1)); }
DI f32x4 mfma(s16x8 a, s16x8 b, f32x4 c) {
  return __builtin_amdgcn_mfma_f32_16x16x32_bf16(__builtin_bit_cast(bf16x8, a), __builtin_bit_cast(bf16x8, b), c, 0, 0, 0);
}
DI unsigned pk2(float a, float b) { f32x2 v = {a, b}; return __builtin_bit_cast(unsigned, __builtin_convertvector(v, bf16x2)); }
DI float u2f(unsigned u) { return __builtin_bit_cast(float, u); }
DI unsigned f2u(float u) { return __builtin_bit_cast(unsigned, u); }
DI float bflo(unsigned u) { return __builtin_bit_cast(float, u << 16); }
DI float bfhi(unsigned u) { return __builtin_bit_cast(float, u & 0xffff0000u); }
template <int CTRL> DI float dpp_add(float v) {
  return v + __builtin_bit_cast(float, __builtin_amdgcn_update_dpp(0, __builtin_bit_cast(int, v), CTRL, 0xf, 0xf, true));
}
DI float row16_sum(float v) {
  v = dpp_add<0xB1>(v);
  v = dpp_add<0x4E>(v);
  v = dpp_add<0x141>(v);
  v = dpp_add<0x140>(v);
  return v;
}
DI float smul(float a, float b) { float d; asm("v_mul_f32 %0, %1, %2" : "=v"(d) : "v"(a), "v"(b)); return d; }
DI float sadd(float a, float b) { float d; asm("v_add_f32 %0, %1, %2" : "=v"(d) : "v"(a), "v"(b)); return d; }
DI float sfma(float a, float b, float c) { float d; asm("v_fma_f32 %0, %1, %2, %3" : "=v"(d) : "v"(a), "v"(b), "v"(c)); return d; }
DI float sfms(float a, float b, float c) { float d; asm("v_fma_f32 %0, %1, %2, -%3" : "=v"(d) : "v"(a), "v"(b), "v"(c)); return d; }
DI void zero4(f32x4& v) {
  float a, b, c, d;
  asm volatile("v_mov_b32 %0, 0\n\tv_mov_b32 %1, 0\n\tv_mov_b32 %2, 0\n\tv_mov_b32 %3, 0" : "=v"(a), "=v"(b), "=v"(c), "=v"(d));
  v = (f32x4){a, b, c, d};
}
DI u32x2 swap32(float a, float b) { asm volatile("s_nop 1" : "+v"(a), "+v"(b)); return __builtin_amdgcn_permlane32_swap(f2u(a), f2u(b), false, false); }
DI u32x2 swap16(float a, float b) { asm volatile("s_nop 1" : "+v"(a), "+v"(b)); return __builtin_amdgcn_permlane16_swap(f2u(a), f2u(b), false, false); }
DI f32x2 pfma(f32x2 a, f32x2 b, f32x2 c) { return __builtin_elementwise_fma(a, b, c); }
DI f32x2 bc(float a) { return (f32x2){a, a}; }
DI f32x2 bco(float a) { f32x2 v = {a, a}; asm("" : "+v"(v)); return v; }
DI f32x2 cmul(f32x2 a, f32x2 b) { return (f32x2){sfms(a[0], b[0], smul(a[1], b[1])), sfma(a[0], b[1], smul(a[1], b[0]))}; }
DI f32x2 cfma(f32x2 a, f32x2 b, f32x2 c) { return (f32x2){sfms(a[0], b[0], sfms(a[1], b[1], c[0])), sfma(a[0], b[1], sfma(a[1], b[0], c[1]))}; }
DI f32x2 csel(bool p, f32x2 a, f32x2 b) { return (f32x2){p ? a[0] : b[0], p ? a[1] : b[1]}; }
DI void prog_set(unsigned a, unsigned v) {
  asm volatile("s_mov_b64 exec, 1\n\tds_write_b32 %0, %1\n\ts_mov_b64 exec, -1" ::"v"(a), "v"(v) : "memory");
}
DI void prog_wait(unsigned a, unsigned v) {
#pragma unroll 1
  for (int spin = 0; spin < SPIN_CAP * 16; ++spin) {
    if (__builtin_amdgcn_readfirstlane(*(volatile LAS unsigned*)(size_t)a) >= v) break;
    __builtin_amdgcn_s_sleep(1);
  }
  asm volatile("" ::: "memory");
}
DI void bar() { asm volatile("" ::: "memory"); __builtin_amdgcn_s_barrier(); asm volatile("" ::: "memory"); }
DI void dma1_dev(unsigned voff, const char* sbase, unsigned ldsaddr) {
  asm volatile("s_mov_b32 m0, %2\n\ts_nop 0\n\tglobal_load_lds_dwordx4 %0, %1 sc1" ::"v"(voff), "s"(sbase), "s"(ldsaddr) : "memory");
}
DI void dma2_dev(unsigned voff, const char* s0, const char* s1, unsigned ldsaddr) {
  asm volatile("s_mov_b32 m0, %3\n\ts_nop 0\n\tglobal_load_lds_dwordx4 %0, %1 sc1\n\tglobal_load_lds_dwordx4 %0, %2 offset:2048 sc1" ::"v"(voff), "s"(s0), "s"(s1), "s"(ldsaddr) : "memory");
}
DI void dma4_dev(unsigned voff, const char* s0, const char* s1, unsigned ldsaddr) {
  asm volatile("s_mov_b32 m0, %3\n\ts_nop 0\n\tglobal_load_lds_dwordx4 %0, %1 sc1\n\tglobal_load_lds_dwordx4 %0, %1 offset:1024 sc1\n\t"
               "global_load_lds_dwordx4 %0, %2 offset:2048 sc1\n\tglobal_load_lds_dwordx4 %0, %2 offset:3072 sc1" ::"v"(voff), "s"(s0), "s"(s1), "s"(ldsaddr) : "memory");
}
DI int kstep_of(int slot, int i) {
  const int q = slot + 4 * i + (slot >= 4 ? 28 : 0), cls = (q >= 14) + (q >= 28) + (q >= 42);
  return 4 * (q - 14 * cls) + ((0x9C >> (2 * cls)) & 3);
}
DI unsigned flag_of(int j, int c) {
  const int q = 14 * ((0x78 >> (2 * c)) & 3) + j;
  return q < 32 ? (q & 3) * 128 + (q >> 2) * 8 : 512 + (q & 3) * 128 + ((q - 32) >> 2) * 8;
}
DI void cnt_add(unsigned a) {
  asm volatile("s_mov_b64 exec, 1\n\tds_add_u32 %0, %1\n\ts_mov_b64 exec, -1" ::"v"(a), "v"(1u) : "memory");
}
DI f32x2 lo2(f32x4 v) { return __builtin_shufflevector(v, v, 0, 1); }
DI f32x2 hi2(f32x4 v) { return __builtin_shufflevector(v, v, 2, 3); }
DI void rows4(float v, float* o) {
  asm volatile("s_nop 1" : "+v"(v));
  const unsigned u = f2u(v);
  const u32x2 h = __builtin_amdgcn_permlane32_swap(u, u, false, false);
  const u32x2 a = __builtin_amdgcn_permlane16_swap(h.x, h.x, false, false), b = __builtin_amdgcn_permlane16_swap(h.y, h.y, false, false);
  o[0] = u2f(a.x); o[1] = u2f(a.y); o[2] = u2f(b.x); o[3] = u2f(b.y);
}
struct Col { f32x2 cs[4], bias[4]; };
DI void mkcol(Col& c, f32x4 cs0, f32x4 cs1, u32x2 b0, u32x2 b1) {
  c.cs[0] = lo2(cs0); c.cs[1] = hi2(cs0); c.cs[2] = lo2(cs1); c.cs[3] = hi2(cs1);
  c.bias[0] = (f32x2){bflo(b0.x), bfhi(b0.x)}; c.bias[1] = (f32x2){bflo(b0.y), bfhi(b0.y)};
  c.bias[2] = (f32x2){bflo(b1.x), bfhi(b1.x)}; c.bias[3] = (f32x2){bflo(b1.y), bfhi(b1.y)};
}
DI u32x4 gelu8(f32x4 a0, f32x4 a1, const Col& c, f32x2 rs, f32x2 nmrs) {
  f32x2 v[4], z[4];
  u32x4 o;
#define ST4(stmt) _Pragma("unroll") for (int h = 0; h < 4; ++h) { stmt; } SCHED();
  ST4(v[h] = pfma(c.cs[h], nmrs, c.bias[h]))
  ST4(v[h] = pfma(h == 0 ? lo2(a0) : h == 1 ? hi2(a0) : h == 2 ? lo2(a1) : hi2(a1), rs, v[h]))
  ST4(z[h] = v[h] * v[h])
  ST4(z[h] = pfma(z[h], bc(-0.10294324f), bc(-2.30220820f)))
  ST4(z[h] = z[h] * v[h])
  ST4((z[h] = (f32x2){__builtin_amdgcn_exp2f(z[h][0]), __builtin_amdgcn_exp2f(z[h][1])}))
  ST4(z[h] = z[h] + bc(1.f))
  ST4((z[h] = (f32x2){__builtin_amdgcn_rcpf(z[h][0]), __builtin_amdgcn_rcpf(z[h][1])}))
  ST4(v[h] = v[h] * z[h])
  ST4(o[h] = pk2(v[h][0], v[h][1]))
#undef ST4
  return o;
}

#define TRACE(i) do {} while (0)
#define TRACE2(i) do {} while (0)
#define TRACE3(i) do {} while (0)
#define TRACE4(i) do {} while (0)
#define STAMP(k) do {} while (0)
#define STAMPC(k) do {} while (0)
#define COUNT(k) do {} while (0)

__global__ void __launch_bounds__(512, 2) fused_kernel(Params p) {
  __shared__ __attribute__((aligned(16))) char lds_mem[LDS_BYTES];
  unsigned long long pc;
  asm volatile("s_getpc_b64 %0" : "=s"(pc));
  const unsigned LB = (unsigned)(size_t)(LAS char*)lds_mem;
  const int tid = threadIdx.x, lane = tid & 63;
  const int w = (__builtin_amdgcn_readfirstlane(tid >> 6) + WOFF) & 7;
  if (w == CPW) {
    const unsigned co = ((unsigned)(lane & 15) * 32u + (blockIdx.x >> 3)) * 128u;
    dma4w(co < CODE_BYTES ? co : 0u, (const char*)(pc & ~255ull), LB + L_DUMMY + 512);
  }
  const int b = blockIdx.x, idx = b >> 3, xcd = b & 7;
  const int r = 8 * (xcd >> 2) + (idx >> 2), j = 4 * (xcd & 3) + (idx & 3);
  const int rot = (2 * (xcd & 3) + 4 * (xcd >> 2)) & 7;
  const int mtile = 4 * xcd + (idx >> 3), ntile = (idx + NOFF) & 7;
  const int i16 = lane & 15, g = lane >> 4, qq = i16 >> 2, pp = i16 & 3;
  const int d = w & 1;
  const int jm = j < 14 ? j : 13, lq = j & 1;
  const bool mlp = j < 14;
  u32x4 pre[4];
  unsigned v_lre, v_lim, v_ldt;
  if ((w >> 1) == 2) {
    const unsigned po = (unsigned)(32 * j + 16 * d + i16) * 2u;
    v_lre = *(const GAS unsigned short*)(p.lre + po); v_lim = *(const GAS unsigned short*)(p.lim + po); v_ldt = *(const GAS unsigned short*)(p.logdt + po);
    asm volatile("" ::: "memory");
  }
  u32x4 prb[2];
  if (w < 4) {
    const bool isb = w >= 2;
    const char* pb = isb ? p.w1 + (64 * (r & 7) + 32 * d) * 3584 + jm * 256 + (r >> 3) * 128 : p.x + (128 * r + 8 * j + 4 * d) * 1024;
    const unsigned po = isb ? (lane >> 3) * 3584 + (lane & 7) * 16 : g * 1024 + i16 * 16, ps = isb ? 28672 : 256;
#pragma unroll
    for (int i = 0; i < 4; ++i) pre[i] = *(const GAS u32x4*)(pb + i * ps + po);
    if (isb) {
#pragma unroll
      for (int i = 0; i < 2; ++i) prb[i] = *(const GAS u32x4*)((r & 8 ? p.bim : p.bre) + (32 * j + 4 * (r & 7) + 2 * d) * 1024 + i * 512 + (lane >> 5) * 1024 + (lane & 31) * 16);
    }
    asm volatile("" ::: "memory");
  }
  const int kgo = 2 * (idx >> 3), late = ((w + 2) >> 2) & 1, kg = 2 * (((w & 2) + d + KROT * ntile) & 3) + late;
  auto slot_of = [&](int role) { return (((role + 1) & 2) << 1) + (((((role + 1) & 2) ? 0 : kgo) + ((role & 1) ? 0 : 2) + d) & 3); };
  const int slot = slot_of(w >> 1);
  const u32x2 ep = *(const __attribute__((address_space(4))) u32x2*)(p.ws + WS_EPOCH);
  unsigned tagA, tagB;
  auto mktag = [&]() { tagA = ep.x; tagB = ep.y; asm volatile("" : "+s"(tagA), "+s"(tagB)); tagA += 1u; tagB += 1u; };
  const __amdgpu_buffer_rsrc_t wsr = __builtin_amdgcn_make_buffer_rsrc(p.ws, 0, WS_END, 0x00020000);
  const i32x4 wss = {(int)(size_t)p.ws, (int)((size_t)p.ws >> 32) & 0xffff, (int)WS_END, 0x00020000};
  auto fresh = [](int o) { asm volatile("" : "+s"(o) :: "memory"); return o; };
  const unsigned HB = LB + d * HALF;
  const unsigned PW = LB + L_FLG;
  s16x8 we[8][4];
  f32x4 acc2[4][4];
  const unsigned home = HB + ((2 - (w >> 1)) & 3) * SLOT;
  const unsigned r2l = (4 * g + qq) * 128 + pp * 8, r2x = (2 * g + (qq >> 1)) & 3;
  auto absorb = [&](int i, unsigned page) {
#pragma unroll
    for (int n = 0; n < 4; ++n) { const unsigned a = page + r2l + ((n ^ r2x) * 32); we[i][n] = ldstr(a, a + 2048); }
  };
  auto mkvc = [&](unsigned* vc) {
#pragma unroll
    for (int i = 0; i < 8; ++i) vc[i] = (lane >> 3) * 1024 + (((lane & 7) ^ (4 * (i & 1) + g)) * 16) + 4096 + i * 7168;
  };
  auto mkv2 = [&](unsigned* v2) {
#pragma unroll
    for (int i = 0; i < 4; ++i) v2[i] = (lane >> 3) * 1024 + (((((lane & 7) >> 1) ^ g) << 1) | (lane & 1)) * 16 + i * 7168;
  };
  auto loadC = [&](const unsigned* vc, int kgc, unsigned dst) {
    const unsigned o = ntile * 65536 + kgc * 128;
    dma_x8(vc, p.cre + o, dst);
    dma_x8(vc, p.cim + o, dst + 8192);
  };
  const char* w2b = p.w2 + ntile * 128;
  auto adv = [&](unsigned& a, int dl) { a += dl; asm volatile("" : "+v"(a)); };
  const int tok = 32 * qq + 16 * (i16 & 1) + ((i16 >> 1) & 1);
  const unsigned kx = (g ^ (i16 >> 1)) * 16;
  const unsigned awb = LB + SL_W + (8 * g + qq) * 256 + pp * 8, awx = qq | ((g & 1) << 2);
  auto sum32 = [&](const u32x4* v) {
    float s0 = 0.f, s1 = 0.f;
#pragma unroll
    for (int i = 0; i < 4; ++i)
#pragma unroll
      for (int e = 0; e < 4; ++e) { s0 += bflo(v[i][e]); s1 += bfhi(v[i][e]); }
    return row16_sum(s0 + s1);
  };
  auto touch = [&]() {
    dma4w(d ? (lane & 15) * 128 : (lane < 56 ? lane : 0) * 128, d ? p.cre + b * 2048 : p.w2 + b * 7168, LB + L_DUMMY + d * 256);
    if (d) dma4w((lane & 15) * 128, p.cim + b * 2048, LB + L_DUMMY + d * 256);
  };
  auto zero2 = [&]() {
#pragma unroll
    for (int n = 0; n < 4; ++n) {
#pragma unroll
      for (int m = 0; m < 4; ++m) acc2[n][m] = (f32x4){0, 0, 0, 0};
      asm volatile("" : "+v"(acc2[n][0]), "+v"(acc2[n][1]), "+v"(acc2[n][2]), "+v"(acc2[n][3]));
    }
  };
  int ksl;
  unsigned hb, crd, pubo = 0xffff0000u;
  const int fm = WS_FLAG + mtile * 2048 + slot * 128, fs = WS_FLAG + mtile * 2048 + 1024 + (kg >> 1) * 64;
  u64x8 fl;
  auto snap = [&](bool ssm) { fl = __builtin_bit_cast(u64x8, sbl16(wss, fresh(ssm ? fs : fm), 1)); SCHED(); };
  auto seen = [&]() { SCHED(); asm volatile("" : "+s"(fl)); };
  auto setup2 = [&](int nm) {
    ksl = (lane < nm ? kstep_of(slot, lane) : 56 + 4 * kg + late + ((lane - nm) & 7)) * 4096;
    if (NOC & 16) ksl = (w * 11 + (lane & 1)) * 4096;
    ksl += w == 1 ? 2048 : 0;
    hb = WS_H + (unsigned)mtile * H_MT + lane * 16;
    crd = home + i16 * 128 + (g & 1) * 8 + (((g >> 1) ^ (i16 >> 1)) * 16);
    asm volatile("" : "+v"(ksl), "+v"(hb), "+v"(crd));
  };

  auto roleD = [&]() __attribute__((always_inline)) {
    __builtin_amdgcn_s_waitcnt(0x0F70);
    __builtin_amdgcn_s_setprio(3);
    unsigned vx[8], vw[8], vb[4], v2[4];
#pragma unroll
    for (int i = 0; i < 8; ++i) vw[i] = g * 3584 + ((((i16 >> 1) ^ (g | (((i >> 1) & 1) << 2))) << 1) | (i16 & 1)) * 16 + 4096 + i * (14336 - 1024);
    const char* wb = p.w1 + jm * 256 + d * (32 * 3584);
    auto part1 = [&](int c, unsigned slot) { if (mlp) dma_x8(vw, wb + ((c + rot) & 7) * (64 * 3584), LB + slot + SL_W + d * 8192); };
    asm volatile("" :: "s"(wb));
    STAMP(18);
    part1(0, 0);
#pragma unroll
    for (int i = 0; i < 8; ++i)
      vx[i] = (32 * (g >> 1) + 16 * ((lane >> 3) & 1) + (g & 1)) * 1024 + (((lane & 7) ^ (4 * (i & 1) + g)) * 16) + 4096 + (i & 1) * 65536 + (i >> 1) * 2048 - i * 1024;
#pragma unroll
    for (int i = 0; i < 4; ++i) vb[i] = (lane >> 3) * 1024 + (((lane & 7) ^ (4 * (i & 1) + g)) * 16) + i * 7168;
    const char* xb = p.x + (r * 128 + d * 8) * 1024;
    const char* bb = (d ? p.bim : p.bre) + j * 32768;
    auto partx = [&](int c, unsigned slot) { dma_x8(vx, xb + ((c + rot) & 7) * 128, LB + slot + d * 8192); };
    auto partb = [&](int c, unsigned slot) { dma_x4(vb, bb + ((c + rot) & 7) * 128, LB + slot + SL_B + d * 4096); };
    auto part0 = [&](int c, unsigned slot) { partx(c, slot); partb(c, slot); };
    mkv2(v2);
    unsigned ra[4];
#pragma unroll
    for (int n = 0; n < 4; ++n) ra[n] = home + r2l + ((n ^ r2x) * 32);
    int kt = kstep_of(slot_of((lane >> 3) & 3), lane & 7) * 32768;
    asm volatile("" : "+v"(kt), "+v"(ra[0]), "+v"(ra[1]), "+v"(ra[2]), "+v"(ra[3]), "+v"(v2[0]), "+v"(v2[1]), "+v"(v2[2]), "+v"(v2[3]));
    bool low = false;
    auto page = [&](int role, int i, int grp, int pg) { if ((NOC & 4) && low) return; dma_x4(v2, w2b + (unsigned)__builtin_amdgcn_readlane(kt, 8 * role + i), HB + grp * SLOT + pg * 4096, low); };
    auto take = [&](int i) {
      SCHED();
#pragma unroll
      for (int n = 0; n < 4; ++n) we[i][n] = ldstr(ra[n] + PG(i) * 4096, ra[n] + PG(i) * 4096 + 2048);
      SCHED();
    };
    auto own = [&](int i) { page(3, i, 3, PG(i)); };
    unsigned l16 = lane * 16, la = home + 12288 + lane * 16, lb = LB + L_ST1 + d * 1024 + lane * 16, lc = LB + L_BS + d * 512 + (lane & 31) * 16;
    unsigned wa = LB + L_CS + d * 256 + (lane & 31) * 8 + (lane >> 5) * 4, wb2 = LB + L_LS + (2 * d + (lane >> 5)) * 288 + i16 * 16 + (g & 1) * 4;
    asm volatile("" : "+v"(l16), "+v"(la), "+v"(lb), "+v"(lc), "+v"(wa), "+v"(wb2));
    auto gather = [&](bool bs) {
      const char* cp = p.ws + WS_CS + jm * 8192 + d * 4096;
      dma4_dev(l16, cp, cp, home + 12288);
      dma2_dev(l16, p.ws + WS_STAT + r * 2048 + d * 1024, p.ws + WS_STAT + (mtile >> 1) * 2048 + d * 1024 - 2048, LB + L_ST1 + d * 1024);
      if (bs) dma1_dev(l16, p.ws + WS_BS + j * 1024, LB + L_BS);
    };
    auto check = [&](auto between) -> bool {
      auto ld16 = [](unsigned a) { return *(volatile LAS u32x4*)(size_t)a; };
      auto bad = [&](unsigned a, u32x4 v) { return __builtin_amdgcn_bitop3_b32(v.z, tagB, __builtin_amdgcn_bitop3_b32(v.x, tagA, a, 0xBE), 0xBE); };
      const u32x4 c0 = ld16(la), c1 = ld16(la + 1024), c2 = ld16(la + 2048), c3 = ld16(la + 3072), s1 = ld16(lb), s2 = ld16(lb + 2048), s3 = ld16(lc);
      SCHED(); between(); SCHED();
      const u32x2 h = swap32(sadd(sadd(u2f(c0.y), u2f(c1.y)), sadd(u2f(c2.y), u2f(c3.y))), sadd(sadd(u2f(c0.w), u2f(c1.w)), sadd(u2f(c2.w), u2f(c3.w))));
      *(LAS float*)(size_t)wa = sadd(u2f(h.x), u2f(h.y));
      *(LAS unsigned*)(size_t)wb2 = s1.y;
      *(LAS unsigned*)(size_t)(wb2 + 8) = s1.w;
      const unsigned e = bad(bad(bad(bad(bad(bad(bad(0u, c0), c1), c2), c3) & (mlp ? ~0u : 0u), s1), s2), s3);
      return __builtin_amdgcn_ballot_w64(e != 0) == 0;
    };
    const unsigned me = PW + w * 4;
    zero2();
    setup2(8);
    auto landed = [&]() { if (mlp) wait_vm<20>(); else wait_vm<12>(); };
    SCHED();
    part0(0, 0);
    if (TOUCH == 4) touch();
    part1(1, SLOT); part0(1, SLOT);
    STAMP(1);
    mktag();
    if (mlp) wait_vm<20 + (TOUCH == 4)>(); else wait_vm<12 + (TOUCH == 4)>();
    STAMP(19);
    bar();
    STAMP(2);
    part1(2, 2 * SLOT); part0(2, 2 * SLOT); landed(); if (TOUCH == 1) touch();
    bar();
    if (d) dma4w(lane * 4, p.b1 + jm * 256, LB + L_B1);
    part1(3, 0); part0(3, 0); landed();
    unsigned vt = (lane < 56 ? lane : 0) * 128;
    asm volatile("" : "+v"(vt));
    auto l2t = [&](int t) { if (t < L2T) dma4w(vt, p.w2 + (t * 64 + 2 * idx + d) * 7168, LB + L_DUMMY + d * 256); };
    auto l2c = [&](int t) { if (t < L2C) dma4w(l16 * 8, (t ? p.cim : p.cre) + (2 * idx + d) * 8192, LB + L_DUMMY + d * 256); };
    auto s3 = [&]() { if (L2T < 4) dma4w((lane < 28 ? lane : 0) * 1024, p.w2 + (2 * idx + d) * 28672 + ((3u - ((unsigned)(size_t)p.w2 >> 7)) & 7u) * 128, LB + L_DUMMY + d * 256); };
    if (S3AT == 1) s3();
    l2t(0);
    bar();
    STAMPC(21);
    dma_x4(vx, p.x + (128 * (mtile >> 1) + 8 * (mtile & 1) + 4 * d) * 1024 + ntile * 128 - 4096, home + 12288);
    part1(4, SLOT); part0(4, SLOT); landed(); if (S3AT == 2) s3();
    bar();
    own(0); part1(5, 2 * SLOT); part0(5, 2 * SLOT); landed(); if (S3AT == 3) s3();
    bar();
    gather(d); part1(6, 0); take(0); part0(6, 0); landed(); if (S3AT == 4) s3();
    bar();
    part1(7, SLOT);
    const bool ok = check([&]() { partx(7, SLOT); });
    SCHED();
    partb(7, SLOT);
    if (__builtin_expect(!ok, 0)) {
#pragma unroll 1
      for (int spin = 0; spin < SPIN_CAP; ++spin) {
        COUNT(20);
        __builtin_amdgcn_s_sleep(10);
        gather(true);
        wait_vm<0>();
        if (check([]() {})) break;
      }
    }
    landed();
    barrier();
    STAMP(3); STAMPC(22);
    page(0, 0, 2, 0); page(0, 1, 2, 1); page(0, 2, 2, 2); page(0, 3, 2, 3); page(0, 4, 2, 4); own(1);
    wait_vm<12>();
    bar();
    STAMP(4);
    own(2); page(2, 0, 0, 0); page(2, 1, 0, 1); page(2, 2, 0, 2); page(2, 3, 0, 3); page(2, 4, 0, 4);
    wait_vm<20>();
    STAMP(5);
    bar();
    TRACE4(31);
    low = true;
    STAMP(6);
    take(1); take(2);
    page(0, 5, 2, 0); page(0, 6, 2, 1); page(0, 7, 2, 2);
    wait_vm<12>(); prog_set(me, 1);
    own(3); own(4); wait_lgkm0(); own(5); own(6);
    wait_vm<16>(); prog_set(me, 2);
    page(1, 0, 1, 0); page(1, 1, 1, 1); page(1, 2, 1, 2);
    wait_vm<16>(); take(3); take(4); take(5); wait_lgkm0();
    own(7);
    page(1, 3, 1, 3); page(1, 4, 1, 4);
    auto c8 = [&](const char* src, unsigned dst) { if (NOC & 1) return; dma_x4(vb, src, dst, true); dma_x4(vb, src + 32768, dst + 4096, true); };
    const unsigned co = ntile * 65536 + kg * 128;
    c8(p.cre + co, home);
    wait_vm<8>();
    prog_set(me, 3);
    take(6); take(7);
    prog_wait(PW + d * 4, 1);
    c8(p.cim + co, home + 8192);
    STAMP(7);
    snap(false);
    c8(p.cre + (co ^ 512), HB + 2 * SLOT); c8(p.cim + (co ^ 512), HB + 2 * SLOT + 8192);
    seen();
    STAMP(8);
  };
  auto roleL = [&]() __attribute__((always_inline)) {
    if (TOUCH == 0) touch();
    if (TLBW && d) { dma4w(0, p.cre + ntile * 65536, LB + L_DUMMY + 256); dma4w(0, p.cim + ntile * 65536, LB + L_DUMMY + 256); dma4w((lane & 1) * 0x1bf000, p.w2, LB + L_DUMMY + 256); }
    f32x4 acr[8], aci[8];
#pragma unroll
    for (int m = 0; m < 8; ++m) { acr[m] = (f32x4){0, 0, 0, 0}; aci[m] = (f32x4){0, 0, 0, 0}; asm volatile("" : "+v"(acr[m]), "+v"(aci[m])); }
    unsigned ax[2], ab[2];
    ax[0] = LB + i16 * 128 + kx; ax[1] = ax[0] ^ 64;
    ab[0] = LB + SL_B + (16 * d + i16) * 128 + kx; ab[1] = ab[0] ^ 64;
    unsigned sa = LB + L_LS + g * 288, csa = LB + L_BS + (16 * d + i16) * 16 + 4;
    asm volatile("" : "+v"(sa), "+v"(csa), "+v"(ax[0]), "+v"(ax[1]), "+v"(ab[0]), "+v"(ab[1]));
    s16x8 xf[2][8], bf[2][2];
    auto rd = [&](int set, int i) {
      if (i < 2) bf[set][i] = lds128(ab[set] + i * 4096);
      else xf[set][i - 2] = lds128(ax[set] + (i - 2) * 2048);
    };
    auto mm = [&](int set, int i) {
      if (i & 1) aci[i >> 1] = mfma(xf[set][i >> 1], bf[set][1], aci[i >> 1]);
      else acr[i >> 1] = mfma(xf[set][i >> 1], bf[set][0], acr[i >> 1]);
    };
    asm volatile("" : "+v"(v_lre), "+v"(v_lim), "+v"(v_ldt));
    const float lr = bflo(v_lre), li = bflo(v_lim);
    const float dt = bflo(pk2(expf(bflo(v_ldt)), 0.f));
    const float ang = smul(dt, li);
    const float rv = smul(ang, 0.15915494f);
    const float rev = (rv - __builtin_rintf(rv)) + sfma(ang, 6.4206382e-9f, sfms(ang, 0.15915494f, rv));
    float sn = __builtin_amdgcn_sinf(rev), cn = __builtin_amdgcn_cosf(rev);
    asm volatile("s_nop 0" : "+v"(sn), "+v"(cn));
    const float ex = expf(smul(dt, lr));
    f32x2 a1 = {smul(ex, cn), smul(ex, sn)};
    const float nr = a1[0] - 1.f, ni = a1[1], iden = 2.f / sfma(lr, lr, smul(li, li));
    f32x2 kk = {smul(sfma(nr, lr, smul(ni, li)), iden), smul(sfms(ni, lr, smul(nr, li)), iden)};
    f32x2 t = cmul(a1, a1); t = cmul(t, t); t = cmul(t, t);
    f32x2 a16 = cmul(t, t);
    f32x2 a32 = cmul(a16, a16);
    const f32x2 a64 = cmul(a32, a32), a128 = cmul(a64, a64), a256 = cmul(a128, a128);
    f32x2 a512 = cmul(a256, a256);
    const f32x2 one = {1.f, 0.f};
    f32x2 aseg = cmul(kk, cmul(csel(g & 1, a32, one), csel(g & 2, a64, one)));
    const int e4 = (r - 1 - g) & 3;
    f32x2 acar = cmul(csel(e4 & 1, a128, one), csel(e4 & 2, a256, one));
    const f32x2 ar = bco(a1[0]), ai = bco(a1[1]);
    asm volatile("" :: "v"(ar), "v"(ai));
    asm volatile("" : "+v"(kk), "+v"(a16), "+v"(a32), "+v"(a512), "+v"(aseg), "+v"(acar));
    const unsigned on = (qq == g) ? ((pp & 1) ? 0xffff0000u : 0xffffu) : 0u;
    unsigned m0 = (pp & 2) ? 0u : on, m1 = (pp & 2) ? on : 0u;
    auto scan = [&](f32x2 h0, f32x2 h1, const f32x2* ur, const f32x2* ui, auto out) {
      const u32x4 z0 = {m0 & 0x3f803f80u, m1 & 0x3f803f80u, 0u, 0u}, z1 = {0u, 0u, z0.x, z0.y};
      const f32x4 zz = {0, 0, 0, 0};
      f32x2 hr = {h0[0], h1[0]}, hi = {-h0[1], -h1[1]};
      u32x4 fr[4], fi[4];
      f32x4 tr[2], ti[2];
      SCHED();
#pragma unroll
      for (int mp = 0; mp < 5; ++mp) {
        if (mp > 0) {
#pragma unroll
          for (int k = 0; k < 2; ++k) {
            tr[k] = mfma(__builtin_bit_cast(s16x8, fr[mp - 1]), __builtin_bit_cast(s16x8, k ? z1 : z0), zz);
            ti[k] = mfma(__builtin_bit_cast(s16x8, fi[mp - 1]), __builtin_bit_cast(s16x8, k ? z1 : z0), zz);
          }
          SCHED();
        }
        if (mp < 4) {
#pragma unroll
          for (int e = 0; e < 4; ++e) {
            const int s = 4 * mp + e;
            const f32x2 t1 = ur ? pfma(hr, -ai, ui[s]) : hr * -ai; SCHED();
            const f32x2 t0 = ur ? pfma(hi, ai, ur[s]) : hi * ai; SCHED();
            hi = pfma(hi, ar, t1); SCHED();
            hr = pfma(hr, ar, t0); SCHED();
            fi[mp][e] = pk2(hi[0], hi[1]); SCHED();
            fr[mp][e] = pk2(hr[0], hr[1]); SCHED();
          }
        }
        if (mp > 0) { out(2 * mp - 2, tr[0], ti[0]); out(2 * mp - 1, tr[1], ti[1]); SCHED(); }
      }
    };
    auto pack = [](f32x4 a, f32x4 b) { return (u32x4){pk2(a[0], a[1]), pk2(a[2], a[3]), pk2(b[0], b[1]), pk2(b[2], b[3])}; };
    u32x4 yb[8];
    auto resp = [&]() { scan(aseg, cmul(a16, aseg), nullptr, nullptr, [&](int q, f32x4 a, f32x4 b) { yb[q] = pack(a, b); asm volatile("" : "+v"(yb[q])); }); };
    if (YPRE) resp();
    mktag();
    __builtin_amdgcn_s_waitcnt(0x0F70);
    __builtin_amdgcn_s_setprio(1);
    STAMP(1);
    bar();
    STAMP(2); TRACE3(1);
#pragma unroll
    for (int i = 0; i < 10; ++i) rd(0, i);
    wait_lgkm0();
    auto stage1 = [&](int dl) {
      SCHED();
#pragma unroll
      for (int i = 0; i < 16; ++i) {
        mm(0, i); SCHED();
        if (i < 10) rd(1, i);
        if (i == 11) { adv(ax[0], dl); adv(ab[0], dl); }
        SCHED();
      }
    };
    stage1(SLOT);
    barrier();
    TRACE3(2);
    int ph = 0;
#pragma unroll 1
    for (int c = 2; c <= 8; ++c) {
      const int dl = ph == 2 ? -2 * (int)SLOT : (int)SLOT;
      ph = ph == 2 ? 0 : ph + 1;
      SCHED();
#pragma unroll
      for (int i = 0; i < 16; ++i) {
        mm(1, i); SCHED();
        if (i < 10) rd(0, i);
        if (i == 11) { adv(ax[1], dl); adv(ab[1], dl); }
        SCHED();
      }
      wait_lgkm0();
      stage1(ph == 2 ? -2 * (int)SLOT : (int)SLOT);
      TRACE(2 * c);
      barrier();
      TRACE(2 * c + 1); TRACE3(c + 1);
    }
    __builtin_amdgcn_s_setprio(LPRIO);
    STAMP(4);
    f32x2 rs[16], mrs[16];
    float csr, csi;
#pragma unroll
    for (int i = 0; i < 16; ++i) {
      mm(1, i); SCHED();
      if (i == 0) { csr = ldsf1(csa); csi = ldsf1(csa + 512); }
      if (i < 8) { const f32x4 t = ldsf4(sa + i * 16); rs[i] = lo2(t); mrs[i] = hi2(t); }
      SCHED();
    }
    const f32x2 ncr = bco(-csr), nci = bco(-csi);
    STAMP(5);
    f32x2 ur[16], ui[16], Er, Ei;
    SCHED();
#pragma unroll
    for (int s = 0; s < 16; ++s) {
      if (s < 8) { const f32x4 t = ldsf4(sa + (s + 8) * 16); rs[s + 8] = lo2(t); mrs[s + 8] = hi2(t); SCHED(); }
      ur[s] = mrs[s] * ncr; SCHED();
      ui[s] = mrs[s] * nci; SCHED();
      ur[s] = pfma((s & 1) ? hi2(acr[s >> 1]) : lo2(acr[s >> 1]), rs[s], ur[s]); SCHED();
      ui[s] = pfma((s & 1) ? hi2(aci[s >> 1]) : lo2(aci[s >> 1]), rs[s], ui[s]); SCHED();
      if (s == 0) { Er = ur[0]; Ei = ui[0]; }
      else {
        const f32x2 t0 = pfma(Ei, -ai, ur[s]); SCHED();
        const f32x2 t1 = pfma(Er, ai, ui[s]); SCHED();
        Er = pfma(Er, ar, t0); SCHED();
        Ei = pfma(Ei, ar, t1); SCHED();
      }
    }
    TRACE2(1);
    const f32x2 E0 = {Er[0], Ei[0]};
    const f32x2 E = cfma(a16, E0, (f32x2){Er[1], Ei[1]});
    f32x2 P = {0.f, 0.f}, Tt = {0.f, 0.f};
    float er4[4], ei4[4];
    rows4(E[0], er4); rows4(E[1], ei4);
#pragma unroll
    for (int s = 0; s < 4; ++s) {
      const f32x2 e = {er4[s], ei4[s]};
      P = csel(s == g, Tt, P);
      Tt = s ? cfma(a32, Tt, e) : e;
    }
    const unsigned totb = WS_TOT + (unsigned)(j * 2 + d) * 4096;
    if (g == 0) __builtin_amdgcn_raw_buffer_store_b128((u32x4){tagA, f2u(Tt[0]), tagB, f2u(Tt[1])}, wsr, totb + r * 256 + i16 * 16, 0, 16);
    STAMP(6);
    TRACE2(2);
    {
      const f32x2 kr = bco(kk[0]), nki = bco(-kk[1]);
#pragma unroll
      for (int s = 0; s < 16; ++s) {
        const f32x2 t0 = ur[s] * kr; SCHED();
        const f32x2 t1 = ur[s] * nki; SCHED();
        ur[s] = pfma(ui[s], nki, t0); SCHED();
        ui[s] = pfma(ui[s], -kr, t1); SCHED();
      }
    }
#pragma unroll
    for (int s = 0; s < 16; s += 4) asm volatile("" : "+v"(ur[s]), "+v"(ui[s]), "+v"(ur[s + 1]), "+v"(ui[s + 1]), "+v"(ur[s + 2]), "+v"(ui[s + 2]), "+v"(ur[s + 3]), "+v"(ui[s + 3]));
    f32x4 hr4[8], hi4[8];
    {
      const f32x2 kP = cmul(kk, P);
      scan(kP, cfma(a16, kP, cmul(kk, E0)), ur, ui, [&](int q, f32x4 a, f32x4 b) { hr4[q] = a; hi4[q] = b; asm volatile("" : "+v"(hr4[q]), "+v"(hi4[q])); });
    }
    TRACE2(3);
    prog_wait(PW + (6 + d) * 4, 1);
#pragma unroll
    for (int i = 0; i < 5; ++i) absorb(i, home + i * 4096);
    unsigned v2[4], vt[4];
    mkv2(v2);
#pragma unroll
    for (int i = 0; i < 4; ++i) { vt[i] = (4 * i + g < r ? g * 256 : -i * 1024) + i16 * 16 + 4096; asm volatile("" : "+v"(vt[i]), "+v"(v2[i])); }
    {
      const i32x4 tr = {wss[0], wss[1], (int)(totb + r * 256), wss[3]};
#pragma unroll 1
      for (int spin = 0; spin < 64; ++spin) {
        int n, t[15];
        asm volatile("s_buffer_load_dword %1, %16, %17 offset:0 glc\n\ts_buffer_load_dword %2, %16, %17 offset:256 glc\n\ts_buffer_load_dword %3, %16, %17 offset:512 glc\n\ts_buffer_load_dword %4, %16, %17 offset:768 glc\n\ts_buffer_load_dword %5, %16, %17 offset:1024 glc\n\ts_buffer_load_dword %6, %16, %17 offset:1280 glc\n\ts_buffer_load_dword %7, %16, %17 offset:1536 glc\n\ts_buffer_load_dword %8, %16, %17 offset:1792 glc\n\ts_buffer_load_dword %9, %16, %17 offset:2048 glc\n\ts_buffer_load_dword %10, %16, %17 offset:2304 glc\n\ts_buffer_load_dword %11, %16, %17 offset:2560 glc\n\ts_buffer_load_dword %12, %16, %17 offset:2816 glc\n\ts_buffer_load_dword %13, %16, %17 offset:3072 glc\n\ts_buffer_load_dword %14, %16, %17 offset:3328 glc\n\ts_buffer_load_dword %15, %16, %17 offset:3584 glc\n\t"
                     "s_mov_b32 %0, 0\n\ts_waitcnt lgkmcnt(0)\n\t"
                     "s_cmp_eq_u32 %1, %18\n\ts_addc_u32 %0, %0, 0\n\ts_cmp_eq_u32 %2, %18\n\ts_addc_u32 %0, %0, 0\n\ts_cmp_eq_u32 %3, %18\n\ts_addc_u32 %0, %0, 0\n\ts_cmp_eq_u32 %4, %18\n\ts_addc_u32 %0, %0, 0\n\ts_cmp_eq_u32 %5, %18\n\ts_addc_u32 %0, %0, 0\n\ts_cmp_eq_u32 %6, %18\n\ts_addc_u32 %0, %0, 0\n\ts_cmp_eq_u32 %7, %18\n\ts_addc_u32 %0, %0, 0\n\ts_cmp_eq_u32 %8, %18\n\ts_addc_u32 %0, %0, 0\n\ts_cmp_eq_u32 %9, %18\n\ts_addc_u32 %0, %0, 0\n\ts_cmp_eq_u32 %10, %18\n\ts_addc_u32 %0, %0, 0\n\ts_cmp_eq_u32 %11, %18\n\ts_addc_u32 %0, %0, 0\n\ts_cmp_eq_u32 %12, %18\n\ts_addc_u32 %0, %0, 0\n\ts_cmp_eq_u32 %13, %18\n\ts_addc_u32 %0, %0, 0\n\ts_cmp_eq_u32 %14, %18\n\ts_addc_u32 %0, %0, 0\n\ts_cmp_eq_u32 %15, %18\n\ts_addc_u32 %0, %0, 0"
                     : "=&s"(n), "=&s"(t[0]), "=&s"(t[1]), "=&s"(t[2]), "=&s"(t[3]), "=&s"(t[4]), "=&s"(t[5]), "=&s"(t[6]), "=&s"(t[7]), "=&s"(t[8]), "=&s"(t[9]), "=&s"(t[10]), "=&s"(t[11]), "=&s"(t[12]), "=&s"(t[13]), "=&s"(t[14]) : "s"(tr), "s"(totb), "s"(tagA) : "scc", "memory");
        if (n >= r) break;
        COUNT(20);
      }
    }
    STAMP(7);
    auto fetch = [&]() {
      asm volatile("s_mov_b32 m0, %5\n\ts_nop 0\n\tglobal_load_lds_dwordx4 %0, %4 sc1\n\tglobal_load_lds_dwordx4 %1, %4 offset:1024 sc1\n\t"
                   "global_load_lds_dwordx4 %2, %4 offset:2048 sc1\n\tglobal_load_lds_dwordx4 %3, %4 offset:3072 sc1"
                   ::"v"(vt[0]), "v"(vt[1]), "v"(vt[2]), "v"(vt[3]), "s"(p.ws + totb - 4096), "s"(home + 16384) : "memory");
    };
    wait_lgkm0();
    if (r > 0) fetch();
    TRACE2(14);
    if (!YPRE) resp();
    f32x2 wt[4];
#pragma unroll
    for (int i = 3; i >= 0; --i) {
      const bool v = 4 * i + g < r;
      wt[i] = csel(v, acar, (f32x2){0.f, 0.f});
      if (i) acar = csel(v, cmul(acar, a512), acar);
      asm volatile("" : "+v"(wt[i]));
    }
    setup2(6);
    unsigned ho = WS_H + (unsigned)(2 * r) * H_MT + (unsigned)(56 + 2 * j + d) * 4096 + lane * 16, ta = home + 16384 + lane * 16;
    asm volatile("" : "+v"(ho), "+v"(ta), "+v"(m0), "+v"(m1));
    TRACE2(4);
    wait_vm<0>();
    TRACE2(15);
    float cr = 0.f, ci = 0.f;
    if (r > 0) {
#pragma unroll 1
      for (int spin = 0; spin < SPIN_CAP; ++spin) {
        u32x4 tv[4];
        unsigned e = 0;
#pragma unroll
        for (int i = 0; i < 4; ++i) tv[i] = *(volatile LAS u32x4*)(size_t)(ta + i * 1024);
        f32x2 c[2];
#pragma unroll
        for (int i = 0; i < 4; ++i) {
          const f32x2 t = {u2f(tv[i].y), u2f(tv[i].w)};
          c[i & 1] = i < 2 ? cmul(wt[i], t) : cfma(wt[i], t, c[i & 1]);
          e = __builtin_amdgcn_bitop3_b32(tv[i].z, tagB, __builtin_amdgcn_bitop3_b32(tv[i].x, tagA, e, 0xBE), 0xBE);
        }
        const u32x2 h = swap32(sadd(c[0][0], c[1][0]), sadd(c[0][1], c[1][1]));
        const float x = sadd(u2f(h.x), u2f(h.y));
        const u32x2 h2 = swap16(x, x);
        const float y = sadd(u2f(h2.x), u2f(h2.y));
        const u32x2 h3 = swap32(y, y);
        cr = u2f(h3.x); ci = u2f(h3.y);
        if (__builtin_expect(__builtin_amdgcn_ballot_w64(e != 0) == 0, 1)) break;
        COUNT(20);
        __builtin_amdgcn_s_sleep(8);
        fetch();
        wait_vm<0>();
      }
    }
    STAMP(8);
    {
      const unsigned c0 = pk2(cr, cr), c1 = pk2(ci, ci), c2 = pk2(-ci, -ci);
      const s16x8 are = __builtin_bit_cast(s16x8, (u32x4){m0 & c0, m1 & c0, m0 & c1, m1 & c1}), aim = __builtin_bit_cast(s16x8, (u32x4){m0 & c2, m1 & c2, m0 & c0, m1 & c0});
      u32x4 o[8];
      SCHED();
#pragma unroll
      for (int q = 0; q < 10; ++q) {
        if (q < 8) {
          hr4[q] = mfma(are, __builtin_bit_cast(s16x8, yb[q]), hr4[q]);
          hi4[q] = mfma(aim, __builtin_bit_cast(s16x8, yb[q]), hi4[q]);
          SCHED();
        }
        if (q >= 2) { o[q - 2] = pack(hr4[q - 2], hi4[q - 2]); __builtin_amdgcn_raw_buffer_store_b128(o[q - 2], wsr, ho + ((q - 2) & 3) * 1024, ((q - 2) >> 2) * H_MT, HST); }
        SCHED();
      }
      TRACE2(5);
      dma_x4(v2, w2b + kstep_of(slot, 5) * 32768, home + 16384);
      asm volatile("" :: "v"(o[0]), "v"(o[1]), "v"(o[2]), "v"(o[3]), "v"(o[4]), "v"(o[5]), "v"(o[6]), "v"(o[7]), "v"(ho));
      STAMP(19);
      pubo = lane < 2 ? WS_FLAG + (2 * r + lane) * 2048 + 1024 + (2 * j + d) * 8 : pubo;
    }
    STAMP(18);
  };
  auto roleB = [&]() __attribute__((always_inline)) {
    mktag();
    {
      float s0 = 0.f, s1 = 0.f;
#pragma unroll
      for (int e = 0; e < 8; ++e) { s0 = sadd(s0, bflo(prb[e >> 2][e & 3])); s1 = sadd(s1, bfhi(prb[e >> 2][e & 3])); }
      *(LAS float*)(size_t)(LB + L_LS + 512 + (4 * d + g) * 4) = row16_sum(s0 + s1);
      __builtin_amdgcn_s_waitcnt(0x0F70);
    }
    if (mlp) {
      f32x4 acc[6][4];
#pragma unroll
      for (int s = 0; s < 6; ++s) {
#pragma unroll
        for (int m = 0; m < 4; ++m) acc[s][m] = (f32x4){0, 0, 0, 0};
        asm volatile("" : "+v"(acc[s][0]), "+v"(acc[s][1]), "+v"(acc[s][2]), "+v"(acc[s][3]));
      }
      unsigned ax[2], aw[6];
      ax[0] = LB + (64 * d + i16) * 128 + kx; ax[1] = ax[0] ^ 64;
#pragma unroll
      for (int s = 0; s < 6; ++s) aw[s] = awb + ((s ^ awx) * 32);
      unsigned sta = LB + L_LS + qq * 288 + (8 * d + ((i16 >> 1) & 1)) * 16 + (i16 & 1) * 4, csa = LB + L_CS + g * 16;
      asm volatile("" : "+v"(ax[0]), "+v"(ax[1]), "+v"(aw[0]), "+v"(aw[1]), "+v"(aw[2]), "+v"(aw[3]), "+v"(aw[4]), "+v"(aw[5]), "+v"(sta), "+v"(csa));
      s16x8 xf[2][4];
      s16x4 wf[2][12];
      auto rd = [&](int set, int i) {
        if (i < 4) xf[set][i] = lds128(ax[set] + i * 2048);
        else wf[set][i - 4] = ldstr4(aw[(i - 4) >> 1] + set * 8192 + (i & 1) * 1024);
      };
      auto mm = [&](int set, int i) { const int s = i >> 2, m = i & 3; acc[s][m] = mfma(cat(wf[set][2 * s], wf[set][2 * s + 1]), xf[set][m], acc[s][m]); };
      {
        float t[8];
#pragma unroll
        for (int e = 0; e < 8; ++e)
          t[e] = (e & 1) ? sadd(sadd(bfhi(pre[0][e >> 1]), bfhi(pre[1][e >> 1])), sadd(bfhi(pre[2][e >> 1]), bfhi(pre[3][e >> 1])))
                         : sadd(sadd(bflo(pre[0][e >> 1]), bflo(pre[1][e >> 1])), sadd(bflo(pre[2][e >> 1]), bflo(pre[3][e >> 1])));
#pragma unroll
        for (int e = 0; e < 4; ++e) { const u32x2 h = swap32(t[e], t[e + 4]); t[e] = sadd(u2f(h.x), u2f(h.y)); }
#pragma unroll
        for (int e = 0; e < 2; ++e) { const u32x2 h = swap16(t[e], t[e + 2]); t[e] = dpp_add<0x128>(sadd(u2f(h.x), u2f(h.y))); }
        *(LAS f32x2*)(size_t)(LB + L_LS + d * 256 + (4 * (lane & 7) + g) * 8) = (f32x2){t[0], t[1]};
      }
      __builtin_amdgcn_s_setprio(1);
      STAMP(1);
      barrier();
      STAMP(2); STAMPC(21);
#pragma unroll
      for (int i = 0; i < 16; ++i) rd(0, i);
      wait_lgkm0();
      auto stage1 = [&](int c, int dl) {
        SCHED();
#pragma unroll
        for (int i = 0; i < 24; ++i) {
          mm(0, i); SCHED();
          if (i < 16) rd(1, i);
          else if (i < 22) adv(aw[i - 16], dl);
          else if (i == 22) adv(ax[0], dl);
          SCHED();
        }
        TRACE(2 * c);
        barrier();
        TRACE(2 * c + 1);
        SCHED();
      };
      int ph = 0;
#pragma unroll 1
      for (int c = 1; c < 8; ++c) {
        const int dl = ph == 2 ? -2 * (int)SLOT : (int)SLOT;
        ph = ph == 2 ? 0 : ph + 1;
        stage1(c, dl);
#pragma unroll
        for (int i = 0; i < 24; ++i) {
          mm(1, i); SCHED();
          if (i < 16) rd(0, i);
          else if (i == 16) adv(ax[1], dl);
          else if (i == 23) wait_lgkm0();
          SCHED();
        }
      }
      stage1(8, 0);
      unsigned b1a = LB + L_B1 + g * 8;
      asm volatile("" : "+v"(b1a));
      STAMP(4); STAMPC(22);
      f32x2 rs[4], nm[4];
      f32x4 cs[6];
      u32x2 b1v[6];
      f32x2 st[4];
      auto colrd = [&](int s) { cs[s] = ldsf4(csa + 64 * s); b1v[s] = *(LAS u32x2*)(size_t)(b1a + 32 * s); };
#pragma unroll
      for (int i = 0; i < 4; ++i) st[i] = (f32x2){ldsf1(sta + i * 32), ldsf1(sta + i * 32 + 8)};
      colrd(0); colrd(1);
      SCHED();
#pragma unroll
      for (int i = 0; i < BSPLIT; ++i) mm(1, i);
      SCHED();
#pragma unroll
      for (int i = 0; i < 4; ++i) { rs[i] = bco(st[i][0]); nm[i] = bco(-st[i][1]); }
      STAMP(5);
      const unsigned fo = WS_FLAG + (2 * r + d) * 2048;
      pubo = lane ? pubo : 0u;
      u32x4 ob[12];
      const unsigned ho = WS_H + (unsigned)(2 * r + d) * H_MT + (unsigned)(4 * jm) * 4096 + lane * 16;
#pragma unroll
      for (int pr = 0; pr < 3; ++pr) {
        Col col;
        mkcol(col, cs[2 * pr], cs[2 * pr + 1], b1v[2 * pr], b1v[2 * pr + 1]);
        if (pr < 2) { colrd(2 * pr + 2); colrd(2 * pr + 3); }
        SCHED();
        if (pr == 1) {
#pragma unroll
          for (int i = BSPLIT; i < 24; ++i) mm(1, i);
          SCHED();
          STAMP(7);
        }
#pragma unroll
        for (int m = 0; m < 4; ++m) {
          ob[4 * pr + m] = gelu8(acc[2 * pr][m], acc[2 * pr + 1][m], col, rs[m], nm[m]);
          __builtin_amdgcn_raw_buffer_store_b128(ob[4 * pr + m], wsr, ho + m * 1024, pr * 4096, HST);
          if (4 * pr + m >= KEEP) asm volatile("" :: "v"(ob[4 * pr + m - KEEP]));
          TRACE2(1 + 4 * pr + m);
          if (pr > 0 && m == (pr == 1 ? BFLAG0 : BFLAG)) {
            if (pr == 1) wait_vm<BFLAG0 + 1>(); else wait_vm<BFLAG + 1>();
            __builtin_amdgcn_raw_buffer_store_b64((u32x2){tagA, tagB}, wsr, pubo, fo + flag_of(jm, pr - 1), 16);
          }
          SCHED();
        }
      }
      pubo |= fo + flag_of(jm, 2);
      STAMP(6);
    } else {
      const int q = 4 * r + 2 * lq + d;
      const unsigned vt = (lane < 56 ? lane : 0) * 128, dm = LB + L_DUMMY + d * 256;
#pragma unroll 1
      for (int c = 0; c < 9; ++c) {
        if (TOUCH == 7) {
          const int t = (c - TST) * TPP;
#pragma unroll
          for (int e = 0; e < TPP; ++e) {
            if (t + e >= 0 && t + e < 4) dma4w(vt, p.w2 + ((t + e) * 64 + q) * 7168, dm);
            else if (t + e == 4 || t + e == 5) dma4w(lane * 128, (t + e == 4 ? p.cre : p.cim) + q * 8192, dm);
          }
        }
        barrier();
      }
    }
    prog_wait(PW + (6 + d) * 4, 3);
#pragma unroll
    for (int i = 0; i < 5; ++i) absorb(i, home + i * 4096);
    unsigned v2[4];
    mkv2(v2);
    setup2(6);
#pragma unroll
    for (int i = 0; i < 4; ++i) asm volatile("" : "+v"(v2[i]));
    wait_lgkm0();
    STAMP(8);
    dma_x4(v2, w2b + kstep_of(slot, 5) * 32768, home + 16384);
  };
  auto roleA = [&]() __attribute__((always_inline)) {
    const int srow = 128 * r + 8 * j + 4 * d + g;
    u32x2 dv[4], bv[4];
#pragma unroll
    for (int n = 0; n < 4; ++n) { dv[n] = *(const GAS u32x2*)(p.dsk + ntile * 128 + n * 32 + g * 8); bv[n] = *(const GAS u32x2*)(p.b2 + ntile * 128 + n * 32 + g * 8); }
    if (lane < 8) *(LAS unsigned*)(size_t)(PW + lane * 4) = 0u;
    f32x4 acc[2][4];
#pragma unroll
    for (int m = 0; m < 4; ++m) { acc[0][m] = (f32x4){0, 0, 0, 0}; acc[1][m] = (f32x4){0, 0, 0, 0}; asm volatile("" : "+v"(acc[0][m]), "+v"(acc[1][m])); }
    unsigned ax0 = LB + (64 * d + i16) * 128 + kx, ax1 = ax0 ^ 64, aw0 = awb + ((6 ^ awx) * 32), aw1 = awb + ((7 ^ awx) * 32);
    unsigned sta = LB + L_LS + qq * 288 + (8 * d + ((i16 >> 1) & 1)) * 16 + (i16 & 1) * 4, csa = LB + L_CS + 384 + g * 16;
    asm volatile("" : "+v"(ax0), "+v"(ax1), "+v"(aw0), "+v"(aw1), "+v"(sta), "+v"(csa));
    int ph = 0;
    auto chunk = [&](auto side) {
      s16x8 xf[2][4], wf[2][2];
      if (ADELAY) __builtin_amdgcn_s_sleep(ADELAY);
#pragma unroll
      for (int k = 0; k < 2; ++k) {
        wf[k][0] = ldstr(aw0 + k * 8192, aw0 + k * 8192 + 1024);
        wf[k][1] = ldstr(aw1 + k * 8192, aw1 + k * 8192 + 1024);
#pragma unroll
        for (int m = 0; m < 4; ++m) xf[k][m] = lds128((k ? ax1 : ax0) + m * 2048);
      }
      SCHED();
      const int dl = ph == 2 ? -2 * (int)SLOT : (int)SLOT;
      ph = ph == 2 ? 0 : ph + 1;
      adv(ax0, dl); adv(ax1, dl); adv(aw0, dl); adv(aw1, dl);
      side(8);
      SCHED();
#pragma unroll
      for (int k = 0; k < 2; ++k)
#pragma unroll
        for (int m = 0; m < 4; ++m) {
          acc[0][m] = mfma(wf[k][0], xf[k][m], acc[0][m]); acc[1][m] = mfma(wf[k][1], xf[k][m], acc[1][m]);
          SCHED();
          side(4 * k + m);
          SCHED();
        }
    };
    auto none = [](int) {};
    mktag();
    asm volatile("" : "+v"(pre[3]));
    STAMP(19);
    {
      const float mean = sum32(pre) * (1.f / 512.f);
      float v0 = 0.f, v1 = 0.f;
#pragma unroll
      for (int i = 0; i < 4; ++i)
#pragma unroll
        for (int e = 0; e < 4; ++e) {
          const float d0 = bflo(pre[i][e]) - mean, d1 = bfhi(pre[i][e]) - mean;
          v0 = sfma(d0, d0, v0); v1 = sfma(d1, d1, v1);
        }
      const float rsd = __builtin_amdgcn_rsqf(row16_sum(v0 + v1) * (1.f / 512.f) + 1e-5f);
      if (i16 == 0) __builtin_amdgcn_raw_buffer_store_b128((u32x4){tagA, f2u(rsd), tagB, f2u(mean * rsd)}, wsr, WS_STAT + srow * 16, 0, 16);
    }
    __builtin_amdgcn_s_setprio(APRIO);
    STAMP(1);
    barrier();
    STAMP(2);
    chunk(none);
    if (d) {
      const f32x2 c0 = ldsf2(LB + L_LS + 512 + (lane & 3) * 8);
      if (lane < 4) __builtin_amdgcn_raw_buffer_store_b128((u32x4){tagA, f2u(sadd(c0[0], c0[1])), tagB, 0u}, wsr, WS_BS + j * 1024 + (4 * r + lane) * 16, 0, 16);
    } else if (mlp) {
      const f32x2 c0 = ldsf2(LB + L_LS + (lane & 31) * 8), c1 = ldsf2(LB + L_LS + 256 + (lane & 31) * 8);
      if (lane < 32) __builtin_amdgcn_raw_buffer_store_b128((u32x4){tagA, f2u(sadd(c0[0], c1[0])), tagB, f2u(sadd(c0[1], c1[1]))}, wsr, WS_CS + (jm * 16 + r) * 512 + lane * 16, 0, 16);
    }
    barrier();
#pragma unroll 1
    for (int c = 1; c < 3; ++c) { if (!ASKIP || mlp) chunk(none); barrier(); }
    chunk(none);
    asm volatile("" : "+v"(dv[0]), "+v"(dv[1]), "+v"(dv[2]), "+v"(dv[3]), "+v"(bv[0]), "+v"(bv[1]), "+v"(bv[2]), "+v"(bv[3]));
    __builtin_amdgcn_s_waitcnt(0x0F70);
    u32x2 xv[2][4];
#pragma unroll
    for (int m = 0; m < 2; ++m)
#pragma unroll
      for (int n = 0; n < 4; ++n) xv[m][n] = *(LAS u32x2*)(size_t)(HB + 3 * SLOT + 12288 + (16 * m + i16) * 128 + (g & 1) * 8 + (((2 * n + (g >> 1)) ^ (i16 >> 1)) * 16));
    barrier();
#pragma unroll 1
    for (int c = 4; c < 6; ++c) { if (!ASKIP || mlp) chunk(none); barrier(); }
    f32x2 st[4];
#pragma unroll
    for (int m = 0; m < 4; ++m) st[m] = (f32x2){ldsf1(sta + m * 32), ldsf1(sta + m * 32 + 8)};
    unsigned ra[4], ho = mlp ? WS_H + (unsigned)(2 * r + d) * H_MT + (unsigned)(4 * jm + 3) * 4096 + lane * 16 : 0xffff0000u;
    f32x2 rs[4], nm[4];
    chunk([&](int k) {
      if (k < 4) { rs[k] = bco(st[k][0]); nm[k] = bco(-st[k][1]); asm volatile("" : "+v"(rs[k]), "+v"(nm[k])); }
      if (k < 8) return;
#pragma unroll
      for (int n = 0; n < 4; ++n) ra[n] = home + r2l + ((n ^ r2x) * 32);
      asm volatile("" : "+v"(ra[0]), "+v"(ra[1]), "+v"(ra[2]), "+v"(ra[3]), "+v"(ho));
    });
    TRACE(14); barrier(); TRACE(15);
    STAMP(4);
    const f32x4 cs0 = ldsf4(csa), cs1 = ldsf4(csa + 64);
    const u32x2 b10 = *(LAS u32x2*)(size_t)(LB + L_B1 + 192 + g * 8), b11 = *(LAS u32x2*)(size_t)(LB + L_B1 + 224 + g * 8);
    Col col;
    auto take = [&](int i, int pg) {
#pragma unroll
      for (int n = 0; n < 4; ++n) we[i][n] = ldstr(ra[n] + pg * 4096, ra[n] + pg * 4096 + 2048);
    };
    chunk([&](int k) {
      if (k == 2) { mkcol(col, cs0, cs1, b10, b11); asm volatile("" : "+v"(col.bias[0]), "+v"(col.bias[1]), "+v"(col.bias[2]), "+v"(col.bias[3])); }
      if (k >= 3 && k < 6) take(k - 3, k - 3);
    });
    barrier();
    __builtin_amdgcn_s_setprio(2);
    STAMP(5);
    u32x4 oa[4];
#pragma unroll
    for (int m = 0; m < 4; ++m) {
      TRACE2(1 + 2 * m);
      oa[m] = gelu8(acc[0][m], acc[1][m], col, rs[m], nm[m]);
      TRACE2(2 + 2 * m);
      __builtin_amdgcn_raw_buffer_store_b128(oa[m], wsr, ho + m * 1024, 0, HST);
      if (m >= 1) asm volatile("" :: "v"(oa[m - 1]));
      if (m == 0) { take(3, 3); take(4, 4); }
      SCHED();
    }
    STAMP(6);
    prog_wait(PW + (6 + d) * 4, 2);
#pragma unroll
    for (int i = 5; i < 8; ++i) take(i, i - 5);
    u32x4 s2v[2];
#pragma unroll
    for (int m = 0; m < 2; ++m) s2v[m] = *(LAS u32x4*)(size_t)(LB + L_ST2 + (tok + 8 * (mtile & 1) + 4 * d + 2 * m) * 16);
    setup2(8);
    wait_lgkm0();
    asm volatile("" :: "v"(oa[3]));
    prog_set(PW + w * 4, 1);
    snap(false);
#pragma unroll
    for (int m = 0; m < 2; ++m) {
      const u32x4 s2 = s2v[m];
      const f32x2 r2 = bco(u2f(s2.y)), n2 = bco(-u2f(s2.w));
#pragma unroll
      for (int n = 0; n < 4; ++n) {
        f32x2 o[2];
#pragma unroll
        for (int h = 0; h < 2; ++h) {
          const f32x2 xx = {bflo(xv[m][n][h]), bfhi(xv[m][n][h])};
          o[h] = pfma(pfma(xx, r2, n2), (f32x2){bflo(dv[n][h]), bfhi(dv[n][h])}, xx + (f32x2){bflo(bv[n][h]), bfhi(bv[n][h])});
        }
        acc2[n][m] = __builtin_shufflevector(o[0], o[1], 0, 1, 2, 3);
        acc2[n][2 + m] = (f32x4){0, 0, 0, 0};
        asm volatile("" : "+v"(acc2[n][m]), "+v"(acc2[n][2 + m]));
      }
    }
    pubo = mlp && lane == 0 ? WS_FLAG + (2 * r + d) * 2048 + flag_of(jm, 3) : pubo;
    seen();
    STAMP(8);
  };

  auto phase2 = [&](auto nm_) __attribute__((always_inline)) {
    STAMP(9);
    constexpr int NM = decltype(nm_)::value, S0 = NM == 8 ? 0 : 5, N = NM == 8 ? 13 : 9;
    const u64 tag = ((u64)tagB << 32) | tagA;
    auto has = [&](int k) { return fl[k < NM ? k : k - NM + S0] == tag; };
    auto need = [&](int k) {
      if (__builtin_expect(!has(k), 0)) {
#pragma unroll 1
        for (int spin = 1; spin < SPIN_CAP; ++spin) {
          COUNT(20);
          snap(k >= NM);
          if (has(k)) break;
          if (!(spin & 31)) {
            const u32x2 f = __builtin_amdgcn_raw_buffer_load_b64(wsr, 0, k < NM ? fm + 8 * k : fs + 8 * (k - NM + S0), 16);
            if (__builtin_amdgcn_readfirstlane(f.x) == tagA && __builtin_amdgcn_readfirstlane(f.y) == tagB) break;
          }
          __builtin_amdgcn_s_sleep(PSLEEP);
        }
      }
      asm volatile("" ::: "memory");
    };
    auto publish = [&]() { __builtin_amdgcn_raw_buffer_store_b64((u32x2){tagA, tagB}, wsr, pubo, 0, 16); };
    auto loadH = [&](int i, int m) -> u32x4 {
      if (m == 0) asm volatile("" : "+v"(hb));
      if ((NOC & 8) && (m & 1)) { u32x4 z; asm volatile("" : "=v"(z)); return z; }
      return __builtin_amdgcn_raw_buffer_load_b128(wsr, hb + (m & 1) * 1024, __builtin_amdgcn_readlane(ksl, i) ^ ((m & 2) * 1024), HAUX);
    };
    u32x4 hf[N][4];
    auto load4 = [&](int i) {
#pragma unroll
      for (int m = 0; m < 4; ++m) hf[i][m] = loadH(i, m);
    };
    STAMP(10);
    if (NM == 8) {
      if (__builtin_expect(!(has(0) && has(1)), 0)) { wait_vm<0>(); publish(); pubo = 0xffff0000u; }
      need(0); load4(0);
      need(1); load4(1);
      wait_vm<8>(); publish();
      __builtin_amdgcn_s_setprio(EPRI);
    } else {
      unsigned vc[8];
      mkvc(vc);
      asm volatile("" : "+v"(vc[0]), "+v"(vc[1]), "+v"(vc[2]), "+v"(vc[3]), "+v"(vc[4]), "+v"(vc[5]), "+v"(vc[6]), "+v"(vc[7]));
      const unsigned o = ntile * 65536 + kg * 128, hs = fresh(HB + (w < 4 ? SLOT : 0));
      __builtin_amdgcn_s_setprio(0);
      dma_x8(vc, p.cre + o, hs);
      wait_vm<8>(); publish();
      snap(false);
      dma_x8(vc, p.cim + o, hs + 8192);
      seen();
      need(0); load4(0);
      need(1); load4(1);
    }
    STAMP(11);
    s16x8 cfr[N - NM][4];
    auto rdc = [&](int q, unsigned base) {
#pragma unroll
      for (int n = 0; n < 4; ++n) {
        unsigned a = base + n * 2048;
        asm volatile("" : "+v"(a));
        cfr[q][n] = cat(*(LAS s16x4*)(size_t)a, *(LAS s16x4*)(size_t)(a + 8192));
      }
    };
    SCHED();
#pragma unroll
    for (int i = 0; i < N; ++i) {
      const int k = i + (i < NM - 2 ? 2 : SDEP);
      if (i == 2) STAMP(12);
      if (!(i & 1) || k >= NM) TRACE4(2 * i + 1);
      if (i == NM - 2) {
        STAMP(13);
#pragma unroll
        for (int e = NM; e < NM + SDEP - 2; ++e) { need(e); load4(e); }
      }
      if (k < NM) {
        if (NM == 6 && i == 0) snap(false);
        else if (NM == 6 && i == 2) { need(2); load4(2); need(3); load4(3); }
        if (!(i & 1) && (NM == 8 || i)) { need(k); need(k + 1); TRACE4(2 * i + 2); if (NM == 6 && i == 2) absorb(5, home + 16384); if (!(ESEP && i == 0)) snap(k + 2 == NM); }
      } else if (k < N) { need(k); TRACE4(2 * i + 2); }
      if (i == NM - 2) STAMP(14);
      if (NM == 8 && i == 1) prog_set(PW + w * 4, 4);
      if (NM == 8 && i == 6) prog_wait(PW + (w | 6) * 4, 4);
      if (NM == 6 && i == 1) prog_set(PW + w * 4, 1);
      if (NM == 8 && i == EOFF) __builtin_amdgcn_s_setprio(0);
      if (NM == 8 && i == PSTEP) { prog_wait(PW + (w ^ 4) * 4, 1); rdc(4, crd - 2 * SLOT); }
      if (NM == 8 && i == PSTEP + 1) { wait_lgkm0(); prog_set(PW + w * 4, 5); }
      if (i >= NM - 1 && i < NM + (NM == 8 ? 3 : 2)) rdc(i + 1 - NM, crd ^ (32 * (i + 1 - NM + (NM == 8 ? 0 : 1))));
      SCHED();
#pragma unroll
      for (int m = 0; m < 4; ++m) {
#pragma unroll
        for (int n = 0; n < 4; ++n)
          if (NM == 8 && i == 0) asm volatile("v_mfma_f32_16x16x32_bf16 %0, %1, %2, %0" : "+v"(acc2[n][m]) : "v"(we[0][n]), "v"(hf[0][m]));
          else acc2[n][m] = mfma(i < NM ? we[i < NM ? i : 0][n] : cfr[i < NM ? 0 : i - NM][n], __builtin_bit_cast(s16x8, hf[i][m]), NM == 6 && i == 0 ? (f32x4){0, 0, 0, 0} : acc2[n][m]);
        SCHED();
        if (k < N && (NM == 8 || i > 1)) hf[k < N ? k : 0][m] = loadH(k, m);
        SCHED();
      }
      if (ESEP && NM == 8 && i < 2) TRACE4(3 + i);
      if (ESEP && NM == 8 && i == 1) snap(false);
      if ((i & 1) && k < NM) seen();
    }
    TRACE4(30);
    if (NM == 6) prog_wait(PW + (w ^ 4) * 4, 5);
  };
  if (__builtin_expect(w >= 6 || w < 2, 1)) {
    if (__builtin_expect(w >= 6, 1)) roleD(); else roleA();
    phase2(std::integral_constant<int, 8>{});
  } else {
    if (w >= 4) roleL(); else roleB();
    phase2(std::integral_constant<int, 6>{});
  }
  STAMP(15);
  int tl = threadIdx.x;
  asm volatile("" : "+v"(tl));
  const int i16t = tl & 15, gt = (tl >> 4) & 3;
#pragma unroll
  for (int m = 0; m < 4; ++m)
#pragma unroll
    for (int n = 0; n < 4; ++n) *(LAS f32x4*)(size_t)(home + ((16 * m + i16t) ^ (w == 1 ? 32 : 0)) * 256 + (((4 * n + gt) ^ i16t) * 16)) = acc2[n][m];
  const int ck = tl & 15;
  const int orow = 32 * ((tl >> 6) & 3) + 16 * ((tl >> 4) & 1) + ((tl >> 5) & 1) + 2 * (tl >> 8) + 8 * (mtile & 1);
  char* op = p.out + (128 * (mtile >> 1) + orow) * 2048 + ntile * 256 + ck * 16;
  unsigned ra[4];
#pragma unroll
  for (int k = 0; k < 4; ++k) ra[k] = LB + (tl >> 4) * 256 + ((ck ^ ((tl >> 4) & 15)) * 16) + k * SLOT;
  asm volatile("" : "+v"(ra[0]), "+v"(ra[1]), "+v"(ra[2]), "+v"(ra[3]), "+v"(op));
  barrier();
  STAMP(16);
  {
    f32x4 pt[2][8];
#pragma unroll
    for (int h = 0; h < 2; ++h)
#pragma unroll
      for (int k = 0; k < 8; ++k) pt[h][k] = ldsf4(ra[k >> 1] + (k & 1) * HALF + h * 8192);
#pragma unroll
    for (int h = 0; h < 2; ++h) {
      const f32x4 s = ((pt[h][0] + pt[h][1]) + (pt[h][2] + pt[h][3])) + ((pt[h][4] + pt[h][5]) + (pt[h][6] + pt[h][7]));
      __builtin_nontemporal_store(s, (GAS f32x4*)(op + h * 8192));
    }
  }
  if (b == 255 && tl == 0) __builtin_amdgcn_raw_buffer_store_b64((u32x2){tagA, tagB}, wsr, WS_EPOCH, 0, 16);
}

extern "C" void kernel_launch(void* const* d_in, const int* in_sizes, int n_in, void* d_out, int out_size, void* d_ws,
                              size_t ws_size, hipStream_t stream) {
  Params p;
  p.x = (const char*)d_in[0]; p.lre = (const char*)d_in[1]; p.lim = (const char*)d_in[2];
  p.bre = (const char*)d_in[3]; p.bim = (const char*)d_in[4]; p.cre = (const char*)d_in[5]; p.cim = (const char*)d_in[6];
  p.logdt = (const char*)d_in[7]; p.dsk = (const char*)d_in[8];
  p.w1 = (const char*)d_in[10]; p.b1 = (const char*)d_in[11]; p.w2 = (const char*)d_in[12]; p.b2 = (const char*)d_in[13];
  p.out = (char*)d_out; p.ws = (char*)d_ws;
  fused_kernel<<<256, 512, 0, stream>>>(p);
}
```

```cpp
#include <hip/hip_runtime.h>
#include <stdint.h>
#include <type_traits>

typedef short s16x4 __attribute__((ext_vector_type(4)));
typedef short s16x8 __attribute__((ext_vector_type(8)));
typedef __bf16 bf16x8 __attribute__((ext_vector_type(8)));
typedef __bf16 bf16x2 __attribute__((ext_vector_type(2)));
typedef float f32x4 __attribute__((ext_vector_type(4)));
typedef float f32x2 __attribute__((ext_vector_type(2)));
typedef unsigned u32x4 __attribute__((ext_vector_type(4)));
typedef unsigned u32x2 __attribute__((ext_vector_type(2)));
typedef int i32x4 __attribute__((ext_vector_type(4)));
typedef unsigned long long u64;
typedef u64 u64x4 __attribute__((ext_vector_type(4)));
typedef u64 u64x8 __attribute__((ext_vector_type(8)));
typedef int i32x8 __attribute__((ext_vector_type(8)));
typedef int i32x16 __attribute__((ext_vector_type(16)));
__device__ i32x8 sbl8(i32x4, int, int) __asm("llvm.amdgcn.s.buffer.load.v8i32");
__device__ i32x16 sbl16(i32x4, int, int) __asm("llvm.amdgcn.s.buffer.load.v16i32");
#define LAS __attribute__((address_space(3)))
#define GAS __attribute__((address_space(1)))
#define DI __device__ __forceinline__

#define STAMPS 0
#define TRACING 0
#define GATHER_AT 4
#define ROT 1
#define TOUCH 7
#define TLBW 0
#define NOFF 0
#define PSLEEP 4
#define BFLAG 2
#define ABAR 0
#define KROT 0
#define LPRIO 3
#define CHK 0
#define BSPLIT 8
#define APRIO 0
#define ADELAY 0
#define BFLAG0 1
#define DEPE 2
#define DEPL 2
#define LB_SLEEP 8
#define CHECK_AT 5
#define CPF 1
#define HAUX 16
#define LSNAP 1
#define BSNAP 1
#define L2T 0
#define L2C 0
#define SDEP 2
#define YPRE 1
#define KEEP 2
#define LSEP 0
#define PSTEP 9
#define NOC 0
#define ESEP 0
#define HST 16
#define EPRI 1
#define EOFF 6
#define WOFF 4
#define CPW 5
#define TST 1
#define TPP 3
#define S3AT 3
#define JM 0
#define ASKIP 0
#define MAP 1

struct Params {
  const char *x, *lre, *lim, *bre, *bim, *cre, *cim, *logdt, *dsk, *w1, *b1, *w2, *b2;
  char *out, *ws;
};

constexpr unsigned WS_EPOCH = 0;
constexpr unsigned WS_STAT = 4096;
constexpr unsigned WS_TOT = WS_STAT + 2048 * 16;
constexpr unsigned WS_CS = WS_TOT + 16 * 2 * 16 * 256;
constexpr unsigned WS_BS = WS_CS + 16 * 16 * 512;
constexpr unsigned WS_FLAG = WS_BS + 16 * 1024;
constexpr unsigned WS_H = WS_FLAG + 32 * 2048;
constexpr unsigned H_MT = 88 * 4096;
constexpr unsigned WS_END = WS_H + 32 * H_MT;
constexpr unsigned WS_STAMP = 32u << 20;
constexpr unsigned WS_B2B = WS_STAMP + (4u << 20);
constexpr unsigned SLOT = 40960, HALF = 20480, SL_W = 16384, SL_B = 32768;
constexpr unsigned L_ST1 = 3 * SLOT + 16384, L_ST2 = L_ST1 + 2048;
constexpr unsigned L_CS = 3 * SLOT + HALF + 16384, L_FLG = L_CS + 512, L_B1 = L_CS + 768, L_BS = L_CS + 1024, L_LS = L_CS + 2048, L_DUMMY = L_CS + 3328;
constexpr unsigned LDS_BYTES = 163840;
constexpr int SPIN_CAP = 40000;
constexpr unsigned CODE_BYTES = 44032;

DI void dma4w(unsigned voff, const char* sbase, unsigned ldsaddr) {
  asm volatile("s_mov_b32 m0, %2\n\ts_nop 0\n\tglobal_load_lds_dword %0, %1" ::"v"(voff), "s"(sbase), "s"(ldsaddr));
}
DI void dma_x4(const unsigned* v, const char* sbase, unsigned ldsaddr, bool low = false) {
  if (low) {
    asm volatile("s_mov_b32 m0, %5\n\ts_setprio 0\n\t"
               "global_load_lds_dwordx4 %0, %4\n\tglobal_load_lds_dwordx4 %1, %4 offset:1024\n\t"
               "global_load_lds_dwordx4 %2, %4 offset:2048\n\tglobal_load_lds_dwordx4 %3, %4 offset:3072\n\ts_setprio 3"
               ::"v"(v[0]), "v"(v[1]), "v"(v[2]), "v"(v[3]), "s"(sbase), "s"(ldsaddr));
    return;
  }
  asm volatile("s_mov_b32 m0, %5\n\ts_nop 0\n\t"
               "global_load_lds_dwordx4 %0, %4\n\tglobal_load_lds_dwordx4 %1, %4 offset:1024\n\t"
               "global_load_lds_dwordx4 %2, %4 offset:2048\n\tglobal_load_lds_dwordx4 %3, %4 offset:3072"
               ::"v"(v[0]), "v"(v[1]), "v"(v[2]), "v"(v[3]), "s"(sbase), "s"(ldsaddr));
}
DI void dma_x8(const unsigned* v, const char* sbase, unsigned ldsaddr) {
  asm volatile("s_mov_b32 m0, %9\n\ts_nop 0\n\t"
               "global_load_lds_dwordx4 %0, %8 offset:-4096\n\tglobal_load_lds_dwordx4 %1, %8 offset:-3072\n\t"
               "global_load_lds_dwordx4 %2, %8 offset:-2048\n\tglobal_load_lds_dwordx4 %3, %8 offset:-1024\n\t"
               "global_load_lds_dwordx4 %4, %8\n\tglobal_load_lds_dwordx4 %5, %8 offset:1024\n\t"
               "global_load_lds_dwordx4 %6, %8 offset:2048\n\tglobal_load_lds_dwordx4 %7, %8 offset:3072"
               ::"v"(v[0]), "v"(v[1]), "v"(v[2]), "v"(v[3]), "v"(v[4]), "v"(v[5]), "v"(v[6]), "v"(v[7]), "s"(sbase), "s"(ldsaddr + 4096u));
}
constexpr int PG(int i) { return i & 3; }
template <int N> DI void wait_vm() { asm volatile("s_waitcnt vmcnt(%0)" ::"i"(N) : "memory"); }
DI void wait_lgkm0() { asm volatile("" ::: "memory"); __builtin_amdgcn_s_waitcnt(0xC07F); asm volatile("" ::: "memory"); }
DI void barrier() {
  asm volatile("" ::: "memory");
  __builtin_amdgcn_s_waitcnt(0xC07F);
  __builtin_amdgcn_s_barrier();
  asm volatile("" ::: "memory");
}
#define SCHED() __builtin_amdgcn_sched_barrier(0)
DI s16x8 lds128(unsigned a) { return *(LAS s16x8*)(size_t)a; }
DI f32x4 ldsf4(unsigned a) { return *(LAS f32x4*)(size_t)a; }
DI f32x2 ldsf2(unsigned a) { return *(LAS f32x2*)(size_t)a; }
DI float ldsf1(unsigned a) { return *(LAS float*)(size_t)a; }
DI s16x4 ldstr4(unsigned a) { return __builtin_amdgcn_ds_read_tr16_b64_v4i16((LAS s16x4*)(size_t)a); }
DI s16x8 cat(s16x4 lo, s16x4 hi) { return __builtin_shufflevector(lo, hi, 0, 1, 2, 3, 4, 5, 6, 7); }
DI s16x8 ldstr(unsigned a0, unsigned a1) { return cat(ldstr4(a0), ldstr4(a1)); }
DI f32x4 mfma(s16x8 a, s16x8 b, f32x4 c) {
  return __builtin_amdgcn_mfma_f32_16x16x32_bf16(__builtin_bit_cast(bf16x8, a), __builtin_bit_cast(bf16x8, b), c, 0, 0, 0);
}
DI unsigned pk2(float a, float b) { f32x2 v = {a, b}; return __builtin_bit_cast(unsigned, __builtin_convertvector(v, bf16x2)); }
DI float u2f(unsigned u) { return __builtin_bit_cast(float, u); }
DI unsigned f2u(float u) { return __builtin_bit_cast(unsigned, u); }
DI float bflo(unsigned u) { return __builtin_bit_cast(float, u << 16); }
DI float bfhi(unsigned u) { return __builtin_bit_cast(float, u & 0xffff0000u); }
template <int CTRL> DI float dpp_add(float v) {
  return v + __builtin_bit_cast(float, __builtin_amdgcn_update_dpp(0, __builtin_bit_cast(int, v), CTRL, 0xf, 0xf, true));
}
DI float row16_sum(float v) {
  v = dpp_add<0xB1>(v);
  v = dpp_add<0x4E>(v);
  v = dpp_add<0x141>(v);
  v = dpp_add<0x140>(v);
  return v;
}
DI float smul(float a, float b) { float d; asm("v_mul_f32 %0, %1, %2" : "=v"(d) : "v"(a), "v"(b)); return d; }
DI float sadd(float a, float b) { float d; asm("v_add_f32 %0, %1, %2" : "=v"(d) : "v"(a), "v"(b)); return d; }
DI float sfma(float a, float b, float c) { float d; asm("v_fma_f32 %0, %1, %2, %3" : "=v"(d) : "v"(a), "v"(b), "v"(c)); return d; }
DI float sfms(float a, float b, float c) { float d; asm("v_fma_f32 %0, %1, %2, -%3" : "=v"(d) : "v"(a), "v"(b), "v"(c)); return d; }
DI void zero4(f32x4& v) {
  float a, b, c, d;
  asm volatile("v_mov_b32 %0, 0\n\tv_mov_b32 %1, 0\n\tv_mov_b32 %2, 0\n\tv_mov_b32 %3, 0" : "=v"(a), "=v"(b), "=v"(c), "=v"(d));
  v = (f32x4){a, b, c, d};
}
DI u32x2 swap32(float a, float b) { asm volatile("s_nop 1" : "+v"(a), "+v"(b)); return __builtin_amdgcn_permlane32_swap(f2u(a), f2u(b), false, false); }
DI u32x2 swap16(float a, float b) { asm volatile("s_nop 1" : "+v"(a), "+v"(b)); return __builtin_amdgcn_permlane16_swap(f2u(a), f2u(b), false, false); }
DI f32x2 pfma(f32x2 a, f32x2 b, f32x2 c) { return __builtin_elementwise_fma(a, b, c); }
DI f32x2 bc(float a) { return (f32x2){a, a}; }
DI f32x2 bco(float a) { f32x2 v = {a, a}; asm("" : "+v"(v)); return v; }
DI f32x2 cmul(f32x2 a, f32x2 b) { return (f32x2){sfms(a[0], b[0], smul(a[1], b[1])), sfma(a[0], b[1], smul(a[1], b[0]))}; }
DI f32x2 cfma(f32x2 a, f32x2 b, f32x2 c) { return (f32x2){sfms(a[0], b[0], sfms(a[1], b[1], c[0])), sfma(a[0], b[1], sfma(a[1], b[0], c[1]))}; }
DI f32x2 csel(bool p, f32x2 a, f32x2 b) { return (f32x2){p ? a[0] : b[0], p ? a[1] : b[1]}; }
DI void prog_set(unsigned a, unsigned v) {
  asm volatile("s_mov_b64 exec, 1\n\tds_write_b32 %0, %1\n\ts_mov_b64 exec, -1" ::"v"(a), "v"(v) : "memory");
}
DI void prog_wait(unsigned a, unsigned v) {
#pragma unroll 1
  for (int spin = 0; spin < SPIN_CAP * 16; ++spin) {
    if (__builtin_amdgcn_readfirstlane(*(volatile LAS unsigned*)(size_t)a) >= v) break;
    __builtin_amdgcn_s_sleep(1);
  }
  asm volatile("" ::: "memory");
}
DI void bar() { asm volatile("" ::: "memory"); __builtin_amdgcn_s_barrier(); asm volatile("" ::: "memory"); }
DI void dma1_dev(unsigned voff, const char* sbase, unsigned ldsaddr) {
  asm volatile("s_mov_b32 m0, %2\n\ts_nop 0\n\tglobal_load_lds_dwordx4 %0, %1 sc1" ::"v"(voff), "s"(sbase), "s"(ldsaddr) : "memory");
}
DI void dma2_dev(unsigned voff, const char* s0, const char* s1, unsigned ldsaddr) {
  asm volatile("s_mov_b32 m0, %3\n\ts_nop 0\n\tglobal_load_lds_dwordx4 %0, %1 sc1\n\tglobal_load_lds_dwordx4 %0, %2 offset:2048 sc1" ::"v"(voff), "s"(s0), "s"(s1), "s"(ldsaddr) : "memory");
}
DI void dma4_dev(unsigned voff, const char* s0, const char* s1, unsigned ldsaddr) {
  asm volatile("s_mov_b32 m0, %3\n\ts_nop 0\n\tglobal_load_lds_dwordx4 %0, %1 sc1\n\tglobal_load_lds_dwordx4 %0, %1 offset:1024 sc1\n\t"
               "global_load_lds_dwordx4 %0, %2 offset:2048 sc1\n\tglobal_load_lds_dwordx4 %0, %2 offset:3072 sc1" ::"v"(voff), "s"(s0), "s"(s1), "s"(ldsaddr) : "memory");
}
DI int kstep_of(int slot, int i) {
  const int q = slot + 4 * i + (slot >= 4 ? 28 : 0), cls = (q >= 14) + (q >= 28) + (q >= 42);
  return 4 * (q - 14 * cls) + ((0x9C >> (2 * cls)) & 3);
}
DI unsigned flag_of(int j, int c) {
  const int q = 14 * ((0x78 >> (2 * c)) & 3) + j;
  return q < 32 ? (q & 3) * 128 + (q >> 2) * 8 : 512 + (q & 3) * 128 + ((q - 32) >> 2) * 8;
}
DI void cnt_add(unsigned a) {
  asm volatile("s_mov_b64 exec, 1\n\tds_add_u32 %0, %1\n\ts_mov_b64 exec, -1" ::"v"(a), "v"(1u) : "memory");
}
DI f32x2 lo2(f32x4 v) { return __builtin_shufflevector(v, v, 0, 1); }
DI f32x2 hi2(f32x4 v) { return __builtin_shufflevector(v, v, 2, 3); }
DI void rows4(float v, float* o) {
  asm volatile("s_nop 1" : "+v"(v));
  const unsigned u = f2u(v);
  const u32x2 h = __builtin_amdgcn_permlane32_swap(u, u, false, false);
  const u32x2 a = __builtin_amdgcn_permlane16_swap(h.x, h.x, false, false), b = __builtin_amdgcn_permlane16_swap(h.y, h.y, false, false);
  o[0] = u2f(a.x); o[1] = u2f(a.y); o[2] = u2f(b.x); o[3] = u2f(b.y);
}
struct Col { f32x2 cs[4], bias[4]; };
DI void mkcol(Col& c, f32x4 cs0, f32x4 cs1, u32x2 b0, u32x2 b1) {
  c.cs[0] = lo2(cs0); c.cs[1] = hi2(cs0); c.cs[2] = lo2(cs1); c.cs[3] = hi2(cs1);
  c.bias[0] = (f32x2){bflo(b0.x), bfhi(b0.x)}; c.bias[1] = (f32x2){bflo(b0.y), bfhi(b0.y)};
  c.bias[2] = (f32x2){bflo(b1.x), bfhi(b1.x)}; c.bias[3] = (f32x2){bflo(b1.y), bfhi(b1.y)};
}
DI u32x4 gelu8(f32x4 a0, f32x4 a1, const Col& c, f32x2 rs, f32x2 nmrs) {
  f32x2 v[4], z[4];
  u32x4 o;
#define ST4(stmt) _Pragma("unroll") for (int h = 0; h < 4; ++h) { stmt; } SCHED();
  ST4(v[h] = pfma(c.cs[h], nmrs, c.bias[h]))
  ST4(v[h] = pfma(h == 0 ? lo2(a0) : h == 1 ? hi2(a0) : h == 2 ? lo2(a1) : hi2(a1), rs, v[h]))
  ST4(z[h] = v[h] * v[h])
  ST4(z[h] = pfma(z[h], bc(-0.10294324f), bc(-2.30220820f)))
  ST4(z[h] = z[h] * v[h])
  ST4((z[h] = (f32x2){__builtin_amdgcn_exp2f(z[h][0]), __builtin_amdgcn_exp2f(z[h][1])}))
  ST4(z[h] = z[h] + bc(1.f))
  ST4((z[h] = (f32x2){__builtin_amdgcn_rcpf(z[h][0]), __builtin_amdgcn_rcpf(z[h][1])}))
  ST4(v[h] = v[h] * z[h])
  ST4(o[h] = pk2(v[h][0], v[h][1]))
#undef ST4
  return o;
}

#define TRACE(i) do {} while (0)
#define TRACE2(i) do {} while (0)
#define TRACE3(i) do {} while (0)
#define TRACE4(i) do {} while (0)
#define STAMP(k) do {} while (0)
#define STAMPC(k) do {} while (0)
#define COUNT(k) do {} while (0)

__global__ void __launch_bounds__(512, 2) fused_kernel(Params p) {
  __shared__ __attribute__((aligned(16))) char lds_mem[LDS_BYTES];
  unsigned t_x = 0;
  const unsigned long long t_start = __builtin_amdgcn_s_memrealtime();
  unsigned long long pc;
  asm volatile("s_getpc_b64 %0" : "=s"(pc));
  const unsigned LB = (unsigned)(size_t)(LAS char*)lds_mem;
  const int tid = threadIdx.x, lane = tid & 63;
  const int w = (__builtin_amdgcn_readfirstlane(tid >> 6) + WOFF) & 7;
  if (w == CPW) {
    const unsigned co = ((unsigned)(lane & 15) * 32u + (blockIdx.x >> 3)) * 128u;
    dma4w(co < CODE_BYTES ? co : 0u, (const char*)(pc & ~255ull), LB + L_DUMMY + 512);
  }
  const int b = blockIdx.x, idx = b >> 3, xcd = b & 7;
  const int r = 8 * (xcd >> 2) + (idx >> 2), j = 4 * (xcd & 3) + (idx & 3);
  const int rot = (2 * (xcd & 3) + 4 * (xcd >> 2)) & 7;
  const int mtile = 4 * xcd + (idx >> 3), ntile = (idx + NOFF) & 7;
  const int i16 = lane & 15, g = lane >> 4, qq = i16 >> 2, pp = i16 & 3;
  const int d = w & 1;
  const int jm = j < 14 ? j : 13, lq = j & 1;
  const bool mlp = j < 14;
  u32x4 pre[4];
  unsigned v_lre, v_lim, v_ldt;
  if ((w >> 1) == 2) {
    const unsigned po = (unsigned)(32 * j + 16 * d + i16) * 2u;
    v_lre = *(const GAS unsigned short*)(p.lre + po); v_lim = *(const GAS unsigned short*)(p.lim + po); v_ldt = *(const GAS unsigned short*)(p.logdt + po);
    asm volatile("" ::: "memory");
  }
  u32x4 prb[2];
  if (w < 4) {
    const bool isb = w >= 2;
    const char* pb = isb ? p.w1 + (64 * (r & 7) + 32 * d) * 3584 + jm * 256 + (r >> 3) * 128 : p.x + (128 * r + 8 * j + 4 * d) * 1024;
    const unsigned po = isb ? (lane >> 3) * 3584 + (lane & 7) * 16 : g * 1024 + i16 * 16, ps = isb ? 28672 : 256;
#pragma unroll
    for (int i = 0; i < 4; ++i) pre[i] = *(const GAS u32x4*)(pb + i * ps + po);
    if (isb) {
#pragma unroll
      for (int i = 0; i < 2; ++i) prb[i] = *(const GAS u32x4*)((r & 8 ? p.bim : p.bre) + (32 * j + 4 * (r & 7) + 2 * d) * 1024 + i * 512 + (lane >> 5) * 1024 + (lane & 31) * 16);
    }
    asm volatile("" ::: "memory");
  }
  const int kgo = 2 * (idx >> 3), late = ((w + 2) >> 2) & 1, kg = 2 * (((w & 2) + d + KROT * ntile) & 3) + late;
  auto slot_of = [&](int role) { return (((role + 1) & 2) << 1) + (((((role + 1) & 2) ? 0 : kgo) + ((role & 1) ? 0 : 2) + d) & 3); };
  const int slot = slot_of(w >> 1);
  const u32x2 ep = *(const __attribute__((address_space(4))) u32x2*)(p.ws + WS_EPOCH);
  unsigned tagA, tagB;
  auto mktag = [&]() { tagA = ep.x; tagB = ep.y; asm volatile("" : "+s"(tagA), "+s"(tagB)); tagA += 1u; tagB += 1u; };
  const __amdgpu_buffer_rsrc_t wsr = __builtin_amdgcn_make_buffer_rsrc(p.ws, 0, WS_END, 0x00020000);
  const i32x4 wss = {(int)(size_t)p.ws, (int)((size_t)p.ws >> 32) & 0xffff, (int)WS_END, 0x00020000};
  auto fresh = [](int o) { asm volatile("" : "+s"(o) :: "memory"); return o; };
  const unsigned HB = LB + d * HALF;
  const unsigned PW = LB + L_FLG;
  s16x8 we[8][4];
  f32x4 acc2[4][4];
  const unsigned home = HB + ((2 - (w >> 1)) & 3) * SLOT;
  const unsigned r2l = (4 * g + qq) * 128 + pp * 8, r2x = (2 * g + (qq >> 1)) & 3;
  auto absorb = [&](int i, unsigned page) {
#pragma unroll
    for (int n = 0; n < 4; ++n) { const unsigned a = page + r2l + ((n ^ r2x) * 32); we[i][n] = ldstr(a, a + 2048); }
  };
  auto mkvc = [&](unsigned* vc) {
#pragma unroll
    for (int i = 0; i < 8; ++i) vc[i] = (lane >> 3) * 1024 + (((lane & 7) ^ (4 * (i & 1) + g)) * 16) + 4096 + i * 7168;
  };
  auto mkv2 = [&](unsigned* v2) {
#pragma unroll
    for (int i = 0; i < 4; ++i) v2[i] = (lane >> 3) * 1024 + (((((lane & 7) >> 1) ^ g) << 1) | (lane & 1)) * 16 + i * 7168;
  };
  auto loadC = [&](const unsigned* vc, int kgc, unsigned dst) {
    const unsigned o = ntile * 65536 + kgc * 128;
    dma_x8(vc, p.cre + o, dst);
    dma_x8(vc, p.cim + o, dst + 8192);
  };
  const char* w2b = p.w2 + ntile * 128;
  auto adv = [&](unsigned& a, int dl) { a += dl; asm volatile("" : "+v"(a)); };
  const int tok = 32 * qq + 16 * (i16 & 1) + ((i16 >> 1) & 1);
  const unsigned kx = (g ^ (i16 >> 1)) * 16;
  const unsigned awb = LB + SL_W + (8 * g + qq) * 256 + pp * 8, awx = qq | ((g & 1) << 2);
  auto sum32 = [&](const u32x4* v) {
    float s0 = 0.f, s1 = 0.f;
#pragma unroll
    for (int i = 0; i < 4; ++i)
#pragma unroll
      for (int e = 0; e < 4; ++e) { s0 += bflo(v[i][e]); s1 += bfhi(v[i][e]); }
    return row16_sum(s0 + s1);
  };
  auto touch = [&]() {
    dma4w(d ? (lane & 15) * 128 : (lane < 56 ? lane : 0) * 128, d ? p.cre + b * 2048 : p.w2 + b * 7168, LB + L_DUMMY + d * 256);
    if (d) dma4w((lane & 15) * 128, p.cim + b * 2048, LB + L_DUMMY + d * 256);
  };
  auto zero2 = [&]() {
#pragma unroll
    for (int n = 0; n < 4; ++n) {
#pragma unroll
      for (int m = 0; m < 4; ++m) acc2[n][m] = (f32x4){0, 0, 0, 0};
      asm volatile("" : "+v"(acc2[n][0]), "+v"(acc2[n][1]), "+v"(acc2[n][2]), "+v"(acc2[n][3]));
    }
  };
  int ksl;
  unsigned hb, crd, pubo = 0xffff0000u;
  const int fm = WS_FLAG + mtile * 2048 + slot * 128, fs = WS_FLAG + mtile * 2048 + 1024 + (kg >> 1) * 64;
  u64x8 fl;
  auto snap = [&](bool ssm) { fl = __builtin_bit_cast(u64x8, sbl16(wss, fresh(ssm ? fs : fm), 1)); SCHED(); };
  auto seen = [&]() { SCHED(); asm volatile("" : "+s"(fl)); };
  auto setup2 = [&](int nm) {
    ksl = (lane < nm ? kstep_of(slot, lane) : 56 + 4 * kg + late + ((lane - nm) & 7)) * 4096;
    if (NOC & 16) ksl = (w * 11 + (lane & 1)) * 4096;
    ksl += w == 1 ? 2048 : 0;
    hb = WS_H + (unsigned)mtile * H_MT + lane * 16;
    crd = home + i16 * 128 + (g & 1) * 8 + (((g >> 1) ^ (i16 >> 1)) * 16);
    asm volatile("" : "+v"(ksl), "+v"(hb), "+v"(crd));
  };

  auto roleD = [&]() __attribute__((always_inline)) {
    __builtin_amdgcn_s_waitcnt(0x0F70);
    __builtin_amdgcn_s_setprio(3);
    unsigned vx[8], vw[8], vb[4], v2[4];
#pragma unroll
    for (int i = 0; i < 8; ++i) vw[i] = g * 3584 + ((((i16 >> 1) ^ (g | (((i >> 1) & 1) << 2))) << 1) | (i16 & 1)) * 16 + 4096 + i * (14336 - 1024);
    const char* wb = p.w1 + jm * 256 + d * (32 * 3584);
    auto part1 = [&](int c, unsigned slot) { if (mlp) dma_x8(vw, wb + ((c + rot) & 7) * (64 * 3584), LB + slot + SL_W + d * 8192); };
    asm volatile("" :: "s"(wb));
    STAMP(18); t_x = (unsigned)(__builtin_amdgcn_s_memrealtime() - t_start); asm volatile("" : "+s"(t_x));
    part1(0, 0);
#pragma unroll
    for (int i = 0; i < 8; ++i)
      vx[i] = (32 * (g >> 1) + 16 * ((lane >> 3) & 1) + (g & 1)) * 1024 + (((lane & 7) ^ (4 * (i & 1) + g)) * 16) + 4096 + (i & 1) * 65536 + (i >> 1) * 2048 - i * 1024;
#pragma unroll
    for (int i = 0; i < 4; ++i) vb[i] = (lane >> 3) * 1024 + (((lane & 7) ^ (4 * (i & 1) + g)) * 16) + i * 7168;
    const char* xb = p.x + (r * 128 + d * 8) * 1024;
    const char* bb = (d ? p.bim : p.bre) + j * 32768;
    auto partx = [&](int c, unsigned slot) { dma_x8(vx, xb + ((c + rot) & 7) * 128, LB + slot + d * 8192); };
    auto partb = [&](int c, unsigned slot) { dma_x4(vb, bb + ((c + rot) & 7) * 128, LB + slot + SL_B + d * 4096); };
    auto part0 = [&](int c, unsigned slot) { partx(c, slot); partb(c, slot); };
    mkv2(v2);
    unsigned ra[4];
#pragma unroll
    for (int n = 0; n < 4; ++n) ra[n] = home + r2l + ((n ^ r2x) * 32);
    int kt = kstep_of(slot_of((lane >> 3) & 3), lane & 7) * 32768;
    asm volatile("" : "+v"(kt), "+v"(ra[0]), "+v"(ra[1]), "+v"(ra[2]), "+v"(ra[3]), "+v"(v2[0]), "+v"(v2[1]), "+v"(v2[2]), "+v"(v2[3]));
    bool low = false;
    auto page = [&](int role, int i, int grp, int pg) { if ((NOC & 4) && low) return; dma_x4(v2, w2b + (unsigned)__builtin_amdgcn_readlane(kt, 8 * role + i), HB + grp * SLOT + pg * 4096, low); };
    auto take = [&](int i) {
      SCHED();
#pragma unroll
      for (int n = 0; n < 4; ++n) we[i][n] = ldstr(ra[n] + PG(i) * 4096, ra[n] + PG(i) * 4096 + 2048);
      SCHED();
    };
    auto own = [&](int i) { page(3, i, 3, PG(i)); };
    unsigned l16 = lane * 16, la = home + 12288 + lane * 16, lb = LB + L_ST1 + d * 1024 + lane * 16, lc = LB + L_BS + d * 512 + (lane & 31) * 16;
    unsigned wa = LB + L_CS + d * 256 + (lane & 31) * 8 + (lane >> 5) * 4, wb2 = LB + L_LS + (2 * d + (lane >> 5)) * 288 + i16 * 16 + (g & 1) * 4;
    asm volatile("" : "+v"(l16), "+v"(la), "+v"(lb), "+v"(lc), "+v"(wa), "+v"(wb2));
    auto gather = [&](bool bs) {
      const char* cp = p.ws + WS_CS + jm * 8192 + d * 4096;
      dma4_dev(l16, cp, cp, home + 12288);
      dma2_dev(l16, p.ws + WS_STAT + r * 2048 + d * 1024, p.ws + WS_STAT + (mtile >> 1) * 2048 + d * 1024 - 2048, LB + L_ST1 + d * 1024);
      if (bs) dma1_dev(l16, p.ws + WS_BS + j * 1024, LB + L_BS);
    };
    auto check = [&](auto between) -> bool {
      auto ld16 = [](unsigned a) { return *(volatile LAS u32x4*)(size_t)a; };
      auto bad = [&](unsigned a, u32x4 v) { return __builtin_amdgcn_bitop3_b32(v.z, tagB, __builtin_amdgcn_bitop3_b32(v.x, tagA, a, 0xBE), 0xBE); };
      const u32x4 c0 = ld16(la), c1 = ld16(la + 1024), c2 = ld16(la + 2048), c3 = ld16(la + 3072), s1 = ld16(lb), s2 = ld16(lb + 2048), s3 = ld16(lc);
      SCHED(); between(); SCHED();
      const u32x2 h = swap32(sadd(sadd(u2f(c0.y), u2f(c1.y)), sadd(u2f(c2.y), u2f(c3.y))), sadd(sadd(u2f(c0.w), u2f(c1.w)), sadd(u2f(c2.w), u2f(c3.w))));
      *(LAS float*)(size_t)wa = sadd(u2f(h.x), u2f(h.y));
      *(LAS unsigned*)(size_t)wb2 = s1.y;
      *(LAS unsigned*)(size_t)(wb2 + 8) = s1.w;
      const unsigned e = bad(bad(bad(bad(bad(bad(bad(0u, c0), c1), c2), c3) & (mlp ? ~0u : 0u), s1), s2), s3);
      return __builtin_amdgcn_ballot_w64(e != 0) == 0;
    };
    const unsigned me = PW + w * 4;
    zero2();
    setup2(8);
    auto landed = [&]() { if (mlp) wait_vm<20>(); else wait_vm<12>(); };
    SCHED();
    part0(0, 0);
    if (TOUCH == 4) touch();
    part1(1, SLOT); part0(1, SLOT);
    STAMP(1);
    mktag();
    if (mlp) wait_vm<20 + (TOUCH == 4)>(); else wait_vm<12 + (TOUCH == 4)>();
    STAMP(19);
    bar();
    STAMP(2);
    part1(2, 2 * SLOT); part0(2, 2 * SLOT); landed(); if (TOUCH == 1) touch();
    bar();
    if (d) dma4w(lane * 4, p.b1 + jm * 256, LB + L_B1);
    part1(3, 0); part0(3, 0); landed();
    unsigned vt = (lane < 56 ? lane : 0) * 128;
    asm volatile("" : "+v"(vt));
    auto l2t = [&](int t) { if (t < L2T) dma4w(vt, p.w2 + (t * 64 + 2 * idx + d) * 7168, LB + L_DUMMY + d * 256); };
    auto l2c = [&](int t) { if (t < L2C) dma4w(l16 * 8, (t ? p.cim : p.cre) + (2 * idx + d) * 8192, LB + L_DUMMY + d * 256); };
    auto s3 = [&]() { if (L2T < 4) dma4w((lane < 28 ? lane : 0) * 1024, p.w2 + (2 * idx + d) * 28672 + ((3u - ((unsigned)(size_t)p.w2 >> 7)) & 7u) * 128, LB + L_DUMMY + d * 256); };
    if (S3AT == 1) s3();
    l2t(0);
    bar();
    STAMPC(21);
    dma_x4(vx, p.x + (128 * (mtile >> 1) + 8 * (mtile & 1) + 4 * d) * 1024 + ntile * 128 - 4096, home + 12288);
    part1(4, SLOT); part0(4, SLOT); landed(); if (S3AT == 2) s3();
    bar();
    own(0); part1(5, 2 * SLOT); part0(5, 2 * SLOT); landed(); if (S3AT == 3) s3();
    bar();
    gather(d); part1(6, 0); take(0); part0(6, 0); landed(); if (S3AT == 4) s3();
    bar();
    part1(7, SLOT);
    const bool ok = check([&]() { partx(7, SLOT); });
    SCHED();
    partb(7, SLOT);
    if (__builtin_expect(!ok, 0)) {
#pragma unroll 1
      for (int spin = 0; spin < SPIN_CAP; ++spin) {
        COUNT(20);
        __builtin_amdgcn_s_sleep(10);
        gather(true);
        wait_vm<0>();
        if (check([]() {})) break;
      }
    }
    landed();
    barrier();
    STAMP(3); STAMPC(22);
    page(0, 0, 2, 0); page(0, 1, 2, 1); page(0, 2, 2, 2); page(0, 3, 2, 3); page(0, 4, 2, 4); own(1);
    wait_vm<12>();
    bar();
    STAMP(4);
    own(2); page(2, 0, 0, 0); page(2, 1, 0, 1); page(2, 2, 0, 2); page(2, 3, 0, 3); page(2, 4, 0, 4);
    wait_vm<20>();
    STAMP(5);
    bar();
    TRACE4(31);
    low = true;
    STAMP(6);
    take(1); take(2);
    page(0, 5, 2, 0); page(0, 6, 2, 1); page(0, 7, 2, 2);
    wait_vm<12>(); prog_set(me, 1);
    own(3); own(4); wait_lgkm0(); own(5); own(6);
    wait_vm<16>(); prog_set(me, 2);
    page(1, 0, 1, 0); page(1, 1, 1, 1); page(1, 2, 1, 2);
    wait_vm<16>(); take(3); take(4); take(5); wait_lgkm0();
    own(7);
    page(1, 3, 1, 3); page(1, 4, 1, 4);
    auto c8 = [&](const char* src, unsigned dst) { if (NOC & 1) return; dma_x4(vb, src, dst, true); dma_x4(vb, src + 32768, dst + 4096, true); };
    const unsigned co = ntile * 65536 + kg * 128;
    c8(p.cre + co, home);
    wait_vm<8>();
    prog_set(me, 3);
    take(6); take(7);
    prog_wait(PW + d * 4, 1);
    c8(p.cim + co, home + 8192);
    STAMP(7);
    snap(false);
    c8(p.cre + (co ^ 512), HB + 2 * SLOT); c8(p.cim + (co ^ 512), HB + 2 * SLOT + 8192);
    seen();
    STAMP(8);
  };
  auto roleL = [&]() __attribute__((always_inline)) {
    if (TOUCH == 0) touch();
    if (TLBW && d) { dma4w(0, p.cre + ntile * 65536, LB + L_DUMMY + 256); dma4w(0, p.cim + ntile * 65536, LB + L_DUMMY + 256); dma4w((lane & 1) * 0x1bf000, p.w2, LB + L_DUMMY + 256); }
    f32x4 acr[8], aci[8];
#pragma unroll
    for (int m = 0; m < 8; ++m) { acr[m] = (f32x4){0, 0, 0, 0}; aci[m] = (f32x4){0, 0, 0, 0}; asm volatile("" : "+v"(acr[m]), "+v"(aci[m])); }
    unsigned ax[2], ab[2];
    ax[0] = LB + i16 * 128 + kx; ax[1] = ax[0] ^ 64;
    ab[0] = LB + SL_B + (16 * d + i16) * 128 + kx; ab[1] = ab[0] ^ 64;
    unsigned sa = LB + L_LS + g * 288, csa = LB + L_BS + (16 * d + i16) * 16 + 4;
    asm volatile("" : "+v"(sa), "+v"(csa), "+v"(ax[0]), "+v"(ax[1]), "+v"(ab[0]), "+v"(ab[1]));
    s16x8 xf[2][8], bf[2][2];
    auto rd = [&](int set, int i) {
      if (i < 2) bf[set][i] = lds128(ab[set] + i * 4096);
      else xf[set][i - 2] = lds128(ax[set] + (i - 2) * 2048);
    };
    auto mm = [&](int set, int i) {
      if (i & 1) aci[i >> 1] = mfma(xf[set][i >> 1], bf[set][1], aci[i >> 1]);
      else acr[i >> 1] = mfma(xf[set][i >> 1], bf[set][0], acr[i >> 1]);
    };
    asm volatile("" : "+v"(v_lre), "+v"(v_lim), "+v"(v_ldt));
    const float lr = bflo(v_lre), li = bflo(v_lim);
    const float dt = bflo(pk2(expf(bflo(v_ldt)), 0.f));
    const float ang = smul(dt, li);
    const float rv = smul(ang, 0.15915494f);
    const float rev = (rv - __builtin_rintf(rv)) + sfma(ang, 6.4206382e-9f, sfms(ang, 0.15915494f, rv));
    float sn = __builtin_amdgcn_sinf(rev), cn = __builtin_amdgcn_cosf(rev);
    asm volatile("s_nop 0" : "+v"(sn), "+v"(cn));
    const float ex = expf(smul(dt, lr));
    f32x2 a1 = {smul(ex, cn), smul(ex, sn)};
    const float nr = a1[0] - 1.f, ni = a1[1], iden = 2.f / sfma(lr, lr, smul(li, li));
    f32x2 kk = {smul(sfma(nr, lr, smul(ni, li)), iden), smul(sfms(ni, lr, smul(nr, li)), iden)};
    f32x2 t = cmul(a1, a1); t = cmul(t, t); t = cmul(t, t);
    f32x2 a16 = cmul(t, t);
    f32x2 a32 = cmul(a16, a16);
    const f32x2 a64 = cmul(a32, a32), a128 = cmul(a64, a64), a256 = cmul(a128, a128);
    f32x2 a512 = cmul(a256, a256);
    const f32x2 one = {1.f, 0.f};
    f32x2 aseg = cmul(kk, cmul(csel(g & 1, a32, one), csel(g & 2, a64, one)));
    const int e4 = (r - 1 - g) & 3;
    f32x2 acar = cmul(csel(e4 & 1, a128, one), csel(e4 & 2, a256, one));
    const f32x2 ar = bco(a1[0]), ai = bco(a1[1]);
    asm volatile("" :: "v"(ar), "v"(ai));
    asm volatile("" : "+v"(kk), "+v"(a16), "+v"(a32), "+v"(a512), "+v"(aseg), "+v"(acar));
    const unsigned on = (qq == g) ? ((pp & 1) ? 0xffff0000u : 0xffffu) : 0u;
    unsigned m0 = (pp & 2) ? 0u : on, m1 = (pp & 2) ? on : 0u;
    auto scan = [&](f32x2 h0, f32x2 h1, const f32x2* ur, const f32x2* ui, auto out) {
      const u32x4 z0 = {m0 & 0x3f803f80u, m1 & 0x3f803f80u, 0u, 0u}, z1 = {0u, 0u, z0.x, z0.y};
      const f32x4 zz = {0, 0, 0, 0};
      f32x2 hr = {h0[0], h1[0]}, hi = {-h0[1], -h1[1]};
      u32x4 fr[4], fi[4];
      f32x4 tr[2], ti[2];
      SCHED();
#pragma unroll
      for (int mp = 0; mp < 5; ++mp) {
        if (mp > 0) {
#pragma unroll
          for (int k = 0; k < 2; ++k) {
            tr[k] = mfma(__builtin_bit_cast(s16x8, fr[mp - 1]), __builtin_bit_cast(s16x8, k ? z1 : z0), zz);
            ti[k] = mfma(__builtin_bit_cast(s16x8, fi[mp - 1]), __builtin_bit_cast(s16x8, k ? z1 : z0), zz);
          }
          SCHED();
        }
        if (mp < 4) {
#pragma unroll
          for (int e = 0; e < 4; ++e) {
            const int s = 4 * mp + e;
            const f32x2 t1 = ur ? pfma(hr, -ai, ui[s]) : hr * -ai; SCHED();
            const f32x2 t0 = ur ? pfma(hi, ai, ur[s]) : hi * ai; SCHED();
            hi = pfma(hi, ar, t1); SCHED();
            hr = pfma(hr, ar, t0); SCHED();
            fi[mp][e] = pk2(hi[0], hi[1]); SCHED();
            fr[mp][e] = pk2(hr[0], hr[1]); SCHED();
          }
        }
        if (mp > 0) { out(2 * mp - 2, tr[0], ti[0]); out(2 * mp - 1, tr[1], ti[1]); SCHED(); }
      }
    };
    auto pack = [](f32x4 a, f32x4 b) { return (u32x4){pk2(a[0], a[1]), pk2(a[2], a[3]), pk2(b[0], b[1]), pk2(b[2], b[3])}; };
    u32x4 yb[8];
    auto resp = [&]() { scan(aseg, cmul(a16, aseg), nullptr, nullptr, [&](int q, f32x4 a, f32x4 b) { yb[q] = pack(a, b); asm volatile("" : "+v"(yb[q])); }); };
    if (YPRE) resp();
    mktag();
    __builtin_amdgcn_s_waitcnt(0x0F70);
    __builtin_amdgcn_s_setprio(1);
    STAMP(1);
    bar();
    STAMP(2); TRACE3(1);
#pragma unroll
    for (int i = 0; i < 10; ++i) rd(0, i);
    wait_lgkm0();
    auto stage1 = [&](int dl) {
      SCHED();
#pragma unroll
      for (int i = 0; i < 16; ++i) {
        mm(0, i); SCHED();
        if (i < 10) rd(1, i);
        if (i == 11) { adv(ax[0], dl); adv(ab[0], dl); }
        SCHED();
      }
    };
    stage1(SLOT);
    barrier();
    TRACE3(2);
    int ph = 0;
#pragma unroll 1
    for (int c = 2; c <= 8; ++c) {
      const int dl = ph == 2 ? -2 * (int)SLOT : (int)SLOT;
      ph = ph == 2 ? 0 : ph + 1;
      SCHED();
#pragma unroll
      for (int i = 0; i < 16; ++i) {
        mm(1, i); SCHED();
        if (i < 10) rd(0, i);
        if (i == 11) { adv(ax[1], dl); adv(ab[1], dl); }
        SCHED();
      }
      wait_lgkm0();
      stage1(ph == 2 ? -2 * (int)SLOT : (int)SLOT);
      TRACE(2 * c);
      barrier();
      TRACE(2 * c + 1); TRACE3(c + 1);
    }
    __builtin_amdgcn_s_setprio(LPRIO);
    STAMP(4);
    f32x2 rs[16], mrs[16];
    float csr, csi;
#pragma unroll
    for (int i = 0; i < 16; ++i) {
      mm(1, i); SCHED();
      if (i == 0) { csr = ldsf1(csa); csi = ldsf1(csa + 512); }
      if (i < 8) { const f32x4 t = ldsf4(sa + i * 16); rs[i] = lo2(t); mrs[i] = hi2(t); }
      SCHED();
    }
    const f32x2 ncr = bco(-csr), nci = bco(-csi);
    STAMP(5);
    f32x2 ur[16], ui[16], Er, Ei;
    SCHED();
#pragma unroll
    for (int s = 0; s < 16; ++s) {
      if (s < 8) { const f32x4 t = ldsf4(sa + (s + 8) * 16); rs[s + 8] = lo2(t); mrs[s + 8] = hi2(t); SCHED(); }
      ur[s] = mrs[s] * ncr; SCHED();
      ui[s] = mrs[s] * nci; SCHED();
      ur[s] = pfma((s & 1) ? hi2(acr[s >> 1]) : lo2(acr[s >> 1]), rs[s], ur[s]); SCHED();
      ui[s] = pfma((s & 1) ? hi2(aci[s >> 1]) : lo2(aci[s >> 1]), rs[s], ui[s]); SCHED();
      if (s == 0) { Er = ur[0]; Ei = ui[0]; }
      else {
        const f32x2 t0 = pfma(Ei, -ai, ur[s]); SCHED();
        const f32x2 t1 = pfma(Er, ai, ui[s]); SCHED();
        Er = pfma(Er, ar, t0); SCHED();
        Ei = pfma(Ei, ar, t1); SCHED();
      }
    }
    TRACE2(1);
    const f32x2 E0 = {Er[0], Ei[0]};
    const f32x2 E = cfma(a16, E0, (f32x2){Er[1], Ei[1]});
    f32x2 P = {0.f, 0.f}, Tt = {0.f, 0.f};
    float er4[4], ei4[4];
    rows4(E[0], er4); rows4(E[1], ei4);
#pragma unroll
    for (int s = 0; s < 4; ++s) {
      const f32x2 e = {er4[s], ei4[s]};
      P = csel(s == g, Tt, P);
      Tt = s ? cfma(a32, Tt, e) : e;
    }
    const unsigned totb = WS_TOT + (unsigned)(j * 2 + d) * 4096;
    if (g == 0) __builtin_amdgcn_raw_buffer_store_b128((u32x4){tagA, f2u(Tt[0]), tagB, f2u(Tt[1])}, wsr, totb + r * 256 + i16 * 16, 0, 16);
    STAMP(6);
    TRACE2(2);
    {
      const f32x2 kr = bco(kk[0]), nki = bco(-kk[1]);
#pragma unroll
      for (int s = 0; s < 16; ++s) {
        const f32x2 t0 = ur[s] * kr; SCHED();
        const f32x2 t1 = ur[s] * nki; SCHED();
        ur[s] = pfma(ui[s], nki, t0); SCHED();
        ui[s] = pfma(ui[s], -kr, t1); SCHED();
      }
    }
#pragma unroll
    for (int s = 0; s < 16; s += 4) asm volatile("" : "+v"(ur[s]), "+v"(ui[s]), "+v"(ur[s + 1]), "+v"(ui[s + 1]), "+v"(ur[s + 2]), "+v"(ui[s + 2]), "+v"(ur[s + 3]), "+v"(ui[s + 3]));
    f32x4 hr4[8], hi4[8];
    {
      const f32x2 kP = cmul(kk, P);
      scan(kP, cfma(a16, kP, cmul(kk, E0)), ur, ui, [&](int q, f32x4 a, f32x4 b) { hr4[q] = a; hi4[q] = b; asm volatile("" : "+v"(hr4[q]), "+v"(hi4[q])); });
    }
    TRACE2(3);
    prog_wait(PW + (6 + d) * 4, 1);
#pragma unroll
    for (int i = 0; i < 5; ++i) absorb(i, home + i * 4096);
    unsigned v2[4], vt[4];
    mkv2(v2);
#pragma unroll
    for (int i = 0; i < 4; ++i) { vt[i] = (4 * i + g < r ? g * 256 : -i * 1024) + i16 * 16 + 4096; asm volatile("" : "+v"(vt[i]), "+v"(v2[i])); }
    {
      const i32x4 tr = {wss[0], wss[1], (int)(totb + r * 256), wss[3]};
#pragma unroll 1
      for (int spin = 0; spin < 64; ++spin) {
        int n, t[15];
        asm volatile("s_buffer_load_dword %1, %16, %17 offset:0 glc\n\ts_buffer_load_dword %2, %16, %17 offset:256 glc\n\ts_buffer_load_dword %3, %16, %17 offset:512 glc\n\ts_buffer_load_dword %4, %16, %17 offset:768 glc\n\ts_buffer_load_dword %5, %16, %17 offset:1024 glc\n\ts_buffer_load_dword %6, %16, %17 offset:1280 glc\n\ts_buffer_load_dword %7, %16, %17 offset:1536 glc\n\ts_buffer_load_dword %8, %16, %17 offset:1792 glc\n\ts_buffer_load_dword %9, %16, %17 offset:2048 glc\n\ts_buffer_load_dword %10, %16, %17 offset:2304 glc\n\ts_buffer_load_dword %11, %16, %17 offset:2560 glc\n\ts_buffer_load_dword %12, %16, %17 offset:2816 glc\n\ts_buffer_load_dword %13, %16, %17 offset:3072 glc\n\ts_buffer_load_dword %14, %16, %17 offset:3328 glc\n\ts_buffer_load_dword %15, %16, %17 offset:3584 glc\n\t"
                     "s_mov_b32 %0, 0\n\ts_waitcnt lgkmcnt(0)\n\t"
                     "s_cmp_eq_u32 %1, %18\n\ts_addc_u32 %0, %0, 0\n\ts_cmp_eq_u32 %2, %18\n\ts_addc_u32 %0, %0, 0\n\ts_cmp_eq_u32 %3, %18\n\ts_addc_u32 %0, %0, 0\n\ts_cmp_eq_u32 %4, %18\n\ts_addc_u32 %0, %0, 0\n\ts_cmp_eq_u32 %5, %18\n\ts_addc_u32 %0, %0, 0\n\ts_cmp_eq_u32 %6, %18\n\ts_addc_u32 %0, %0, 0\n\ts_cmp_eq_u32 %7, %18\n\ts_addc_u32 %0, %0, 0\n\ts_cmp_eq_u32 %8, %18\n\ts_addc_u32 %0, %0, 0\n\ts_cmp_eq_u32 %9, %18\n\ts_addc_u32 %0, %0, 0\n\ts_cmp_eq_u32 %10, %18\n\ts_addc_u32 %0, %0, 0\n\ts_cmp_eq_u32 %11, %18\n\ts_addc_u32 %0, %0, 0\n\ts_cmp_eq_u32 %12, %18\n\ts_addc_u32 %0, %0, 0\n\ts_cmp_eq_u32 %13, %18\n\ts_addc_u32 %0, %0, 0\n\ts_cmp_eq_u32 %14, %18\n\ts_addc_u32 %0, %0, 0\n\ts_cmp_eq_u32 %15, %18\n\ts_addc_u32 %0, %0, 0"
                     : "=&s"(n), "=&s"(t[0]), "=&s"(t[1]), "=&s"(t[2]), "=&s"(t[3]), "=&s"(t[4]), "=&s"(t[5]), "=&s"(t[6]), "=&s"(t[7]), "=&s"(t[8]), "=&s"(t[9]), "=&s"(t[10]), "=&s"(t[11]), "=&s"(t[12]), "=&s"(t[13]), "=&s"(t[14]) : "s"(tr), "s"(totb), "s"(tagA) : "scc", "memory");
        if (n >= r) break;
        COUNT(20);
      }
    }
    STAMP(7);
    auto fetch = [&]() {
      asm volatile("s_mov_b32 m0, %5\n\ts_nop 0\n\tglobal_load_lds_dwordx4 %0, %4 sc1\n\tglobal_load_lds_dwordx4 %1, %4 offset:1024 sc1\n\t"
                   "global_load_lds_dwordx4 %2, %4 offset:2048 sc1\n\tglobal_load_lds_dwordx4 %3, %4 offset:3072 sc1"
                   ::"v"(vt[0]), "v"(vt[1]), "v"(vt[2]), "v"(vt[3]), "s"(p.ws + totb - 4096), "s"(home + 16384) : "memory");
    };
    wait_lgkm0();
    if (r > 0) fetch();
    TRACE2(14);
    if (!YPRE) resp();
    f32x2 wt[4];
#pragma unroll
    for (int i = 3; i >= 0; --i) {
      const bool v = 4 * i + g < r;
      wt[i] = csel(v, acar, (f32x2){0.f, 0.f});
      if (i) acar = csel(v, cmul(acar, a512), acar);
      asm volatile("" : "+v"(wt[i]));
    }
    setup2(6);
    unsigned ho = WS_H + (unsigned)(2 * r) * H_MT + (unsigned)(56 + 2 * j + d) * 4096 + lane * 16, ta = home + 16384 + lane * 16;
    asm volatile("" : "+v"(ho), "+v"(ta), "+v"(m0), "+v"(m1));
    TRACE2(4);
    wait_vm<0>();
    TRACE2(15);
    float cr = 0.f, ci = 0.f;
    if (r > 0) {
#pragma unroll 1
      for (int spin = 0; spin < SPIN_CAP; ++spin) {
        u32x4 tv[4];
        unsigned e = 0;
#pragma unroll
        for (int i = 0; i < 4; ++i) tv[i] = *(volatile LAS u32x4*)(size_t)(ta + i * 1024);
        f32x2 c[2];
#pragma unroll
        for (int i = 0; i < 4; ++i) {
          const f32x2 t = {u2f(tv[i].y), u2f(tv[i].w)};
          c[i & 1] = i < 2 ? cmul(wt[i], t) : cfma(wt[i], t, c[i & 1]);
          e = __builtin_amdgcn_bitop3_b32(tv[i].z, tagB, __builtin_amdgcn_bitop3_b32(tv[i].x, tagA, e, 0xBE), 0xBE);
        }
        const u32x2 h = swap32(sadd(c[0][0], c[1][0]), sadd(c[0][1], c[1][1]));
        const float x = sadd(u2f(h.x), u2f(h.y));
        const u32x2 h2 = swap16(x, x);
        const float y = sadd(u2f(h2.x), u2f(h2.y));
        const u32x2 h3 = swap32(y, y);
        cr = u2f(h3.x); ci = u2f(h3.y);
        if (__builtin_expect(__builtin_amdgcn_ballot_w64(e != 0) == 0, 1)) break;
        COUNT(20);
        __builtin_amdgcn_s_sleep(8);
        fetch();
        wait_vm<0>();
      }
    }
    STAMP(8);
    {
      const unsigned c0 = pk2(cr, cr), c1 = pk2(ci, ci), c2 = pk2(-ci, -ci);
      const s16x8 are = __builtin_bit_cast(s16x8, (u32x4){m0 & c0, m1 & c0, m0 & c1, m1 & c1}), aim = __builtin_bit_cast(s16x8, (u32x4){m0 & c2, m1 & c2, m0 & c0, m1 & c0});
      u32x4 o[8];
      SCHED();
#pragma unroll
      for (int q = 0; q < 10; ++q) {
        if (q < 8) {
          hr4[q] = mfma(are, __builtin_bit_cast(s16x8, yb[q]), hr4[q]);
          hi4[q] = mfma(aim, __builtin_bit_cast(s16x8, yb[q]), hi4[q]);
          SCHED();
        }
        if (q >= 2) { o[q - 2] = pack(hr4[q - 2], hi4[q - 2]); __builtin_amdgcn_raw_buffer_store_b128(o[q - 2], wsr, ho + ((q - 2) & 3) * 1024, ((q - 2) >> 2) * H_MT, HST); }
        SCHED();
      }
      TRACE2(5);
      dma_x4(v2, w2b + kstep_of(slot, 5) * 32768, home + 16384);
      asm volatile("" :: "v"(o[0]), "v"(o[1]), "v"(o[2]), "v"(o[3]), "v"(o[4]), "v"(o[5]), "v"(o[6]), "v"(o[7]), "v"(ho));
      STAMP(19);
      pubo = lane < 2 ? WS_FLAG + (2 * r + lane) * 2048 + 1024 + (2 * j + d) * 8 : pubo;
    }
    STAMP(18);
  };
  auto roleB = [&]() __attribute__((always_inline)) {
    mktag();
    {
      float s0 = 0.f, s1 = 0.f;
#pragma unroll
      for (int e = 0; e < 8; ++e) { s0 = sadd(s0, bflo(prb[e >> 2][e & 3])); s1 = sadd(s1, bfhi(prb[e >> 2][e & 3])); }
      *(LAS float*)(size_t)(LB + L_LS + 512 + (4 * d + g) * 4) = row16_sum(s0 + s1);
      __builtin_amdgcn_s_waitcnt(0x0F70);
    }
    if (mlp) {
      f32x4 acc[6][4];
#pragma unroll
      for (int s = 0; s < 6; ++s) {
#pragma unroll
        for (int m = 0; m < 4; ++m) acc[s][m] = (f32x4){0, 0, 0, 0};
        asm volatile("" : "+v"(acc[s][0]), "+v"(acc[s][1]), "+v"(acc[s][2]), "+v"(acc[s][3]));
      }
      unsigned ax[2], aw[6];
      ax[0] = LB + (64 * d + i16) * 128 + kx; ax[1] = ax[0] ^ 64;
#pragma unroll
      for (int s = 0; s < 6; ++s) aw[s] = awb + ((s ^ awx) * 32);
      unsigned sta = LB + L_LS + qq * 288 + (8 * d + ((i16 >> 1) & 1)) * 16 + (i16 & 1) * 4, csa = LB + L_CS + g * 16;
      asm volatile("" : "+v"(ax[0]), "+v"(ax[1]), "+v"(aw[0]), "+v"(aw[1]), "+v"(aw[2]), "+v"(aw[3]), "+v"(aw[4]), "+v"(aw[5]), "+v"(sta), "+v"(csa));
      s16x8 xf[2][4];
      s16x4 wf[2][12];
      auto rd = [&](int set, int i) {
        if (i < 4) xf[set][i] = lds128(ax[set] + i * 2048);
        else wf[set][i - 4] = ldstr4(aw[(i - 4) >> 1] + set * 8192 + (i & 1) * 1024);
      };
      auto mm = [&](int set, int i) { const int s = i >> 2, m = i & 3; acc[s][m] = mfma(cat(wf[set][2 * s], wf[set][2 * s + 1]), xf[set][m], acc[s][m]); };
      {
        float t[8];
#pragma unroll
        for (int e = 0; e < 8; ++e)
          t[e] = (e & 1) ? sadd(sadd(bfhi(pre[0][e >> 1]), bfhi(pre[1][e >> 1])), sadd(bfhi(pre[2][e >> 1]), bfhi(pre[3][e >> 1])))
                         : sadd(sadd(bflo(pre[0][e >> 1]), bflo(pre[1][e >> 1])), sadd(bflo(pre[2][e >> 1]), bflo(pre[3][e >> 1])));
#pragma unroll
        for (int e = 0; e < 4; ++e) { const u32x2 h = swap32(t[e], t[e + 4]); t[e] = sadd(u2f(h.x), u2f(h.y)); }
#pragma unroll
        for (int e = 0; e < 2; ++e) { const u32x2 h = swap16(t[e], t[e + 2]); t[e] = dpp_add<0x128>(sadd(u2f(h.x), u2f(h.y))); }
        *(LAS f32x2*)(size_t)(LB + L_LS + d * 256 + (4 * (lane & 7) + g) * 8) = (f32x2){t[0], t[1]};
      }
      __builtin_amdgcn_s_setprio(1);
      STAMP(1);
      barrier();
      STAMP(2); STAMPC(21);
#pragma unroll
      for (int i = 0; i < 16; ++i) rd(0, i);
      wait_lgkm0();
      auto stage1 = [&](int c, int dl) {
        SCHED();
#pragma unroll
        for (int i = 0; i < 24; ++i) {
          mm(0, i); SCHED();
          if (i < 16) rd(1, i);
          else if (i < 22) adv(aw[i - 16], dl);
          else if (i == 22) adv(ax[0], dl);
          SCHED();
        }
        TRACE(2 * c);
        barrier();
        TRACE(2 * c + 1);
        SCHED();
      };
      int ph = 0;
#pragma unroll 1
      for (int c = 1; c < 8; ++c) {
        const int dl = ph == 2 ? -2 * (int)SLOT : (int)SLOT;
        ph = ph == 2 ? 0 : ph + 1;
        stage1(c, dl);
#pragma unroll
        for (int i = 0; i < 24; ++i) {
          mm(1, i); SCHED();
          if (i < 16) rd(0, i);
          else if (i == 16) adv(ax[1], dl);
          else if (i == 23) wait_lgkm0();
          SCHED();
        }
      }
      stage1(8, 0);
      unsigned b1a = LB + L_B1 + g * 8;
      asm volatile("" : "+v"(b1a));
      STAMP(4); STAMPC(22);
      f32x2 rs[4], nm[4];
      f32x4 cs[6];
      u32x2 b1v[6];
      f32x2 st[4];
      auto colrd = [&](int s) { cs[s] = ldsf4(csa + 64 * s); b1v[s] = *(LAS u32x2*)(size_t)(b1a + 32 * s); };
#pragma unroll
      for (int i = 0; i < 4; ++i) st[i] = (f32x2){ldsf1(sta + i * 32), ldsf1(sta + i * 32 + 8)};
      colrd(0); colrd(1);
      SCHED();
#pragma unroll
      for (int i = 0; i < BSPLIT; ++i) mm(1, i);
      SCHED();
#pragma unroll
      for (int i = 0; i < 4; ++i) { rs[i] = bco(st[i][0]); nm[i] = bco(-st[i][1]); }
      STAMP(5);
      const unsigned fo = WS_FLAG + (2 * r + d) * 2048;
      pubo = lane ? pubo : 0u;
      u32x4 ob[12];
      const unsigned ho = WS_H + (unsigned)(2 * r + d) * H_MT + (unsigned)(4 * jm) * 4096 + lane * 16;
#pragma unroll
      for (int pr = 0; pr < 3; ++pr) {
        Col col;
        mkcol(col, cs[2 * pr], cs[2 * pr + 1], b1v[2 * pr], b1v[2 * pr + 1]);
        if (pr < 2) { colrd(2 * pr + 2); colrd(2 * pr + 3); }
        SCHED();
        if (pr == 1) {
#pragma unroll
          for (int i = BSPLIT; i < 24; ++i) mm(1, i);
          SCHED();
          STAMP(7);
        }
#pragma unroll
        for (int m = 0; m < 4; ++m) {
          ob[4 * pr + m] = gelu8(acc[2 * pr][m], acc[2 * pr + 1][m], col, rs[m], nm[m]);
          __builtin_amdgcn_raw_buffer_store_b128(ob[4 * pr + m], wsr, ho + m * 1024, pr * 4096, HST);
          if (4 * pr + m >= KEEP) asm volatile("" :: "v"(ob[4 * pr + m - KEEP]));
          TRACE2(1 + 4 * pr + m);
          if (pr > 0 && m == (pr == 1 ? BFLAG0 : BFLAG)) {
            if (pr == 1) wait_vm<BFLAG0 + 1>(); else wait_vm<BFLAG + 1>();
            __builtin_amdgcn_raw_buffer_store_b64((u32x2){tagA, tagB}, wsr, pubo, fo + flag_of(jm, pr - 1), 16);
          }
          SCHED();
        }
      }
      pubo |= fo + flag_of(jm, 2);
      STAMP(6);
    } else {
      const int q = 4 * r + 2 * lq + d;
      const unsigned vt = (lane < 56 ? lane : 0) * 128, dm = LB + L_DUMMY + d * 256;
#pragma unroll 1
      for (int c = 0; c < 9; ++c) {
        if (TOUCH == 7) {
          const int t = (c - TST) * TPP;
#pragma unroll
          for (int e = 0; e < TPP; ++e) {
            if (t + e >= 0 && t + e < 4) dma4w(vt, p.w2 + ((t + e) * 64 + q) * 7168, dm);
            else if (t + e == 4 || t + e == 5) dma4w(lane * 128, (t + e == 4 ? p.cre : p.cim) + q * 8192, dm);
          }
        }
        barrier();
      }
    }
    prog_wait(PW + (6 + d) * 4, 3);
#pragma unroll
    for (int i = 0; i < 5; ++i) absorb(i, home + i * 4096);
    unsigned v2[4];
    mkv2(v2);
    setup2(6);
#pragma unroll
    for (int i = 0; i < 4; ++i) asm volatile("" : "+v"(v2[i]));
    wait_lgkm0();
    STAMP(8);
    dma_x4(v2, w2b + kstep_of(slot, 5) * 32768, home + 16384);
  };
  auto roleA = [&]() __attribute__((always_inline)) {
    const int srow = 128 * r + 8 * j + 4 * d + g;
    u32x2 dv[4], bv[4];
#pragma unroll
    for (int n = 0; n < 4; ++n) { dv[n] = *(const GAS u32x2*)(p.dsk + ntile * 128 + n * 32 + g * 8); bv[n] = *(const GAS u32x2*)(p.b2 + ntile * 128 + n * 32 + g * 8); }
    if (lane < 8) *(LAS unsigned*)(size_t)(PW + lane * 4) = 0u;
    f32x4 acc[2][4];
#pragma unroll
    for (int m = 0; m < 4; ++m) { acc[0][m] = (f32x4){0, 0, 0, 0}; acc[1][m] = (f32x4){0, 0, 0, 0}; asm volatile("" : "+v"(acc[0][m]), "+v"(acc[1][m])); }
    unsigned ax0 = LB + (64 * d + i16) * 128 + kx, ax1 = ax0 ^ 64, aw0 = awb + ((6 ^ awx) * 32), aw1 = awb + ((7 ^ awx) * 32);
    unsigned sta = LB + L_LS + qq * 288 + (8 * d + ((i16 >> 1) & 1)) * 16 + (i16 & 1) * 4, csa = LB + L_CS + 384 + g * 16;
    asm volatile("" : "+v"(ax0), "+v"(ax1), "+v"(aw0), "+v"(aw1), "+v"(sta), "+v"(csa));
    int ph = 0;
    auto chunk = [&](auto side) {
      s16x8 xf[2][4], wf[2][2];
      if (ADELAY) __builtin_amdgcn_s_sleep(ADELAY);
#pragma unroll
      for (int k = 0; k < 2; ++k) {
        wf[k][0] = ldstr(aw0 + k * 8192, aw0 + k * 8192 + 1024);
        wf[k][1] = ldstr(aw1 + k * 8192, aw1 + k * 8192 + 1024);
#pragma unroll
        for (int m = 0; m < 4; ++m) xf[k][m] = lds128((k ? ax1 : ax0) + m * 2048);
      }
      SCHED();
      const int dl = ph == 2 ? -2 * (int)SLOT : (int)SLOT;
      ph = ph == 2 ? 0 : ph + 1;
      adv(ax0, dl); adv(ax1, dl); adv(aw0, dl); adv(aw1, dl);
      side(8);
      SCHED();
#pragma unroll
      for (int k = 0; k < 2; ++k)
#pragma unroll
        for (int m = 0; m < 4; ++m) {
          acc[0][m] = mfma(wf[k][0], xf[k][m], acc[0][m]); acc[1][m] = mfma(wf[k][1], xf[k][m], acc[1][m]);
          SCHED();
          side(4 * k + m);
          SCHED();
        }
    };
    auto none = [](int) {};
    mktag();
    asm volatile("" : "+v"(pre[3]));
    STAMP(19);
    {
      const float mean = sum32(pre) * (1.f / 512.f);
      float v0 = 0.f, v1 = 0.f;
#pragma unroll
      for (int i = 0; i < 4; ++i)
#pragma unroll
        for (int e = 0; e < 4; ++e) {
          const float d0 = bflo(pre[i][e]) - mean, d1 = bfhi(pre[i][e]) - mean;
          v0 = sfma(d0, d0, v0); v1 = sfma(d1, d1, v1);
        }
      const float rsd = __builtin_amdgcn_rsqf(row16_sum(v0 + v1) * (1.f / 512.f) + 1e-5f);
      if (i16 == 0) __builtin_amdgcn_raw_buffer_store_b128((u32x4){tagA, f2u(rsd), tagB, f2u(mean * rsd)}, wsr, WS_STAT + srow * 16, 0, 16);
    }
    __builtin_amdgcn_s_setprio(APRIO);
    STAMP(1);
    barrier();
    STAMP(2);
    chunk(none);
    if (d) {
      const f32x2 c0 = ldsf2(LB + L_LS + 512 + (lane & 3) * 8);
      if (lane < 4) __builtin_amdgcn_raw_buffer_store_b128((u32x4){tagA, f2u(sadd(c0[0], c0[1])), tagB, 0u}, wsr, WS_BS + j * 1024 + (4 * r + lane) * 16, 0, 16);
    } else if (mlp) {
      const f32x2 c0 = ldsf2(LB + L_LS + (lane & 31) * 8), c1 = ldsf2(LB + L_LS + 256 + (lane & 31) * 8);
      if (lane < 32) __builtin_amdgcn_raw_buffer_store_b128((u32x4){tagA, f2u(sadd(c0[0], c1[0])), tagB, f2u(sadd(c0[1], c1[1]))}, wsr, WS_CS + (jm * 16 + r) * 512 + lane * 16, 0, 16);
    }
    barrier();
#pragma unroll 1
    for (int c = 1; c < 3; ++c) { if (!ASKIP || mlp) chunk(none); barrier(); }
    chunk(none);
    asm volatile("" : "+v"(dv[0]), "+v"(dv[1]), "+v"(dv[2]), "+v"(dv[3]), "+v"(bv[0]), "+v"(bv[1]), "+v"(bv[2]), "+v"(bv[3]));
    __builtin_amdgcn_s_waitcnt(0x0F70);
    u32x2 xv[2][4];
#pragma unroll
    for (int m = 0; m < 2; ++m)
#pragma unroll
      for (int n = 0; n < 4; ++n) xv[m][n] = *(LAS u32x2*)(size_t)(HB + 3 * SLOT + 12288 + (16 * m + i16) * 128 + (g & 1) * 8 + (((2 * n + (g >> 1)) ^ (i16 >> 1)) * 16));
    barrier();
#pragma unroll 1
    for (int c = 4; c < 6; ++c) { if (!ASKIP || mlp) chunk(none); barrier(); }
    f32x2 st[4];
#pragma unroll
    for (int m = 0; m < 4; ++m) st[m] = (f32x2){ldsf1(sta + m * 32), ldsf1(sta + m * 32 + 8)};
    unsigned ra[4], ho = mlp ? WS_H + (unsigned)(2 * r + d) * H_MT + (unsigned)(4 * jm + 3) * 4096 + lane * 16 : 0xffff0000u;
    f32x2 rs[4], nm[4];
    chunk([&](int k) {
      if (k < 4) { rs[k] = bco(st[k][0]); nm[k] = bco(-st[k][1]); asm volatile("" : "+v"(rs[k]), "+v"(nm[k])); }
      if (k < 8) return;
#pragma unroll
      for (int n = 0; n < 4; ++n) ra[n] = home + r2l + ((n ^ r2x) * 32);
      asm volatile("" : "+v"(ra[0]), "+v"(ra[1]), "+v"(ra[2]), "+v"(ra[3]), "+v"(ho));
    });
    TRACE(14); barrier(); TRACE(15);
    STAMP(4);
    const f32x4 cs0 = ldsf4(csa), cs1 = ldsf4(csa + 64);
    const u32x2 b10 = *(LAS u32x2*)(size_t)(LB + L_B1 + 192 + g * 8), b11 = *(LAS u32x2*)(size_t)(LB + L_B1 + 224 + g * 8);
    Col col;
    auto take = [&](int i, int pg) {
#pragma unroll
      for (int n = 0; n < 4; ++n) we[i][n] = ldstr(ra[n] + pg * 4096, ra[n] + pg * 4096 + 2048);
    };
    chunk([&](int k) {
      if (k == 2) { mkcol(col, cs0, cs1, b10, b11); asm volatile("" : "+v"(col.bias[0]), "+v"(col.bias[1]), "+v"(col.bias[2]), "+v"(col.bias[3])); }
      if (k >= 3 && k < 6) take(k - 3, k - 3);
    });
    barrier();
    __builtin_amdgcn_s_setprio(2);
    STAMP(5);
    u32x4 oa[4];
#pragma unroll
    for (int m = 0; m < 4; ++m) {
      TRACE2(1 + 2 * m);
      oa[m] = gelu8(acc[0][m], acc[1][m], col, rs[m], nm[m]);
      TRACE2(2 + 2 * m);
      __builtin_amdgcn_raw_buffer_store_b128(oa[m], wsr, ho + m * 1024, 0, HST);
      if (m >= 1) asm volatile("" :: "v"(oa[m - 1]));
      if (m == 0) { take(3, 3); take(4, 4); }
      SCHED();
    }
    STAMP(6);
    prog_wait(PW + (6 + d) * 4, 2);
#pragma unroll
    for (int i = 5; i < 8; ++i) take(i, i - 5);
    u32x4 s2v[2];
#pragma unroll
    for (int m = 0; m < 2; ++m) s2v[m] = *(LAS u32x4*)(size_t)(LB + L_ST2 + (tok + 8 * (mtile & 1) + 4 * d + 2 * m) * 16);
    setup2(8);
    wait_lgkm0();
    asm volatile("" :: "v"(oa[3]));
    prog_set(PW + w * 4, 1);
    snap(false);
#pragma unroll
    for (int m = 0; m < 2; ++m) {
      const u32x4 s2 = s2v[m];
      const f32x2 r2 = bco(u2f(s2.y)), n2 = bco(-u2f(s2.w));
#pragma unroll
      for (int n = 0; n < 4; ++n) {
        f32x2 o[2];
#pragma unroll
        for (int h = 0; h < 2; ++h) {
          const f32x2 xx = {bflo(xv[m][n][h]), bfhi(xv[m][n][h])};
          o[h] = pfma(pfma(xx, r2, n2), (f32x2){bflo(dv[n][h]), bfhi(dv[n][h])}, xx + (f32x2){bflo(bv[n][h]), bfhi(bv[n][h])});
        }
        acc2[n][m] = __builtin_shufflevector(o[0], o[1], 0, 1, 2, 3);
        acc2[n][2 + m] = (f32x4){0, 0, 0, 0};
        asm volatile("" : "+v"(acc2[n][m]), "+v"(acc2[n][2 + m]));
      }
    }
    pubo = mlp && lane == 0 ? WS_FLAG + (2 * r + d) * 2048 + flag_of(jm, 3) : pubo;
    seen();
    STAMP(8);
  };

  auto phase2 = [&](auto nm_) __attribute__((always_inline)) {
    STAMP(9);
    constexpr int NM = decltype(nm_)::value, S0 = NM == 8 ? 0 : 5, N = NM == 8 ? 13 : 9;
    const u64 tag = ((u64)tagB << 32) | tagA;
    auto has = [&](int k) { return fl[k < NM ? k : k - NM + S0] == tag; };
    auto need = [&](int k) {
      if (__builtin_expect(!has(k), 0)) {
#pragma unroll 1
        for (int spin = 1; spin < SPIN_CAP; ++spin) {
          COUNT(20);
          snap(k >= NM);
          if (has(k)) break;
          if (!(spin & 31)) {
            const u32x2 f = __builtin_amdgcn_raw_buffer_load_b64(wsr, 0, k < NM ? fm + 8 * k : fs + 8 * (k - NM + S0), 16);
            if (__builtin_amdgcn_readfirstlane(f.x) == tagA && __builtin_amdgcn_readfirstlane(f.y) == tagB) break;
          }
          __builtin_amdgcn_s_sleep(PSLEEP);
        }
      }
      asm volatile("" ::: "memory");
    };
    auto publish = [&]() { __builtin_amdgcn_raw_buffer_store_b64((u32x2){tagA, tagB}, wsr, pubo, 0, 16); };
    auto loadH = [&](int i, int m) -> u32x4 {
      if (m == 0) asm volatile("" : "+v"(hb));
      if ((NOC & 8) && (m & 1)) { u32x4 z; asm volatile("" : "=v"(z)); return z; }
      return __builtin_amdgcn_raw_buffer_load_b128(wsr, hb + (m & 1) * 1024, __builtin_amdgcn_readlane(ksl, i) ^ ((m & 2) * 1024), HAUX);
    };
    u32x4 hf[N][4];
    auto load4 = [&](int i) {
#pragma unroll
      for (int m = 0; m < 4; ++m) hf[i][m] = loadH(i, m);
    };
    STAMP(10);
    if (NM == 8) {
      if (__builtin_expect(!(has(0) && has(1)), 0)) { wait_vm<0>(); publish(); pubo = 0xffff0000u; }
      need(0); load4(0);
      need(1); load4(1);
      wait_vm<8>(); publish();
      __builtin_amdgcn_s_setprio(EPRI);
    } else {
      unsigned vc[8];
      mkvc(vc);
      asm volatile("" : "+v"(vc[0]), "+v"(vc[1]), "+v"(vc[2]), "+v"(vc[3]), "+v"(vc[4]), "+v"(vc[5]), "+v"(vc[6]), "+v"(vc[7]));
      const unsigned o = ntile * 65536 + kg * 128, hs = fresh(HB + (w < 4 ? SLOT : 0));
      __builtin_amdgcn_s_setprio(0);
      dma_x8(vc, p.cre + o, hs);
      wait_vm<8>(); publish();
      snap(false);
      dma_x8(vc, p.cim + o, hs + 8192);
      seen();
      need(0); load4(0);
      need(1); load4(1);
    }
    STAMP(11);
    s16x8 cfr[N - NM][4];
    auto rdc = [&](int q, unsigned base) {
#pragma unroll
      for (int n = 0; n < 4; ++n) {
        unsigned a = base + n * 2048;
        asm volatile("" : "+v"(a));
        cfr[q][n] = cat(*(LAS s16x4*)(size_t)a, *(LAS s16x4*)(size_t)(a + 8192));
      }
    };
    SCHED();
#pragma unroll
    for (int i = 0; i < N; ++i) {
      const int k = i + (i < NM - 2 ? 2 : SDEP);
      if (i == 2) STAMP(12);
      if (!(i & 1) || k >= NM) TRACE4(2 * i + 1);
      if (i == NM - 2) {
        STAMP(13);
#pragma unroll
        for (int e = NM; e < NM + SDEP - 2; ++e) { need(e); load4(e); }
      }
      if (k < NM) {
        if (NM == 6 && i == 0) snap(false);
        else if (NM == 6 && i == 2) { need(2); load4(2); need(3); load4(3); }
        if (!(i & 1) && (NM == 8 || i)) { need(k); need(k + 1); TRACE4(2 * i + 2); if (NM == 6 && i == 2) absorb(5, home + 16384); if (!(ESEP && i == 0)) snap(k + 2 == NM); }
      } else if (k < N) { need(k); TRACE4(2 * i + 2); }
      if (i == NM - 2) STAMP(14);
      if (NM == 8 && i == 1) prog_set(PW + w * 4, 4);
      if (NM == 8 && i == 6) prog_wait(PW + (w | 6) * 4, 4);
      if (NM == 6 && i == 1) prog_set(PW + w * 4, 1);
      if (NM == 8 && i == EOFF) __builtin_amdgcn_s_setprio(0);
      if (NM == 8 && i == PSTEP) { prog_wait(PW + (w ^ 4) * 4, 1); rdc(4, crd - 2 * SLOT); }
      if (NM == 8 && i == PSTEP + 1) { wait_lgkm0(); prog_set(PW + w * 4, 5); }
      if (i >= NM - 1 && i < NM + (NM == 8 ? 3 : 2)) rdc(i + 1 - NM, crd ^ (32 * (i + 1 - NM + (NM == 8 ? 0 : 1))));
      SCHED();
#pragma unroll
      for (int m = 0; m < 4; ++m) {
#pragma unroll
        for (int n = 0; n < 4; ++n)
          if (NM == 8 && i == 0) asm volatile("v_mfma_f32_16x16x32_bf16 %0, %1, %2, %0" : "+v"(acc2[n][m]) : "v"(we[0][n]), "v"(hf[0][m]));
          else acc2[n][m] = mfma(i < NM ? we[i < NM ? i : 0][n] : cfr[i < NM ? 0 : i - NM][n], __builtin_bit_cast(s16x8, hf[i][m]), NM == 6 && i == 0 ? (f32x4){0, 0, 0, 0} : acc2[n][m]);
        SCHED();
        if (k < N && (NM == 8 || i > 1)) hf[k < N ? k : 0][m] = loadH(k, m);
        SCHED();
      }
      if (ESEP && NM == 8 && i < 2) TRACE4(3 + i);
      if (ESEP && NM == 8 && i == 1) snap(false);
      if ((i & 1) && k < NM) seen();
    }
    TRACE4(30);
    if (NM == 6) prog_wait(PW + (w ^ 4) * 4, 5);
  };
  if (__builtin_expect(w >= 6 || w < 2, 1)) {
    if (__builtin_expect(w >= 6, 1)) roleD(); else roleA();
    phase2(std::integral_constant<int, 8>{});
  } else {
    if (w >= 4) roleL(); else roleB();
    phase2(std::integral_constant<int, 6>{});
  }
  STAMP(15);
  int tl = threadIdx.x;
  asm volatile("" : "+v"(tl));
  const int i16t = tl & 15, gt = (tl >> 4) & 3;
#pragma unroll
  for (int m = 0; m < 4; ++m)
#pragma unroll
    for (int n = 0; n < 4; ++n) *(LAS f32x4*)(size_t)(home + ((16 * m + i16t) ^ (w == 1 ? 32 : 0)) * 256 + (((4 * n + gt) ^ i16t) * 16)) = acc2[n][m];
  const int ck = tl & 15;
  const int orow = 32 * ((tl >> 6) & 3) + 16 * ((tl >> 4) & 1) + ((tl >> 5) & 1) + 2 * (tl >> 8) + 8 * (mtile & 1);
  char* op = p.out + (128 * (mtile >> 1) + orow) * 2048 + ntile * 256 + ck * 16;
  unsigned ra[4];
#pragma unroll
  for (int k = 0; k < 4; ++k) ra[k] = LB + (tl >> 4) * 256 + ((ck ^ ((tl >> 4) & 15)) * 16) + k * SLOT;
  asm volatile("" : "+v"(ra[0]), "+v"(ra[1]), "+v"(ra[2]), "+v"(ra[3]), "+v"(op));
  barrier();
  STAMP(16);
  {
    f32x4 pt[2][8];
#pragma unroll
    for (int h = 0; h < 2; ++h)
#pragma unroll
      for (int k = 0; k < 8; ++k) pt[h][k] = ldsf4(ra[k >> 1] + (k & 1) * HALF + h * 8192);
#pragma unroll
    for (int h = 0; h < 2; ++h) {
      const f32x4 s = ((pt[h][0] + pt[h][1]) + (pt[h][2] + pt[h][3])) + ((pt[h][4] + pt[h][5]) + (pt[h][6] + pt[h][7]));
      __builtin_nontemporal_store(s, (GAS f32x4*)(op + h * 8192));
    }
  }
  if (b == 255 && tl == 0) __builtin_amdgcn_raw_buffer_store_b64((u32x2){tagA, tagB}, wsr, WS_EPOCH, 0, 16);
  while ((unsigned)(__builtin_amdgcn_s_memrealtime() - t_start) < 3000 + t_x) __builtin_amdgcn_s_sleep(2);
}

extern "C" void kernel_launch(void* const* d_in, const int* in_sizes, int n_in, void* d_out, int out_size, void* d_ws,
                              size_t ws_size, hipStream_t stream) {
  Params p;
  p.x = (const char*)d_in[0]; p.lre = (const char*)d_in[1]; p.lim = (const char*)d_in[2];
  p.bre = (const char*)d_in[3]; p.bim = (const char*)d_in[4]; p.cre = (const char*)d_in[5]; p.cim = (const char*)d_in[6];
  p.logdt = (const char*)d_in[7]; p.dsk = (const char*)d_in[8];
  p.w1 = (const char*)d_in[10]; p.b1 = (const char*)d_in[11]; p.w2 = (const char*)d_in[12]; p.b2 = (const char*)d_in[13];
  p.out = (char*)d_out; p.ws = (char*)d_ws;
  fused_kernel<<<256, 512, 0, stream>>>(p);
}
```

```cpp
#include <hip/hip_runtime.h>
#include <stdint.h>
#include <type_traits>

typedef short s16x4 __attribute__((ext_vector_type(4)));
typedef short s16x8 __attribute__((ext_vector_type(8)));
typedef __bf16 bf16x8 __attribute__((ext_vector_type(8)));
typedef __bf16 bf16x2 __attribute__((ext_vector_type(2)));
typedef float f32x4 __attribute__((ext_vector_type(4)));
typedef float f32x2 __attribute__((ext_vector_type(2)));
typedef unsigned u32x4 __attribute__((ext_vector_type(4)));
typedef unsigned u32x2 __attribute__((ext_vector_type(2)));
typedef int i32x4 __attribute__((ext_vector_type(4)));
typedef unsigned long long u64;
typedef u64 u64x4 __attribute__((ext_vector_type(4)));
typedef u64 u64x8 __attribute__((ext_vector_type(8)));
typedef int i32x8 __attribute__((ext_vector_type(8)));
typedef int i32x16 __attribute__((ext_vector_type(16)));
__device__ i32x8 sbl8(i32x4, int, int) __asm("llvm.amdgcn.s.buffer.load.v8i32");
__device__ i32x16 sbl16(i32x4, int, int) __asm("llvm.amdgcn.s.buffer.load.v16i32");
#define LAS __attribute__((address_space(3)))
#define GAS __attribute__((address_space(1)))
#define DI __device__ __forceinline__

#define STAMPS 0
#define TRACING 0
#define GATHER_AT 4
#define ROT 1
#define TOUCH 7
#define TLBW 0
#define NOFF 0
#define PSLEEP 4
#define BFLAG 2
#define ABAR 0
#define KROT 0
#define LPRIO 3
#define CHK 0
#define BSPLIT 8
#define APRIO 0
#define ADELAY 0
#define BFLAG0 1
#define DEPE 2
#define DEPL 2
#define LB_SLEEP 8
#define CHECK_AT 5
#define CPF 1
#define HAUX 16
#define LSNAP 1
#define BSNAP 1
#define L2T 0
#define L2C 0
#define SDEP 2
#define YPRE 1
#define KEEP 2
#define LSEP 0
#define PSTEP 9
#define NOC 0
#define ESEP 0
#define HST 16
#define EPRI 1
#define EOFF 6
#define WOFF 4
#define CPW 5
#define TST 1
#define TPP 3
#define S3AT 3
#define JM 0
#define ASKIP 0
#define TLBH 1
#define MAP 1

struct Params {
  const char *x, *lre, *lim, *bre, *bim, *cre, *cim, *logdt, *dsk, *w1, *b1, *w2, *b2;
  char *out, *ws;
};

constexpr unsigned WS_EPOCH = 0;
constexpr unsigned WS_STAT = 4096;
constexpr unsigned WS_TOT = WS_STAT + 2048 * 16;
constexpr unsigned WS_CS = WS_TOT + 16 * 2 * 16 * 256;
constexpr unsigned WS_BS = WS_CS + 16 * 16 * 512;
constexpr unsigned WS_FLAG = WS_BS + 16 * 1024;
constexpr unsigned WS_H = WS_FLAG + 32 * 2048;
constexpr unsigned H_MT = 88 * 4096;
constexpr unsigned WS_END = WS_H + 32 * H_MT;
constexpr unsigned WS_STAMP = 32u << 20;
constexpr unsigned WS_B2B = WS_STAMP + (4u << 20);
constexpr unsigned SLOT = 40960, HALF = 20480, SL_W = 16384, SL_B = 32768;
constexpr unsigned L_ST1 = 3 * SLOT + 16384, L_ST2 = L_ST1 + 2048;
constexpr unsigned L_CS = 3 * SLOT + HALF + 16384, L_FLG = L_CS + 512, L_B1 = L_CS + 768, L_BS = L_CS + 1024, L_LS = L_CS + 2048, L_DUMMY = L_CS + 3328;
constexpr unsigned LDS_BYTES = 163840;
constexpr int SPIN_CAP = 40000;
constexpr unsigned CODE_BYTES = 44032;

DI void dma4w(unsigned voff, const char* sbase, unsigned ldsaddr) {
  asm volatile("s_mov_b32 m0, %2\n\ts_nop 0\n\tglobal_load_lds_dword %0, %1" ::"v"(voff), "s"(sbase), "s"(ldsaddr));
}
DI void dma_x4(const unsigned* v, const char* sbase, unsigned ldsaddr, bool low = false) {
  if (low) {
    asm volatile("s_mov_b32 m0, %5\n\ts_setprio 0\n\t"
               "global_load_lds_dwordx4 %0, %4\n\tglobal_load_lds_dwordx4 %1, %4 offset:1024\n\t"
               "global_load_lds_dwordx4 %2, %4 offset:2048\n\tglobal_load_lds_dwordx4 %3, %4 offset:3072\n\ts_setprio 3"
               ::"v"(v[0]), "v"(v[1]), "v"(v[2]), "v"(v[3]), "s"(sbase), "s"(ldsaddr));
    return;
  }
  asm volatile("s_mov_b32 m0, %5\n\ts_nop 0\n\t"
               "global_load_lds_dwordx4 %0, %4\n\tglobal_load_lds_dwordx4 %1, %4 offset:1024\n\t"
               "global_load_lds_dwordx4 %2, %4 offset:2048\n\tglobal_load_lds_dwordx4 %3, %4 offset:3072"
               ::"v"(v[0]), "v"(v[1]), "v"(v[2]), "v"(v[3]), "s"(sbase), "s"(ldsaddr));
}
DI void dma_x8(const unsigned* v, const char* sbase, unsigned ldsaddr) {
  asm volatile("s_mov_b32 m0, %9\n\ts_nop 0\n\t"
               "global_load_lds_dwordx4 %0, %8 offset:-4096\n\tglobal_load_lds_dwordx4 %1, %8 offset:-3072\n\t"
               "global_load_lds_dwordx4 %2, %8 offset:-2048\n\tglobal_load_lds_dwordx4 %3, %8 offset:-1024\n\t"
               "global_load_lds_dwordx4 %4, %8\n\tglobal_load_lds_dwordx4 %5, %8 offset:1024\n\t"
               "global_load_lds_dwordx4 %6, %8 offset:2048\n\tglobal_load_lds_dwordx4 %7, %8 offset:3072"
               ::"v"(v[0]), "v"(v[1]), "v"(v[2]), "v"(v[3]), "v"(v[4]), "v"(v[5]), "v"(v[6]), "v"(v[7]), "s"(sbase), "s"(ldsaddr + 4096u));
}
constexpr int PG(int i) { return i & 3; }
template <int N> DI void wait_vm() { asm volatile("s_waitcnt vmcnt(%0)" ::"i"(N) : "memory"); }
DI void wait_lgkm0() { asm volatile("" ::: "memory"); __builtin_amdgcn_s_waitcnt(0xC07F); asm volatile("" ::: "memory"); }
DI void barrier() {
  asm volatile("" ::: "memory");
  __builtin_amdgcn_s_waitcnt(0xC07F);
  __builtin_amdgcn_s_barrier();
  asm volatile("" ::: "memory");
}
#define SCHED() __builtin_amdgcn_sched_barrier(0)
DI s16x8 lds128(unsigned a) { return *(LAS s16x8*)(size_t)a; }
DI f32x4 ldsf4(unsigned a) { return *(LAS f32x4*)(size_t)a; }
DI f32x2 ldsf2(unsigned a) { return *(LAS f32x2*)(size_t)a; }
DI float ldsf1(unsigned a) { return *(LAS float*)(size_t)a; }
DI s16x4 ldstr4(unsigned a) { return __builtin_amdgcn_ds_read_tr16_b64_v4i16((LAS s16x4*)(size_t)a); }
DI s16x8 cat(s16x4 lo, s16x4 hi) { return __builtin_shufflevector(lo, hi, 0, 1, 2, 3, 4, 5, 6, 7); }
DI s16x8 ldstr(unsigned a0, unsigned a1) { return cat(ldstr4(a0), ldstr4(a1)); }
DI f32x4 mfma(s16x8 a, s16x8 b, f32x4 c) {
  return __builtin_amdgcn_mfma_f32_16x16x32_bf16(__builtin_bit_cast(bf16x8, a), __builtin_bit_cast(bf16x8, b), c, 0, 0, 0);
}
DI unsigned pk2(float a, float b) { f32x2 v = {a, b}; return __builtin_bit_cast(unsigned, __builtin_convertvector(v, bf16x2)); }
DI float u2f(unsigned u) { return __builtin_bit_cast(float, u); }
DI unsigned f2u(float u) { return __builtin_bit_cast(unsigned, u); }
DI float bflo(unsigned u) { return __builtin_bit_cast(float, u << 16); }
DI float bfhi(unsigned u) { return __builtin_bit_cast(float, u & 0xffff0000u); }
template <int CTRL> DI float dpp_add(float v) {
  return v + __builtin_bit_cast(float, __builtin_amdgcn_update_dpp(0, __builtin_bit_cast(int, v), CTRL, 0xf, 0xf, true));
}
DI float row16_sum(float v) {
  v = dpp_add<0xB1>(v);
  v = dpp_add<0x4E>(v);
  v = dpp_add<0x141>(v);
  v = dpp_add<0x140>(v);
  return v;
}
DI float smul(float a, float b) { float d; asm("v_mul_f32 %0, %1, %2" : "=v"(d) : "v"(a), "v"(b)); return d; }
DI float sadd(float a, float b) { float d; asm("v_add_f32 %0, %1, %2" : "=v"(d) : "v"(a), "v"(b)); return d; }
DI float sfma(float a, float b, float c) { float d; asm("v_fma_f32 %0, %1, %2, %3" : "=v"(d) : "v"(a), "v"(b), "v"(c)); return d; }
DI float sfms(float a, float b, float c) { float d; asm("v_fma_f32 %0, %1, %2, -%3" : "=v"(d) : "v"(a), "v"(b), "v"(c)); return d; }
DI void zero4(f32x4& v) {
  float a, b, c, d;
  asm volatile("v_mov_b32 %0, 0\n\tv_mov_b32 %1, 0\n\tv_mov_b32 %2, 0\n\tv_mov_b32 %3, 0" : "=v"(a), "=v"(b), "=v"(c), "=v"(d));
  v = (f32x4){a, b, c, d};
}
DI u32x2 swap32(float a, float b) { asm volatile("s_nop 1" : "+v"(a), "+v"(b)); return __builtin_amdgcn_permlane32_swap(f2u(a), f2u(b), false, false); }
DI u32x2 swap16(float a, float b) { asm volatile("s_nop 1" : "+v"(a), "+v"(b)); return __builtin_amdgcn_permlane16_swap(f2u(a), f2u(b), false, false); }
DI f32x2 pfma(f32x2 a, f32x2 b, f32x2 c) { return __builtin_elementwise_fma(a, b, c); }
DI f32x2 bc(float a) { return (f32x2){a, a}; }
DI f32x2 bco(float a) { f32x2 v = {a, a}; asm("" : "+v"(v)); return v; }
DI f32x2 cmul(f32x2 a, f32x2 b) { return (f32x2){sfms(a[0], b[0], smul(a[1], b[1])), sfma(a[0], b[1], smul(a[1], b[0]))}; }
DI f32x2 cfma(f32x2 a, f32x2 b, f32x2 c) { return (f32x2){sfms(a[0], b[0], sfms(a[1], b[1], c[0])), sfma(a[0], b[1], sfma(a[1], b[0], c[1]))}; }
DI f32x2 csel(bool p, f32x2 a, f32x2 b) { return (f32x2){p ? a[0] : b[0], p ? a[1] : b[1]}; }
DI void prog_set(unsigned a, unsigned v) {
  asm volatile("s_mov_b64 exec, 1\n\tds_write_b32 %0, %1\n\ts_mov_b64 exec, -1" ::"v"(a), "v"(v) : "memory");
}
DI void prog_wait(unsigned a, unsigned v) {
#pragma unroll 1
  for (int spin = 0; spin < SPIN_CAP * 16; ++spin) {
    if (__builtin_amdgcn_readfirstlane(*(volatile LAS unsigned*)(size_t)a) >= v) break;
    __builtin_amdgcn_s_sleep(1);
  }
  asm volatile("" ::: "memory");
}
DI void bar() { asm volatile("" ::: "memory"); __builtin_amdgcn_s_barrier(); asm volatile("" ::: "memory"); }
DI void dma1_dev(unsigned voff, const char* sbase, unsigned ldsaddr) {
  asm volatile("s_mov_b32 m0, %2\n\ts_nop 0\n\tglobal_load_lds_dwordx4 %0, %1 sc1" ::"v"(voff), "s"(sbase), "s"(ldsaddr) : "memory");
}
DI void dma2_dev(unsigned voff, const char* s0, const char* s1, unsigned ldsaddr) {
  asm volatile("s_mov_b32 m0, %3\n\ts_nop 0\n\tglobal_load_lds_dwordx4 %0, %1 sc1\n\tglobal_load_lds_dwordx4 %0, %2 offset:2048 sc1" ::"v"(voff), "s"(s0), "s"(s1), "s"(ldsaddr) : "memory");
}
DI void dma4_dev(unsigned voff, const char* s0, const char* s1, unsigned ldsaddr) {
  asm volatile("s_mov_b32 m0, %3\n\ts_nop 0\n\tglobal_load_lds_dwordx4 %0, %1 sc1\n\tglobal_load_lds_dwordx4 %0, %1 offset:1024 sc1\n\t"
               "global_load_lds_dwordx4 %0, %2 offset:2048 sc1\n\tglobal_load_lds_dwordx4 %0, %2 offset:3072 sc1" ::"v"(voff), "s"(s0), "s"(s1), "s"(ldsaddr) : "memory");
}
DI int kstep_of(int slot, int i) {
  const int q = slot + 4 * i + (slot >= 4 ? 28 : 0), cls = (q >= 14) + (q >= 28) + (q >= 42);
  return 4 * (q - 14 * cls) + ((0x9C >> (2 * cls)) & 3);
}
DI unsigned flag_of(int j, int c) {
  const int q = 14 * ((0x78 >> (2 * c)) & 3) + j;
  return q < 32 ? (q & 3) * 128 + (q >> 2) * 8 : 512 + (q & 3) * 128 + ((q - 32) >> 2) * 8;
}
DI void cnt_add(unsigned a) {
  asm volatile("s_mov_b64 exec, 1\n\tds_add_u32 %0, %1\n\ts_mov_b64 exec, -1" ::"v"(a), "v"(1u) : "memory");
}
DI f32x2 lo2(f32x4 v) { return __builtin_shufflevector(v, v, 0, 1); }
DI f32x2 hi2(f32x4 v) { return __builtin_shufflevector(v, v, 2, 3); }
DI void rows4(float v, float* o) {
  asm volatile("s_nop 1" : "+v"(v));
  const unsigned u = f2u(v);
  const u32x2 h = __builtin_amdgcn_permlane32_swap(u, u, false, false);
  const u32x2 a = __builtin_amdgcn_permlane16_swap(h.x, h.x, false, false), b = __builtin_amdgcn_permlane16_swap(h.y, h.y, false, false);
  o[0] = u2f(a.x); o[1] = u2f(a.y); o[2] = u2f(b.x); o[3] = u2f(b.y);
}
struct Col { f32x2 cs[4], bias[4]; };
DI void mkcol(Col& c, f32x4 cs0, f32x4 cs1, u32x2 b0, u32x2 b1) {
  c.cs[0] = lo2(cs0); c.cs[1] = hi2(cs0); c.cs[2] = lo2(cs1); c.cs[3] = hi2(cs1);
  c.bias[0] = (f32x2){bflo(b0.x), bfhi(b0.x)}; c.bias[1] = (f32x2){bflo(b0.y), bfhi(b0.y)};
  c.bias[2] = (f32x2){bflo(b1.x), bfhi(b1.x)}; c.bias[3] = (f32x2){bflo(b1.y), bfhi(b1.y)};
}
DI u32x4 gelu8(f32x4 a0, f32x4 a1, const Col& c, f32x2 rs, f32x2 nmrs) {
  f32x2 v[4], z[4];
  u32x4 o;
#define ST4(stmt) _Pragma("unroll") for (int h = 0; h < 4; ++h) { stmt; } SCHED();
  ST4(v[h] = pfma(c.cs[h], nmrs, c.bias[h]))
  ST4(v[h] = pfma(h == 0 ? lo2(a0) : h == 1 ? hi2(a0) : h == 2 ? lo2(a1) : hi2(a1), rs, v[h]))
  ST4(z[h] = v[h] * v[h])
  ST4(z[h] = pfma(z[h], bc(-0.10294324f), bc(-2.30220820f)))
  ST4(z[h] = z[h] * v[h])
  ST4((z[h] = (f32x2){__builtin_amdgcn_exp2f(z[h][0]), __builtin_amdgcn_exp2f(z[h][1])}))
  ST4(z[h] = z[h] + bc(1.f))
  ST4((z[h] = (f32x2){__builtin_amdgcn_rcpf(z[h][0]), __builtin_amdgcn_rcpf(z[h][1])}))
  ST4(v[h] = v[h] * z[h])
  ST4(o[h] = pk2(v[h][0], v[h][1]))
#undef ST4
  return o;
}

#define TRACE(i) do {} while (0)
#define TRACE2(i) do {} while (0)
#define TRACE3(i) do {} while (0)
#define TRACE4(i) do {} while (0)
#define STAMP(k) do {} while (0)
#define STAMPC(k) do {} while (0)
#define COUNT(k) do {} while (0)

__global__ void __launch_bounds__(512, 2) fused_kernel(Params p) {
  __shared__ __attribute__((aligned(16))) char lds_mem[LDS_BYTES];
  unsigned long long pc;
  asm volatile("s_getpc_b64 %0" : "=s"(pc));
  const unsigned LB = (unsigned)(size_t)(LAS char*)lds_mem;
  const int tid = threadIdx.x, lane = tid & 63;
  const int w = (__builtin_amdgcn_readfirstlane(tid >> 6) + WOFF) & 7;
  if (w == CPW) {
    const unsigned co = ((unsigned)(lane & 15) * 32u + (blockIdx.x >> 3)) * 128u;
    dma4w(co < CODE_BYTES ? co : 0u, (const char*)(pc & ~255ull), LB + L_DUMMY + 512);
  }
  const int b = blockIdx.x, idx = b >> 3, xcd = b & 7;
  const int r = 8 * (xcd >> 2) + (idx >> 2), j = 4 * (xcd & 3) + (idx & 3);
  const int rot = (2 * (xcd & 3) + 4 * (xcd >> 2)) & 7;
  const int mtile = 4 * xcd + (idx >> 3), ntile = (idx + NOFF) & 7;
  const int i16 = lane & 15, g = lane >> 4, qq = i16 >> 2, pp = i16 & 3;
  const int d = w & 1;
  const int jm = j < 14 ? j : 13, lq = j & 1;
  const bool mlp = j < 14;
  u32x4 pre[4];
  unsigned v_lre, v_lim, v_ldt;
  if ((w >> 1) == 2) {
    const unsigned po = (unsigned)(32 * j + 16 * d + i16) * 2u;
    v_lre = *(const GAS unsigned short*)(p.lre + po); v_lim = *(const GAS unsigned short*)(p.lim + po); v_ldt = *(const GAS unsigned short*)(p.logdt + po);
    asm volatile("" ::: "memory");
  }
  u32x4 prb[2];
  if (w < 4) {
    const bool isb = w >= 2;
    const char* pb = isb ? p.w1 + (64 * (r & 7) + 32 * d) * 3584 + jm * 256 + (r >> 3) * 128 : p.x + (128 * r + 8 * j + 4 * d) * 1024;
    const unsigned po = isb ? (lane >> 3) * 3584 + (lane & 7) * 16 : g * 1024 + i16 * 16, ps = isb ? 28672 : 256;
#pragma unroll
    for (int i = 0; i < 4; ++i) pre[i] = *(const GAS u32x4*)(pb + i * ps + po);
    if (isb) {
#pragma unroll
      for (int i = 0; i < 2; ++i) prb[i] = *(const GAS u32x4*)((r & 8 ? p.bim : p.bre) + (32 * j + 4 * (r & 7) + 2 * d) * 1024 + i * 512 + (lane >> 5) * 1024 + (lane & 31) * 16);
    }
    asm volatile("" ::: "memory");
  }
  const int kgo = 2 * (idx >> 3), late = ((w + 2) >> 2) & 1, kg = 2 * (((w & 2) + d + KROT * ntile) & 3) + late;
  auto slot_of = [&](int role) { return (((role + 1) & 2) << 1) + (((((role + 1) & 2) ? 0 : kgo) + ((role & 1) ? 0 : 2) + d) & 3); };
  const int slot = slot_of(w >> 1);
  const u32x2 ep = *(const __attribute__((address_space(4))) u32x2*)(p.ws + WS_EPOCH);
  unsigned tagA, tagB;
  auto mktag = [&]() { tagA = ep.x; tagB = ep.y; asm volatile("" : "+s"(tagA), "+s"(tagB)); tagA += 1u; tagB += 1u; };
  const __amdgpu_buffer_rsrc_t wsr = __builtin_amdgcn_make_buffer_rsrc(p.ws, 0, WS_END, 0x00020000);
  const i32x4 wss = {(int)(size_t)p.ws, (int)((size_t)p.ws >> 32) & 0xffff, (int)WS_END, 0x00020000};
  auto fresh = [](int o) { asm volatile("" : "+s"(o) :: "memory"); return o; };
  const unsigned HB = LB + d * HALF;
  const unsigned PW = LB + L_FLG;
  s16x8 we[8][4];
  f32x4 acc2[4][4];
  const unsigned home = HB + ((2 - (w >> 1)) & 3) * SLOT;
  const unsigned r2l = (4 * g + qq) * 128 + pp * 8, r2x = (2 * g + (qq >> 1)) & 3;
  auto absorb = [&](int i, unsigned page) {
#pragma unroll
    for (int n = 0; n < 4; ++n) { const unsigned a = page + r2l + ((n ^ r2x) * 32); we[i][n] = ldstr(a, a + 2048); }
  };
  auto mkvc = [&](unsigned* vc) {
#pragma unroll
    for (int i = 0; i < 8; ++i) vc[i] = (lane >> 3) * 1024 + (((lane & 7) ^ (4 * (i & 1) + g)) * 16) + 4096 + i * 7168;
  };
  auto mkv2 = [&](unsigned* v2) {
#pragma unroll
    for (int i = 0; i < 4; ++i) v2[i] = (lane >> 3) * 1024 + (((((lane & 7) >> 1) ^ g) << 1) | (lane & 1)) * 16 + i * 7168;
  };
  auto loadC = [&](const unsigned* vc, int kgc, unsigned dst) {
    const unsigned o = ntile * 65536 + kgc * 128;
    dma_x8(vc, p.cre + o, dst);
    dma_x8(vc, p.cim + o, dst + 8192);
  };
  const char* w2b = p.w2 + ntile * 128;
  auto adv = [&](unsigned& a, int dl) { a += dl; asm volatile("" : "+v"(a)); };
  const int tok = 32 * qq + 16 * (i16 & 1) + ((i16 >> 1) & 1);
  const unsigned kx = (g ^ (i16 >> 1)) * 16;
  const unsigned awb = LB + SL_W + (8 * g + qq) * 256 + pp * 8, awx = qq | ((g & 1) << 2);
  auto sum32 = [&](const u32x4* v) {
    float s0 = 0.f, s1 = 0.f;
#pragma unroll
    for (int i = 0; i < 4; ++i)
#pragma unroll
      for (int e = 0; e < 4; ++e) { s0 += bflo(v[i][e]); s1 += bfhi(v[i][e]); }
    return row16_sum(s0 + s1);
  };
  auto touch = [&]() {
    dma4w(d ? (lane & 15) * 128 : (lane < 56 ? lane : 0) * 128, d ? p.cre + b * 2048 : p.w2 + b * 7168, LB + L_DUMMY + d * 256);
    if (d) dma4w((lane & 15) * 128, p.cim + b * 2048, LB + L_DUMMY + d * 256);
  };
  auto zero2 = [&]() {
#pragma unroll
    for (int n = 0; n < 4; ++n) {
#pragma unroll
      for (int m = 0; m < 4; ++m) acc2[n][m] = (f32x4){0, 0, 0, 0};
      asm volatile("" : "+v"(acc2[n][0]), "+v"(acc2[n][1]), "+v"(acc2[n][2]), "+v"(acc2[n][3]));
    }
  };
  int ksl;
  unsigned hb, crd, pubo = 0xffff0000u;
  const int fm = WS_FLAG + mtile * 2048 + slot * 128, fs = WS_FLAG + mtile * 2048 + 1024 + (kg >> 1) * 64;
  u64x8 fl;
  auto snap = [&](bool ssm) { fl = __builtin_bit_cast(u64x8, sbl16(wss, fresh(ssm ? fs : fm), 1)); SCHED(); };
  auto seen = [&]() { SCHED(); asm volatile("" : "+s"(fl)); };
  auto setup2 = [&](int nm) {
    ksl = (lane < nm ? kstep_of(slot, lane) : 56 + 4 * kg + late + ((lane - nm) & 7)) * 4096;
    if (NOC & 16) ksl = (w * 11 + (lane & 1)) * 4096;
    ksl += w == 1 ? 2048 : 0;
    hb = WS_H + (unsigned)mtile * H_MT + lane * 16;
    crd = home + i16 * 128 + (g & 1) * 8 + (((g >> 1) ^ (i16 >> 1)) * 16);
    asm volatile("" : "+v"(ksl), "+v"(hb), "+v"(crd));
  };

  auto roleD = [&]() __attribute__((always_inline)) {
    __builtin_amdgcn_s_waitcnt(0x0F70);
    __builtin_amdgcn_s_setprio(3);
    unsigned vx[8], vw[8], vb[4], v2[4];
#pragma unroll
    for (int i = 0; i < 8; ++i) vw[i] = g * 3584 + ((((i16 >> 1) ^ (g | (((i >> 1) & 1) << 2))) << 1) | (i16 & 1)) * 16 + 4096 + i * (14336 - 1024);
    const char* wb = p.w1 + jm * 256 + d * (32 * 3584);
    auto part1 = [&](int c, unsigned slot) { if (mlp) dma_x8(vw, wb + ((c + rot) & 7) * (64 * 3584), LB + slot + SL_W + d * 8192); };
    asm volatile("" :: "s"(wb));
    STAMP(18);
    part1(0, 0);
#pragma unroll
    for (int i = 0; i < 8; ++i)
      vx[i] = (32 * (g >> 1) + 16 * ((lane >> 3) & 1) + (g & 1)) * 1024 + (((lane & 7) ^ (4 * (i & 1) + g)) * 16) + 4096 + (i & 1) * 65536 + (i >> 1) * 2048 - i * 1024;
#pragma unroll
    for (int i = 0; i < 4; ++i) vb[i] = (lane >> 3) * 1024 + (((lane & 7) ^ (4 * (i & 1) + g)) * 16) + i * 7168;
    const char* xb = p.x + (r * 128 + d * 8) * 1024;
    const char* bb = (d ? p.bim : p.bre) + j * 32768;
    auto partx = [&](int c, unsigned slot) { dma_x8(vx, xb + ((c + rot) & 7) * 128, LB + slot + d * 8192); };
    auto partb = [&](int c, unsigned slot) { dma_x4(vb, bb + ((c + rot) & 7) * 128, LB + slot + SL_B + d * 4096); };
    auto part0 = [&](int c, unsigned slot) { partx(c, slot); partb(c, slot); };
    mkv2(v2);
    unsigned ra[4];
#pragma unroll
    for (int n = 0; n < 4; ++n) ra[n] = home + r2l + ((n ^ r2x) * 32);
    int kt = kstep_of(slot_of((lane >> 3) & 3), lane & 7) * 32768;
    asm volatile("" : "+v"(kt), "+v"(ra[0]), "+v"(ra[1]), "+v"(ra[2]), "+v"(ra[3]), "+v"(v2[0]), "+v"(v2[1]), "+v"(v2[2]), "+v"(v2[3]));
    bool low = false;
    auto page = [&](int role, int i, int grp, int pg) { if ((NOC & 4) && low) return; dma_x4(v2, w2b + (unsigned)__builtin_amdgcn_readlane(kt, 8 * role + i), HB + grp * SLOT + pg * 4096, low); };
    auto take = [&](int i) {
      SCHED();
#pragma unroll
      for (int n = 0; n < 4; ++n) we[i][n] = ldstr(ra[n] + PG(i) * 4096, ra[n] + PG(i) * 4096 + 2048);
      SCHED();
    };
    auto own = [&](int i) { page(3, i, 3, PG(i)); };
    unsigned l16 = lane * 16, la = home + 12288 + lane * 16, lb = LB + L_ST1 + d * 1024 + lane * 16, lc = LB + L_BS + d * 512 + (lane & 31) * 16;
    unsigned wa = LB + L_CS + d * 256 + (lane & 31) * 8 + (lane >> 5) * 4, wb2 = LB + L_LS + (2 * d + (lane >> 5)) * 288 + i16 * 16 + (g & 1) * 4;
    asm volatile("" : "+v"(l16), "+v"(la), "+v"(lb), "+v"(lc), "+v"(wa), "+v"(wb2));
    auto gather = [&](bool bs) {
      const char* cp = p.ws + WS_CS + jm * 8192 + d * 4096;
      dma4_dev(l16, cp, cp, home + 12288);
      dma2_dev(l16, p.ws + WS_STAT + r * 2048 + d * 1024, p.ws + WS_STAT + (mtile >> 1) * 2048 + d * 1024 - 2048, LB + L_ST1 + d * 1024);
      if (bs) dma1_dev(l16, p.ws + WS_BS + j * 1024, LB + L_BS);
    };
    auto check = [&](auto between) -> bool {
      auto ld16 = [](unsigned a) { return *(volatile LAS u32x4*)(size_t)a; };
      auto bad = [&](unsigned a, u32x4 v) { return __builtin_amdgcn_bitop3_b32(v.z, tagB, __builtin_amdgcn_bitop3_b32(v.x, tagA, a, 0xBE), 0xBE); };
      const u32x4 c0 = ld16(la), c1 = ld16(la + 1024), c2 = ld16(la + 2048), c3 = ld16(la + 3072), s1 = ld16(lb), s2 = ld16(lb + 2048), s3 = ld16(lc);
      SCHED(); between(); SCHED();
      const u32x2 h = swap32(sadd(sadd(u2f(c0.y), u2f(c1.y)), sadd(u2f(c2.y), u2f(c3.y))), sadd(sadd(u2f(c0.w), u2f(c1.w)), sadd(u2f(c2.w), u2f(c3.w))));
      *(LAS float*)(size_t)wa = sadd(u2f(h.x), u2f(h.y));
      *(LAS unsigned*)(size_t)wb2 = s1.y;
      *(LAS unsigned*)(size_t)(wb2 + 8) = s1.w;
      const unsigned e = bad(bad(bad(bad(bad(bad(bad(0u, c0), c1), c2), c3) & (mlp ? ~0u : 0u), s1), s2), s3);
      return __builtin_amdgcn_ballot_w64(e != 0) == 0;
    };
    const unsigned me = PW + w * 4;
    zero2();
    setup2(8);
    auto landed = [&]() { if (mlp) wait_vm<20>(); else wait_vm<12>(); };
    SCHED();
    part0(0, 0);
    if (TOUCH == 4) touch();
    part1(1, SLOT); part0(1, SLOT);
    STAMP(1);
    mktag();
    if (mlp) wait_vm<20 + (TOUCH == 4)>(); else wait_vm<12 + (TOUCH == 4)>();
    STAMP(19);
    bar();
    STAMP(2);
    part1(2, 2 * SLOT); part0(2, 2 * SLOT); landed(); if (TOUCH == 1) touch();
    bar();
    if (d) dma4w(lane * 4, p.b1 + jm * 256, LB + L_B1);
    part1(3, 0); part0(3, 0); landed();
    unsigned vt = (lane < 56 ? lane : 0) * 128;
    asm volatile("" : "+v"(vt));
    auto l2t = [&](int t) { if (t < L2T) dma4w(vt, p.w2 + (t * 64 + 2 * idx + d) * 7168, LB + L_DUMMY + d * 256); };
    auto l2c = [&](int t) { if (t < L2C) dma4w(l16 * 8, (t ? p.cim : p.cre) + (2 * idx + d) * 8192, LB + L_DUMMY + d * 256); };
    auto s3 = [&]() { if (L2T < 4) dma4w((lane < 28 ? lane : 0) * 1024, p.w2 + (2 * idx + d) * 28672 + ((3u - ((unsigned)(size_t)p.w2 >> 7)) & 7u) * 128, LB + L_DUMMY + d * 256); };
    if (S3AT == 1) s3();
    l2t(0);
    bar();
    STAMPC(21);
    dma_x4(vx, p.x + (128 * (mtile >> 1) + 8 * (mtile & 1) + 4 * d) * 1024 + ntile * 128 - 4096, home + 12288);
    part1(4, SLOT); part0(4, SLOT); landed(); if (S3AT == 2) s3();
    bar();
    own(0); part1(5, 2 * SLOT); part0(5, 2 * SLOT); landed(); if (S3AT == 3) s3();
    bar();
    gather(d); part1(6, 0); take(0); part0(6, 0); landed(); if (S3AT == 4) s3();
    bar();
    part1(7, SLOT);
    const bool ok = check([&]() { partx(7, SLOT); });
    SCHED();
    partb(7, SLOT);
    if (__builtin_expect(!ok, 0)) {
#pragma unroll 1
      for (int spin = 0; spin < SPIN_CAP; ++spin) {
        COUNT(20);
        __builtin_amdgcn_s_sleep(10);
        gather(true);
        wait_vm<0>();
        if (check([]() {})) break;
      }
    }
    landed();
    barrier();
    STAMP(3); STAMPC(22);
    page(0, 0, 2, 0); page(0, 1, 2, 1); page(0, 2, 2, 2); page(0, 3, 2, 3); page(0, 4, 2, 4); own(1);
    wait_vm<12>();
    bar();
    STAMP(4);
    own(2); page(2, 0, 0, 0); page(2, 1, 0, 1); page(2, 2, 0, 2); page(2, 3, 0, 3); page(2, 4, 0, 4);
    wait_vm<20>();
    STAMP(5);
    bar();
    TRACE4(31);
    low = true;
    STAMP(6);
    take(1); take(2);
    page(0, 5, 2, 0); page(0, 6, 2, 1); page(0, 7, 2, 2);
    wait_vm<12>(); prog_set(me, 1);
    own(3); own(4); wait_lgkm0(); own(5); own(6);
    wait_vm<16>(); prog_set(me, 2);
    page(1, 0, 1, 0); page(1, 1, 1, 1); page(1, 2, 1, 2);
    wait_vm<16>(); take(3); take(4); take(5); wait_lgkm0();
    own(7);
    page(1, 3, 1, 3); page(1, 4, 1, 4);
    auto c8 = [&](const char* src, unsigned dst) { if (NOC & 1) return; dma_x4(vb, src, dst, true); dma_x4(vb, src + 32768, dst + 4096, true); };
    const unsigned co = ntile * 65536 + kg * 128;
    c8(p.cre + co, home);
    wait_vm<8>();
    prog_set(me, 3);
    take(6); take(7);
    prog_wait(PW + d * 4, 1);
    c8(p.cim + co, home + 8192);
    STAMP(7);
    snap(false);
    c8(p.cre + (co ^ 512), HB + 2 * SLOT); c8(p.cim + (co ^ 512), HB + 2 * SLOT + 8192);
    seen();
    STAMP(8);
  };
  auto roleL = [&]() __attribute__((always_inline)) {
    if (TOUCH == 0) touch();
    if (TLBW && d) { dma4w(0, p.cre + ntile * 65536, LB + L_DUMMY + 256); dma4w(0, p.cim + ntile * 65536, LB + L_DUMMY + 256); dma4w((lane & 1) * 0x1bf000, p.w2, LB + L_DUMMY + 256); }
    f32x4 acr[8], aci[8];
#pragma unroll
    for (int m = 0; m < 8; ++m) { acr[m] = (f32x4){0, 0, 0, 0}; aci[m] = (f32x4){0, 0, 0, 0}; asm volatile("" : "+v"(acr[m]), "+v"(aci[m])); }
    unsigned ax[2], ab[2];
    ax[0] = LB + i16 * 128 + kx; ax[1] = ax[0] ^ 64;
    ab[0] = LB + SL_B + (16 * d + i16) * 128 + kx; ab[1] = ab[0] ^ 64;
    unsigned sa = LB + L_LS + g * 288, csa = LB + L_BS + (16 * d + i16) * 16 + 4;
    asm volatile("" : "+v"(sa), "+v"(csa), "+v"(ax[0]), "+v"(ax[1]), "+v"(ab[0]), "+v"(ab[1]));
    s16x8 xf[2][8], bf[2][2];
    auto rd = [&](int set, int i) {
      if (i < 2) bf[set][i] = lds128(ab[set] + i * 4096);
      else xf[set][i - 2] = lds128(ax[set] + (i - 2) * 2048);
    };
    auto mm = [&](int set, int i) {
      if (i & 1) aci[i >> 1] = mfma(xf[set][i >> 1], bf[set][1], aci[i >> 1]);
      else acr[i >> 1] = mfma(xf[set][i >> 1], bf[set][0], acr[i >> 1]);
    };
    asm volatile("" : "+v"(v_lre), "+v"(v_lim), "+v"(v_ldt));
    const float lr = bflo(v_lre), li = bflo(v_lim);
    const float dt = bflo(pk2(expf(bflo(v_ldt)), 0.f));
    const float ang = smul(dt, li);
    const float rv = smul(ang, 0.15915494f);
    const float rev = (rv - __builtin_rintf(rv)) + sfma(ang, 6.4206382e-9f, sfms(ang, 0.15915494f, rv));
    float sn = __builtin_amdgcn_sinf(rev), cn = __builtin_amdgcn_cosf(rev);
    asm volatile("s_nop 0" : "+v"(sn), "+v"(cn));
    const float ex = expf(smul(dt, lr));
    f32x2 a1 = {smul(ex, cn), smul(ex, sn)};
    const float nr = a1[0] - 1.f, ni = a1[1], iden = 2.f / sfma(lr, lr, smul(li, li));
    f32x2 kk = {smul(sfma(nr, lr, smul(ni, li)), iden), smul(sfms(ni, lr, smul(nr, li)), iden)};
    f32x2 t = cmul(a1, a1); t = cmul(t, t); t = cmul(t, t);
    f32x2 a16 = cmul(t, t);
    f32x2 a32 = cmul(a16, a16);
    const f32x2 a64 = cmul(a32, a32), a128 = cmul(a64, a64), a256 = cmul(a128, a128);
    f32x2 a512 = cmul(a256, a256);
    const f32x2 one = {1.f, 0.f};
    f32x2 aseg = cmul(kk, cmul(csel(g & 1, a32, one), csel(g & 2, a64, one)));
    const int e4 = (r - 1 - g) & 3;
    f32x2 acar = cmul(csel(e4 & 1, a128, one), csel(e4 & 2, a256, one));
    const f32x2 ar = bco(a1[0]), ai = bco(a1[1]);
    asm volatile("" :: "v"(ar), "v"(ai));
    asm volatile("" : "+v"(kk), "+v"(a16), "+v"(a32), "+v"(a512), "+v"(aseg), "+v"(acar));
    const unsigned on = (qq == g) ? ((pp & 1) ? 0xffff0000u : 0xffffu) : 0u;
    unsigned m0 = (pp & 2) ? 0u : on, m1 = (pp & 2) ? on : 0u;
    auto scan = [&](f32x2 h0, f32x2 h1, const f32x2* ur, const f32x2* ui, auto out) {
      const u32x4 z0 = {m0 & 0x3f803f80u, m1 & 0x3f803f80u, 0u, 0u}, z1 = {0u, 0u, z0.x, z0.y};
      const f32x4 zz = {0, 0, 0, 0};
      f32x2 hr = {h0[0], h1[0]}, hi = {-h0[1], -h1[1]};
      u32x4 fr[4], fi[4];
      f32x4 tr[2], ti[2];
      SCHED();
#pragma unroll
      for (int mp = 0; mp < 5; ++mp) {
        if (mp > 0) {
#pragma unroll
          for (int k = 0; k < 2; ++k) {
            tr[k] = mfma(__builtin_bit_cast(s16x8, fr[mp - 1]), __builtin_bit_cast(s16x8, k ? z1 : z0), zz);
            ti[k] = mfma(__builtin_bit_cast(s16x8, fi[mp - 1]), __builtin_bit_cast(s16x8, k ? z1 : z0), zz);
          }
          SCHED();
        }
        if (mp < 4) {
#pragma unroll
          for (int e = 0; e < 4; ++e) {
            const int s = 4 * mp + e;
            const f32x2 t1 = ur ? pfma(hr, -ai, ui[s]) : hr * -ai; SCHED();
            const f32x2 t0 = ur ? pfma(hi, ai, ur[s]) : hi * ai; SCHED();
            hi = pfma(hi, ar, t1); SCHED();
            hr = pfma(hr, ar, t0); SCHED();
            fi[mp][e] = pk2(hi[0], hi[1]); SCHED();
            fr[mp][e] = pk2(hr[0], hr[1]); SCHED();
          }
        }
        if (mp > 0) { out(2 * mp - 2, tr[0], ti[0]); out(2 * mp - 1, tr[1], ti[1]); SCHED(); }
      }
    };
    auto pack = [](f32x4 a, f32x4 b) { return (u32x4){pk2(a[0], a[1]), pk2(a[2], a[3]), pk2(b[0], b[1]), pk2(b[2], b[3])}; };
    u32x4 yb[8];
    auto resp = [&]() { scan(aseg, cmul(a16, aseg), nullptr, nullptr, [&](int q, f32x4 a, f32x4 b) { yb[q] = pack(a, b); asm volatile("" : "+v"(yb[q])); }); };
    if (YPRE) resp();
    mktag();
    __builtin_amdgcn_s_waitcnt(0x0F70);
    auto tlb = [&]() {
      const int l = lane & 15;
      const unsigned o = d ? (128 * (mtile >> 1) + (l & 7) * 16) * 2048 + ntile * 256
                           : l < 11 ? WS_H + mtile * H_MT + l * 32768 : l == 15 ? WS_FLAG + mtile * 2048 + 1920 : WS_H + (2 * r + (l & 1)) * H_MT + (l & 2 ? 4 * jm : 56 + 2 * j) * 4096;
      asm volatile("s_mov_b32 m0, %2\n\ts_nop 0\n\tglobal_load_lds_dword %0, %1 sc1" ::"v"(o), "s"(d ? p.out : p.ws), "s"(LB + L_DUMMY + 512));
    };
    if (TLBH == 1) tlb();
    __builtin_amdgcn_s_setprio(1);
    STAMP(1);
    bar();
    STAMP(2); TRACE3(1);
#pragma unroll
    for (int i = 0; i < 10; ++i) rd(0, i);
    wait_lgkm0();
    auto stage1 = [&](int dl) {
      SCHED();
#pragma unroll
      for (int i = 0; i < 16; ++i) {
        mm(0, i); SCHED();
        if (i < 10) rd(1, i);
        if (i == 11) { adv(ax[0], dl); adv(ab[0], dl); }
        SCHED();
      }
    };
    stage1(SLOT);
    if (TLBH == 2) tlb();
    barrier();
    TRACE3(2);
    int ph = 0;
#pragma unroll 1
    for (int c = 2; c <= 8; ++c) {
      const int dl = ph == 2 ? -2 * (int)SLOT : (int)SLOT;
      ph = ph == 2 ? 0 : ph + 1;
      SCHED();
#pragma unroll
      for (int i = 0; i < 16; ++i) {
        mm(1, i); SCHED();
        if (i < 10) rd(0, i);
        if (i == 11) { adv(ax[1], dl); adv(ab[1], dl); }
        SCHED();
      }
      wait_lgkm0();
      stage1(ph == 2 ? -2 * (int)SLOT : (int)SLOT);
      TRACE(2 * c);
      barrier();
      TRACE(2 * c + 1); TRACE3(c + 1);
    }
    __builtin_amdgcn_s_setprio(LPRIO);
    STAMP(4);
    f32x2 rs[16], mrs[16];
    float csr, csi;
#pragma unroll
    for (int i = 0; i < 16; ++i) {
      mm(1, i); SCHED();
      if (i == 0) { csr = ldsf1(csa); csi = ldsf1(csa + 512); }
      if (i < 8) { const f32x4 t = ldsf4(sa + i * 16); rs[i] = lo2(t); mrs[i] = hi2(t); }
      SCHED();
    }
    const f32x2 ncr = bco(-csr), nci = bco(-csi);
    STAMP(5);
    f32x2 ur[16], ui[16], Er, Ei;
    SCHED();
#pragma unroll
    for (int s = 0; s < 16; ++s) {
      if (s < 8) { const f32x4 t = ldsf4(sa + (s + 8) * 16); rs[s + 8] = lo2(t); mrs[s + 8] = hi2(t); SCHED(); }
      ur[s] = mrs[s] * ncr; SCHED();
      ui[s] = mrs[s] * nci; SCHED();
      ur[s] = pfma((s & 1) ? hi2(acr[s >> 1]) : lo2(acr[s >> 1]), rs[s], ur[s]); SCHED();
      ui[s] = pfma((s & 1) ? hi2(aci[s >> 1]) : lo2(aci[s >> 1]), rs[s], ui[s]); SCHED();
      if (s == 0) { Er = ur[0]; Ei = ui[0]; }
      else {
        const f32x2 t0 = pfma(Ei, -ai, ur[s]); SCHED();
        const f32x2 t1 = pfma(Er, ai, ui[s]); SCHED();
        Er = pfma(Er, ar, t0); SCHED();
        Ei = pfma(Ei, ar, t1); SCHED();
      }
    }
    TRACE2(1);
    const f32x2 E0 = {Er[0], Ei[0]};
    const f32x2 E = cfma(a16, E0, (f32x2){Er[1], Ei[1]});
    f32x2 P = {0.f, 0.f}, Tt = {0.f, 0.f};
    float er4[4], ei4[4];
    rows4(E[0], er4); rows4(E[1], ei4);
#pragma unroll
    for (int s = 0; s < 4; ++s) {
      const f32x2 e = {er4[s], ei4[s]};
      P = csel(s == g, Tt, P);
      Tt = s ? cfma(a32, Tt, e) : e;
    }
    const unsigned totb = WS_TOT + (unsigned)(j * 2 + d) * 4096;
    if (g == 0) __builtin_amdgcn_raw_buffer_store_b128((u32x4){tagA, f2u(Tt[0]), tagB, f2u(Tt[1])}, wsr, totb + r * 256 + i16 * 16, 0, 16);
    STAMP(6);
    TRACE2(2);
    {
      const f32x2 kr = bco(kk[0]), nki = bco(-kk[1]);
#pragma unroll
      for (int s = 0; s < 16; ++s) {
        const f32x2 t0 = ur[s] * kr; SCHED();
        const f32x2 t1 = ur[s] * nki; SCHED();
        ur[s] = pfma(ui[s], nki, t0); SCHED();
        ui[s] = pfma(ui[s], -kr, t1); SCHED();
      }
    }
#pragma unroll
    for (int s = 0; s < 16; s += 4) asm volatile("" : "+v"(ur[s]), "+v"(ui[s]), "+v"(ur[s + 1]), "+v"(ui[s + 1]), "+v"(ur[s + 2]), "+v"(ui[s + 2]), "+v"(ur[s + 3]), "+v"(ui[s + 3]));
    f32x4 hr4[8], hi4[8];
    {
      const f32x2 kP = cmul(kk, P);
      scan(kP, cfma(a16, kP, cmul(kk, E0)), ur, ui, [&](int q, f32x4 a, f32x4 b) { hr4[q] = a; hi4[q] = b; asm volatile("" : "+v"(hr4[q]), "+v"(hi4[q])); });
    }
    TRACE2(3);
    prog_wait(PW + (6 + d) * 4, 1);
#pragma unroll
    for (int i = 0; i < 5; ++i) absorb(i, home + i * 4096);
    unsigned v2[4], vt[4];
    mkv2(v2);
#pragma unroll
    for (int i = 0; i < 4; ++i) { vt[i] = (4 * i + g < r ? g * 256 : -i * 1024) + i16 * 16 + 4096; asm volatile("" : "+v"(vt[i]), "+v"(v2[i])); }
    {
      const i32x4 tr = {wss[0], wss[1], (int)(totb + r * 256), wss[3]};
#pragma unroll 1
      for (int spin = 0; spin < 64; ++spin) {
        int n, t[15];
        asm volatile("s_buffer_load_dword %1, %16, %17 offset:0 glc\n\ts_buffer_load_dword %2, %16, %17 offset:256 glc\n\ts_buffer_load_dword %3, %16, %17 offset:512 glc\n\ts_buffer_load_dword %4, %16, %17 offset:768 glc\n\ts_buffer_load_dword %5, %16, %17 offset:1024 glc\n\ts_buffer_load_dword %6, %16, %17 offset:1280 glc\n\ts_buffer_load_dword %7, %16, %17 offset:1536 glc\n\ts_buffer_load_dword %8, %16, %17 offset:1792 glc\n\ts_buffer_load_dword %9, %16, %17 offset:2048 glc\n\ts_buffer_load_dword %10, %16, %17 offset:2304 glc\n\ts_buffer_load_dword %11, %16, %17 offset:2560 glc\n\ts_buffer_load_dword %12, %16, %17 offset:2816 glc\n\ts_buffer_load_dword %13, %16, %17 offset:3072 glc\n\ts_buffer_load_dword %14, %16, %17 offset:3328 glc\n\ts_buffer_load_dword %15, %16, %17 offset:3584 glc\n\t"
                     "s_mov_b32 %0, 0\n\ts_waitcnt lgkmcnt(0)\n\t"
                     "s_cmp_eq_u32 %1, %18\n\ts_addc_u32 %0, %0, 0\n\ts_cmp_eq_u32 %2, %18\n\ts_addc_u32 %0, %0, 0\n\ts_cmp_eq_u32 %3, %18\n\ts_addc_u32 %0, %0, 0\n\ts_cmp_eq_u32 %4, %18\n\ts_addc_u32 %0, %0, 0\n\ts_cmp_eq_u32 %5, %18\n\ts_addc_u32 %0, %0, 0\n\ts_cmp_eq_u32 %6, %18\n\ts_addc_u32 %0, %0, 0\n\ts_cmp_eq_u32 %7, %18\n\ts_addc_u32 %0, %0, 0\n\ts_cmp_eq_u32 %8, %18\n\ts_addc_u32 %0, %0, 0\n\ts_cmp_eq_u32 %9, %18\n\ts_addc_u32 %0, %0, 0\n\ts_cmp_eq_u32 %10, %18\n\ts_addc_u32 %0, %0, 0\n\ts_cmp_eq_u32 %11, %18\n\ts_addc_u32 %0, %0, 0\n\ts_cmp_eq_u32 %12, %18\n\ts_addc_u32 %0, %0, 0\n\ts_cmp_eq_u32 %13, %18\n\ts_addc_u32 %0, %0, 0\n\ts_cmp_eq_u32 %14, %18\n\ts_addc_u32 %0, %0, 0\n\ts_cmp_eq_u32 %15, %18\n\ts_addc_u32 %0, %0, 0"
                     : "=&s"(n), "=&s"(t[0]), "=&s"(t[1]), "=&s"(t[2]), "=&s"(t[3]), "=&s"(t[4]), "=&s"(t[5]), "=&s"(t[6]), "=&s"(t[7]), "=&s"(t[8]), "=&s"(t[9]), "=&s"(t[10]), "=&s"(t[11]), "=&s"(t[12]), "=&s"(t[13]), "=&s"(t[14]) : "s"(tr), "s"(totb), "s"(tagA) : "scc", "memory");
        if (n >= r) break;
        COUNT(20);
      }
    }
    STAMP(7);
    auto fetch = [&]() {
      asm volatile("s_mov_b32 m0, %5\n\ts_nop 0\n\tglobal_load_lds_dwordx4 %0, %4 sc1\n\tglobal_load_lds_dwordx4 %1, %4 offset:1024 sc1\n\t"
                   "global_load_lds_dwordx4 %2, %4 offset:2048 sc1\n\tglobal_load_lds_dwordx4 %3, %4 offset:3072 sc1"
                   ::"v"(vt[0]), "v"(vt[1]), "v"(vt[2]), "v"(vt[3]), "s"(p.ws + totb - 4096), "s"(home + 16384) : "memory");
    };
    wait_lgkm0();
    if (r > 0) fetch();
    TRACE2(14);
    if (!YPRE) resp();
    f32x2 wt[4];
#pragma unroll
    for (int i = 3; i >= 0; --i) {
      const bool v = 4 * i + g < r;
      wt[i] = csel(v, acar, (f32x2){0.f, 0.f});
      if (i) acar = csel(v, cmul(acar, a512), acar);
      asm volatile("" : "+v"(wt[i]));
    }
    setup2(6);
    unsigned ho = WS_H + (unsigned)(2 * r) * H_MT + (unsigned)(56 + 2 * j + d) * 4096 + lane * 16, ta = home + 16384 + lane * 16;
    asm volatile("" : "+v"(ho), "+v"(ta), "+v"(m0), "+v"(m1));
    TRACE2(4);
    wait_vm<0>();
    TRACE2(15);
    float cr = 0.f, ci = 0.f;
    if (r > 0) {
#pragma unroll 1
      for (int spin = 0; spin < SPIN_CAP; ++spin) {
        u32x4 tv[4];
        unsigned e = 0;
#pragma unroll
        for (int i = 0; i < 4; ++i) tv[i] = *(volatile LAS u32x4*)(size_t)(ta + i * 1024);
        f32x2 c[2];
#pragma unroll
        for (int i = 0; i < 4; ++i) {
          const f32x2 t = {u2f(tv[i].y), u2f(tv[i].w)};
          c[i & 1] = i < 2 ? cmul(wt[i], t) : cfma(wt[i], t, c[i & 1]);
          e = __builtin_amdgcn_bitop3_b32(tv[i].z, tagB, __builtin_amdgcn_bitop3_b32(tv[i].x, tagA, e, 0xBE), 0xBE);
        }
        const u32x2 h = swap32(sadd(c[0][0], c[1][0]), sadd(c[0][1], c[1][1]));
        const float x = sadd(u2f(h.x), u2f(h.y));
        const u32x2 h2 = swap16(x, x);
        const float y = sadd(u2f(h2.x), u2f(h2.y));
        const u32x2 h3 = swap32(y, y);
        cr = u2f(h3.x); ci = u2f(h3.y);
        if (__builtin_expect(__builtin_amdgcn_ballot_w64(e != 0) == 0, 1)) break;
        COUNT(20);
        __builtin_amdgcn_s_sleep(8);
        fetch();
        wait_vm<0>();
      }
    }
    STAMP(8);
    {
      const unsigned c0 = pk2(cr, cr), c1 = pk2(ci, ci), c2 = pk2(-ci, -ci);
      const s16x8 are = __builtin_bit_cast(s16x8, (u32x4){m0 & c0, m1 & c0, m0 & c1, m1 & c1}), aim = __builtin_bit_cast(s16x8, (u32x4){m0 & c2, m1 & c2, m0 & c0, m1 & c0});
      u32x4 o[8];
      SCHED();
#pragma unroll
      for (int q = 0; q < 10; ++q) {
        if (q < 8) {
          hr4[q] = mfma(are, __builtin_bit_cast(s16x8, yb[q]), hr4[q]);
          hi4[q] = mfma(aim, __builtin_bit_cast(s16x8, yb[q]), hi4[q]);
          SCHED();
        }
        if (q >= 2) { o[q - 2] = pack(hr4[q - 2], hi4[q - 2]); __builtin_amdgcn_raw_buffer_store_b128(o[q - 2], wsr, ho + ((q - 2) & 3) * 1024, ((q - 2) >> 2) * H_MT, HST); }
        SCHED();
      }
      TRACE2(5);
      dma_x4(v2, w2b + kstep_of(slot, 5) * 32768, home + 16384);
      asm volatile("" :: "v"(o[0]), "v"(o[1]), "v"(o[2]), "v"(o[3]), "v"(o[4]), "v"(o[5]), "v"(o[6]), "v"(o[7]), "v"(ho));
      STAMP(19);
      pubo = lane < 2 ? WS_FLAG + (2 * r + lane) * 2048 + 1024 + (2 * j + d) * 8 : pubo;
    }
    STAMP(18);
  };
  auto roleB = [&]() __attribute__((always_inline)) {
    mktag();
    {
      float s0 = 0.f, s1 = 0.f;
#pragma unroll
      for (int e = 0; e < 8; ++e) { s0 = sadd(s0, bflo(prb[e >> 2][e & 3])); s1 = sadd(s1, bfhi(prb[e >> 2][e & 3])); }
      *(LAS float*)(size_t)(LB + L_LS + 512 + (4 * d + g) * 4) = row16_sum(s0 + s1);
      __builtin_amdgcn_s_waitcnt(0x0F70);
    }
    if (mlp) {
      f32x4 acc[6][4];
#pragma unroll
      for (int s = 0; s < 6; ++s) {
#pragma unroll
        for (int m = 0; m < 4; ++m) acc[s][m] = (f32x4){0, 0, 0, 0};
        asm volatile("" : "+v"(acc[s][0]), "+v"(acc[s][1]), "+v"(acc[s][2]), "+v"(acc[s][3]));
      }
      unsigned ax[2], aw[6];
      ax[0] = LB + (64 * d + i16) * 128 + kx; ax[1] = ax[0] ^ 64;
#pragma unroll
      for (int s = 0; s < 6; ++s) aw[s] = awb + ((s ^ awx) * 32);
      unsigned sta = LB + L_LS + qq * 288 + (8 * d + ((i16 >> 1) & 1)) * 16 + (i16 & 1) * 4, csa = LB + L_CS + g * 16;
      asm volatile("" : "+v"(ax[0]), "+v"(ax[1]), "+v"(aw[0]), "+v"(aw[1]), "+v"(aw[2]), "+v"(aw[3]), "+v"(aw[4]), "+v"(aw[5]), "+v"(sta), "+v"(csa));
      s16x8 xf[2][4];
      s16x4 wf[2][12];
      auto rd = [&](int set, int i) {
        if (i < 4) xf[set][i] = lds128(ax[set] + i * 2048);
        else wf[set][i - 4] = ldstr4(aw[(i - 4) >> 1] + set * 8192 + (i & 1) * 1024);
      };
      auto mm = [&](int set, int i) { const int s = i >> 2, m = i & 3; acc[s][m] = mfma(cat(wf[set][2 * s], wf[set][2 * s + 1]), xf[set][m], acc[s][m]); };
      {
        float t[8];
#pragma unroll
        for (int e = 0; e < 8; ++e)
          t[e] = (e & 1) ? sadd(sadd(bfhi(pre[0][e >> 1]), bfhi(pre[1][e >> 1])), sadd(bfhi(pre[2][e >> 1]), bfhi(pre[3][e >> 1])))
                         : sadd(sadd(bflo(pre[0][e >> 1]), bflo(pre[1][e >> 1])), sadd(bflo(pre[2][e >> 1]), bflo(pre[3][e >> 1])));
#pragma unroll
        for (int e = 0; e < 4; ++e) { const u32x2 h = swap32(t[e], t[e + 4]); t[e] = sadd(u2f(h.x), u2f(h.y)); }
#pragma unroll
        for (int e = 0; e < 2; ++e) { const u32x2 h = swap16(t[e], t[e + 2]); t[e] = dpp_add<0x128>(sadd(u2f(h.x), u2f(h.y))); }
        *(LAS f32x2*)(size_t)(LB + L_LS + d * 256 + (4 * (lane & 7) + g) * 8) = (f32x2){t[0], t[1]};
      }
      __builtin_amdgcn_s_setprio(1);
      STAMP(1);
      barrier();
      STAMP(2); STAMPC(21);
#pragma unroll
      for (int i = 0; i < 16; ++i) rd(0, i);
      wait_lgkm0();
      auto stage1 = [&](int c, int dl) {
        SCHED();
#pragma unroll
        for (int i = 0; i < 24; ++i) {
          mm(0, i); SCHED();
          if (i < 16) rd(1, i);
          else if (i < 22) adv(aw[i - 16], dl);
          else if (i == 22) adv(ax[0], dl);
          SCHED();
        }
        TRACE(2 * c);
        barrier();
        TRACE(2 * c + 1);
        SCHED();
      };
      int ph = 0;
#pragma unroll 1
      for (int c = 1; c < 8; ++c) {
        const int dl = ph == 2 ? -2 * (int)SLOT : (int)SLOT;
        ph = ph == 2 ? 0 : ph + 1;
        stage1(c, dl);
#pragma unroll
        for (int i = 0; i < 24; ++i) {
          mm(1, i); SCHED();
          if (i < 16) rd(0, i);
          else if (i == 16) adv(ax[1], dl);
          else if (i == 23) wait_lgkm0();
          SCHED();
        }
      }
      stage1(8, 0);
      unsigned b1a = LB + L_B1 + g * 8;
      asm volatile("" : "+v"(b1a));
      STAMP(4); STAMPC(22);
      f32x2 rs[4], nm[4];
      f32x4 cs[6];
      u32x2 b1v[6];
      f32x2 st[4];
      auto colrd = [&](int s) { cs[s] = ldsf4(csa + 64 * s); b1v[s] = *(LAS u32x2*)(size_t)(b1a + 32 * s); };
#pragma unroll
      for (int i = 0; i < 4; ++i) st[i] = (f32x2){ldsf1(sta + i * 32), ldsf1(sta + i * 32 + 8)};
      colrd(0); colrd(1);
      SCHED();
#pragma unroll
      for (int i = 0; i < BSPLIT; ++i) mm(1, i);
      SCHED();
#pragma unroll
      for (int i = 0; i < 4; ++i) { rs[i] = bco(st[i][0]); nm[i] = bco(-st[i][1]); }
      STAMP(5);
      const unsigned fo = WS_FLAG + (2 * r + d) * 2048;
      pubo = lane ? pubo : 0u;
      u32x4 ob[12];
      const unsigned ho = WS_H + (unsigned)(2 * r + d) * H_MT + (unsigned)(4 * jm) * 4096 + lane * 16;
#pragma unroll
      for (int pr = 0; pr < 3; ++pr) {
        Col col;
        mkcol(col, cs[2 * pr], cs[2 * pr + 1], b1v[2 * pr], b1v[2 * pr + 1]);
        if (pr < 2) { colrd(2 * pr + 2); colrd(2 * pr + 3); }
        SCHED();
        if (pr == 1) {
#pragma unroll
          for (int i = BSPLIT; i < 24; ++i) mm(1, i);
          SCHED();
          STAMP(7);
        }
#pragma unroll
        for (int m = 0; m < 4; ++m) {
          ob[4 * pr + m] = gelu8(acc[2 * pr][m], acc[2 * pr + 1][m], col, rs[m], nm[m]);
          __builtin_amdgcn_raw_buffer_store_b128(ob[4 * pr + m], wsr, ho + m * 1024, pr * 4096, HST);
          if (4 * pr + m >= KEEP) asm volatile("" :: "v"(ob[4 * pr + m - KEEP]));
          TRACE2(1 + 4 * pr + m);
          if (pr > 0 && m == (pr == 1 ? BFLAG0 : BFLAG)) {
            if (pr == 1) wait_vm<BFLAG0 + 1>(); else wait_vm<BFLAG + 1>();
            __builtin_amdgcn_raw_buffer_store_b64((u32x2){tagA, tagB}, wsr, pubo, fo + flag_of(jm, pr - 1), 16);
          }
          SCHED();
        }
      }
      pubo |= fo + flag_of(jm, 2);
      STAMP(6);
    } else {
      const int q = 4 * r + 2 * lq + d;
      const unsigned vt = (lane < 56 ? lane : 0) * 128, dm = LB + L_DUMMY + d * 256;
#pragma unroll 1
      for (int c = 0; c < 9; ++c) {
        if (TOUCH == 7) {
          const int t = (c - TST) * TPP;
#pragma unroll
          for (int e = 0; e < TPP; ++e) {
            if (t + e >= 0 && t + e < 4) dma4w(vt, p.w2 + ((t + e) * 64 + q) * 7168, dm);
            else if (t + e == 4 || t + e == 5) dma4w(lane * 128, (t + e == 4 ? p.cre : p.cim) + q * 8192, dm);
          }
        }
        barrier();
      }
    }
    prog_wait(PW + (6 + d) * 4, 3);
#pragma unroll
    for (int i = 0; i < 5; ++i) absorb(i, home + i * 4096);
    unsigned v2[4];
    mkv2(v2);
    setup2(6);
#pragma unroll
    for (int i = 0; i < 4; ++i) asm volatile("" : "+v"(v2[i]));
    wait_lgkm0();
    STAMP(8);
    dma_x4(v2, w2b + kstep_of(slot, 5) * 32768, home + 16384);
  };
  auto roleA = [&]() __attribute__((always_inline)) {
    const int srow = 128 * r + 8 * j + 4 * d + g;
    u32x2 dv[4], bv[4];
#pragma unroll
    for (int n = 0; n < 4; ++n) { dv[n] = *(const GAS u32x2*)(p.dsk + ntile * 128 + n * 32 + g * 8); bv[n] = *(const GAS u32x2*)(p.b2 + ntile * 128 + n * 32 + g * 8); }
    if (lane < 8) *(LAS unsigned*)(size_t)(PW + lane * 4) = 0u;
    f32x4 acc[2][4];
#pragma unroll
    for (int m = 0; m < 4; ++m) { acc[0][m] = (f32x4){0, 0, 0, 0}; acc[1][m] = (f32x4){0, 0, 0, 0}; asm volatile("" : "+v"(acc[0][m]), "+v"(acc[1][m])); }
    unsigned ax0 = LB + (64 * d + i16) * 128 + kx, ax1 = ax0 ^ 64, aw0 = awb + ((6 ^ awx) * 32), aw1 = awb + ((7 ^ awx) * 32);
    unsigned sta = LB + L_LS + qq * 288 + (8 * d + ((i16 >> 1) & 1)) * 16 + (i16 & 1) * 4, csa = LB + L_CS + 384 + g * 16;
    asm volatile("" : "+v"(ax0), "+v"(ax1), "+v"(aw0), "+v"(aw1), "+v"(sta), "+v"(csa));
    int ph = 0;
    auto chunk = [&](auto side) {
      s16x8 xf[2][4], wf[2][2];
      if (ADELAY) __builtin_amdgcn_s_sleep(ADELAY);
#pragma unroll
      for (int k = 0; k < 2; ++k) {
        wf[k][0] = ldstr(aw0 + k * 8192, aw0 + k * 8192 + 1024);
        wf[k][1] = ldstr(aw1 + k * 8192, aw1 + k * 8192 + 1024);
#pragma unroll
        for (int m = 0; m < 4; ++m) xf[k][m] = lds128((k ? ax1 : ax0) + m * 2048);
      }
      SCHED();
      const int dl = ph == 2 ? -2 * (int)SLOT : (int)SLOT;
      ph = ph == 2 ? 0 : ph + 1;
      adv(ax0, dl); adv(ax1, dl); adv(aw0, dl); adv(aw1, dl);
      side(8);
      SCHED();
#pragma unroll
      for (int k = 0; k < 2; ++k)
#pragma unroll
        for (int m = 0; m < 4; ++m) {
          acc[0][m] = mfma(wf[k][0], xf[k][m], acc[0][m]); acc[1][m] = mfma(wf[k][1], xf[k][m], acc[1][m]);
          SCHED();
          side(4 * k + m);
          SCHED();
        }
    };
    auto none = [](int) {};
    mktag();
    asm volatile("" : "+v"(pre[3]));
    STAMP(19);
    {
      const float mean = sum32(pre) * (1.f / 512.f);
      float v0 = 0.f, v1 = 0.f;
#pragma unroll
      for (int i = 0; i < 4; ++i)
#pragma unroll
        for (int e = 0; e < 4; ++e) {
          const float d0 = bflo(pre[i][e]) - mean, d1 = bfhi(pre[i][e]) - mean;
          v0 = sfma(d0, d0, v0); v1 = sfma(d1, d1, v1);
        }
      const float rsd = __builtin_amdgcn_rsqf(row16_sum(v0 + v1) * (1.f / 512.f) + 1e-5f);
      if (i16 == 0) __builtin_amdgcn_raw_buffer_store_b128((u32x4){tagA, f2u(rsd), tagB, f2u(mean * rsd)}, wsr, WS_STAT + srow * 16, 0, 16);
    }
    __builtin_amdgcn_s_setprio(APRIO);
    STAMP(1);
    barrier();
    STAMP(2);
    chunk(none);
    if (d) {
      const f32x2 c0 = ldsf2(LB + L_LS + 512 + (lane & 3) * 8);
      if (lane < 4) __builtin_amdgcn_raw_buffer_store_b128((u32x4){tagA, f2u(sadd(c0[0], c0[1])), tagB, 0u}, wsr, WS_BS + j * 1024 + (4 * r + lane) * 16, 0, 16);
    } else if (mlp) {
      const f32x2 c0 = ldsf2(LB + L_LS + (lane & 31) * 8), c1 = ldsf2(LB + L_LS + 256 + (lane & 31) * 8);
      if (lane < 32) __builtin_amdgcn_raw_buffer_store_b128((u32x4){tagA, f2u(sadd(c0[0], c1[0])), tagB, f2u(sadd(c0[1], c1[1]))}, wsr, WS_CS + (jm * 16 + r) * 512 + lane * 16, 0, 16);
    }
    barrier();
#pragma unroll 1
    for (int c = 1; c < 3; ++c) { if (!ASKIP || mlp) chunk(none); barrier(); }
    chunk(none);
    asm volatile("" : "+v"(dv[0]), "+v"(dv[1]), "+v"(dv[2]), "+v"(dv[3]), "+v"(bv[0]), "+v"(bv[1]), "+v"(bv[2]), "+v"(bv[3]));
    __builtin_amdgcn_s_waitcnt(0x0F70);
    u32x2 xv[2][4];
#pragma unroll
    for (int m = 0; m < 2; ++m)
#pragma unroll
      for (int n = 0; n < 4; ++n) xv[m][n] = *(LAS u32x2*)(size_t)(HB + 3 * SLOT + 12288 + (16 * m + i16) * 128 + (g & 1) * 8 + (((2 * n + (g >> 1)) ^ (i16 >> 1)) * 16));
    barrier();
#pragma unroll 1
    for (int c = 4; c < 6; ++c) { if (!ASKIP || mlp) chunk(none); barrier(); }
    f32x2 st[4];
#pragma unroll
    for (int m = 0; m < 4; ++m) st[m] = (f32x2){ldsf1(sta + m * 32), ldsf1(sta + m * 32 + 8)};
    unsigned ra[4], ho = mlp ? WS_H + (unsigned)(2 * r + d) * H_MT + (unsigned)(4 * jm + 3) * 4096 + lane * 16 : 0xffff0000u;
    f32x2 rs[4], nm[4];
    chunk([&](int k) {
      if (k < 4) { rs[k] = bco(st[k][0]); nm[k] = bco(-st[k][1]); asm volatile("" : "+v"(rs[k]), "+v"(nm[k])); }
      if (k < 8) return;
#pragma unroll
      for (int n = 0; n < 4; ++n) ra[n] = home + r2l + ((n ^ r2x) * 32);
      asm volatile("" : "+v"(ra[0]), "+v"(ra[1]), "+v"(ra[2]), "+v"(ra[3]), "+v"(ho));
    });
    TRACE(14); barrier(); TRACE(15);
    STAMP(4);
    const f32x4 cs0 = ldsf4(csa), cs1 = ldsf4(csa + 64);
    const u32x2 b10 = *(LAS u32x2*)(size_t)(LB + L_B1 + 192 + g * 8), b11 = *(LAS u32x2*)(size_t)(LB + L_B1 + 224 + g * 8);
    Col col;
    auto take = [&](int i, int pg) {
#pragma unroll
      for (int n = 0; n < 4; ++n) we[i][n] = ldstr(ra[n] + pg * 4096, ra[n] + pg * 4096 + 2048);
    };
    chunk([&](int k) {
      if (k == 2) { mkcol(col, cs0, cs1, b10, b11); asm volatile("" : "+v"(col.bias[0]), "+v"(col.bias[1]), "+v"(col.bias[2]), "+v"(col.bias[3])); }
      if (k >= 3 && k < 6) take(k - 3, k - 3);
    });
    barrier();
    __builtin_amdgcn_s_setprio(2);
    STAMP(5);
    u32x4 oa[4];
#pragma unroll
    for (int m = 0; m < 4; ++m) {
      TRACE2(1 + 2 * m);
      oa[m] = gelu8(acc[0][m], acc[1][m], col, rs[m], nm[m]);
      TRACE2(2 + 2 * m);
      __builtin_amdgcn_raw_buffer_store_b128(oa[m], wsr, ho + m * 1024, 0, HST);
      if (m >= 1) asm volatile("" :: "v"(oa[m - 1]));
      if (m == 0) { take(3, 3); take(4, 4); }
      SCHED();
    }
    STAMP(6);
    prog_wait(PW + (6 + d) * 4, 2);
#pragma unroll
    for (int i = 5; i < 8; ++i) take(i, i - 5);
    u32x4 s2v[2];
#pragma unroll
    for (int m = 0; m < 2; ++m) s2v[m] = *(LAS u32x4*)(size_t)(LB + L_ST2 + (tok + 8 * (mtile & 1) + 4 * d + 2 * m) * 16);
    setup2(8);
    wait_lgkm0();
    asm volatile("" :: "v"(oa[3]));
    prog_set(PW + w * 4, 1);
    snap(false);
#pragma unroll
    for (int m = 0; m < 2; ++m) {
      const u32x4 s2 = s2v[m];
      const f32x2 r2 = bco(u2f(s2.y)), n2 = bco(-u2f(s2.w));
#pragma unroll
      for (int n = 0; n < 4; ++n) {
        f32x2 o[2];
#pragma unroll
        for (int h = 0; h < 2; ++h) {
          const f32x2 xx = {bflo(xv[m][n][h]), bfhi(xv[m][n][h])};
          o[h] = pfma(pfma(xx, r2, n2), (f32x2){bflo(dv[n][h]), bfhi(dv[n][h])}, xx + (f32x2){bflo(bv[n][h]), bfhi(bv[n][h])});
        }
        acc2[n][m] = __builtin_shufflevector(o[0], o[1], 0, 1, 2, 3);
        acc2[n][2 + m] = (f32x4){0, 0, 0, 0};
        asm volatile("" : "+v"(acc2[n][m]), "+v"(acc2[n][2 + m]));
      }
    }
    pubo = mlp && lane == 0 ? WS_FLAG + (2 * r + d) * 2048 + flag_of(jm, 3) : pubo;
    seen();
    STAMP(8);
  };

  auto phase2 = [&](auto nm_) __attribute__((always_inline)) {
    STAMP(9);
    constexpr int NM = decltype(nm_)::value, S0 = NM == 8 ? 0 : 5, N = NM == 8 ? 13 : 9;
    const u64 tag = ((u64)tagB << 32) | tagA;
    auto has = [&](int k) { return fl[k < NM ? k : k - NM + S0] == tag; };
    auto need = [&](int k) {
      if (__builtin_expect(!has(k), 0)) {
#pragma unroll 1
        for (int spin = 1; spin < SPIN_CAP; ++spin) {
          COUNT(20);
          snap(k >= NM);
          if (has(k)) break;
          if (!(spin & 31)) {
            const u32x2 f = __builtin_amdgcn_raw_buffer_load_b64(wsr, 0, k < NM ? fm + 8 * k : fs + 8 * (k - NM + S0), 16);
            if (__builtin_amdgcn_readfirstlane(f.x) == tagA && __builtin_amdgcn_readfirstlane(f.y) == tagB) break;
          }
          __builtin_amdgcn_s_sleep(PSLEEP);
        }
      }
      asm volatile("" ::: "memory");
    };
    auto publish = [&]() { __builtin_amdgcn_raw_buffer_store_b64((u32x2){tagA, tagB}, wsr, pubo, 0, 16); };
    auto loadH = [&](int i, int m) -> u32x4 {
      if (m == 0) asm volatile("" : "+v"(hb));
      if ((NOC & 8) && (m & 1)) { u32x4 z; asm volatile("" : "=v"(z)); return z; }
      return __builtin_amdgcn_raw_buffer_load_b128(wsr, hb + (m & 1) * 1024, __builtin_amdgcn_readlane(ksl, i) ^ ((m & 2) * 1024), HAUX);
    };
    u32x4 hf[N][4];
    auto load4 = [&](int i) {
#pragma unroll
      for (int m = 0; m < 4; ++m) hf[i][m] = loadH(i, m);
    };
    STAMP(10);
    if (NM == 8) {
      if (__builtin_expect(!(has(0) && has(1)), 0)) { wait_vm<0>(); publish(); pubo = 0xffff0000u; }
      need(0); load4(0);
      need(1); load4(1);
      wait_vm<8>(); publish();
      __builtin_amdgcn_s_setprio(EPRI);
    } else {
      unsigned vc[8];
      mkvc(vc);
      asm volatile("" : "+v"(vc[0]), "+v"(vc[1]), "+v"(vc[2]), "+v"(vc[3]), "+v"(vc[4]), "+v"(vc[5]), "+v"(vc[6]), "+v"(vc[7]));
      const unsigned o = ntile * 65536 + kg * 128, hs = fresh(HB + (w < 4 ? SLOT : 0));
      __builtin_amdgcn_s_setprio(0);
      dma_x8(vc, p.cre + o, hs);
      wait_vm<8>(); publish();
      snap(false);
      dma_x8(vc, p.cim + o, hs + 8192);
      seen();
      need(0); load4(0);
      need(1); load4(1);
    }
    STAMP(11);
    s16x8 cfr[N - NM][4];
    auto rdc = [&](int q, unsigned base) {
#pragma unroll
      for (int n = 0; n < 4; ++n) {
        unsigned a = base + n * 2048;
        asm volatile("" : "+v"(a));
        cfr[q][n] = cat(*(LAS s16x4*)(size_t)a, *(LAS s16x4*)(size_t)(a + 8192));
      }
    };
    SCHED();
#pragma unroll
    for (int i = 0; i < N; ++i) {
      const int k = i + (i < NM - 2 ? 2 : SDEP);
      if (i == 2) STAMP(12);
      if (!(i & 1) || k >= NM) TRACE4(2 * i + 1);
      if (i == NM - 2) {
        STAMP(13);
#pragma unroll
        for (int e = NM; e < NM + SDEP - 2; ++e) { need(e); load4(e); }
      }
      if (k < NM) {
        if (NM == 6 && i == 0) snap(false);
        else if (NM == 6 && i == 2) { need(2); load4(2); need(3); load4(3); }
        if (!(i & 1) && (NM == 8 || i)) { need(k); need(k + 1); TRACE4(2 * i + 2); if (NM == 6 && i == 2) absorb(5, home + 16384); if (!(ESEP && i == 0)) snap(k + 2 == NM); }
      } else if (k < N) { need(k); TRACE4(2 * i + 2); }
      if (i == NM - 2) STAMP(14);
      if (NM == 8 && i == 1) prog_set(PW + w * 4, 4);
      if (NM == 8 && i == 6) prog_wait(PW + (w | 6) * 4, 4);
      if (NM == 6 && i == 1) prog_set(PW + w * 4, 1);
      if (NM == 8 && i == EOFF) __builtin_amdgcn_s_setprio(0);
      if (NM == 8 && i == PSTEP) { prog_wait(PW + (w ^ 4) * 4, 1); rdc(4, crd - 2 * SLOT); }
      if (NM == 8 && i == PSTEP + 1) { wait_lgkm0(); prog_set(PW + w * 4, 5); }
      if (i >= NM - 1 && i < NM + (NM == 8 ? 3 : 2)) rdc(i + 1 - NM, crd ^ (32 * (i + 1 - NM + (NM == 8 ? 0 : 1))));
      SCHED();
#pragma unroll
      for (int m = 0; m < 4; ++m) {
#pragma unroll
        for (int n = 0; n < 4; ++n)
          if (NM == 8 && i == 0) asm volatile("v_mfma_f32_16x16x32_bf16 %0, %1, %2, %0" : "+v"(acc2[n][m]) : "v"(we[0][n]), "v"(hf[0][m]));
          else acc2[n][m] = mfma(i < NM ? we[i < NM ? i : 0][n] : cfr[i < NM ? 0 : i - NM][n], __builtin_bit_cast(s16x8, hf[i][m]), NM == 6 && i == 0 ? (f32x4){0, 0, 0, 0} : acc2[n][m]);
        SCHED();
        if (k < N && (NM == 8 || i > 1)) hf[k < N ? k : 0][m] = loadH(k, m);
        SCHED();
      }
      if (ESEP && NM == 8 && i < 2) TRACE4(3 + i);
      if (ESEP && NM == 8 && i == 1) snap(false);
      if ((i & 1) && k < NM) seen();
    }
    TRACE4(30);
    if (NM == 6) prog_wait(PW + (w ^ 4) * 4, 5);
  };
  if (__builtin_expect(w >= 6 || w < 2, 1)) {
    if (__builtin_expect(w >= 6, 1)) roleD(); else roleA();
    phase2(std::integral_constant<int, 8>{});
  } else {
    if (w >= 4) roleL(); else roleB();
    phase2(std::integral_constant<int, 6>{});
  }
  STAMP(15);
  int tl = threadIdx.x;
  asm volatile("" : "+v"(tl));
  const int i16t = tl & 15, gt = (tl >> 4) & 3;
#pragma unroll
  for (int m = 0; m < 4; ++m)
#pragma unroll
    for (int n = 0; n < 4; ++n) *(LAS f32x4*)(size_t)(home + ((16 * m + i16t) ^ (w == 1 ? 32 : 0)) * 256 + (((4 * n + gt) ^ i16t) * 16)) = acc2[n][m];
  const int ck = tl & 15;
  const int orow = 32 * ((tl >> 6) & 3) + 16 * ((tl >> 4) & 1) + ((tl >> 5) & 1) + 2 * (tl >> 8) + 8 * (mtile & 1);
  char* op = p.out + (128 * (mtile >> 1) + orow) * 2048 + ntile * 256 + ck * 16;
  unsigned ra[4];
#pragma unroll
  for (int k = 0; k < 4; ++k) ra[k] = LB + (tl >> 4) * 256 + ((ck ^ ((tl >> 4) & 15)) * 16) + k * SLOT;
  asm volatile("" : "+v"(ra[0]), "+v"(ra[1]), "+v"(ra[2]), "+v"(ra[3]), "+v"(op));
  barrier();
  STAMP(16);
  {
    f32x4 pt[2][8];
#pragma unroll
    for (int h = 0; h < 2; ++h)
#pragma unroll
      for (int k = 0; k < 8; ++k) pt[h][k] = ldsf4(ra[k >> 1] + (k & 1) * HALF + h * 8192);
#pragma unroll
    for (int h = 0; h < 2; ++h) {
      const f32x4 s = ((pt[h][0] + pt[h][1]) + (pt[h][2] + pt[h][3])) + ((pt[h][4] + pt[h][5]) + (pt[h][6] + pt[h][7]));
      __builtin_nontemporal_store(s, (GAS f32x4*)(op + h * 8192));
    }
  }
  if (b == 255 && tl == 0) __builtin_amdgcn_raw_buffer_store_b64((u32x2){tagA, tagB}, wsr, WS_EPOCH, 0, 16);
}

extern "C" void kernel_launch(void* const* d_in, const int* in_sizes, int n_in, void* d_out, int out_size, void* d_ws,
                              size_t ws_size, hipStream_t stream) {
  Params p;
  p.x = (const char*)d_in[0]; p.lre = (const char*)d_in[1]; p.lim = (const char*)d_in[2];
  p.bre = (const char*)d_in[3]; p.bim = (const char*)d_in[4]; p.cre = (const char*)d_in[5]; p.cim = (const char*)d_in[6];
  p.logdt = (const char*)d_in[7]; p.dsk = (const char*)d_in[8];
  p.w1 = (const char*)d_in[10]; p.b1 = (const char*)d_in[11]; p.w2 = (const char*)d_in[12]; p.b2 = (const char*)d_in[13];
  p.out = (char*)d_out; p.ws = (char*)d_ws;
  fused_kernel<<<256, 512, 0, stream>>>(p);
}
```

```cpp
#include <hip/hip_runtime.h>
#include <stdint.h>
#include <type_traits>

typedef short s16x4 __attribute__((ext_vector_type(4)));
typedef short s16x8 __attribute__((ext_vector_type(8)));
typedef __bf16 bf16x8 __attribute__((ext_vector_type(8)));
typedef __bf16 bf16x2 __attribute__((ext_vector_type(2)));
typedef float f32x4 __attribute__((ext_vector_type(4)));
typedef float f32x2 __attribute__((ext_vector_type(2)));
typedef unsigned u32x4 __attribute__((ext_vector_type(4)));
typedef unsigned u32x2 __attribute__((ext_vector_type(2)));
typedef int i32x4 __attribute__((ext_vector_type(4)));
typedef unsigned long long u64;
typedef u64 u64x4 __attribute__((ext_vector_type(4)));
typedef u64 u64x8 __attribute__((ext_vector_type(8)));
typedef int i32x8 __attribute__((ext_vector_type(8)));
typedef int i32x16 __attribute__((ext_vector_type(16)));
__device__ i32x8 sbl8(i32x4, int, int) __asm("llvm.amdgcn.s.buffer.load.v8i32");
__device__ i32x16 sbl16(i32x4, int, int) __asm("llvm.amdgcn.s.buffer.load.v16i32");
#define LAS __attribute__((address_space(3)))
#define GAS __attribute__((address_space(1)))
#define DI __device__ __forceinline__

#define STAMPS 0
#define TRACING 0
#define GATHER_AT 4
#define ROT 1
#define TOUCH 7
#define TLBW 0
#define NOFF 0
#define PSLEEP 4
#define BFLAG 2
#define ABAR 0
#define KROT 0
#define LPRIO 3
#define CHK 0
#define BSPLIT 8
#define APRIO 0
#define ADELAY 0
#define BFLAG0 1
#define DEPE 2
#define DEPL 2
#define LB_SLEEP 8
#define CHECK_AT 5
#define CPF 1
#define HAUX 16
#define LSNAP 1
#define BSNAP 1
#define L2T 0
#define L2C 0
#define SDEP 2
#define YPRE 1
#define KEEP 2
#define LSEP 0
#define PSTEP 9
#define NOC 0
#define ESEP 0
#define HST 16
#define EPRI 1
#define EOFF 6
#define WOFF 4
#define CPW 5
#define TST 1
#define TPP 3
#define S3AT 3
#define JM 0
#define ASKIP 1
#define APUB 3
#define LPUB 1
#define NOPRE 0
#define RAMP1 0
#define RAMP2 0
#define RAMP3 0
#define MAP 1

struct Params {
  const char *x, *lre, *lim, *bre, *bim, *cre, *cim, *logdt, *dsk, *w1, *b1, *w2, *b2;
  char *out, *ws;
};

constexpr unsigned WS_EPOCH = 0;
constexpr unsigned WS_STAT = 4096;
constexpr unsigned WS_TOT = WS_STAT + 2048 * 16;
constexpr unsigned WS_CS = WS_TOT + 16 * 2 * 16 * 256;
constexpr unsigned WS_BS = WS_CS + 16 * 16 * 512;
constexpr unsigned WS_FLAG = WS_BS + 16 * 1024;
constexpr unsigned WS_H = WS_FLAG + 32 * 2048;
constexpr unsigned H_MT = 88 * 4096;
constexpr unsigned WS_END = WS_H + 32 * H_MT;
constexpr unsigned WS_STAMP = 32u << 20;
constexpr unsigned WS_B2B = WS_STAMP + (4u << 20);
constexpr unsigned SLOT = 40960, HALF = 20480, SL_W = 16384, SL_B = 32768;
constexpr unsigned L_ST1 = 3 * SLOT + 16384, L_ST2 = L_ST1 + 2048;
constexpr unsigned L_CS = 3 * SLOT + HALF + 16384, L_FLG = L_CS + 512, L_B1 = L_CS + 768, L_BS = L_CS + 1024, L_LS = L_CS + 2048, L_DUMMY = L_CS + 3328;
constexpr unsigned LDS_BYTES = 163840;
constexpr int SPIN_CAP = 40000;
constexpr unsigned CODE_BYTES = 45056;

DI void dma4w(unsigned voff, const char* sbase, unsigned ldsaddr) {
  asm volatile("s_mov_b32 m0, %2\n\ts_nop 0\n\tglobal_load_lds_dword %0, %1" ::"v"(voff), "s"(sbase), "s"(ldsaddr));
}
DI void dma_x4(const unsigned* v, const char* sbase, unsigned ldsaddr, bool low = false) {
  if (low) {
    asm volatile("s_mov_b32 m0, %5\n\ts_setprio 0\n\t"
               "global_load_lds_dwordx4 %0, %4\n\tglobal_load_lds_dwordx4 %1, %4 offset:1024\n\t"
               "global_load_lds_dwordx4 %2, %4 offset:2048\n\tglobal_load_lds_dwordx4 %3, %4 offset:3072\n\ts_setprio 3"
               ::"v"(v[0]), "v"(v[1]), "v"(v[2]), "v"(v[3]), "s"(sbase), "s"(ldsaddr));
    return;
  }
  asm volatile("s_mov_b32 m0, %5\n\ts_nop 0\n\t"
               "global_load_lds_dwordx4 %0, %4\n\tglobal_load_lds_dwordx4 %1, %4 offset:1024\n\t"
               "global_load_lds_dwordx4 %2, %4 offset:2048\n\tglobal_load_lds_dwordx4 %3, %4 offset:3072"
               ::"v"(v[0]), "v"(v[1]), "v"(v[2]), "v"(v[3]), "s"(sbase), "s"(ldsaddr));
}
DI void dma_x8(const unsigned* v, const char* sbase, unsigned ldsaddr) {
  asm volatile("s_mov_b32 m0, %9\n\ts_nop 0\n\t"
               "global_load_lds_dwordx4 %0, %8 offset:-4096\n\tglobal_load_lds_dwordx4 %1, %8 offset:-3072\n\t"
               "global_load_lds_dwordx4 %2, %8 offset:-2048\n\tglobal_load_lds_dwordx4 %3, %8 offset:-1024\n\t"
               "global_load_lds_dwordx4 %4, %8\n\tglobal_load_lds_dwordx4 %5, %8 offset:1024\n\t"
               "global_load_lds_dwordx4 %6, %8 offset:2048\n\tglobal_load_lds_dwordx4 %7, %8 offset:3072"
               ::"v"(v[0]), "v"(v[1]), "v"(v[2]), "v"(v[3]), "v"(v[4]), "v"(v[5]), "v"(v[6]), "v"(v[7]), "s"(sbase), "s"(ldsaddr + 4096u));
}
constexpr int PG(int i) { return i & 3; }
template <int N> DI void wait_vm() { asm volatile("s_waitcnt vmcnt(%0)" ::"i"(N) : "memory"); }
DI void wait_lgkm0() { asm volatile("" ::: "memory"); __builtin_amdgcn_s_waitcnt(0xC07F); asm volatile("" ::: "memory"); }
DI void barrier() {
  asm volatile("" ::: "memory");
  __builtin_amdgcn_s_waitcnt(0xC07F);
  __builtin_amdgcn_s_barrier();
  asm volatile("" ::: "memory");
}
#define SCHED() __builtin_amdgcn_sched_barrier(0)
DI s16x8 lds128(unsigned a) { return *(LAS s16x8*)(size_t)a; }
DI f32x4 ldsf4(unsigned a) { return *(LAS f32x4*)(size_t)a; }
DI f32x2 ldsf2(unsigned a) { return *(LAS f32x2*)(size_t)a; }
DI float ldsf1(unsigned a) { return *(LAS float*)(size_t)a; }
DI s16x4 ldstr4(unsigned a) { return __builtin_amdgcn_ds_read_tr16_b64_v4i16((LAS s16x4*)(size_t)a); }
DI s16x8 cat(s16x4 lo, s16x4 hi) { return __builtin_shufflevector(lo, hi, 0, 1, 2, 3, 4, 5, 6, 7); }
DI s16x8 ldstr(unsigned a0, unsigned a1) { return cat(ldstr4(a0), ldstr4(a1)); }
DI f32x4 mfma(s16x8 a, s16x8 b, f32x4 c) {
  return __builtin_amdgcn_mfma_f32_16x16x32_bf16(__builtin_bit_cast(bf16x8, a), __builtin_bit_cast(bf16x8, b), c, 0, 0, 0);
}
DI unsigned pk2(float a, float b) { f32x2 v = {a, b}; return __builtin_bit_cast(unsigned, __builtin_convertvector(v, bf16x2)); }
DI float u2f(unsigned u) { return __builtin_bit_cast(float, u); }
DI unsigned f2u(float u) { return __builtin_bit_cast(unsigned, u); }
DI float bflo(unsigned u) { return __builtin_bit_cast(float, u << 16); }
DI float bfhi(unsigned u) { return __builtin_bit_cast(float, u & 0xffff0000u); }
template <int CTRL> DI float dpp_add(float v) {
  return v + __builtin_bit_cast(float, __builtin_amdgcn_update_dpp(0, __builtin_bit_cast(int, v), CTRL, 0xf, 0xf, true));
}
DI float row16_sum(float v) {
  v = dpp_add<0xB1>(v);
  v = dpp_add<0x4E>(v);
  v = dpp_add<0x141>(v);
  v = dpp_add<0x140>(v);
  return v;
}
DI float smul(float a, float b) { float d; asm("v_mul_f32 %0, %1, %2" : "=v"(d) : "v"(a), "v"(b)); return d; }
DI float sadd(float a, float b) { float d; asm("v_add_f32 %0, %1, %2" : "=v"(d) : "v"(a), "v"(b)); return d; }
DI float sfma(float a, float b, float c) { float d; asm("v_fma_f32 %0, %1, %2, %3" : "=v"(d) : "v"(a), "v"(b), "v"(c)); return d; }
DI float sfms(float a, float b, float c) { float d; asm("v_fma_f32 %0, %1, %2, -%3" : "=v"(d) : "v"(a), "v"(b), "v"(c)); return d; }
DI void zero4(f32x4& v) {
  float a, b, c, d;
  asm volatile("v_mov_b32 %0, 0\n\tv_mov_b32 %1, 0\n\tv_mov_b32 %2, 0\n\tv_mov_b32 %3, 0" : "=v"(a), "=v"(b), "=v"(c), "=v"(d));
  v = (f32x4){a, b, c, d};
}
DI u32x2 swap32(float a, float b) { asm volatile("s_nop 1" : "+v"(a), "+v"(b)); return __builtin_amdgcn_permlane32_swap(f2u(a), f2u(b), false, false); }
DI u32x2 swap16(float a, float b) { asm volatile("s_nop 1" : "+v"(a), "+v"(b)); return __builtin_amdgcn_permlane16_swap(f2u(a), f2u(b), false, false); }
DI f32x2 pfma(f32x2 a, f32x2 b, f32x2 c) { return __builtin_elementwise_fma(a, b, c); }
DI f32x2 bc(float a) { return (f32x2){a, a}; }
DI f32x2 bco(float a) { f32x2 v = {a, a}; asm("" : "+v"(v)); return v; }
DI f32x2 cmul(f32x2 a, f32x2 b) { return (f32x2){sfms(a[0], b[0], smul(a[1], b[1])), sfma(a[0], b[1], smul(a[1], b[0]))}; }
DI f32x2 cfma(f32x2 a, f32x2 b, f32x2 c) { return (f32x2){sfms(a[0], b[0], sfms(a[1], b[1], c[0])), sfma(a[0], b[1], sfma(a[1], b[0], c[1]))}; }
DI f32x2 csel(bool p, f32x2 a, f32x2 b) { return (f32x2){p ? a[0] : b[0], p ? a[1] : b[1]}; }
DI void prog_set(unsigned a, unsigned v) {
  asm volatile("s_mov_b64 exec, 1\n\tds_write_b32 %0, %1\n\ts_mov_b64 exec, -1" ::"v"(a), "v"(v) : "memory");
}
DI void prog_wait(unsigned a, unsigned v) {
#pragma unroll 1
  for (int spin = 0; spin < SPIN_CAP * 16; ++spin) {
    if (__builtin_amdgcn_readfirstlane(*(volatile LAS unsigned*)(size_t)a) >= v) break;
    __builtin_amdgcn_s_sleep(1);
  }
  asm volatile("" ::: "memory");
}
DI void bar() { asm volatile("" ::: "memory"); __builtin_amdgcn_s_barrier(); asm volatile("" ::: "memory"); }
DI void dma1_dev(unsigned voff, const char* sbase, unsigned ldsaddr) {
  asm volatile("s_mov_b32 m0, %2\n\ts_nop 0\n\tglobal_load_lds_dwordx4 %0, %1 sc1" ::"v"(voff), "s"(sbase), "s"(ldsaddr) : "memory");
}
DI void dma2_dev(unsigned voff, const char* s0, const char* s1, unsigned ldsaddr) {
  asm volatile("s_mov_b32 m0, %3\n\ts_nop 0\n\tglobal_load_lds_dwordx4 %0, %1 sc1\n\tglobal_load_lds_dwordx4 %0, %2 offset:2048 sc1" ::"v"(voff), "s"(s0), "s"(s1), "s"(ldsaddr) : "memory");
}
DI void dma4_dev(unsigned voff, const char* s0, const char* s1, unsigned ldsaddr) {
  asm volatile("s_mov_b32 m0, %3\n\ts_nop 0\n\tglobal_load_lds_dwordx4 %0, %1 sc1\n\tglobal_load_lds_dwordx4 %0, %1 offset:1024 sc1\n\t"
               "global_load_lds_dwordx4 %0, %2 offset:2048 sc1\n\tglobal_load_lds_dwordx4 %0, %2 offset:3072 sc1" ::"v"(voff), "s"(s0), "s"(s1), "s"(ldsaddr) : "memory");
}
DI int kstep_of(int slot, int i) {
  const int q = slot + 4 * i + (slot >= 4 ? 28 : 0), cls = (q >= 14) + (q >= 28) + (q >= 42);
  return 4 * (q - 14 * cls) + ((0x9C >> (2 * cls)) & 3);
}
DI unsigned flag_of(int j, int c) {
  const int q = 14 * ((0x78 >> (2 * c)) & 3) + j;
  return q < 32 ? (q & 3) * 128 + (q >> 2) * 8 : 512 + (q & 3) * 128 + ((q - 32) >> 2) * 8;
}
DI void cnt_add(unsigned a) {
  asm volatile("s_mov_b64 exec, 1\n\tds_add_u32 %0, %1\n\ts_mov_b64 exec, -1" ::"v"(a), "v"(1u) : "memory");
}
DI f32x2 lo2(f32x4 v) { return __builtin_shufflevector(v, v, 0, 1); }
DI f32x2 hi2(f32x4 v) { return __builtin_shufflevector(v, v, 2, 3); }
DI void rows4(float v, float* o) {
  asm volatile("s_nop 1" : "+v"(v));
  const unsigned u = f2u(v);
  const u32x2 h = __builtin_amdgcn_permlane32_swap(u, u, false, false);
  const u32x2 a = __builtin_amdgcn_permlane16_swap(h.x, h.x, false, false), b = __builtin_amdgcn_permlane16_swap(h.y, h.y, false, false);
  o[0] = u2f(a.x); o[1] = u2f(a.y); o[2] = u2f(b.x); o[3] = u2f(b.y);
}
struct Col { f32x2 cs[4], bias[4]; };
DI void mkcol(Col& c, f32x4 cs0, f32x4 cs1, u32x2 b0, u32x2 b1) {
  c.cs[0] = lo2(cs0); c.cs[1] = hi2(cs0); c.cs[2] = lo2(cs1); c.cs[3] = hi2(cs1);
  c.bias[0] = (f32x2){bflo(b0.x), bfhi(b0.x)}; c.bias[1] = (f32x2){bflo(b0.y), bfhi(b0.y)};
  c.bias[2] = (f32x2){bflo(b1.x), bfhi(b1.x)}; c.bias[3] = (f32x2){bflo(b1.y), bfhi(b1.y)};
}
DI u32x4 gelu8(f32x4 a0, f32x4 a1, const Col& c, f32x2 rs, f32x2 nmrs) {
  f32x2 v[4], z[4];
  u32x4 o;
#define ST4(stmt) _Pragma("unroll") for (int h = 0; h < 4; ++h) { stmt; } SCHED();
  ST4(v[h] = pfma(c.cs[h], nmrs, c.bias[h]))
  ST4(v[h] = pfma(h == 0 ? lo2(a0) : h == 1 ? hi2(a0) : h == 2 ? lo2(a1) : hi2(a1), rs, v[h]))
  ST4(z[h] = v[h] * v[h])
  ST4(z[h] = pfma(z[h], bc(-0.10294324f), bc(-2.30220820f)))
  ST4(z[h] = z[h] * v[h])
  ST4((z[h] = (f32x2){__builtin_amdgcn_exp2f(z[h][0]), __builtin_amdgcn_exp2f(z[h][1])}))
  ST4(z[h] = z[h] + bc(1.f))
  ST4((z[h] = (f32x2){__builtin_amdgcn_rcpf(z[h][0]), __builtin_amdgcn_rcpf(z[h][1])}))
  ST4(v[h] = v[h] * z[h])
  ST4(o[h] = pk2(v[h][0], v[h][1]))
#undef ST4
  return o;
}

#define TRACE(i) do {} while (0)
#define TRACE2(i) do {} while (0)
#define TRACE3(i) do {} while (0)
#define TRACE4(i) do {} while (0)
#define STAMP(k) do {} while (0)
#define STAMPC(k) do {} while (0)
#define COUNT(k) do {} while (0)

__global__ void __launch_bounds__(512, 2) fused_kernel(Params p) {
  __shared__ __attribute__((aligned(16))) char lds_mem[LDS_BYTES];
  unsigned long long pc;
  asm volatile("s_getpc_b64 %0" : "=s"(pc));
  const unsigned LB = (unsigned)(size_t)(LAS char*)lds_mem;
  const int tid = threadIdx.x, lane = tid & 63;
  const int w = (__builtin_amdgcn_readfirstlane(tid >> 6) + WOFF) & 7;
  if (w == CPW) {
    const unsigned co = ((unsigned)(lane & 15) * 32u + (blockIdx.x >> 3)) * 128u;
    dma4w(co < CODE_BYTES ? co : 0u, (const char*)(pc & ~255ull), LB + L_DUMMY + 512);
  }
  const int b = blockIdx.x, idx = b >> 3, xcd = b & 7;
  const int r = 8 * (xcd >> 2) + (idx >> 2), j = 4 * (xcd & 3) + (idx & 3);
  const int rot = (2 * (xcd & 3) + 4 * (xcd >> 2)) & 7;
  const int mtile = 4 * xcd + (idx >> 3), ntile = (idx + NOFF) & 7;
  const int i16 = lane & 15, g = lane >> 4, qq = i16 >> 2, pp = i16 & 3;
  const int d = w & 1;
  const int jm = j < 14 ? j : 13, lq = j & 1;
  const bool mlp = j < 14;
  u32x4 pre[4];
  unsigned v_lre, v_lim, v_ldt;
  if ((w >> 1) == 2) {
    const unsigned po = (unsigned)(32 * j + 16 * d + i16) * 2u;
    v_lre = *(const GAS unsigned short*)(p.lre + po); v_lim = *(const GAS unsigned short*)(p.lim + po); v_ldt = *(const GAS unsigned short*)(p.logdt + po);
    asm volatile("" ::: "memory");
  }
  u32x4 prb[2];
  if (w < 4) {
    if (NOPRE > 1) __builtin_amdgcn_s_sleep(NOPRE);
    const bool isb = w >= 2;
    const char* pb = isb ? p.w1 + (64 * (r & 7) + 32 * d) * 3584 + jm * 256 + (r >> 3) * 128 : p.x + (128 * r + 8 * j + 4 * d) * 1024;
    const unsigned po = isb ? (lane >> 3) * 3584 + (lane & 7) * 16 : g * 1024 + i16 * 16, ps = isb ? 28672 : 256;
#pragma unroll
    for (int i = 0; i < 4; ++i) pre[i] = *(const GAS u32x4*)(pb + i * ps + po);
    if (isb) {
#pragma unroll
      for (int i = 0; i < 2; ++i) prb[i] = *(const GAS u32x4*)((r & 8 ? p.bim : p.bre) + (32 * j + 4 * (r & 7) + 2 * d) * 1024 + i * 512 + (lane >> 5) * 1024 + (lane & 31) * 16);
    }
    asm volatile("" ::: "memory");
  }
  const int kgo = 2 * (idx >> 3), late = ((w + 2) >> 2) & 1, kg = 2 * (((w & 2) + d + KROT * ntile) & 3) + late;
  auto slot_of = [&](int role) { return (((role + 1) & 2) << 1) + (((((role + 1) & 2) ? 0 : kgo) + ((role & 1) ? 0 : 2) + d) & 3); };
  const int slot = slot_of(w >> 1);
  const u32x2 ep = *(const __attribute__((address_space(4))) u32x2*)(p.ws + WS_EPOCH);
  unsigned tagA, tagB;
  auto mktag = [&]() { tagA = ep.x; tagB = ep.y; asm volatile("" : "+s"(tagA), "+s"(tagB)); tagA += 1u; tagB += 1u; };
  const __amdgpu_buffer_rsrc_t wsr = __builtin_amdgcn_make_buffer_rsrc(p.ws, 0, WS_END, 0x00020000);
  const i32x4 wss = {(int)(size_t)p.ws, (int)((size_t)p.ws >> 32) & 0xffff, (int)WS_END, 0x00020000};
  auto fresh = [](int o) { asm volatile("" : "+s"(o) :: "memory"); return o; };
  const unsigned HB = LB + d * HALF;
  const unsigned PW = LB + L_FLG;
  s16x8 we[8][4];
  f32x4 acc2[4][4];
  const unsigned home = HB + ((2 - (w >> 1)) & 3) * SLOT;
  const unsigned r2l = (4 * g + qq) * 128 + pp * 8, r2x = (2 * g + (qq >> 1)) & 3;
  auto absorb = [&](int i, unsigned page) {
#pragma unroll
    for (int n = 0; n < 4; ++n) { const unsigned a = page + r2l + ((n ^ r2x) * 32); we[i][n] = ldstr(a, a + 2048); }
  };
  auto mkvc = [&](unsigned* vc) {
#pragma unroll
    for (int i = 0; i < 8; ++i) vc[i] = (lane >> 3) * 1024 + (((lane & 7) ^ (4 * (i & 1) + g)) * 16) + 4096 + i * 7168;
  };
  auto mkv2 = [&](unsigned* v2) {
#pragma unroll
    for (int i = 0; i < 4; ++i) v2[i] = (lane >> 3) * 1024 + (((((lane & 7) >> 1) ^ g) << 1) | (lane & 1)) * 16 + i * 7168;
  };
  auto loadC = [&](const unsigned* vc, int kgc, unsigned dst) {
    const unsigned o = ntile * 65536 + kgc * 128;
    dma_x8(vc, p.cre + o, dst);
    dma_x8(vc, p.cim + o, dst + 8192);
  };
  const char* w2b = p.w2 + ntile * 128;
  auto adv = [&](unsigned& a, int dl) { a += dl; asm volatile("" : "+v"(a)); };
  const int tok = 32 * qq + 16 * (i16 & 1) + ((i16 >> 1) & 1);
  const unsigned kx = (g ^ (i16 >> 1)) * 16;
  const unsigned awb = LB + SL_W + (8 * g + qq) * 256 + pp * 8, awx = qq | ((g & 1) << 2);
  auto sum32 = [&](const u32x4* v) {
    float s0 = 0.f, s1 = 0.f;
#pragma unroll
    for (int i = 0; i < 4; ++i)
#pragma unroll
      for (int e = 0; e < 4; ++e) { s0 += bflo(v[i][e]); s1 += bfhi(v[i][e]); }
    return row16_sum(s0 + s1);
  };
  auto touch = [&]() {
    dma4w(d ? (lane & 15) * 128 : (lane < 56 ? lane : 0) * 128, d ? p.cre + b * 2048 : p.w2 + b * 7168, LB + L_DUMMY + d * 256);
    if (d) dma4w((lane & 15) * 128, p.cim + b * 2048, LB + L_DUMMY + d * 256);
  };
  auto zero2 = [&]() {
#pragma unroll
    for (int n = 0; n < 4; ++n) {
#pragma unroll
      for (int m = 0; m < 4; ++m) acc2[n][m] = (f32x4){0, 0, 0, 0};
      asm volatile("" : "+v"(acc2[n][0]), "+v"(acc2[n][1]), "+v"(acc2[n][2]), "+v"(acc2[n][3]));
    }
  };
  int ksl;
  unsigned hb, crd, pubo = 0xffff0000u;
  const int fm = WS_FLAG + mtile * 2048 + slot * 128, fs = WS_FLAG + mtile * 2048 + 1024 + (kg >> 1) * 64;
  u64x8 fl;
  auto snap = [&](bool ssm) { fl = __builtin_bit_cast(u64x8, sbl16(wss, fresh(ssm ? fs : fm), 1)); SCHED(); };
  auto seen = [&]() { SCHED(); asm volatile("" : "+s"(fl)); };
  auto setup2 = [&](int nm) {
    ksl = (lane < nm ? kstep_of(slot, lane) : 56 + 4 * kg + late + ((lane - nm) & 7)) * 4096;
    if (NOC & 16) ksl = (w * 11 + (lane & 1)) * 4096;
    ksl += w == 1 ? 2048 : 0;
    hb = WS_H + (unsigned)mtile * H_MT + lane * 16;
    crd = home + i16 * 128 + (g & 1) * 8 + (((g >> 1) ^ (i16 >> 1)) * 16);
    asm volatile("" : "+v"(ksl), "+v"(hb), "+v"(crd));
  };

  auto roleD = [&]() __attribute__((always_inline)) {
    __builtin_amdgcn_s_waitcnt(0x0F70);
    __builtin_amdgcn_s_setprio(3);
    unsigned vx[8], vw[8], vb[4], v2[4];
#pragma unroll
    for (int i = 0; i < 8; ++i) vw[i] = g * 3584 + ((((i16 >> 1) ^ (g | (((i >> 1) & 1) << 2))) << 1) | (i16 & 1)) * 16 + 4096 + i * (14336 - 1024);
    const char* wb = p.w1 + jm * 256 + d * (32 * 3584);
    auto part1 = [&](int c, unsigned slot) { if (mlp) dma_x8(vw, wb + ((c + rot) & 7) * (64 * 3584), LB + slot + SL_W + d * 8192); };
    asm volatile("" :: "s"(wb));
    STAMP(18);
    part1(0, 0);
#pragma unroll
    for (int i = 0; i < 8; ++i)
      vx[i] = (32 * (g >> 1) + 16 * ((lane >> 3) & 1) + (g & 1)) * 1024 + (((lane & 7) ^ (4 * (i & 1) + g)) * 16) + 4096 + (i & 1) * 65536 + (i >> 1) * 2048 - i * 1024;
#pragma unroll
    for (int i = 0; i < 4; ++i) vb[i] = (lane >> 3) * 1024 + (((lane & 7) ^ (4 * (i & 1) + g)) * 16) + i * 7168;
    const char* xb = p.x + (r * 128 + d * 8) * 1024;
    const char* bb = (d ? p.bim : p.bre) + j * 32768;
    auto partx = [&](int c, unsigned slot) { dma_x8(vx, xb + ((c + rot) & 7) * 128, LB + slot + d * 8192); };
    auto partb = [&](int c, unsigned slot) { dma_x4(vb, bb + ((c + rot) & 7) * 128, LB + slot + SL_B + d * 4096); };
    auto part0 = [&](int c, unsigned slot) { partx(c, slot); partb(c, slot); };
    mkv2(v2);
    unsigned ra[4];
#pragma unroll
    for (int n = 0; n < 4; ++n) ra[n] = home + r2l + ((n ^ r2x) * 32);
    int kt = kstep_of(slot_of((lane >> 3) & 3), lane & 7) * 32768;
    asm volatile("" : "+v"(kt), "+v"(ra[0]), "+v"(ra[1]), "+v"(ra[2]), "+v"(ra[3]), "+v"(v2[0]), "+v"(v2[1]), "+v"(v2[2]), "+v"(v2[3]));
    bool low = false;
    auto page = [&](int role, int i, int grp, int pg) { if ((NOC & 4) && low) return; dma_x4(v2, w2b + (unsigned)__builtin_amdgcn_readlane(kt, 8 * role + i), HB + grp * SLOT + pg * 4096, low); };
    auto take = [&](int i) {
      SCHED();
#pragma unroll
      for (int n = 0; n < 4; ++n) we[i][n] = ldstr(ra[n] + PG(i) * 4096, ra[n] + PG(i) * 4096 + 2048);
      SCHED();
    };
    auto own = [&](int i) { page(3, i, 3, PG(i)); };
    unsigned l16 = lane * 16, la = home + 12288 + lane * 16, lb = LB + L_ST1 + d * 1024 + lane * 16, lc = LB + L_BS + d * 512 + (lane & 31) * 16;
    unsigned wa = LB + L_CS + d * 256 + (lane & 31) * 8 + (lane >> 5) * 4, wb2 = LB + L_LS + (2 * d + (lane >> 5)) * 288 + i16 * 16 + (g & 1) * 4;
    asm volatile("" : "+v"(l16), "+v"(la), "+v"(lb), "+v"(lc), "+v"(wa), "+v"(wb2));
    auto gather = [&](bool bs) {
      const char* cp = p.ws + WS_CS + jm * 8192 + d * 4096;
      dma4_dev(l16, cp, cp, home + 12288);
      dma2_dev(l16, p.ws + WS_STAT + r * 2048 + d * 1024, p.ws + WS_STAT + (mtile >> 1) * 2048 + d * 1024 - 2048, LB + L_ST1 + d * 1024);
      if (bs) dma1_dev(l16, p.ws + WS_BS + j * 1024, LB + L_BS);
    };
    auto check = [&](auto between) -> bool {
      auto ld16 = [](unsigned a) { return *(volatile LAS u32x4*)(size_t)a; };
      auto bad = [&](unsigned a, u32x4 v) { return __builtin_amdgcn_bitop3_b32(v.z, tagB, __builtin_amdgcn_bitop3_b32(v.x, tagA, a, 0xBE), 0xBE); };
      const u32x4 c0 = ld16(la), c1 = ld16(la + 1024), c2 = ld16(la + 2048), c3 = ld16(la + 3072), s1 = ld16(lb), s2 = ld16(lb + 2048), s3 = ld16(lc);
      SCHED(); between(); SCHED();
      const u32x2 h = swap32(sadd(sadd(u2f(c0.y), u2f(c1.y)), sadd(u2f(c2.y), u2f(c3.y))), sadd(sadd(u2f(c0.w), u2f(c1.w)), sadd(u2f(c2.w), u2f(c3.w))));
      *(LAS float*)(size_t)wa = sadd(u2f(h.x), u2f(h.y));
      *(LAS unsigned*)(size_t)wb2 = s1.y;
      *(LAS unsigned*)(size_t)(wb2 + 8) = s1.w;
      const unsigned e = bad(bad(bad(bad(bad(bad(bad(0u, c0), c1), c2), c3) & (mlp ? ~0u : 0u), s1), s2), s3);
      return __builtin_amdgcn_ballot_w64(e != 0) == 0;
    };
    const unsigned me = PW + w * 4;
    zero2();
    setup2(8);
    auto landed = [&]() { if (mlp) wait_vm<20>(); else wait_vm<12>(); };
    SCHED();
    part0(0, 0);
    if (TOUCH == 4) touch();
    part1(1, SLOT); part0(1, SLOT);
    STAMP(1);
    mktag();
    if (mlp) wait_vm<20 + (TOUCH == 4)>(); else wait_vm<12 + (TOUCH == 4)>();
    STAMP(19);
    bar();
    STAMP(2);
    part1(2, 2 * SLOT); part0(2, 2 * SLOT); landed(); if (TOUCH == 1) touch();
    if (RAMP1) __builtin_amdgcn_s_sleep(RAMP1);
    bar();
    if (d) dma4w(lane * 4, p.b1 + jm * 256, LB + L_B1);
    part1(3, 0); part0(3, 0); landed();
    unsigned vt = (lane < 56 ? lane : 0) * 128;
    asm volatile("" : "+v"(vt));
    auto l2t = [&](int t) { if (t < L2T) dma4w(vt, p.w2 + (t * 64 + 2 * idx + d) * 7168, LB + L_DUMMY + d * 256); };
    auto l2c = [&](int t) { if (t < L2C) dma4w(l16 * 8, (t ? p.cim : p.cre) + (2 * idx + d) * 8192, LB + L_DUMMY + d * 256); };
    auto s3 = [&]() { if (L2T < 4) dma4w((lane < 28 ? lane : 0) * 1024, p.w2 + (2 * idx + d) * 28672 + ((3u - ((unsigned)(size_t)p.w2 >> 7)) & 7u) * 128, LB + L_DUMMY + d * 256); };
    if (S3AT == 1) s3();
    l2t(0);
    if (RAMP2) __builtin_amdgcn_s_sleep(RAMP2);
    bar();
    STAMPC(21);
    dma_x4(vx, p.x + (128 * (mtile >> 1) + 8 * (mtile & 1) + 4 * d) * 1024 + ntile * 128 - 4096, home + 12288);
    part1(4, SLOT); part0(4, SLOT); landed(); if (S3AT == 2) s3();
    if (RAMP3) __builtin_amdgcn_s_sleep(RAMP3);
    bar();
    own(0); part1(5, 2 * SLOT); part0(5, 2 * SLOT); landed(); if (S3AT == 3) s3();
    bar();
    gather(d); part1(6, 0); take(0); part0(6, 0); landed(); if (S3AT == 4) s3();
    bar();
    part1(7, SLOT);
    const bool ok = check([&]() { partx(7, SLOT); });
    SCHED();
    partb(7, SLOT);
    if (__builtin_expect(!ok, 0)) {
#pragma unroll 1
      for (int spin = 0; spin < SPIN_CAP; ++spin) {
        COUNT(20);
        __builtin_amdgcn_s_sleep(10);
        gather(true);
        wait_vm<0>();
        if (check([]() {})) break;
      }
    }
    landed();
    barrier();
    STAMP(3); STAMPC(22);
    page(0, 0, 2, 0); page(0, 1, 2, 1); page(0, 2, 2, 2); page(0, 3, 2, 3); page(0, 4, 2, 4); own(1);
    wait_vm<12>();
    bar();
    STAMP(4);
    own(2); page(2, 0, 0, 0); page(2, 1, 0, 1); page(2, 2, 0, 2); page(2, 3, 0, 3); page(2, 4, 0, 4);
    wait_vm<20>();
    STAMP(5);
    bar();
    TRACE4(31);
    low = true;
    STAMP(6);
    take(1); take(2);
    page(0, 5, 2, 0); page(0, 6, 2, 1); page(0, 7, 2, 2);
    wait_vm<12>(); prog_set(me, 1);
    own(3); own(4); wait_lgkm0(); own(5); own(6);
    wait_vm<16>(); prog_set(me, 2);
    page(1, 0, 1, 0); page(1, 1, 1, 1); page(1, 2, 1, 2);
    wait_vm<16>(); take(3); take(4); take(5); wait_lgkm0();
    own(7);
    page(1, 3, 1, 3); page(1, 4, 1, 4);
    auto c8 = [&](const char* src, unsigned dst) { if (NOC & 1) return; dma_x4(vb, src, dst, true); dma_x4(vb, src + 32768, dst + 4096, true); };
    const unsigned co = ntile * 65536 + kg * 128;
    c8(p.cre + co, home);
    wait_vm<8>();
    prog_set(me, 3);
    take(6); take(7);
    prog_wait(PW + d * 4, 1);
    c8(p.cim + co, home + 8192);
    STAMP(7);
    snap(false);
    c8(p.cre + (co ^ 512), HB + 2 * SLOT); c8(p.cim + (co ^ 512), HB + 2 * SLOT + 8192);
    seen();
    STAMP(8);
  };
  auto roleL = [&]() __attribute__((always_inline)) {
    if (TOUCH == 0) touch();
    if (TLBW && d) { dma4w(0, p.cre + ntile * 65536, LB + L_DUMMY + 256); dma4w(0, p.cim + ntile * 65536, LB + L_DUMMY + 256); dma4w((lane & 1) * 0x1bf000, p.w2, LB + L_DUMMY + 256); }
    f32x4 acr[8], aci[8];
#pragma unroll
    for (int m = 0; m < 8; ++m) { acr[m] = (f32x4){0, 0, 0, 0}; aci[m] = (f32x4){0, 0, 0, 0}; asm volatile("" : "+v"(acr[m]), "+v"(aci[m])); }
    unsigned ax[2], ab[2];
    ax[0] = LB + i16 * 128 + kx; ax[1] = ax[0] ^ 64;
    ab[0] = LB + SL_B + (16 * d + i16) * 128 + kx; ab[1] = ab[0] ^ 64;
    unsigned sa = LB + L_LS + g * 288, csa = LB + L_BS + (16 * d + i16) * 16 + 4;
    asm volatile("" : "+v"(sa), "+v"(csa), "+v"(ax[0]), "+v"(ax[1]), "+v"(ab[0]), "+v"(ab[1]));
    s16x8 xf[2][8], bf[2][2];
    auto rd = [&](int set, int i) {
      if (i < 2) bf[set][i] = lds128(ab[set] + i * 4096);
      else xf[set][i - 2] = lds128(ax[set] + (i - 2) * 2048);
    };
    auto mm = [&](int set, int i) {
      if (i & 1) aci[i >> 1] = mfma(xf[set][i >> 1], bf[set][1], aci[i >> 1]);
      else acr[i >> 1] = mfma(xf[set][i >> 1], bf[set][0], acr[i >> 1]);
    };
    asm volatile("" : "+v"(v_lre), "+v"(v_lim), "+v"(v_ldt));
    const float lr = bflo(v_lre), li = bflo(v_lim);
    const float dt = bflo(pk2(expf(bflo(v_ldt)), 0.f));
    const float ang = smul(dt, li);
    const float rv = smul(ang, 0.15915494f);
    const float rev = (rv - __builtin_rintf(rv)) + sfma(ang, 6.4206382e-9f, sfms(ang, 0.15915494f, rv));
    float sn = __builtin_amdgcn_sinf(rev), cn = __builtin_amdgcn_cosf(rev);
    asm volatile("s_nop 0" : "+v"(sn), "+v"(cn));
    const float ex = expf(smul(dt, lr));
    f32x2 a1 = {smul(ex, cn), smul(ex, sn)};
    const float nr = a1[0] - 1.f, ni = a1[1], iden = 2.f / sfma(lr, lr, smul(li, li));
    f32x2 kk = {smul(sfma(nr, lr, smul(ni, li)), iden), smul(sfms(ni, lr, smul(nr, li)), iden)};
    f32x2 t = cmul(a1, a1); t = cmul(t, t); t = cmul(t, t);
    f32x2 a16 = cmul(t, t);
    f32x2 a32 = cmul(a16, a16);
    const f32x2 a64 = cmul(a32, a32), a128 = cmul(a64, a64), a256 = cmul(a128, a128);
    f32x2 a512 = cmul(a256, a256);
    const f32x2 one = {1.f, 0.f};
    f32x2 aseg = cmul(kk, cmul(csel(g & 1, a32, one), csel(g & 2, a64, one)));
    const int e4 = (r - 1 - g) & 3;
    f32x2 acar = cmul(csel(e4 & 1, a128, one), csel(e4 & 2, a256, one));
    const f32x2 ar = bco(a1[0]), ai = bco(a1[1]);
    asm volatile("" :: "v"(ar), "v"(ai));
    asm volatile("" : "+v"(kk), "+v"(a16), "+v"(a32), "+v"(a512), "+v"(aseg), "+v"(acar));
    const unsigned on = (qq == g) ? ((pp & 1) ? 0xffff0000u : 0xffffu) : 0u;
    unsigned m0 = (pp & 2) ? 0u : on, m1 = (pp & 2) ? on : 0u;
    auto scan = [&](f32x2 h0, f32x2 h1, const f32x2* ur, const f32x2* ui, auto out) {
      const u32x4 z0 = {m0 & 0x3f803f80u, m1 & 0x3f803f80u, 0u, 0u}, z1 = {0u, 0u, z0.x, z0.y};
      const f32x4 zz = {0, 0, 0, 0};
      f32x2 hr = {h0[0], h1[0]}, hi = {-h0[1], -h1[1]};
      u32x4 fr[4], fi[4];
      f32x4 tr[2], ti[2];
      SCHED();
#pragma unroll
      for (int mp = 0; mp < 5; ++mp) {
        if (mp > 0) {
#pragma unroll
          for (int k = 0; k < 2; ++k) {
            tr[k] = mfma(__builtin_bit_cast(s16x8, fr[mp - 1]), __builtin_bit_cast(s16x8, k ? z1 : z0), zz);
            ti[k] = mfma(__builtin_bit_cast(s16x8, fi[mp - 1]), __builtin_bit_cast(s16x8, k ? z1 : z0), zz);
          }
          SCHED();
        }
        if (mp < 4) {
#pragma unroll
          for (int e = 0; e < 4; ++e) {
            const int s = 4 * mp + e;
            const f32x2 t1 = ur ? pfma(hr, -ai, ui[s]) : hr * -ai; SCHED();
            const f32x2 t0 = ur ? pfma(hi, ai, ur[s]) : hi * ai; SCHED();
            hi = pfma(hi, ar, t1); SCHED();
            hr = pfma(hr, ar, t0); SCHED();
            fi[mp][e] = pk2(hi[0], hi[1]); SCHED();
            fr[mp][e] = pk2(hr[0], hr[1]); SCHED();
          }
        }
        if (mp > 0) { out(2 * mp - 2, tr[0], ti[0]); out(2 * mp - 1, tr[1], ti[1]); SCHED(); }
      }
    };
    auto pack = [](f32x4 a, f32x4 b) { return (u32x4){pk2(a[0], a[1]), pk2(a[2], a[3]), pk2(b[0], b[1]), pk2(b[2], b[3])}; };
    u32x4 yb[8];
    auto resp = [&]() { scan(aseg, cmul(a16, aseg), nullptr, nullptr, [&](int q, f32x4 a, f32x4 b) { yb[q] = pack(a, b); asm volatile("" : "+v"(yb[q])); }); };
    if (YPRE) resp();
    mktag();
    __builtin_amdgcn_s_waitcnt(0x0F70);
    __builtin_amdgcn_s_setprio(1);
    STAMP(1);
    bar();
    STAMP(2); TRACE3(1);
#pragma unroll
    for (int i = 0; i < 10; ++i) rd(0, i);
    wait_lgkm0();
    auto stage1 = [&](int dl) {
      SCHED();
#pragma unroll
      for (int i = 0; i < 16; ++i) {
        mm(0, i); SCHED();
        if (i < 10) rd(1, i);
        if (i == 11) { adv(ax[0], dl); adv(ab[0], dl); }
        SCHED();
      }
    };
    stage1(SLOT);
    barrier();
    TRACE3(2);
    int ph = 0;
#pragma unroll 1
    for (int c = 2; c <= 8; ++c) {
      const int dl = ph == 2 ? -2 * (int)SLOT : (int)SLOT;
      ph = ph == 2 ? 0 : ph + 1;
      SCHED();
#pragma unroll
      for (int i = 0; i < 16; ++i) {
        mm(1, i); SCHED();
        if (i < 10) rd(0, i);
        if (i == 11) { adv(ax[1], dl); adv(ab[1], dl); }
        SCHED();
      }
      wait_lgkm0();
      stage1(ph == 2 ? -2 * (int)SLOT : (int)SLOT);
      TRACE(2 * c);
      barrier();
      TRACE(2 * c + 1); TRACE3(c + 1);
    }
    __builtin_amdgcn_s_setprio(LPRIO);
    STAMP(4);
    f32x2 rs[16], mrs[16];
    float csr, csi;
#pragma unroll
    for (int i = 0; i < 16; ++i) {
      mm(1, i); SCHED();
      if (i == 0) { csr = ldsf1(csa); csi = ldsf1(csa + 512); }
      if (i < 8) { const f32x4 t = ldsf4(sa + i * 16); rs[i] = lo2(t); mrs[i] = hi2(t); }
      SCHED();
    }
    const f32x2 ncr = bco(-csr), nci = bco(-csi);
    STAMP(5);
    f32x2 ur[16], ui[16], Er, Ei;
    SCHED();
#pragma unroll
    for (int s = 0; s < 16; ++s) {
      if (s < 8) { const f32x4 t = ldsf4(sa + (s + 8) * 16); rs[s + 8] = lo2(t); mrs[s + 8] = hi2(t); SCHED(); }
      ur[s] = mrs[s] * ncr; SCHED();
      ui[s] = mrs[s] * nci; SCHED();
      ur[s] = pfma((s & 1) ? hi2(acr[s >> 1]) : lo2(acr[s >> 1]), rs[s], ur[s]); SCHED();
      ui[s] = pfma((s & 1) ? hi2(aci[s >> 1]) : lo2(aci[s >> 1]), rs[s], ui[s]); SCHED();
      if (s == 0) { Er = ur[0]; Ei = ui[0]; }
      else {
        const f32x2 t0 = pfma(Ei, -ai, ur[s]); SCHED();
        const f32x2 t1 = pfma(Er, ai, ui[s]); SCHED();
        Er = pfma(Er, ar, t0); SCHED();
        Ei = pfma(Ei, ar, t1); SCHED();
      }
    }
    TRACE2(1);
    const f32x2 E0 = {Er[0], Ei[0]};
    const f32x2 E = cfma(a16, E0, (f32x2){Er[1], Ei[1]});
    f32x2 P = {0.f, 0.f}, Tt = {0.f, 0.f};
    float er4[4], ei4[4];
    rows4(E[0], er4); rows4(E[1], ei4);
#pragma unroll
    for (int s = 0; s < 4; ++s) {
      const f32x2 e = {er4[s], ei4[s]};
      P = csel(s == g, Tt, P);
      Tt = s ? cfma(a32, Tt, e) : e;
    }
    const unsigned totb = WS_TOT + (unsigned)(j * 2 + d) * 4096;
    if (g == 0) __builtin_amdgcn_raw_buffer_store_b128((u32x4){tagA, f2u(Tt[0]), tagB, f2u(Tt[1])}, wsr, totb + r * 256 + i16 * 16, 0, 16);
    STAMP(6);
    TRACE2(2);
    {
      const f32x2 kr = bco(kk[0]), nki = bco(-kk[1]);
#pragma unroll
      for (int s = 0; s < 16; ++s) {
        const f32x2 t0 = ur[s] * kr; SCHED();
        const f32x2 t1 = ur[s] * nki; SCHED();
        ur[s] = pfma(ui[s], nki, t0); SCHED();
        ui[s] = pfma(ui[s], -kr, t1); SCHED();
      }
    }
#pragma unroll
    for (int s = 0; s < 16; s += 4) asm volatile("" : "+v"(ur[s]), "+v"(ui[s]), "+v"(ur[s + 1]), "+v"(ui[s + 1]), "+v"(ur[s + 2]), "+v"(ui[s + 2]), "+v"(ur[s + 3]), "+v"(ui[s + 3]));
    f32x4 hr4[8], hi4[8];
    {
      const f32x2 kP = cmul(kk, P);
      scan(kP, cfma(a16, kP, cmul(kk, E0)), ur, ui, [&](int q, f32x4 a, f32x4 b) { hr4[q] = a; hi4[q] = b; asm volatile("" : "+v"(hr4[q]), "+v"(hi4[q])); });
    }
    TRACE2(3);
    prog_wait(PW + (6 + d) * 4, 1);
#pragma unroll
    for (int i = 0; i < 5; ++i) absorb(i, home + i * 4096);
    unsigned v2[4], vt[4];
    mkv2(v2);
#pragma unroll
    for (int i = 0; i < 4; ++i) { vt[i] = (4 * i + g < r ? g * 256 : -i * 1024) + i16 * 16 + 4096; asm volatile("" : "+v"(vt[i]), "+v"(v2[i])); }
    {
      const i32x4 tr = {wss[0], wss[1], (int)(totb + r * 256), wss[3]};
#pragma unroll 1
      for (int spin = 0; spin < 64; ++spin) {
        int n, t[15];
        asm volatile("s_buffer_load_dword %1, %16, %17 offset:0 glc\n\ts_buffer_load_dword %2, %16, %17 offset:256 glc\n\ts_buffer_load_dword %3, %16, %17 offset:512 glc\n\ts_buffer_load_dword %4, %16, %17 offset:768 glc\n\ts_buffer_load_dword %5, %16, %17 offset:1024 glc\n\ts_buffer_load_dword %6, %16, %17 offset:1280 glc\n\ts_buffer_load_dword %7, %16, %17 offset:1536 glc\n\ts_buffer_load_dword %8, %16, %17 offset:1792 glc\n\ts_buffer_load_dword %9, %16, %17 offset:2048 glc\n\ts_buffer_load_dword %10, %16, %17 offset:2304 glc\n\ts_buffer_load_dword %11, %16, %17 offset:2560 glc\n\ts_buffer_load_dword %12, %16, %17 offset:2816 glc\n\ts_buffer_load_dword %13, %16, %17 offset:3072 glc\n\ts_buffer_load_dword %14, %16, %17 offset:3328 glc\n\ts_buffer_load_dword %15, %16, %17 offset:3584 glc\n\t"
                     "s_mov_b32 %0, 0\n\ts_waitcnt lgkmcnt(0)\n\t"
                     "s_cmp_eq_u32 %1, %18\n\ts_addc_u32 %0, %0, 0\n\ts_cmp_eq_u32 %2, %18\n\ts_addc_u32 %0, %0, 0\n\ts_cmp_eq_u32 %3, %18\n\ts_addc_u32 %0, %0, 0\n\ts_cmp_eq_u32 %4, %18\n\ts_addc_u32 %0, %0, 0\n\ts_cmp_eq_u32 %5, %18\n\ts_addc_u32 %0, %0, 0\n\ts_cmp_eq_u32 %6, %18\n\ts_addc_u32 %0, %0, 0\n\ts_cmp_eq_u32 %7, %18\n\ts_addc_u32 %0, %0, 0\n\ts_cmp_eq_u32 %8, %18\n\ts_addc_u32 %0, %0, 0\n\ts_cmp_eq_u32 %9, %18\n\ts_addc_u32 %0, %0, 0\n\ts_cmp_eq_u32 %10, %18\n\ts_addc_u32 %0, %0, 0\n\ts_cmp_eq_u32 %11, %18\n\ts_addc_u32 %0, %0, 0\n\ts_cmp_eq_u32 %12, %18\n\ts_addc_u32 %0, %0, 0\n\ts_cmp_eq_u32 %13, %18\n\ts_addc_u32 %0, %0, 0\n\ts_cmp_eq_u32 %14, %18\n\ts_addc_u32 %0, %0, 0\n\ts_cmp_eq_u32 %15, %18\n\ts_addc_u32 %0, %0, 0"
                     : "=&s"(n), "=&s"(t[0]), "=&s"(t[1]), "=&s"(t[2]), "=&s"(t[3]), "=&s"(t[4]), "=&s"(t[5]), "=&s"(t[6]), "=&s"(t[7]), "=&s"(t[8]), "=&s"(t[9]), "=&s"(t[10]), "=&s"(t[11]), "=&s"(t[12]), "=&s"(t[13]), "=&s"(t[14]) : "s"(tr), "s"(totb), "s"(tagA) : "scc", "memory");
        if (n >= r) break;
        COUNT(20);
      }
    }
    STAMP(7);
    auto fetch = [&]() {
      asm volatile("s_mov_b32 m0, %5\n\ts_nop 0\n\tglobal_load_lds_dwordx4 %0, %4 sc1\n\tglobal_load_lds_dwordx4 %1, %4 offset:1024 sc1\n\t"
                   "global_load_lds_dwordx4 %2, %4 offset:2048 sc1\n\tglobal_load_lds_dwordx4 %3, %4 offset:3072 sc1"
                   ::"v"(vt[0]), "v"(vt[1]), "v"(vt[2]), "v"(vt[3]), "s"(p.ws + totb - 4096), "s"(home + 16384) : "memory");
    };
    wait_lgkm0();
    if (r > 0) fetch();
    TRACE2(14);
    if (!YPRE) resp();
    f32x2 wt[4];
#pragma unroll
    for (int i = 3; i >= 0; --i) {
      const bool v = 4 * i + g < r;
      wt[i] = csel(v, acar, (f32x2){0.f, 0.f});
      if (i) acar = csel(v, cmul(acar, a512), acar);
      asm volatile("" : "+v"(wt[i]));
    }
    setup2(6);
    unsigned ho = WS_H + (unsigned)(2 * r) * H_MT + (unsigned)(56 + 2 * j + d) * 4096 + lane * 16, ta = home + 16384 + lane * 16;
    asm volatile("" : "+v"(ho), "+v"(ta), "+v"(m0), "+v"(m1));
    TRACE2(4);
    wait_vm<0>();
    TRACE2(15);
    float cr = 0.f, ci = 0.f;
    if (r > 0) {
#pragma unroll 1
      for (int spin = 0; spin < SPIN_CAP; ++spin) {
        u32x4 tv[4];
        unsigned e = 0;
#pragma unroll
        for (int i = 0; i < 4; ++i) tv[i] = *(volatile LAS u32x4*)(size_t)(ta + i * 1024);
        f32x2 c[2];
#pragma unroll
        for (int i = 0; i < 4; ++i) {
          const f32x2 t = {u2f(tv[i].y), u2f(tv[i].w)};
          c[i & 1] = i < 2 ? cmul(wt[i], t) : cfma(wt[i], t, c[i & 1]);
          e = __builtin_amdgcn_bitop3_b32(tv[i].z, tagB, __builtin_amdgcn_bitop3_b32(tv[i].x, tagA, e, 0xBE), 0xBE);
        }
        const u32x2 h = swap32(sadd(c[0][0], c[1][0]), sadd(c[0][1], c[1][1]));
        const float x = sadd(u2f(h.x), u2f(h.y));
        const u32x2 h2 = swap16(x, x);
        const float y = sadd(u2f(h2.x), u2f(h2.y));
        const u32x2 h3 = swap32(y, y);
        cr = u2f(h3.x); ci = u2f(h3.y);
        if (__builtin_expect(__builtin_amdgcn_ballot_w64(e != 0) == 0, 1)) break;
        COUNT(20);
        __builtin_amdgcn_s_sleep(8);
        fetch();
        wait_vm<0>();
      }
    }
    STAMP(8);
    {
      const unsigned c0 = pk2(cr, cr), c1 = pk2(ci, ci), c2 = pk2(-ci, -ci);
      const s16x8 are = __builtin_bit_cast(s16x8, (u32x4){m0 & c0, m1 & c0, m0 & c1, m1 & c1}), aim = __builtin_bit_cast(s16x8, (u32x4){m0 & c2, m1 & c2, m0 & c0, m1 & c0});
      u32x4 o[8];
      SCHED();
#pragma unroll
      for (int q = 0; q < 10; ++q) {
        if (q < 8) {
          hr4[q] = mfma(are, __builtin_bit_cast(s16x8, yb[q]), hr4[q]);
          hi4[q] = mfma(aim, __builtin_bit_cast(s16x8, yb[q]), hi4[q]);
          SCHED();
        }
        if (q >= 2) { o[q - 2] = pack(hr4[q - 2], hi4[q - 2]); __builtin_amdgcn_raw_buffer_store_b128(o[q - 2], wsr, ho + ((q - 2) & 3) * 1024, ((q - 2) >> 2) * H_MT, HST); }
        SCHED();
      }
      TRACE2(5);
      dma_x4(v2, w2b + kstep_of(slot, 5) * 32768, home + 16384);
      asm volatile("" :: "v"(o[0]), "v"(o[1]), "v"(o[2]), "v"(o[3]), "v"(o[4]), "v"(o[5]), "v"(o[6]), "v"(o[7]), "v"(ho));
      STAMP(19);
      pubo = lane < 2 ? WS_FLAG + (2 * r + lane) * 2048 + 1024 + (2 * j + d) * 8 : pubo;
    }
    STAMP(18);
  };
  auto roleB = [&]() __attribute__((always_inline)) {
    mktag();
    {
      float s0 = 0.f, s1 = 0.f;
#pragma unroll
      for (int e = 0; e < 8; ++e) { s0 = sadd(s0, bflo(prb[e >> 2][e & 3])); s1 = sadd(s1, bfhi(prb[e >> 2][e & 3])); }
      *(LAS float*)(size_t)(LB + L_LS + 512 + (4 * d + g) * 4) = row16_sum(s0 + s1);
      __builtin_amdgcn_s_waitcnt(0x0F70);
    }
    if (mlp) {
      f32x4 acc[6][4];
#pragma unroll
      for (int s = 0; s < 6; ++s) {
#pragma unroll
        for (int m = 0; m < 4; ++m) acc[s][m] = (f32x4){0, 0, 0, 0};
        asm volatile("" : "+v"(acc[s][0]), "+v"(acc[s][1]), "+v"(acc[s][2]), "+v"(acc[s][3]));
      }
      unsigned ax[2], aw[6];
      ax[0] = LB + (64 * d + i16) * 128 + kx; ax[1] = ax[0] ^ 64;
#pragma unroll
      for (int s = 0; s < 6; ++s) aw[s] = awb + ((s ^ awx) * 32);
      unsigned sta = LB + L_LS + qq * 288 + (8 * d + ((i16 >> 1) & 1)) * 16 + (i16 & 1) * 4, csa = LB + L_CS + g * 16;
      asm volatile("" : "+v"(ax[0]), "+v"(ax[1]), "+v"(aw[0]), "+v"(aw[1]), "+v"(aw[2]), "+v"(aw[3]), "+v"(aw[4]), "+v"(aw[5]), "+v"(sta), "+v"(csa));
      s16x8 xf[2][4];
      s16x4 wf[2][12];
      auto rd = [&](int set, int i) {
        if (i < 4) xf[set][i] = lds128(ax[set] + i * 2048);
        else wf[set][i - 4] = ldstr4(aw[(i - 4) >> 1] + set * 8192 + (i & 1) * 1024);
      };
      auto mm = [&](int set, int i) { const int s = i >> 2, m = i & 3; acc[s][m] = mfma(cat(wf[set][2 * s], wf[set][2 * s + 1]), xf[set][m], acc[s][m]); };
      {
        float t[8];
#pragma unroll
        for (int e = 0; e < 8; ++e)
          t[e] = (e & 1) ? sadd(sadd(bfhi(pre[0][e >> 1]), bfhi(pre[1][e >> 1])), sadd(bfhi(pre[2][e >> 1]), bfhi(pre[3][e >> 1])))
                         : sadd(sadd(bflo(pre[0][e >> 1]), bflo(pre[1][e >> 1])), sadd(bflo(pre[2][e >> 1]), bflo(pre[3][e >> 1])));
#pragma unroll
        for (int e = 0; e < 4; ++e) { const u32x2 h = swap32(t[e], t[e + 4]); t[e] = sadd(u2f(h.x), u2f(h.y)); }
#pragma unroll
        for (int e = 0; e < 2; ++e) { const u32x2 h = swap16(t[e], t[e + 2]); t[e] = dpp_add<0x128>(sadd(u2f(h.x), u2f(h.y))); }
        *(LAS f32x2*)(size_t)(LB + L_LS + d * 256 + (4 * (lane & 7) + g) * 8) = (f32x2){t[0], t[1]};
      }
      __builtin_amdgcn_s_setprio(1);
      STAMP(1);
      barrier();
      STAMP(2); STAMPC(21);
#pragma unroll
      for (int i = 0; i < 16; ++i) rd(0, i);
      wait_lgkm0();
      auto stage1 = [&](int c, int dl) {
        SCHED();
#pragma unroll
        for (int i = 0; i < 24; ++i) {
          mm(0, i); SCHED();
          if (i < 16) rd(1, i);
          else if (i < 22) adv(aw[i - 16], dl);
          else if (i == 22) adv(ax[0], dl);
          SCHED();
        }
        TRACE(2 * c);
        barrier();
        TRACE(2 * c + 1);
        SCHED();
      };
      int ph = 0;
#pragma unroll 1
      for (int c = 1; c < 8; ++c) {
        const int dl = ph == 2 ? -2 * (int)SLOT : (int)SLOT;
        ph = ph == 2 ? 0 : ph + 1;
        stage1(c, dl);
#pragma unroll
        for (int i = 0; i < 24; ++i) {
          mm(1, i); SCHED();
          if (i < 16) rd(0, i);
          else if (i == 16) adv(ax[1], dl);
          else if (i == 23) wait_lgkm0();
          SCHED();
        }
      }
      stage1(8, 0);
      unsigned b1a = LB + L_B1 + g * 8;
      asm volatile("" : "+v"(b1a));
      STAMP(4); STAMPC(22);
      f32x2 rs[4], nm[4];
      f32x4 cs[6];
      u32x2 b1v[6];
      f32x2 st[4];
      auto colrd = [&](int s) { cs[s] = ldsf4(csa + 64 * s); b1v[s] = *(LAS u32x2*)(size_t)(b1a + 32 * s); };
#pragma unroll
      for (int i = 0; i < 4; ++i) st[i] = (f32x2){ldsf1(sta + i * 32), ldsf1(sta + i * 32 + 8)};
      colrd(0); colrd(1);
      SCHED();
#pragma unroll
      for (int i = 0; i < BSPLIT; ++i) mm(1, i);
      SCHED();
#pragma unroll
      for (int i = 0; i < 4; ++i) { rs[i] = bco(st[i][0]); nm[i] = bco(-st[i][1]); }
      STAMP(5);
      const unsigned fo = WS_FLAG + (2 * r + d) * 2048;
      pubo = lane ? pubo : 0u;
      u32x4 ob[12];
      const unsigned ho = WS_H + (unsigned)(2 * r + d) * H_MT + (unsigned)(4 * jm) * 4096 + lane * 16;
#pragma unroll
      for (int pr = 0; pr < 3; ++pr) {
        Col col;
        mkcol(col, cs[2 * pr], cs[2 * pr + 1], b1v[2 * pr], b1v[2 * pr + 1]);
        if (pr < 2) { colrd(2 * pr + 2); colrd(2 * pr + 3); }
        SCHED();
        if (pr == 1) {
#pragma unroll
          for (int i = BSPLIT; i < 24; ++i) mm(1, i);
          SCHED();
          STAMP(7);
        }
#pragma unroll
        for (int m = 0; m < 4; ++m) {
          ob[4 * pr + m] = gelu8(acc[2 * pr][m], acc[2 * pr + 1][m], col, rs[m], nm[m]);
          __builtin_amdgcn_raw_buffer_store_b128(ob[4 * pr + m], wsr, ho + m * 1024, pr * 4096, HST);
          if (4 * pr + m >= KEEP) asm volatile("" :: "v"(ob[4 * pr + m - KEEP]));
          TRACE2(1 + 4 * pr + m);
          if (pr > 0 && m == (pr == 1 ? BFLAG0 : BFLAG)) {
            if (pr == 1) wait_vm<BFLAG0 + 1>(); else wait_vm<BFLAG + 1>();
            __builtin_amdgcn_raw_buffer_store_b64((u32x2){tagA, tagB}, wsr, pubo, fo + flag_of(jm, pr - 1), 16);
          }
          SCHED();
        }
      }
      pubo |= fo + flag_of(jm, 2);
      STAMP(6);
    } else {
      const int q = 4 * r + 2 * lq + d;
      const unsigned vt = (lane < 56 ? lane : 0) * 128, dm = LB + L_DUMMY + d * 256;
#pragma unroll 1
      for (int c = 0; c < 9; ++c) {
        if (TOUCH == 7) {
          const int t = (c - TST) * TPP;
#pragma unroll
          for (int e = 0; e < TPP; ++e) {
            if (t + e >= 0 && t + e < 4) dma4w(vt, p.w2 + ((t + e) * 64 + q) * 7168, dm);
            else if (t + e == 4 || t + e == 5) dma4w(lane * 128, (t + e == 4 ? p.cre : p.cim) + q * 8192, dm);
          }
        }
        barrier();
      }
    }
    prog_wait(PW + (6 + d) * 4, 3);
#pragma unroll
    for (int i = 0; i < 5; ++i) absorb(i, home + i * 4096);
    unsigned v2[4];
    mkv2(v2);
    setup2(6);
#pragma unroll
    for (int i = 0; i < 4; ++i) asm volatile("" : "+v"(v2[i]));
    wait_lgkm0();
    STAMP(8);
    dma_x4(v2, w2b + kstep_of(slot, 5) * 32768, home + 16384);
  };
  auto roleA = [&]() __attribute__((always_inline)) {
    const int srow = 128 * r + 8 * j + 4 * d + g;
    u32x2 dv[4], bv[4];
#pragma unroll
    for (int n = 0; n < 4; ++n) { dv[n] = *(const GAS u32x2*)(p.dsk + ntile * 128 + n * 32 + g * 8); bv[n] = *(const GAS u32x2*)(p.b2 + ntile * 128 + n * 32 + g * 8); }
    if (lane < 8) *(LAS unsigned*)(size_t)(PW + lane * 4) = 0u;
    f32x4 acc[2][4];
#pragma unroll
    for (int m = 0; m < 4; ++m) { acc[0][m] = (f32x4){0, 0, 0, 0}; acc[1][m] = (f32x4){0, 0, 0, 0}; asm volatile("" : "+v"(acc[0][m]), "+v"(acc[1][m])); }
    unsigned ax0 = LB + (64 * d + i16) * 128 + kx, ax1 = ax0 ^ 64, aw0 = awb + ((6 ^ awx) * 32), aw1 = awb + ((7 ^ awx) * 32);
    unsigned sta = LB + L_LS + qq * 288 + (8 * d + ((i16 >> 1) & 1)) * 16 + (i16 & 1) * 4, csa = LB + L_CS + 384 + g * 16;
    asm volatile("" : "+v"(ax0), "+v"(ax1), "+v"(aw0), "+v"(aw1), "+v"(sta), "+v"(csa));
    int ph = 0;
    auto chunk = [&](auto side) {
      s16x8 xf[2][4], wf[2][2];
      if (ADELAY) __builtin_amdgcn_s_sleep(ADELAY);
#pragma unroll
      for (int k = 0; k < 2; ++k) {
        wf[k][0] = ldstr(aw0 + k * 8192, aw0 + k * 8192 + 1024);
        wf[k][1] = ldstr(aw1 + k * 8192, aw1 + k * 8192 + 1024);
#pragma unroll
        for (int m = 0; m < 4; ++m) xf[k][m] = lds128((k ? ax1 : ax0) + m * 2048);
      }
      SCHED();
      const int dl = ph == 2 ? -2 * (int)SLOT : (int)SLOT;
      ph = ph == 2 ? 0 : ph + 1;
      adv(ax0, dl); adv(ax1, dl); adv(aw0, dl); adv(aw1, dl);
      side(8);
      SCHED();
#pragma unroll
      for (int k = 0; k < 2; ++k)
#pragma unroll
        for (int m = 0; m < 4; ++m) {
          acc[0][m] = mfma(wf[k][0], xf[k][m], acc[0][m]); acc[1][m] = mfma(wf[k][1], xf[k][m], acc[1][m]);
          SCHED();
          side(4 * k + m);
          SCHED();
        }
    };
    auto none = [](int) {};
    mktag();
    asm volatile("" : "+v"(pre[3]));
    STAMP(19);
    {
      const float mean = sum32(pre) * (1.f / 512.f);
      float v0 = 0.f, v1 = 0.f;
#pragma unroll
      for (int i = 0; i < 4; ++i)
#pragma unroll
        for (int e = 0; e < 4; ++e) {
          const float d0 = bflo(pre[i][e]) - mean, d1 = bfhi(pre[i][e]) - mean;
          v0 = sfma(d0, d0, v0); v1 = sfma(d1, d1, v1);
        }
      const float rsd = __builtin_amdgcn_rsqf(row16_sum(v0 + v1) * (1.f / 512.f) + 1e-5f);
      if (i16 == 0) __builtin_amdgcn_raw_buffer_store_b128((u32x4){tagA, f2u(rsd), tagB, f2u(mean * rsd)}, wsr, WS_STAT + srow * 16, 0, 16);
    }
    __builtin_amdgcn_s_setprio(APRIO);
    STAMP(1);
    barrier();
    STAMP(2);
    chunk(none);
    if (d) {
      const f32x2 c0 = ldsf2(LB + L_LS + 512 + (lane & 3) * 8);
      if (lane < 4) __builtin_amdgcn_raw_buffer_store_b128((u32x4){tagA, f2u(sadd(c0[0], c0[1])), tagB, 0u}, wsr, WS_BS + j * 1024 + (4 * r + lane) * 16, 0, 16);
    } else if (mlp) {
      const f32x2 c0 = ldsf2(LB + L_LS + (lane & 31) * 8), c1 = ldsf2(LB + L_LS + 256 + (lane & 31) * 8);
      if (lane < 32) __builtin_amdgcn_raw_buffer_store_b128((u32x4){tagA, f2u(sadd(c0[0], c1[0])), tagB, f2u(sadd(c0[1], c1[1]))}, wsr, WS_CS + (jm * 16 + r) * 512 + lane * 16, 0, 16);
    }
    barrier();
#pragma unroll 1
    for (int c = 1; c < 3; ++c) { if (!ASKIP || mlp) chunk(none); barrier(); }
    chunk(none);
    asm volatile("" : "+v"(dv[0]), "+v"(dv[1]), "+v"(dv[2]), "+v"(dv[3]), "+v"(bv[0]), "+v"(bv[1]), "+v"(bv[2]), "+v"(bv[3]));
    __builtin_amdgcn_s_waitcnt(0x0F70);
    u32x2 xv[2][4];
#pragma unroll
    for (int m = 0; m < 2; ++m)
#pragma unroll
      for (int n = 0; n < 4; ++n) xv[m][n] = *(LAS u32x2*)(size_t)(HB + 3 * SLOT + 12288 + (16 * m + i16) * 128 + (g & 1) * 8 + (((2 * n + (g >> 1)) ^ (i16 >> 1)) * 16));
    barrier();
#pragma unroll 1
    for (int c = 4; c < 6; ++c) { if (!ASKIP || mlp) chunk(none); barrier(); }
    f32x2 st[4];
#pragma unroll
    for (int m = 0; m < 4; ++m) st[m] = (f32x2){ldsf1(sta + m * 32), ldsf1(sta + m * 32 + 8)};
    unsigned ra[4], ho = mlp ? WS_H + (unsigned)(2 * r + d) * H_MT + (unsigned)(4 * jm + 3) * 4096 + lane * 16 : 0xffff0000u;
    f32x2 rs[4], nm[4];
    chunk([&](int k) {
      if (k < 4) { rs[k] = bco(st[k][0]); nm[k] = bco(-st[k][1]); asm volatile("" : "+v"(rs[k]), "+v"(nm[k])); }
      if (k < 8) return;
#pragma unroll
      for (int n = 0; n < 4; ++n) ra[n] = home + r2l + ((n ^ r2x) * 32);
      asm volatile("" : "+v"(ra[0]), "+v"(ra[1]), "+v"(ra[2]), "+v"(ra[3]), "+v"(ho));
    });
    TRACE(14); barrier(); TRACE(15);
    STAMP(4);
    const f32x4 cs0 = ldsf4(csa), cs1 = ldsf4(csa + 64);
    const u32x2 b10 = *(LAS u32x2*)(size_t)(LB + L_B1 + 192 + g * 8), b11 = *(LAS u32x2*)(size_t)(LB + L_B1 + 224 + g * 8);
    Col col;
    auto take = [&](int i, int pg) {
#pragma unroll
      for (int n = 0; n < 4; ++n) we[i][n] = ldstr(ra[n] + pg * 4096, ra[n] + pg * 4096 + 2048);
    };
    chunk([&](int k) {
      if (k == 2) { mkcol(col, cs0, cs1, b10, b11); asm volatile("" : "+v"(col.bias[0]), "+v"(col.bias[1]), "+v"(col.bias[2]), "+v"(col.bias[3])); }
      if (k >= 3 && k < 6) take(k - 3, k - 3);
    });
    barrier();
    __builtin_amdgcn_s_setprio(2);
    STAMP(5);
    u32x4 oa[4];
#pragma unroll
    for (int m = 0; m < 4; ++m) {
      TRACE2(1 + 2 * m);
      oa[m] = gelu8(acc[0][m], acc[1][m], col, rs[m], nm[m]);
      TRACE2(2 + 2 * m);
      __builtin_amdgcn_raw_buffer_store_b128(oa[m], wsr, ho + m * 1024, 0, HST);
      if (m >= 1) asm volatile("" :: "v"(oa[m - 1]));
      if (m == 0) { take(3, 3); take(4, 4); }
      SCHED();
    }
    STAMP(6);
    prog_wait(PW + (6 + d) * 4, 2);
#pragma unroll
    for (int i = 5; i < 8; ++i) take(i, i - 5);
    u32x4 s2v[2];
#pragma unroll
    for (int m = 0; m < 2; ++m) s2v[m] = *(LAS u32x4*)(size_t)(LB + L_ST2 + (tok + 8 * (mtile & 1) + 4 * d + 2 * m) * 16);
    setup2(8);
    wait_lgkm0();
    asm volatile("" :: "v"(oa[3]));
    prog_set(PW + w * 4, 1);
    snap(false);
#pragma unroll
    for (int m = 0; m < 2; ++m) {
      const u32x4 s2 = s2v[m];
      const f32x2 r2 = bco(u2f(s2.y)), n2 = bco(-u2f(s2.w));
#pragma unroll
      for (int n = 0; n < 4; ++n) {
        f32x2 o[2];
#pragma unroll
        for (int h = 0; h < 2; ++h) {
          const f32x2 xx = {bflo(xv[m][n][h]), bfhi(xv[m][n][h])};
          o[h] = pfma(pfma(xx, r2, n2), (f32x2){bflo(dv[n][h]), bfhi(dv[n][h])}, xx + (f32x2){bflo(bv[n][h]), bfhi(bv[n][h])});
        }
        acc2[n][m] = __builtin_shufflevector(o[0], o[1], 0, 1, 2, 3);
        acc2[n][2 + m] = (f32x4){0, 0, 0, 0};
        asm volatile("" : "+v"(acc2[n][m]), "+v"(acc2[n][2 + m]));
        if (4 * m + n == APUB) {
          SCHED();
          wait_vm<0>();
          __builtin_amdgcn_raw_buffer_store_b64((u32x2){tagA, tagB}, wsr, mlp && lane == 0 ? 0u : pubo, WS_FLAG + (2 * r + d) * 2048 + flag_of(jm, 3), 16);
          SCHED();
        }
      }
    }
    seen();
    STAMP(8);
  };

  unsigned tw[2][2], ra[4][2];
  u32x4 sl[2];
  char* op;
  auto mktail = [&]() {
    int tl = threadIdx.x;
    asm volatile("" : "+v"(tl));
    const int ck = tl & 15, gt = (tl >> 4) & 3, hw = tl >> 6;
#pragma unroll
    for (int h = 0; h < 2; ++h) {
      tw[0][h] = home + ck * 128 + (((4 * h + gt) ^ (ck & 7)) * 16) + (w == 1 ? 4096 : 0);
      tw[1][h] = tw[0][h] + (w == 1 ? -4096 : 4096);
      asm volatile("" : "+v"(tw[0][h]), "+v"(tw[1][h]));
    }
#pragma unroll
    for (int q = 0; q < 4; ++q) {
      ra[q][0] = LB + gt * HALF + (8 * hw + 2 * q + (ck >> 3)) * 128 + (((ck & 7) ^ (2 * q + (ck >> 3))) * 16);
      ra[q][1] = ra[q][0] + 4 * HALF;
      asm volatile("" : "+v"(ra[q][0]), "+v"(ra[q][1]));
    }
#pragma unroll
    for (int e = 0; e < 8; ++e) sl[e >> 2][e & 3] = (ck >> 1) == e ? ((ck & 1) ? 0x3f800000u : 0x3f80u) : 0u;
    asm volatile("" : "+v"(sl[0]), "+v"(sl[1]));
    const int orow = 64 * (hw & 1) + 16 * (ck >> 3) + 2 * (hw >> 1) + (gt >> 1) + 8 * (mtile & 1);
    op = p.out + (128 * (mtile >> 1) + orow) * 2048 + ntile * 256 + ((ck & 4) * 8 + (gt & 1) * 16 + (ck & 3) * 4) * 4;
    asm volatile("" : "+v"(op));
  };
  auto phase2 = [&](auto nm_) __attribute__((always_inline)) {
    STAMP(9);
    constexpr int NM = decltype(nm_)::value, S0 = NM == 8 ? 0 : 5, N = NM == 8 ? 13 : 9;
    const u64 tag = ((u64)tagB << 32) | tagA;
    auto has = [&](int k) { return fl[k < NM ? k : k - NM + S0] == tag; };
    auto need = [&](int k) {
      if (__builtin_expect(!has(k), 0)) {
#pragma unroll 1
        for (int spin = 1; spin < SPIN_CAP; ++spin) {
          COUNT(20);
          snap(k >= NM);
          if (has(k)) break;
          if (!(spin & 31)) {
            const u32x2 f = __builtin_amdgcn_raw_buffer_load_b64(wsr, 0, k < NM ? fm + 8 * k : fs + 8 * (k - NM + S0), 16);
            if (__builtin_amdgcn_readfirstlane(f.x) == tagA && __builtin_amdgcn_readfirstlane(f.y) == tagB) break;
          }
          __builtin_amdgcn_s_sleep(PSLEEP);
        }
      }
      asm volatile("" ::: "memory");
    };
    auto publish = [&]() { __builtin_amdgcn_raw_buffer_store_b64((u32x2){tagA, tagB}, wsr, pubo, 0, 16); };
    auto loadH = [&](int i, int m) -> u32x4 {
      if (m == 0) asm volatile("" : "+v"(hb));
      if ((NOC & 8) && (m & 1)) { u32x4 z; asm volatile("" : "=v"(z)); return z; }
      return __builtin_amdgcn_raw_buffer_load_b128(wsr, hb + (m & 1) * 1024, __builtin_amdgcn_readlane(ksl, i) ^ ((m & 2) * 1024), HAUX);
    };
    u32x4 hf[N][4];
    auto load4 = [&](int i) {
#pragma unroll
      for (int m = 0; m < 4; ++m) hf[i][m] = loadH(i, m);
    };
    STAMP(10);
    if (NM == 8) {
      __builtin_amdgcn_s_setprio(EPRI);
      need(0); load4(0);
      need(1); load4(1);
    } else {
      unsigned vc[8];
      mkvc(vc);
      asm volatile("" : "+v"(vc[0]), "+v"(vc[1]), "+v"(vc[2]), "+v"(vc[3]), "+v"(vc[4]), "+v"(vc[5]), "+v"(vc[6]), "+v"(vc[7]));
      const unsigned o = ntile * 65536 + kg * 128, hs = fresh(HB + (w < 4 ? SLOT : 0));
      if (LPUB) { wait_vm<4>(); publish(); }
      __builtin_amdgcn_s_setprio(0);
      dma_x8(vc, p.cre + o, hs);
      if (!LPUB) { wait_vm<12>(); publish(); }
      snap(false);
      dma_x8(vc, p.cim + o, hs + 8192);
      seen();
      need(0); load4(0);
      need(1); load4(1);
    }
    STAMP(11);
    s16x8 cfr[N - NM][4];
    auto rdc = [&](int q, unsigned base) {
#pragma unroll
      for (int n = 0; n < 4; ++n) {
        unsigned a = base + n * 2048;
        asm volatile("" : "+v"(a));
        cfr[q][n] = cat(*(LAS s16x4*)(size_t)a, *(LAS s16x4*)(size_t)(a + 8192));
      }
    };
    SCHED();
#pragma unroll
    for (int i = 0; i < N; ++i) {
      const int k = i + (i < NM - 2 ? 2 : SDEP);
      if (i == 2) STAMP(12);
      if (!(i & 1) || k >= NM) TRACE4(2 * i + 1);
      if (i == NM - 2) {
        STAMP(13);
#pragma unroll
        for (int e = NM; e < NM + SDEP - 2; ++e) { need(e); load4(e); }
      }
      if (k < NM) {
        if (NM == 6 && i == 0) snap(false);
        else if (NM == 6 && i == 2) { need(2); load4(2); need(3); load4(3); }
        if (!(i & 1) && (NM == 8 || i)) { need(k); need(k + 1); TRACE4(2 * i + 2); if (NM == 6 && i == 2) absorb(5, home + 16384); if (!(ESEP && i == 0)) snap(k + 2 == NM); }
      } else if (k < N) { need(k); TRACE4(2 * i + 2); }
      if (i == NM - 2) STAMP(14);
      if (NM == 8 && i == 1) prog_set(PW + w * 4, 4);
      if (NM == 8 && i == 6) prog_wait(PW + (w | 6) * 4, 4);
      if (NM == 6 && i == 1) prog_set(PW + w * 4, 1);
      if (NM == 8 && i == EOFF) __builtin_amdgcn_s_setprio(0);
      if (NM == 8 && i == PSTEP) { prog_wait(PW + (w ^ 4) * 4, 1); rdc(4, crd - 2 * SLOT); }
      if (NM == 8 && i == PSTEP + 1) { wait_lgkm0(); prog_set(PW + w * 4, 5); }
      if (i >= NM - 1 && i < NM + (NM == 8 ? 3 : 2)) rdc(i + 1 - NM, crd ^ (32 * (i + 1 - NM + (NM == 8 ? 0 : 1))));
      if (NM == 6 && i == N - 1) prog_wait(PW + (w ^ 4) * 4, 5);
      auto put = [&](int m) {
#pragma unroll
        for (int h = 0; h < 2; ++h) {
          const f32x4 u = acc2[2 * h][m], v = acc2[2 * h + 1][m];
          *(LAS u32x4*)(size_t)(tw[m >> 1][h] + (m & 1) * 2048) = (u32x4){pk2(u[0], u[1]), pk2(u[2], u[3]), pk2(v[0], v[1]), pk2(v[2], v[3])};
        }
        SCHED();
      };
      SCHED();
#pragma unroll
      for (int m = 0; m < 4; ++m) {
#pragma unroll
        for (int n = 0; n < 4; ++n)
          if (NM == 8 && i == 0) asm volatile("v_mfma_f32_16x16x32_bf16 %0, %1, %2, %0" : "+v"(acc2[n][m]) : "v"(we[0][n]), "v"(hf[0][m]));
          else acc2[n][m] = mfma(i < NM ? we[i < NM ? i : 0][n] : cfr[i < NM ? 0 : i - NM][n], __builtin_bit_cast(s16x8, hf[i][m]), NM == 6 && i == 0 ? (f32x4){0, 0, 0, 0} : acc2[n][m]);
        SCHED();
        if (k < N && (NM == 8 || i > 1)) hf[k < N ? k : 0][m] = loadH(k, m);
        if (i == N - 2 && m == 0) mktail();
        if (i == N - 1 && m > 0) put(m - 1);
        SCHED();
      }
      if (i == N - 1) put(3);
      if (ESEP && NM == 8 && i < 2) TRACE4(3 + i);
      if (ESEP && NM == 8 && i == 1) snap(false);
      if ((i & 1) && k < NM) seen();
    }
    TRACE4(30);
  };
  if (__builtin_expect(w >= 6 || w < 2, 1)) {
    if (__builtin_expect(w >= 6, 1)) roleD(); else roleA();
    phase2(std::integral_constant<int, 8>{});
  } else {
    if (w >= 4) roleL(); else roleB();
    phase2(std::integral_constant<int, 6>{});
  }
  STAMP(15);
  const int tl = threadIdx.x;
  barrier();
  STAMP(16);
  {
    s16x8 pt[4][2];
#pragma unroll
    for (int q = 0; q < 4; ++q) { pt[q][0] = lds128(ra[q][0]); pt[q][1] = lds128(ra[q][1]); }
#pragma unroll
    for (int t = 0; t < 2; ++t) {
      f32x4 s = {0, 0, 0, 0};
#pragma unroll
      for (int k = 0; k < 4; ++k) s = mfma(__builtin_bit_cast(s16x8, sl[k >> 1]), pt[2 * t + (k >> 1)][k & 1], s);
      __builtin_nontemporal_store(s, (GAS f32x4*)(op + t * 65536));
    }
  }
  if (b == 255 && tl == 0) __builtin_amdgcn_raw_buffer_store_b64((u32x2){tagA, tagB}, wsr, WS_EPOCH, 0, 16);
}

extern "C" void kernel_launch(void* const* d_in, const int* in_sizes, int n_in, void* d_out, int out_size, void* d_ws,
                              size_t ws_size, hipStream_t stream) {
  Params p;
  p.x = (const char*)d_in[0]; p.lre = (const char*)d_in[1]; p.lim = (const char*)d_in[2];
  p.bre = (const char*)d_in[3]; p.bim = (const char*)d_in[4]; p.cre = (const char*)d_in[5]; p.cim = (const char*)d_in[6];
  p.logdt = (const char*)d_in[7]; p.dsk = (const char*)d_in[8];
  p.w1 = (const char*)d_in[10]; p.b1 = (const char*)d_in[11]; p.w2 = (const char*)d_in[12]; p.b2 = (const char*)d_in[13];
  p.out = (char*)d_out; p.ws = (char*)d_ws;
  fused_kernel<<<256, 512, 0, stream>>>(p);
}
```

```cpp
#include <hip/hip_runtime.h>
#include <stdint.h>
#include <type_traits>

typedef short s16x4 __attribute__((ext_vector_type(4)));
typedef short s16x8 __attribute__((ext_vector_type(8)));
typedef __bf16 bf16x8 __attribute__((ext_vector_type(8)));
typedef __bf16 bf16x2 __attribute__((ext_vector_type(2)));
typedef float f32x4 __attribute__((ext_vector_type(4)));
typedef float f32x2 __attribute__((ext_vector_type(2)));
typedef unsigned u32x4 __attribute__((ext_vector_type(4)));
typedef unsigned u32x2 __attribute__((ext_vector_type(2)));
typedef int i32x4 __attribute__((ext_vector_type(4)));
typedef unsigned long long u64;
typedef u64 u64x4 __attribute__((ext_vector_type(4)));
typedef u64 u64x8 __attribute__((ext_vector_type(8)));
typedef int i32x8 __attribute__((ext_vector_type(8)));
typedef int i32x16 __attribute__((ext_vector_type(16)));
__device__ i32x8 sbl8(i32x4, int, int) __asm("llvm.amdgcn.s.buffer.load.v8i32");
__device__ i32x16 sbl16(i32x4, int, int) __asm("llvm.amdgcn.s.buffer.load.v16i32");
#define LAS __attribute__((address_space(3)))
#define GAS __attribute__((address_space(1)))
#define DI __device__ __forceinline__

#define STAMPS 0
#define TRACING 0
#define GATHER_AT 4
#define ROT 1
#define TOUCH 7
#define TLBW 0
#define NOFF 0
#define PSLEEP 4
#define BFLAG 2
#define ABAR 0
#define KROT 0
#define LPRIO 3
#define CHK 0
#define BSPLIT 8
#define APRIO 0
#define ADELAY 0
#define BFLAG0 1
#define DEPE 2
#define DEPL 2
#define LB_SLEEP 8
#define CHECK_AT 5
#define CPF 1
#define HAUX 16
#define LSNAP 1
#define BSNAP 1
#define L2T 0
#define L2C 0
#define SDEP 2
#define YPRE 1
#define KEEP 2
#define LSEP 0
#define PSTEP 9
#define NOC 0
#define ESEP 0
#define HST 16
#define EPRI 1
#define EOFF 6
#define WOFF 4
#define CPW 5
#define TST 1
#define TPP 3
#define S3AT 3
#define JM 0
#define ASKIP 1
#define APUB 3
#define LPUB 1
#define NOPRE 0
#define RAMP1 0
#define RAMP2 0
#define RAMP3 0
#define MAP 1

struct Params {
  const char *x, *lre, *lim, *bre, *bim, *cre, *cim, *logdt, *dsk, *w1, *b1, *w2, *b2;
  char *out, *ws;
};

constexpr unsigned WS_EPOCH = 0;
constexpr unsigned WS_STAT = 4096;
constexpr unsigned WS_TOT = WS_STAT + 2048 * 16;
constexpr unsigned WS_CS = WS_TOT + 16 * 2 * 16 * 256;
constexpr unsigned WS_BS = WS_CS + 16 * 16 * 512;
constexpr unsigned WS_FLAG = WS_BS + 16 * 1024;
constexpr unsigned WS_H = WS_FLAG + 32 * 2048;
constexpr unsigned H_MT = 88 * 4096;
constexpr unsigned WS_END = WS_H + 32 * H_MT;
constexpr unsigned WS_STAMP = 32u << 20;
constexpr unsigned WS_B2B = WS_STAMP + (4u << 20);
constexpr unsigned SLOT = 40960, HALF = 20480, SL_W = 16384, SL_B = 32768;
constexpr unsigned L_ST1 = 3 * SLOT + 16384, L_ST2 = L_ST1 + 2048;
constexpr unsigned L_CS = 3 * SLOT + HALF + 16384, L_FLG = L_CS + 512, L_B1 = L_CS + 768, L_BS = L_CS + 1024, L_LS = L_CS + 2048, L_DUMMY = L_CS + 3328;
constexpr unsigned LDS_BYTES = 163840;
constexpr int SPIN_CAP = 40000;
constexpr unsigned CODE_BYTES = 45056;

DI void dma4w(unsigned voff, const char* sbase, unsigned ldsaddr) {
  asm volatile("s_mov_b32 m0, %2\n\ts_nop 0\n\tglobal_load_lds_dword %0, %1" ::"v"(voff), "s"(sbase), "s"(ldsaddr));
}
DI void dma_x4(const unsigned* v, const char* sbase, unsigned ldsaddr, bool low = false) {
  if (low) {
    asm volatile("s_mov_b32 m0, %5\n\ts_setprio 0\n\t"
               "global_load_lds_dwordx4 %0, %4\n\tglobal_load_lds_dwordx4 %1, %4 offset:1024\n\t"
               "global_load_lds_dwordx4 %2, %4 offset:2048\n\tglobal_load_lds_dwordx4 %3, %4 offset:3072\n\ts_setprio 3"
               ::"v"(v[0]), "v"(v[1]), "v"(v[2]), "v"(v[3]), "s"(sbase), "s"(ldsaddr));
    return;
  }
  asm volatile("s_mov_b32 m0, %5\n\ts_nop 0\n\t"
               "global_load_lds_dwordx4 %0, %4\n\tglobal_load_lds_dwordx4 %1, %4 offset:1024\n\t"
               "global_load_lds_dwordx4 %2, %4 offset:2048\n\tglobal_load_lds_dwordx4 %3, %4 offset:3072"
               ::"v"(v[0]), "v"(v[1]), "v"(v[2]), "v"(v[3]), "s"(sbase), "s"(ldsaddr));
}
DI void dma_x8(const unsigned* v, const char* sbase, unsigned ldsaddr) {
  asm volatile("s_mov_b32 m0, %9\n\ts_nop 0\n\t"
               "global_load_lds_dwordx4 %0, %8 offset:-4096\n\tglobal_load_lds_dwordx4 %1, %8 offset:-3072\n\t"
               "global_load_lds_dwordx4 %2, %8 offset:-2048\n\tglobal_load_lds_dwordx4 %3, %8 offset:-1024\n\t"
               "global_load_lds_dwordx4 %4, %8\n\tglobal_load_lds_dwordx4 %5, %8 offset:1024\n\t"
               "global_load_lds_dwordx4 %6, %8 offset:2048\n\tglobal_load_lds_dwordx4 %7, %8 offset:3072"
               ::"v"(v[0]), "v"(v[1]), "v"(v[2]), "v"(v[3]), "v"(v[4]), "v"(v[5]), "v"(v[6]), "v"(v[7]), "s"(sbase), "s"(ldsaddr + 4096u));
}
constexpr int PG(int i) { return i & 3; }
template <int N> DI void wait_vm() { asm volatile("s_waitcnt vmcnt(%0)" ::"i"(N) : "memory"); }
DI void wait_lgkm0() { asm volatile("" ::: "memory"); __builtin_amdgcn_s_waitcnt(0xC07F); asm volatile("" ::: "memory"); }
DI void barrier() {
  asm volatile("" ::: "memory");
  __builtin_amdgcn_s_waitcnt(0xC07F);
  __builtin_amdgcn_s_barrier();
  asm volatile("" ::: "memory");
}
#define SCHED() __builtin_amdgcn_sched_barrier(0)
DI s16x8 lds128(unsigned a) { return *(LAS s16x8*)(size_t)a; }
DI f32x4 ldsf4(unsigned a) { return *(LAS f32x4*)(size_t)a; }
DI f32x2 ldsf2(unsigned a) { return *(LAS f32x2*)(size_t)a; }
DI float ldsf1(unsigned a) { return *(LAS float*)(size_t)a; }
DI s16x4 ldstr4(unsigned a) { return __builtin_amdgcn_ds_read_tr16_b64_v4i16((LAS s16x4*)(size_t)a); }
DI s16x8 cat(s16x4 lo, s16x4 hi) { return __builtin_shufflevector(lo, hi, 0, 1, 2, 3, 4, 5, 6, 7); }
DI s16x8 ldstr(unsigned a0, unsigned a1) { return cat(ldstr4(a0), ldstr4(a1)); }
DI f32x4 mfma(s16x8 a, s16x8 b, f32x4 c) {
  return __builtin_amdgcn_mfma_f32_16x16x32_bf16(__builtin_bit_cast(bf16x8, a), __builtin_bit_cast(bf16x8, b), c, 0, 0, 0);
}
DI unsigned pk2(float a, float b) { f32x2 v = {a, b}; return __builtin_bit_cast(unsigned, __builtin_convertvector(v, bf16x2)); }
DI float u2f(unsigned u) { return __builtin_bit_cast(float, u); }
DI unsigned f2u(float u) { return __builtin_bit_cast(unsigned, u); }
DI float bflo(unsigned u) { return __builtin_bit_cast(float, u << 16); }
DI float bfhi(unsigned u) { return __builtin_bit_cast(float, u & 0xffff0000u); }
template <int CTRL> DI float dpp_add(float v) {
  return v + __builtin_bit_cast(float, __builtin_amdgcn_update_dpp(0, __builtin_bit_cast(int, v), CTRL, 0xf, 0xf, true));
}
DI float row16_sum(float v) {
  v = dpp_add<0xB1>(v);
  v = dpp_add<0x4E>(v);
  v = dpp_add<0x141>(v);
  v = dpp_add<0x140>(v);
  return v;
}
DI float smul(float a, float b) { float d; asm("v_mul_f32 %0, %1, %2" : "=v"(d) : "v"(a), "v"(b)); return d; }
DI float sadd(float a, float b) { float d; asm("v_add_f32 %0, %1, %2" : "=v"(d) : "v"(a), "v"(b)); return d; }
DI float sfma(float a, float b, float c) { float d; asm("v_fma_f32 %0, %1, %2, %3" : "=v"(d) : "v"(a), "v"(b), "v"(c)); return d; }
DI float sfms(float a, float b, float c) { float d; asm("v_fma_f32 %0, %1, %2, -%3" : "=v"(d) : "v"(a), "v"(b), "v"(c)); return d; }
DI void zero4(f32x4& v) {
  float a, b, c, d;
  asm volatile("v_mov_b32 %0, 0\n\tv_mov_b32 %1, 0\n\tv_mov_b32 %2, 0\n\tv_mov_b32 %3, 0" : "=v"(a), "=v"(b), "=v"(c), "=v"(d));
  v = (f32x4){a, b, c, d};
}
DI u32x2 swap32(float a, float b) { asm volatile("s_nop 1" : "+v"(a), "+v"(b)); return __builtin_amdgcn_permlane32_swap(f2u(a), f2u(b), false, false); }
DI u32x2 swap16(float a, float b) { asm volatile("s_nop 1" : "+v"(a), "+v"(b)); return __builtin_amdgcn_permlane16_swap(f2u(a), f2u(b), false, false); }
DI f32x2 pfma(f32x2 a, f32x2 b, f32x2 c) { return __builtin_elementwise_fma(a, b, c); }
DI f32x2 bc(float a) { return (f32x2){a, a}; }
DI f32x2 bco(float a) { f32x2 v = {a, a}; asm("" : "+v"(v)); return v; }
DI f32x2 cmul(f32x2 a, f32x2 b) { return (f32x2){sfms(a[0], b[0], smul(a[1], b[1])), sfma(a[0], b[1], smul(a[1], b[0]))}; }
DI f32x2 cfma(f32x2 a, f32x2 b, f32x2 c) { return (f32x2){sfms(a[0], b[0], sfms(a[1], b[1], c[0])), sfma(a[0], b[1], sfma(a[1], b[0], c[1]))}; }
DI f32x2 csel(bool p, f32x2 a, f32x2 b) { return (f32x2){p ? a[0] : b[0], p ? a[1] : b[1]}; }
DI void prog_set(unsigned a, unsigned v) {
  asm volatile("s_mov_b64 exec, 1\n\tds_write_b32 %0, %1\n\ts_mov_b64 exec, -1" ::"v"(a), "v"(v) : "memory");
}
DI void prog_wait(unsigned a, unsigned v) {
#pragma unroll 1
  for (int spin = 0; spin < SPIN_CAP * 16; ++spin) {
    if (__builtin_amdgcn_readfirstlane(*(volatile LAS unsigned*)(size_t)a) >= v) break;
    __builtin_amdgcn_s_sleep(1);
  }
  asm volatile("" ::: "memory");
}
DI void bar() { asm volatile("" ::: "memory"); __builtin_amdgcn_s_barrier(); asm volatile("" ::: "memory"); }
DI void dma1_dev(unsigned voff, const char* sbase, unsigned ldsaddr) {
  asm volatile("s_mov_b32 m0, %2\n\ts_nop 0\n\tglobal_load_lds_dwordx4 %0, %1 sc1" ::"v"(voff), "s"(sbase), "s"(ldsaddr) : "memory");
}
DI void dma2_dev(unsigned voff, const char* s0, const char* s1, unsigned ldsaddr) {
  asm volatile("s_mov_b32 m0, %3\n\ts_nop 0\n\tglobal_load_lds_dwordx4 %0, %1 sc1\n\tglobal_load_lds_dwordx4 %0, %2 offset:2048 sc1" ::"v"(voff), "s"(s0), "s"(s1), "s"(ldsaddr) : "memory");
}
DI void dma4_dev(unsigned voff, const char* s0, const char* s1, unsigned ldsaddr) {
  asm volatile("s_mov_b32 m0, %3\n\ts_nop 0\n\tglobal_load_lds_dwordx4 %0, %1 sc1\n\tglobal_load_lds_dwordx4 %0, %1 offset:1024 sc1\n\t"
               "global_load_lds_dwordx4 %0, %2 offset:2048 sc1\n\tglobal_load_lds_dwordx4 %0, %2 offset:3072 sc1" ::"v"(voff), "s"(s0), "s"(s1), "s"(ldsaddr) : "memory");
}
DI int kstep_of(int slot, int i) {
  const int q = slot + 4 * i + (slot >= 4 ? 28 : 0), cls = (q >= 14) + (q >= 28) + (q >= 42);
  return 4 * (q - 14 * cls) + ((0x9C >> (2 * cls)) & 3);
}
DI unsigned flag_of(int j, int c) {
  const int q = 14 * ((0x78 >> (2 * c)) & 3) + j;
  return q < 32 ? (q & 3) * 128 + (q >> 2) * 8 : 512 + (q & 3) * 128 + ((q - 32) >> 2) * 8;
}
DI void cnt_add(unsigned a) {
  asm volatile("s_mov_b64 exec, 1\n\tds_add_u32 %0, %1\n\ts_mov_b64 exec, -1" ::"v"(a), "v"(1u) : "memory");
}
DI f32x2 lo2(f32x4 v) { return __builtin_shufflevector(v, v, 0, 1); }
DI f32x2 hi2(f32x4 v) { return __builtin_shufflevector(v, v, 2, 3); }
DI void rows4(float v, float* o) {
  asm volatile("s_nop 1" : "+v"(v));
  const unsigned u = f2u(v);
  const u32x2 h = __builtin_amdgcn_permlane32_swap(u, u, false, false);
  const u32x2 a = __builtin_amdgcn_permlane16_swap(h.x, h.x, false, false), b = __builtin_amdgcn_permlane16_swap(h.y, h.y, false, false);
  o[0] = u2f(a.x); o[1] = u2f(a.y); o[2] = u2f(b.x); o[3] = u2f(b.y);
}
struct Col { f32x2 cs[4], bias[4]; };
DI void mkcol(Col& c, f32x4 cs0, f32x4 cs1, u32x2 b0, u32x2 b1) {
  c.cs[0] = lo2(cs0); c.cs[1] = hi2(cs0); c.cs[2] = lo2(cs1); c.cs[3] = hi2(cs1);
  c.bias[0] = (f32x2){bflo(b0.x), bfhi(b0.x)}; c.bias[1] = (f32x2){bflo(b0.y), bfhi(b0.y)};
  c.bias[2] = (f32x2){bflo(b1.x), bfhi(b1.x)}; c.bias[3] = (f32x2){bflo(b1.y), bfhi(b1.y)};
}
DI u32x4 gelu8(f32x4 a0, f32x4 a1, const Col& c, f32x2 rs, f32x2 nmrs) {
  f32x2 v[4], z[4];
  u32x4 o;
#define ST4(stmt) _Pragma("unroll") for (int h = 0; h < 4; ++h) { stmt; } SCHED();
  ST4(v[h] = pfma(c.cs[h], nmrs, c.bias[h]))
  ST4(v[h] = pfma(h == 0 ? lo2(a0) : h == 1 ? hi2(a0) : h == 2 ? lo2(a1) : hi2(a1), rs, v[h]))
  ST4(z[h] = v[h] * v[h])
  ST4(z[h] = pfma(z[h], bc(-0.10294324f), bc(-2.30220820f)))
  ST4(z[h] = z[h] * v[h])
  ST4((z[h] = (f32x2){__builtin_amdgcn_exp2f(z[h][0]), __builtin_amdgcn_exp2f(z[h][1])}))
  ST4(z[h] = z[h] + bc(1.f))
  ST4((z[h] = (f32x2){__builtin_amdgcn_rcpf(z[h][0]), __builtin_amdgcn_rcpf(z[h][1])}))
  ST4(v[h] = v[h] * z[h])
  ST4(o[h] = pk2(v[h][0], v[h][1]))
#undef ST4
  return o;
}

#define TRACE(i) do {} while (0)
#define TRACE2(i) do {} while (0)
#define TRACE3(i) do {} while (0)
#define TRACE4(i) do {} while (0)
#define STAMP(k) do {} while (0)
#define STAMPC(k) do {} while (0)
#define COUNT(k) do {} while (0)

__global__ void __launch_bounds__(512, 2) fused_kernel(Params p) {
  __shared__ __attribute__((aligned(16))) char lds_mem[LDS_BYTES];
  unsigned long long pc;
  asm volatile("s_getpc_b64 %0" : "=s"(pc));
  const unsigned LB = (unsigned)(size_t)(LAS char*)lds_mem;
  const int tid = threadIdx.x, lane = tid & 63;
  const int w = (__builtin_amdgcn_readfirstlane(tid >> 6) + WOFF) & 7;
  if (w == CPW) {
    const unsigned co = ((unsigned)(lane & 15) * 32u + (blockIdx.x >> 3)) * 128u;
    dma4w(co < CODE_BYTES ? co : 0u, (const char*)(pc & ~255ull), LB + L_DUMMY + 512);
  }
  const int b = blockIdx.x, idx = b >> 3, xcd = b & 7;
  const int r = 8 * (xcd >> 2) + (idx >> 2), j = 4 * (xcd & 3) + (idx & 3);
  const int rot = (2 * (xcd & 3) + 4 * (xcd >> 2)) & 7;
  const int mtile = 4 * xcd + (idx >> 3), ntile = (idx + NOFF) & 7;
  const int i16 = lane & 15, g = lane >> 4, qq = i16 >> 2, pp = i16 & 3;
  const int d = w & 1;
  const int jm = j < 14 ? j : 13, lq = j & 1;
  const bool mlp = j < 14;
  u32x4 pre[4];
  unsigned v_lre, v_lim, v_ldt;
  if ((w >> 1) == 2) {
    const unsigned po = (unsigned)(32 * j + 16 * d + i16) * 2u;
    v_lre = *(const GAS unsigned short*)(p.lre + po); v_lim = *(const GAS unsigned short*)(p.lim + po); v_ldt = *(const GAS unsigned short*)(p.logdt + po);
    asm volatile("" ::: "memory");
  }
  u32x4 prb[2];
  if (w < 4) {
    if (NOPRE > 1) __builtin_amdgcn_s_sleep(NOPRE);
    const bool isb = w >= 2;
    const char* pb = isb ? p.w1 + (64 * (r & 7) + 32 * d) * 3584 + jm * 256 + (r >> 3) * 128 : p.x + (128 * r + 8 * j + 4 * d) * 1024;
    const unsigned po = isb ? (lane >> 3) * 3584 + (lane & 7) * 16 : g * 1024 + i16 * 16, ps = isb ? 28672 : 256;
#pragma unroll
    for (int i = 0; i < 4; ++i) pre[i] = *(const GAS u32x4*)(pb + i * ps + po);
    if (isb) {
#pragma unroll
      for (int i = 0; i < 2; ++i) prb[i] = *(const GAS u32x4*)((r & 8 ? p.bim : p.bre) + (32 * j + 4 * (r & 7) + 2 * d) * 1024 + i * 512 + (lane >> 5) * 1024 + (lane & 31) * 16);
    }
    asm volatile("" ::: "memory");
  }
  const int kgo = 2 * (idx >> 3), late = ((w + 2) >> 2) & 1, kg = 2 * (((w & 2) + d + KROT * ntile) & 3) + late;
  auto slot_of = [&](int role) { return (((role + 1) & 2) << 1) + (((((role + 1) & 2) ? 0 : kgo) + ((role & 1) ? 0 : 2) + d) & 3); };
  const int slot = slot_of(w >> 1);
  const u32x2 ep = *(const __attribute__((address_space(4))) u32x2*)(p.ws + WS_EPOCH);
  unsigned tagA, tagB;
  auto mktag = [&]() { tagA = ep.x; tagB = ep.y; asm volatile("" : "+s"(tagA), "+s"(tagB)); tagA += 1u; tagB += 1u; };
  const __amdgpu_buffer_rsrc_t wsr = __builtin_amdgcn_make_buffer_rsrc(p.ws, 0, WS_END, 0x00020000);
  const i32x4 wss = {(int)(size_t)p.ws, (int)((size_t)p.ws >> 32) & 0xffff, (int)WS_END, 0x00020000};
  auto fresh = [](int o) { asm volatile("" : "+s"(o) :: "memory"); return o; };
  const unsigned HB = LB + d * HALF;
  const unsigned PW = LB + L_FLG;
  s16x8 we[8][4];
  f32x4 acc2[4][4];
  const unsigned home = HB + ((2 - (w >> 1)) & 3) * SLOT;
  const unsigned r2l = (4 * g + qq) * 128 + pp * 8, r2x = (2 * g + (qq >> 1)) & 3;
  auto absorb = [&](int i, unsigned page) {
#pragma unroll
    for (int n = 0; n < 4; ++n) { const unsigned a = page + r2l + ((n ^ r2x) * 32); we[i][n] = ldstr(a, a + 2048); }
  };
  auto mkvc = [&](unsigned* vc) {
#pragma unroll
    for (int i = 0; i < 8; ++i) vc[i] = (lane >> 3) * 1024 + (((lane & 7) ^ (4 * (i & 1) + g)) * 16) + 4096 + i * 7168;
  };
  auto mkv2 = [&](unsigned* v2) {
#pragma unroll
    for (int i = 0; i < 4; ++i) v2[i] = (lane >> 3) * 1024 + (((((lane & 7) >> 1) ^ g) << 1) | (lane & 1)) * 16 + i * 7168;
  };
  auto loadC = [&](const unsigned* vc, int kgc, unsigned dst) {
    const unsigned o = ntile * 65536 + kgc * 128;
    dma_x8(vc, p.cre + o, dst);
    dma_x8(vc, p.cim + o, dst + 8192);
  };
  const char* w2b = p.w2 + ntile * 128;
  auto adv = [&](unsigned& a, int dl) { a += dl; asm volatile("" : "+v"(a)); };
  const int tok = 32 * qq + 16 * (i16 & 1) + ((i16 >> 1) & 1);
  const unsigned kx = (g ^ (i16 >> 1)) * 16;
  const unsigned awb = LB + SL_W + (8 * g + qq) * 256 + pp * 8, awx = qq | ((g & 1) << 2);
  auto sum32 = [&](const u32x4* v) {
    float s0 = 0.f, s1 = 0.f;
#pragma unroll
    for (int i = 0; i < 4; ++i)
#pragma unroll
      for (int e = 0; e < 4; ++e) { s0 += bflo(v[i][e]); s1 += bfhi(v[i][e]); }
    return row16_sum(s0 + s1);
  };
  auto touch = [&]() {
    dma4w(d ? (lane & 15) * 128 : (lane < 56 ? lane : 0) * 128, d ? p.cre + b * 2048 : p.w2 + b * 7168, LB + L_DUMMY + d * 256);
    if (d) dma4w((lane & 15) * 128, p.cim + b * 2048, LB + L_DUMMY + d * 256);
  };
  auto zero2 = [&]() {
#pragma unroll
    for (int n = 0; n < 4; ++n) {
#pragma unroll
      for (int m = 0; m < 4; ++m) acc2[n][m] = (f32x4){0, 0, 0, 0};
      asm volatile("" : "+v"(acc2[n][0]), "+v"(acc2[n][1]), "+v"(acc2[n][2]), "+v"(acc2[n][3]));
    }
  };
  int ksl;
  unsigned hb, crd, pubo = 0xffff0000u;
  const int fm = WS_FLAG + mtile * 2048 + slot * 128, fs = WS_FLAG + mtile * 2048 + 1024 + (kg >> 1) * 64;
  u64x8 fl;
  auto snap = [&](bool ssm) { fl = __builtin_bit_cast(u64x8, sbl16(wss, fresh(ssm ? fs : fm), 1)); SCHED(); };
  auto seen = [&]() { SCHED(); asm volatile("" : "+s"(fl)); };
  auto setup2 = [&](int nm) {
    ksl = (lane < nm ? kstep_of(slot, lane) : 56 + 4 * kg + late + ((lane - nm) & 7)) * 4096;
    if (NOC & 16) ksl = (w * 11 + (lane & 1)) * 4096;
    ksl += w == 1 ? 2048 : 0;
    hb = WS_H + (unsigned)mtile * H_MT + lane * 16;
    crd = home + i16 * 128 + (g & 1) * 8 + (((g >> 1) ^ (i16 >> 1)) * 16);
    asm volatile("" : "+v"(ksl), "+v"(hb), "+v"(crd));
  };

  auto roleD = [&]() __attribute__((always_inline)) {
    __builtin_amdgcn_s_waitcnt(0x0F70);
    __builtin_amdgcn_s_setprio(3);
    unsigned vx[8], vw[8], vb[4], v2[4];
#pragma unroll
    for (int i = 0; i < 8; ++i) vw[i] = g * 3584 + ((((i16 >> 1) ^ (g | (((i >> 1) & 1) << 2))) << 1) | (i16 & 1)) * 16 + 4096 + i * (14336 - 1024);
    const char* wb = p.w1 + jm * 256 + d * (32 * 3584);
    auto part1 = [&](int c, unsigned slot) { if (mlp) dma_x8(vw, wb + ((c + rot) & 7) * (64 * 3584), LB + slot + SL_W + d * 8192); };
    asm volatile("" :: "s"(wb));
    STAMP(18);
    part1(0, 0);
#pragma unroll
    for (int i = 0; i < 8; ++i)
      vx[i] = (32 * (g >> 1) + 16 * ((lane >> 3) & 1) + (g & 1)) * 1024 + (((lane & 7) ^ (4 * (i & 1) + g)) * 16) + 4096 + (i & 1) * 65536 + (i >> 1) * 2048 - i * 1024;
#pragma unroll
    for (int i = 0; i < 4; ++i) vb[i] = (lane >> 3) * 1024 + (((lane & 7) ^ (4 * (i & 1) + g)) * 16) + i * 7168;
    const char* xb = p.x + (r * 128 + d * 8) * 1024;
    const char* bb = (d ? p.bim : p.bre) + j * 32768;
    auto partx = [&](int c, unsigned slot) { dma_x8(vx, xb + ((c + rot) & 7) * 128, LB + slot + d * 8192); };
    auto partb = [&](int c, unsigned slot) { dma_x4(vb, bb + ((c + rot) & 7) * 128, LB + slot + SL_B + d * 4096); };
    auto part0 = [&](int c, unsigned slot) { partx(c, slot); partb(c, slot); };
    mkv2(v2);
    unsigned ra[4];
#pragma unroll
    for (int n = 0; n < 4; ++n) ra[n] = home + r2l + ((n ^ r2x) * 32);
    int kt = kstep_of(slot_of((lane >> 3) & 3), lane & 7) * 32768;
    asm volatile("" : "+v"(kt), "+v"(ra[0]), "+v"(ra[1]), "+v"(ra[2]), "+v"(ra[3]), "+v"(v2[0]), "+v"(v2[1]), "+v"(v2[2]), "+v"(v2[3]));
    bool low = false;
    auto page = [&](int role, int i, int grp, int pg) { if ((NOC & 4) && low) return; dma_x4(v2, w2b + (unsigned)__builtin_amdgcn_readlane(kt, 8 * role + i), HB + grp * SLOT + pg * 4096, low); };
    auto take = [&](int i) {
      SCHED();
#pragma unroll
      for (int n = 0; n < 4; ++n) we[i][n] = ldstr(ra[n] + PG(i) * 4096, ra[n] + PG(i) * 4096 + 2048);
      SCHED();
    };
    auto own = [&](int i) { page(3, i, 3, PG(i)); };
    unsigned l16 = lane * 16, la = home + 12288 + lane * 16, lb = LB + L_ST1 + d * 1024 + lane * 16, lc = LB + L_BS + d * 512 + (lane & 31) * 16;
    unsigned wa = LB + L_CS + d * 256 + (lane & 31) * 8 + (lane >> 5) * 4, wb2 = LB + L_LS + (2 * d + (lane >> 5)) * 288 + i16 * 16 + (g & 1) * 4;
    asm volatile("" : "+v"(l16), "+v"(la), "+v"(lb), "+v"(lc), "+v"(wa), "+v"(wb2));
    auto gather = [&](bool bs) {
      const char* cp = p.ws + WS_CS + jm * 8192 + d * 4096;
      dma4_dev(l16, cp, cp, home + 12288);
      dma2_dev(l16, p.ws + WS_STAT + r * 2048 + d * 1024, p.ws + WS_STAT + (mtile >> 1) * 2048 + d * 1024 - 2048, LB + L_ST1 + d * 1024);
      if (bs) dma1_dev(l16, p.ws + WS_BS + j * 1024, LB + L_BS);
    };
    auto check = [&](auto between) -> bool {
      auto ld16 = [](unsigned a) { return *(volatile LAS u32x4*)(size_t)a; };
      auto bad = [&](unsigned a, u32x4 v) { return __builtin_amdgcn_bitop3_b32(v.z, tagB, __builtin_amdgcn_bitop3_b32(v.x, tagA, a, 0xBE), 0xBE); };
      const u32x4 c0 = ld16(la), c1 = ld16(la + 1024), c2 = ld16(la + 2048), c3 = ld16(la + 3072), s1 = ld16(lb), s2 = ld16(lb + 2048), s3 = ld16(lc);
      SCHED(); between(); SCHED();
      const u32x2 h = swap32(sadd(sadd(u2f(c0.y), u2f(c1.y)), sadd(u2f(c2.y), u2f(c3.y))), sadd(sadd(u2f(c0.w), u2f(c1.w)), sadd(u2f(c2.w), u2f(c3.w))));
      *(LAS float*)(size_t)wa = sadd(u2f(h.x), u2f(h.y));
      *(LAS unsigned*)(size_t)wb2 = s1.y;
      *(LAS unsigned*)(size_t)(wb2 + 8) = s1.w;
      const unsigned e = bad(bad(bad(bad(bad(bad(bad(0u, c0), c1), c2), c3) & (mlp ? ~0u : 0u), s1), s2), s3);
      return __builtin_amdgcn_ballot_w64(e != 0) == 0;
    };
    const unsigned me = PW + w * 4;
    zero2();
    setup2(8);
    auto landed = [&]() { if (mlp) wait_vm<20>(); else wait_vm<12>(); };
    SCHED();
    part0(0, 0);
    if (TOUCH == 4) touch();
    part1(1, SLOT); part0(1, SLOT);
    STAMP(1);
    mktag();
    if (mlp) wait_vm<20 + (TOUCH == 4)>(); else wait_vm<12 + (TOUCH == 4)>();
    STAMP(19);
    bar();
    STAMP(2);
    part1(2, 2 * SLOT); part0(2, 2 * SLOT); landed(); if (TOUCH == 1) touch();
    if (RAMP1) __builtin_amdgcn_s_sleep(RAMP1);
    bar();
    if (d) dma4w(lane * 4, p.b1 + jm * 256, LB + L_B1);
    part1(3, 0); part0(3, 0); landed();
    unsigned vt = (lane < 56 ? lane : 0) * 128;
    asm volatile("" : "+v"(vt));
    auto l2t = [&](int t) { if (t < L2T) dma4w(vt, p.w2 + (t * 64 + 2 * idx + d) * 7168, LB + L_DUMMY + d * 256); };
    auto l2c = [&](int t) { if (t < L2C) dma4w(l16 * 8, (t ? p.cim : p.cre) + (2 * idx + d) * 8192, LB + L_DUMMY + d * 256); };
    auto s3 = [&]() { if (L2T < 4) dma4w((lane < 28 ? lane : 0) * 1024, p.w2 + (2 * idx + d) * 28672 + ((3u - ((unsigned)(size_t)p.w2 >> 7)) & 7u) * 128, LB + L_DUMMY + d * 256); };
    if (S3AT == 1) s3();
    l2t(0);
    if (RAMP2) __builtin_amdgcn_s_sleep(RAMP2);
    bar();
    STAMPC(21);
    dma_x4(vx, p.x + (128 * (mtile >> 1) + 8 * (mtile & 1) + 4 * d) * 1024 + ntile * 128 - 4096, home + 12288);
    own(0); own(1); part1(4, SLOT); part0(4, SLOT); landed(); if (S3AT == 2) s3();
    if (RAMP3) __builtin_amdgcn_s_sleep(RAMP3);
    bar();
    own(2); part1(5, 2 * SLOT); take(0); take(1); part0(5, 2 * SLOT); landed(); wait_lgkm0(); if (S3AT == 3) s3();
    bar();
    gather(d); own(4); part1(6, 0); take(2); part0(6, 0); landed(); wait_lgkm0(); if (S3AT == 4) s3();
    bar();
    own(5); part1(7, SLOT); take(4);
    const bool ok = check([&]() { partx(7, SLOT); });
    SCHED();
    partb(7, SLOT);
    if (__builtin_expect(!ok, 0)) {
#pragma unroll 1
      for (int spin = 0; spin < SPIN_CAP; ++spin) {
        COUNT(20);
        __builtin_amdgcn_s_sleep(10);
        gather(true);
        wait_vm<0>();
        if (check([]() {})) break;
      }
    }
    landed();
    barrier();
    STAMP(3); STAMPC(22);
    auto c8 = [&](const char* src, unsigned dst) { dma_x4(vb, src, dst, low); dma_x4(vb, src + 32768, dst + 4096, low); };
    const unsigned co = ntile * 65536 + kg * 128;
    page(0, 0, 2, 0); take(5); page(0, 1, 2, 1); page(0, 2, 2, 2); page(0, 3, 2, 3); page(0, 4, 2, 4); own(3);
    wait_vm<12>();
    bar();
    STAMP(4);
    own(6); page(2, 0, 0, 0); page(2, 1, 0, 1); page(2, 2, 0, 2); page(2, 3, 0, 3); page(2, 4, 0, 4);
    wait_vm<20>();
    STAMP(5);
    bar();
    TRACE4(31);
    low = true;
    STAMP(6);
    take(3); take(6);
    page(0, 5, 2, 0); page(0, 6, 2, 1); page(0, 7, 2, 2);
    wait_lgkm0();
    own(7); page(1, 0, 1, 0);
    wait_vm<20>(); prog_set(me, 1);
    page(1, 1, 1, 1); page(1, 2, 1, 2); page(1, 3, 1, 3); page(1, 4, 1, 4);
    wait_vm<20>(); prog_set(me, 2);
    take(7);
    c8(p.cre + co, home);
    wait_lgkm0();
    c8(p.cim + co, home + 8192);
    wait_vm<16>();
    prog_set(me, 3);
    prog_wait(PW + d * 4, 1);
    STAMP(7);
    snap(false);
    c8(p.cre + (co ^ 512), HB + 2 * SLOT); c8(p.cim + (co ^ 512), HB + 2 * SLOT + 8192);
    seen();
    STAMP(8);
  };
  auto roleL = [&]() __attribute__((always_inline)) {
    if (TOUCH == 0) touch();
    f32x4 acr[8], aci[8];
#pragma unroll
    for (int m = 0; m < 8; ++m) { acr[m] = (f32x4){0, 0, 0, 0}; aci[m] = (f32x4){0, 0, 0, 0}; asm volatile("" : "+v"(acr[m]), "+v"(aci[m])); }
    unsigned ax[2], ab[2];
    ax[0] = LB + i16 * 128 + kx; ax[1] = ax[0] ^ 64;
    ab[0] = LB + SL_B + (16 * d + i16) * 128 + kx; ab[1] = ab[0] ^ 64;
    unsigned sa = LB + L_LS + g * 288, csa = LB + L_BS + (16 * d + i16) * 16 + 4;
    asm volatile("" : "+v"(sa), "+v"(csa), "+v"(ax[0]), "+v"(ax[1]), "+v"(ab[0]), "+v"(ab[1]));
    s16x8 xf[2][8], bf[2][2];
    auto rd = [&](int set, int i) {
      if (i < 2) bf[set][i] = lds128(ab[set] + i * 4096);
      else xf[set][i - 2] = lds128(ax[set] + (i - 2) * 2048);
    };
    auto mm = [&](int set, int i) {
      if (i & 1) aci[i >> 1] = mfma(xf[set][i >> 1], bf[set][1], aci[i >> 1]);
      else acr[i >> 1] = mfma(xf[set][i >> 1], bf[set][0], acr[i >> 1]);
    };
    asm volatile("" : "+v"(v_lre), "+v"(v_lim), "+v"(v_ldt));
    const float lr = bflo(v_lre), li = bflo(v_lim);
    const float dt = bflo(pk2(expf(bflo(v_ldt)), 0.f));
    const float ang = smul(dt, li);
    const float rv = smul(ang, 0.15915494f);
    const float rev = (rv - __builtin_rintf(rv)) + sfma(ang, 6.4206382e-9f, sfms(ang, 0.15915494f, rv));
    float sn = __builtin_amdgcn_sinf(rev), cn = __builtin_amdgcn_cosf(rev);
    asm volatile("s_nop 0" : "+v"(sn), "+v"(cn));
    const float ex = expf(smul(dt, lr));
    f32x2 a1 = {smul(ex, cn), smul(ex, sn)};
    const float nr = a1[0] - 1.f, ni = a1[1], iden = 2.f / sfma(lr, lr, smul(li, li));
    f32x2 kk = {smul(sfma(nr, lr, smul(ni, li)), iden), smul(sfms(ni, lr, smul(nr, li)), iden)};
    f32x2 t = cmul(a1, a1); t = cmul(t, t); t = cmul(t, t);
    f32x2 a16 = cmul(t, t);
    f32x2 a32 = cmul(a16, a16);
    const f32x2 a64 = cmul(a32, a32), a128 = cmul(a64, a64), a256 = cmul(a128, a128);
    f32x2 a512 = cmul(a256, a256);
    const f32x2 one = {1.f, 0.f};
    f32x2 aseg = cmul(kk, cmul(csel(g & 1, a32, one), csel(g & 2, a64, one)));
    const int e4 = (r - 1 - g) & 3;
    f32x2 acar = cmul(csel(e4 & 1, a128, one), csel(e4 & 2, a256, one));
    const f32x2 ar = bco(a1[0]), ai = bco(a1[1]);
    asm volatile("" :: "v"(ar), "v"(ai));
    asm volatile("" : "+v"(kk), "+v"(a16), "+v"(a32), "+v"(a512), "+v"(aseg), "+v"(acar));
    const unsigned on = (qq == g) ? ((pp & 1) ? 0xffff0000u : 0xffffu) : 0u;
    unsigned m0 = (pp & 2) ? 0u : on, m1 = (pp & 2) ? on : 0u;
    auto scan = [&](f32x2 h0, f32x2 h1, const f32x2* ur, const f32x2* ui, auto out) {
      const u32x4 z0 = {m0 & 0x3f803f80u, m1 & 0x3f803f80u, 0u, 0u}, z1 = {0u, 0u, z0.x, z0.y};
      const f32x4 zz = {0, 0, 0, 0};
      f32x2 hr = {h0[0], h1[0]}, hi = {-h0[1], -h1[1]};
      u32x4 fr[4], fi[4];
      f32x4 tr[2], ti[2];
      SCHED();
#pragma unroll
      for (int mp = 0; mp < 5; ++mp) {
        if (mp > 0) {
#pragma unroll
          for (int k = 0; k < 2; ++k) {
            tr[k] = mfma(__builtin_bit_cast(s16x8, fr[mp - 1]), __builtin_bit_cast(s16x8, k ? z1 : z0), zz);
            ti[k] = mfma(__builtin_bit_cast(s16x8, fi[mp - 1]), __builtin_bit_cast(s16x8, k ? z1 : z0), zz);
          }
          SCHED();
        }
        if (mp < 4) {
#pragma unroll
          for (int e = 0; e < 4; ++e) {
            const int s = 4 * mp + e;
            const f32x2 t1 = ur ? pfma(hr, -ai, ui[s]) : hr * -ai; SCHED();
            const f32x2 t0 = ur ? pfma(hi, ai, ur[s]) : hi * ai; SCHED();
            hi = pfma(hi, ar, t1); SCHED();
            hr = pfma(hr, ar, t0); SCHED();
            fi[mp][e] = pk2(hi[0], hi[1]); SCHED();
            fr[mp][e] = pk2(hr[0], hr[1]); SCHED();
          }
        }
        if (mp > 0) { out(2 * mp - 2, tr[0], ti[0]); out(2 * mp - 1, tr[1], ti[1]); SCHED(); }
      }
    };
    auto pack = [](f32x4 a, f32x4 b) { return (u32x4){pk2(a[0], a[1]), pk2(a[2], a[3]), pk2(b[0], b[1]), pk2(b[2], b[3])}; };
    u32x4 yb[8];
    auto resp = [&]() { scan(aseg, cmul(a16, aseg), nullptr, nullptr, [&](int q, f32x4 a, f32x4 b) { yb[q] = pack(a, b); asm volatile("" : "+v"(yb[q])); }); };
    if (YPRE) resp();
    mktag();
    __builtin_amdgcn_s_waitcnt(0x0F70);
    if (TLBW && d) { dma4w(0, p.cre + ntile * 65536, LB + L_DUMMY + 256); dma4w(0, p.cim + ntile * 65536, LB + L_DUMMY + 256); dma4w((lane & 1) * 0x1bf000, p.w2, LB + L_DUMMY + 256); }
    __builtin_amdgcn_s_setprio(1);
    STAMP(1);
    bar();
    STAMP(2); TRACE3(1);
#pragma unroll
    for (int i = 0; i < 10; ++i) rd(0, i);
    wait_lgkm0();
    auto stage1 = [&](int dl) {
      SCHED();
#pragma unroll
      for (int i = 0; i < 16; ++i) {
        mm(0, i); SCHED();
        if (i < 10) rd(1, i);
        if (i == 11) { adv(ax[0], dl); adv(ab[0], dl); }
        SCHED();
      }
    };
    stage1(SLOT);
    barrier();
    TRACE3(2);
    int ph = 0;
#pragma unroll 1
    for (int c = 2; c <= 8; ++c) {
      const int dl = ph == 2 ? -2 * (int)SLOT : (int)SLOT;
      ph = ph == 2 ? 0 : ph + 1;
      SCHED();
#pragma unroll
      for (int i = 0; i < 16; ++i) {
        mm(1, i); SCHED();
        if (i < 10) rd(0, i);
        if (i == 11) { adv(ax[1], dl); adv(ab[1], dl); }
        SCHED();
      }
      wait_lgkm0();
      stage1(ph == 2 ? -2 * (int)SLOT : (int)SLOT);
      TRACE(2 * c);
      barrier();
      TRACE(2 * c + 1); TRACE3(c + 1);
    }
    __builtin_amdgcn_s_setprio(LPRIO);
    STAMP(4);
    f32x2 rs[16], mrs[16];
    float csr, csi;
#pragma unroll
    for (int i = 0; i < 16; ++i) {
      mm(1, i); SCHED();
      if (i == 0) { csr = ldsf1(csa); csi = ldsf1(csa + 512); }
      if (i < 8) { const f32x4 t = ldsf4(sa + i * 16); rs[i] = lo2(t); mrs[i] = hi2(t); }
      SCHED();
    }
    const f32x2 ncr = bco(-csr), nci = bco(-csi);
    STAMP(5);
    f32x2 ur[16], ui[16], Er, Ei;
    SCHED();
#pragma unroll
    for (int s = 0; s < 16; ++s) {
      if (s < 8) { const f32x4 t = ldsf4(sa + (s + 8) * 16); rs[s + 8] = lo2(t); mrs[s + 8] = hi2(t); SCHED(); }
      ur[s] = mrs[s] * ncr; SCHED();
      ui[s] = mrs[s] * nci; SCHED();
      ur[s] = pfma((s & 1) ? hi2(acr[s >> 1]) : lo2(acr[s >> 1]), rs[s], ur[s]); SCHED();
      ui[s] = pfma((s & 1) ? hi2(aci[s >> 1]) : lo2(aci[s >> 1]), rs[s], ui[s]); SCHED();
      if (s == 0) { Er = ur[0]; Ei = ui[0]; }
      else {
        const f32x2 t0 = pfma(Ei, -ai, ur[s]); SCHED();
        const f32x2 t1 = pfma(Er, ai, ui[s]); SCHED();
        Er = pfma(Er, ar, t0); SCHED();
        Ei = pfma(Ei, ar, t1); SCHED();
      }
    }
    TRACE2(1);
    const f32x2 E0 = {Er[0], Ei[0]};
    const f32x2 E = cfma(a16, E0, (f32x2){Er[1], Ei[1]});
    f32x2 P = {0.f, 0.f}, Tt = {0.f, 0.f};
    float er4[4], ei4[4];
    rows4(E[0], er4); rows4(E[1], ei4);
#pragma unroll
    for (int s = 0; s < 4; ++s) {
      const f32x2 e = {er4[s], ei4[s]};
      P = csel(s == g, Tt, P);
      Tt = s ? cfma(a32, Tt, e) : e;
    }
    const unsigned totb = WS_TOT + (unsigned)(j * 2 + d) * 4096;
    if (g == 0) __builtin_amdgcn_raw_buffer_store_b128((u32x4){tagA, f2u(Tt[0]), tagB, f2u(Tt[1])}, wsr, totb + r * 256 + i16 * 16, 0, 16);
    STAMP(6);
    TRACE2(2);
    {
      const f32x2 kr = bco(kk[0]), nki = bco(-kk[1]);
#pragma unroll
      for (int s = 0; s < 16; ++s) {
        const f32x2 t0 = ur[s] * kr; SCHED();
        const f32x2 t1 = ur[s] * nki; SCHED();
        ur[s] = pfma(ui[s], nki, t0); SCHED();
        ui[s] = pfma(ui[s], -kr, t1); SCHED();
      }
    }
#pragma unroll
    for (int s = 0; s < 16; s += 4) asm volatile("" : "+v"(ur[s]), "+v"(ui[s]), "+v"(ur[s + 1]), "+v"(ui[s + 1]), "+v"(ur[s + 2]), "+v"(ui[s + 2]), "+v"(ur[s + 3]), "+v"(ui[s + 3]));
    f32x4 hr4[8], hi4[8];
    {
      const f32x2 kP = cmul(kk, P);
      scan(kP, cfma(a16, kP, cmul(kk, E0)), ur, ui, [&](int q, f32x4 a, f32x4 b) { hr4[q] = a; hi4[q] = b; asm volatile("" : "+v"(hr4[q]), "+v"(hi4[q])); });
    }
    TRACE2(3);
    prog_wait(PW + (6 + d) * 4, 1);
#pragma unroll
    for (int i = 0; i < 5; ++i) absorb(i, home + i * 4096);
    unsigned v2[4], vt[4];
    mkv2(v2);
#pragma unroll
    for (int i = 0; i < 4; ++i) { vt[i] = (4 * i + g < r ? g * 256 : -i * 1024) + i16 * 16 + 4096; asm volatile("" : "+v"(vt[i]), "+v"(v2[i])); }
    {
      const i32x4 tr = {wss[0], wss[1], (int)(totb + r * 256), wss[3]};
#pragma unroll 1
      for (int spin = 0; spin < 64; ++spin) {
        int n, t[15];
        asm volatile("s_buffer_load_dword %1, %16, %17 offset:0 glc\n\ts_buffer_load_dword %2, %16, %17 offset:256 glc\n\ts_buffer_load_dword %3, %16, %17 offset:512 glc\n\ts_buffer_load_dword %4, %16, %17 offset:768 glc\n\ts_buffer_load_dword %5, %16, %17 offset:1024 glc\n\ts_buffer_load_dword %6, %16, %17 offset:1280 glc\n\ts_buffer_load_dword %7, %16, %17 offset:1536 glc\n\ts_buffer_load_dword %8, %16, %17 offset:1792 glc\n\ts_buffer_load_dword %9, %16, %17 offset:2048 glc\n\ts_buffer_load_dword %10, %16, %17 offset:2304 glc\n\ts_buffer_load_dword %11, %16, %17 offset:2560 glc\n\ts_buffer_load_dword %12, %16, %17 offset:2816 glc\n\ts_buffer_load_dword %13, %16, %17 offset:3072 glc\n\ts_buffer_load_dword %14, %16, %17 offset:3328 glc\n\ts_buffer_load_dword %15, %16, %17 offset:3584 glc\n\t"
                     "s_mov_b32 %0, 0\n\ts_waitcnt lgkmcnt(0)\n\t"
                     "s_cmp_eq_u32 %1, %18\n\ts_addc_u32 %0, %0, 0\n\ts_cmp_eq_u32 %2, %18\n\ts_addc_u32 %0, %0, 0\n\ts_cmp_eq_u32 %3, %18\n\ts_addc_u32 %0, %0, 0\n\ts_cmp_eq_u32 %4, %18\n\ts_addc_u32 %0, %0, 0\n\ts_cmp_eq_u32 %5, %18\n\ts_addc_u32 %0, %0, 0\n\ts_cmp_eq_u32 %6, %18\n\ts_addc_u32 %0, %0, 0\n\ts_cmp_eq_u32 %7, %18\n\ts_addc_u32 %0, %0, 0\n\ts_cmp_eq_u32 %8, %18\n\ts_addc_u32 %0, %0, 0\n\ts_cmp_eq_u32 %9, %18\n\ts_addc_u32 %0, %0, 0\n\ts_cmp_eq_u32 %10, %18\n\ts_addc_u32 %0, %0, 0\n\ts_cmp_eq_u32 %11, %18\n\ts_addc_u32 %0, %0, 0\n\ts_cmp_eq_u32 %12, %18\n\ts_addc_u32 %0, %0, 0\n\ts_cmp_eq_u32 %13, %18\n\ts_addc_u32 %0, %0, 0\n\ts_cmp_eq_u32 %14, %18\n\ts_addc_u32 %0, %0, 0\n\ts_cmp_eq_u32 %15, %18\n\ts_addc_u32 %0, %0, 0"
                     : "=&s"(n), "=&s"(t[0]), "=&s"(t[1]), "=&s"(t[2]), "=&s"(t[3]), "=&s"(t[4]), "=&s"(t[5]), "=&s"(t[6]), "=&s"(t[7]), "=&s"(t[8]), "=&s"(t[9]), "=&s"(t[10]), "=&s"(t[11]), "=&s"(t[12]), "=&s"(t[13]), "=&s"(t[14]) : "s"(tr), "s"(totb), "s"(tagA) : "scc", "memory");
        if (n >= r) break;
        COUNT(20);
      }
    }
    STAMP(7);
    auto fetch = [&]() {
      asm volatile("s_mov_b32 m0, %5\n\ts_nop 0\n\tglobal_load_lds_dwordx4 %0, %4 sc1\n\tglobal_load_lds_dwordx4 %1, %4 offset:1024 sc1\n\t"
                   "global_load_lds_dwordx4 %2, %4 offset:2048 sc1\n\tglobal_load_lds_dwordx4 %3, %4 offset:3072 sc1"
                   ::"v"(vt[0]), "v"(vt[1]), "v"(vt[2]), "v"(vt[3]), "s"(p.ws + totb - 4096), "s"(home + 16384) : "memory");
    };
    wait_lgkm0();
    if (r > 0) fetch();
    TRACE2(14);
    if (!YPRE) resp();
    f32x2 wt[4];
#pragma unroll
    for (int i = 3; i >= 0; --i) {
      const bool v = 4 * i + g < r;
      wt[i] = csel(v, acar, (f32x2){0.f, 0.f});
      if (i) acar = csel(v, cmul(acar, a512), acar);
      asm volatile("" : "+v"(wt[i]));
    }
    setup2(6);
    unsigned ho = WS_H + (unsigned)(2 * r) * H_MT + (unsigned)(56 + 2 * j + d) * 4096 + lane * 16, ta = home + 16384 + lane * 16;
    asm volatile("" : "+v"(ho), "+v"(ta), "+v"(m0), "+v"(m1));
    TRACE2(4);
    wait_vm<0>();
    TRACE2(15);
    float cr = 0.f, ci = 0.f;
    if (r > 0) {
#pragma unroll 1
      for (int spin = 0; spin < SPIN_CAP; ++spin) {
        u32x4 tv[4];
        unsigned e = 0;
#pragma unroll
        for (int i = 0; i < 4; ++i) tv[i] = *(volatile LAS u32x4*)(size_t)(ta + i * 1024);
        f32x2 c[2];
#pragma unroll
        for (int i = 0; i < 4; ++i) {
          const f32x2 t = {u2f(tv[i].y), u2f(tv[i].w)};
          c[i & 1] = i < 2 ? cmul(wt[i], t) : cfma(wt[i], t, c[i & 1]);
          e = __builtin_amdgcn_bitop3_b32(tv[i].z, tagB, __builtin_amdgcn_bitop3_b32(tv[i].x, tagA, e, 0xBE), 0xBE);
        }
        const u32x2 h = swap32(sadd(c[0][0], c[1][0]), sadd(c[0][1], c[1][1]));
        const float x = sadd(u2f(h.x), u2f(h.y));
        const u32x2 h2 = swap16(x, x);
        const float y = sadd(u2f(h2.x), u2f(h2.y));
        const u32x2 h3 = swap32(y, y);
        cr = u2f(h3.x); ci = u2f(h3.y);
        if (__builtin_expect(__builtin_amdgcn_ballot_w64(e != 0) == 0, 1)) break;
        COUNT(20);
        __builtin_amdgcn_s_sleep(8);
        fetch();
        wait_vm<0>();
      }
    }
    STAMP(8);
    {
      const unsigned c0 = pk2(cr, cr), c1 = pk2(ci, ci), c2 = pk2(-ci, -ci);
      const s16x8 are = __builtin_bit_cast(s16x8, (u32x4){m0 & c0, m1 & c0, m0 & c1, m1 & c1}), aim = __builtin_bit_cast(s16x8, (u32x4){m0 & c2, m1 & c2, m0 & c0, m1 & c0});
      u32x4 o[8];
      SCHED();
#pragma unroll
      for (int q = 0; q < 10; ++q) {
        if (q < 8) {
          hr4[q] = mfma(are, __builtin_bit_cast(s16x8, yb[q]), hr4[q]);
          hi4[q] = mfma(aim, __builtin_bit_cast(s16x8, yb[q]), hi4[q]);
          SCHED();
        }
        if (q >= 2) { o[q - 2] = pack(hr4[q - 2], hi4[q - 2]); __builtin_amdgcn_raw_buffer_store_b128(o[q - 2], wsr, ho + ((q - 2) & 3) * 1024, ((q - 2) >> 2) * H_MT, HST); }
        SCHED();
      }
      TRACE2(5);
      dma_x4(v2, w2b + kstep_of(slot, 5) * 32768, home + 16384);
      asm volatile("" :: "v"(o[0]), "v"(o[1]), "v"(o[2]), "v"(o[3]), "v"(o[4]), "v"(o[5]), "v"(o[6]), "v"(o[7]), "v"(ho));
      STAMP(19);
      pubo = lane < 2 ? WS_FLAG + (2 * r + lane) * 2048 + 1024 + (2 * j + d) * 8 : pubo;
    }
    STAMP(18);
  };
  auto roleB = [&]() __attribute__((always_inline)) {
    mktag();
    {
      float s0 = 0.f, s1 = 0.f;
#pragma unroll
      for (int e = 0; e < 8; ++e) { s0 = sadd(s0, bflo(prb[e >> 2][e & 3])); s1 = sadd(s1, bfhi(prb[e >> 2][e & 3])); }
      *(LAS float*)(size_t)(LB + L_LS + 512 + (4 * d + g) * 4) = row16_sum(s0 + s1);
      __builtin_amdgcn_s_waitcnt(0x0F70);
    }
    if (mlp) {
      f32x4 acc[6][4];
#pragma unroll
      for (int s = 0; s < 6; ++s) {
#pragma unroll
        for (int m = 0; m < 4; ++m) acc[s][m] = (f32x4){0, 0, 0, 0};
        asm volatile("" : "+v"(acc[s][0]), "+v"(acc[s][1]), "+v"(acc[s][2]), "+v"(acc[s][3]));
      }
      unsigned ax[2], aw[6];
      ax[0] = LB + (64 * d + i16) * 128 + kx; ax[1] = ax[0] ^ 64;
#pragma unroll
      for (int s = 0; s < 6; ++s) aw[s] = awb + ((s ^ awx) * 32);
      unsigned sta = LB + L_LS + qq * 288 + (8 * d + ((i16 >> 1) & 1)) * 16 + (i16 & 1) * 4, csa = LB + L_CS + g * 16;
      asm volatile("" : "+v"(ax[0]), "+v"(ax[1]), "+v"(aw[0]), "+v"(aw[1]), "+v"(aw[2]), "+v"(aw[3]), "+v"(aw[4]), "+v"(aw[5]), "+v"(sta), "+v"(csa));
      s16x8 xf[2][4];
      s16x4 wf[2][12];
      auto rd = [&](int set, int i) {
        if (i < 4) xf[set][i] = lds128(ax[set] + i * 2048);
        else wf[set][i - 4] = ldstr4(aw[(i - 4) >> 1] + set * 8192 + (i & 1) * 1024);
      };
      auto mm = [&](int set, int i) { const int s = i >> 2, m = i & 3; acc[s][m] = mfma(cat(wf[set][2 * s], wf[set][2 * s + 1]), xf[set][m], acc[s][m]); };
      {
        float t[8];
#pragma unroll
        for (int e = 0; e < 8; ++e)
          t[e] = (e & 1) ? sadd(sadd(bfhi(pre[0][e >> 1]), bfhi(pre[1][e >> 1])), sadd(bfhi(pre[2][e >> 1]), bfhi(pre[3][e >> 1])))
                         : sadd(sadd(bflo(pre[0][e >> 1]), bflo(pre[1][e >> 1])), sadd(bflo(pre[2][e >> 1]), bflo(pre[3][e >> 1])));
#pragma unroll
        for (int e = 0; e < 4; ++e) { const u32x2 h = swap32(t[e], t[e + 4]); t[e] = sadd(u2f(h.x), u2f(h.y)); }
#pragma unroll
        for (int e = 0; e < 2; ++e) { const u32x2 h = swap16(t[e], t[e + 2]); t[e] = dpp_add<0x128>(sadd(u2f(h.x), u2f(h.y))); }
        *(LAS f32x2*)(size_t)(LB + L_LS + d * 256 + (4 * (lane & 7) + g) * 8) = (f32x2){t[0], t[1]};
      }
      __builtin_amdgcn_s_setprio(1);
      STAMP(1);
      barrier();
      STAMP(2); STAMPC(21);
#pragma unroll
      for (int i = 0; i < 16; ++i) rd(0, i);
      wait_lgkm0();
      auto stage1 = [&](int c, int dl) {
        SCHED();
#pragma unroll
        for (int i = 0; i < 24; ++i) {
          mm(0, i); SCHED();
          if (i < 16) rd(1, i);
          else if (i < 22) adv(aw[i - 16], dl);
          else if (i == 22) adv(ax[0], dl);
          SCHED();
        }
        TRACE(2 * c);
        barrier();
        TRACE(2 * c + 1);
        SCHED();
      };
      int ph = 0;
#pragma unroll 1
      for (int c = 1; c < 8; ++c) {
        const int dl = ph == 2 ? -2 * (int)SLOT : (int)SLOT;
        ph = ph == 2 ? 0 : ph + 1;
        stage1(c, dl);
#pragma unroll
        for (int i = 0; i < 24; ++i) {
          mm(1, i); SCHED();
          if (i < 16) rd(0, i);
          else if (i == 16) adv(ax[1], dl);
          else if (i == 23) wait_lgkm0();
          SCHED();
        }
      }
      stage1(8, 0);
      unsigned b1a = LB + L_B1 + g * 8;
      asm volatile("" : "+v"(b1a));
      STAMP(4); STAMPC(22);
      f32x2 rs[4], nm[4];
      f32x4 cs[6];
      u32x2 b1v[6];
      f32x2 st[4];
      auto colrd = [&](int s) { cs[s] = ldsf4(csa + 64 * s); b1v[s] = *(LAS u32x2*)(size_t)(b1a + 32 * s); };
#pragma unroll
      for (int i = 0; i < 4; ++i) st[i] = (f32x2){ldsf1(sta + i * 32), ldsf1(sta + i * 32 + 8)};
      colrd(0); colrd(1);
      SCHED();
#pragma unroll
      for (int i = 0; i < BSPLIT; ++i) mm(1, i);
      SCHED();
#pragma unroll
      for (int i = 0; i < 4; ++i) { rs[i] = bco(st[i][0]); nm[i] = bco(-st[i][1]); }
      STAMP(5);
      const unsigned fo = WS_FLAG + (2 * r + d) * 2048;
      pubo = lane ? pubo : 0u;
      u32x4 ob[12];
      const unsigned ho = WS_H + (unsigned)(2 * r + d) * H_MT + (unsigned)(4 * jm) * 4096 + lane * 16;
#pragma unroll
      for (int pr = 0; pr < 3; ++pr) {
        Col col;
        mkcol(col, cs[2 * pr], cs[2 * pr + 1], b1v[2 * pr], b1v[2 * pr + 1]);
        if (pr < 2) { colrd(2 * pr + 2); colrd(2 * pr + 3); }
        SCHED();
        if (pr == 1) {
#pragma unroll
          for (int i = BSPLIT; i < 24; ++i) mm(1, i);
          SCHED();
          STAMP(7);
        }
#pragma unroll
        for (int m = 0; m < 4; ++m) {
          ob[4 * pr + m] = gelu8(acc[2 * pr][m], acc[2 * pr + 1][m], col, rs[m], nm[m]);
          __builtin_amdgcn_raw_buffer_store_b128(ob[4 * pr + m], wsr, ho + m * 1024, pr * 4096, HST);
          if (4 * pr + m >= KEEP) asm volatile("" :: "v"(ob[4 * pr + m - KEEP]));
          TRACE2(1 + 4 * pr + m);
          if (pr > 0 && m == (pr == 1 ? BFLAG0 : BFLAG)) {
            if (pr == 1) wait_vm<BFLAG0 + 1>(); else wait_vm<BFLAG + 1>();
            __builtin_amdgcn_raw_buffer_store_b64((u32x2){tagA, tagB}, wsr, pubo, fo + flag_of(jm, pr - 1), 16);
          }
          SCHED();
        }
      }
      pubo |= fo + flag_of(jm, 2);
      STAMP(6);
    } else {
      const int q = 4 * r + 2 * lq + d;
      const unsigned vt = (lane < 56 ? lane : 0) * 128, dm = LB + L_DUMMY + d * 256;
#pragma unroll 1
      for (int c = 0; c < 9; ++c) {
        if (TOUCH == 7) {
          const int t = (c - TST) * TPP;
#pragma unroll
          for (int e = 0; e < TPP; ++e) {
            if (t + e >= 0 && t + e < 4) dma4w(vt, p.w2 + ((t + e) * 64 + q) * 7168, dm);
            else if (t + e == 4 || t + e == 5) dma4w(lane * 128, (t + e == 4 ? p.cre : p.cim) + q * 8192, dm);
          }
        }
        barrier();
      }
    }
    prog_wait(PW + (6 + d) * 4, 3);
#pragma unroll
    for (int i = 0; i < 5; ++i) absorb(i, home + i * 4096);
    unsigned v2[4];
    mkv2(v2);
    setup2(6);
#pragma unroll
    for (int i = 0; i < 4; ++i) asm volatile("" : "+v"(v2[i]));
    wait_lgkm0();
    STAMP(8);
    dma_x4(v2, w2b + kstep_of(slot, 5) * 32768, home + 16384);
  };
  auto roleA = [&]() __attribute__((always_inline)) {
    const int srow = 128 * r + 8 * j + 4 * d + g;
    u32x2 dv[4], bv[4];
#pragma unroll
    for (int n = 0; n < 4; ++n) { dv[n] = *(const GAS u32x2*)(p.dsk + ntile * 128 + n * 32 + g * 8); bv[n] = *(const GAS u32x2*)(p.b2 + ntile * 128 + n * 32 + g * 8); }
    if (lane < 8) *(LAS unsigned*)(size_t)(PW + lane * 4) = 0u;
    f32x4 acc[2][4];
#pragma unroll
    for (int m = 0; m < 4; ++m) { acc[0][m] = (f32x4){0, 0, 0, 0}; acc[1][m] = (f32x4){0, 0, 0, 0}; asm volatile("" : "+v"(acc[0][m]), "+v"(acc[1][m])); }
    unsigned ax0 = LB + (64 * d + i16) * 128 + kx, ax1 = ax0 ^ 64, aw0 = awb + ((6 ^ awx) * 32), aw1 = awb + ((7 ^ awx) * 32);
    unsigned sta = LB + L_LS + qq * 288 + (8 * d + ((i16 >> 1) & 1)) * 16 + (i16 & 1) * 4, csa = LB + L_CS + 384 + g * 16;
    asm volatile("" : "+v"(ax0), "+v"(ax1), "+v"(aw0), "+v"(aw1), "+v"(sta), "+v"(csa));
    int ph = 0;
    auto chunk = [&](auto side) {
      s16x8 xf[2][4], wf[2][2];
      if (ADELAY) __builtin_amdgcn_s_sleep(ADELAY);
#pragma unroll
      for (int k = 0; k < 2; ++k) {
        wf[k][0] = ldstr(aw0 + k * 8192, aw0 + k * 8192 + 1024);
        wf[k][1] = ldstr(aw1 + k * 8192, aw1 + k * 8192 + 1024);
#pragma unroll
        for (int m = 0; m < 4; ++m) xf[k][m] = lds128((k ? ax1 : ax0) + m * 2048);
      }
      SCHED();
      const int dl = ph == 2 ? -2 * (int)SLOT : (int)SLOT;
      ph = ph == 2 ? 0 : ph + 1;
      adv(ax0, dl); adv(ax1, dl); adv(aw0, dl); adv(aw1, dl);
      side(8);
      SCHED();
#pragma unroll
      for (int k = 0; k < 2; ++k)
#pragma unroll
        for (int m = 0; m < 4; ++m) {
          acc[0][m] = mfma(wf[k][0], xf[k][m], acc[0][m]); acc[1][m] = mfma(wf[k][1], xf[k][m], acc[1][m]);
          SCHED();
          side(4 * k + m);
          SCHED();
        }
    };
    auto none = [](int) {};
    mktag();
    asm volatile("" : "+v"(pre[3]));
    STAMP(19);
    {
      const float mean = sum32(pre) * (1.f / 512.f);
      float v0 = 0.f, v1 = 0.f;
#pragma unroll
      for (int i = 0; i < 4; ++i)
#pragma unroll
        for (int e = 0; e < 4; ++e) {
          const float d0 = bflo(pre[i][e]) - mean, d1 = bfhi(pre[i][e]) - mean;
          v0 = sfma(d0, d0, v0); v1 = sfma(d1, d1, v1);
        }
      const float rsd = __builtin_amdgcn_rsqf(row16_sum(v0 + v1) * (1.f / 512.f) + 1e-5f);
      if (i16 == 0) __builtin_amdgcn_raw_buffer_store_b128((u32x4){tagA, f2u(rsd), tagB, f2u(mean * rsd)}, wsr, WS_STAT + srow * 16, 0, 16);
    }
    __builtin_amdgcn_s_setprio(APRIO);
    STAMP(1);
    barrier();
    STAMP(2);
    chunk(none);
    if (d) {
      const f32x2 c0 = ldsf2(LB + L_LS + 512 + (lane & 3) * 8);
      if (lane < 4) __builtin_amdgcn_raw_buffer_store_b128((u32x4){tagA, f2u(sadd(c0[0], c0[1])), tagB, 0u}, wsr, WS_BS + j * 1024 + (4 * r + lane) * 16, 0, 16);
    } else if (mlp) {
      const f32x2 c0 = ldsf2(LB + L_LS + (lane & 31) * 8), c1 = ldsf2(LB + L_LS + 256 + (lane & 31) * 8);
      if (lane < 32) __builtin_amdgcn_raw_buffer_store_b128((u32x4){tagA, f2u(sadd(c0[0], c1[0])), tagB, f2u(sadd(c0[1], c1[1]))}, wsr, WS_CS + (jm * 16 + r) * 512 + lane * 16, 0, 16);
    }
    barrier();
#pragma unroll 1
    for (int c = 1; c < 3; ++c) { if (!ASKIP || mlp) chunk(none); barrier(); }
    chunk(none);
    asm volatile("" : "+v"(dv[0]), "+v"(dv[1]), "+v"(dv[2]), "+v"(dv[3]), "+v"(bv[0]), "+v"(bv[1]), "+v"(bv[2]), "+v"(bv[3]));
    __builtin_amdgcn_s_waitcnt(0x0F70);
    u32x2 xv[2][4];
#pragma unroll
    for (int m = 0; m < 2; ++m)
#pragma unroll
      for (int n = 0; n < 4; ++n) xv[m][n] = *(LAS u32x2*)(size_t)(HB + 3 * SLOT + 12288 + (16 * m + i16) * 128 + (g & 1) * 8 + (((2 * n + (g >> 1)) ^ (i16 >> 1)) * 16));
    barrier();
#pragma unroll 1
    for (int c = 4; c < 6; ++c) { if (!ASKIP || mlp) chunk(none); barrier(); }
    f32x2 st[4];
#pragma unroll
    for (int m = 0; m < 4; ++m) st[m] = (f32x2){ldsf1(sta + m * 32), ldsf1(sta + m * 32 + 8)};
    unsigned ra[4], ho = mlp ? WS_H + (unsigned)(2 * r + d) * H_MT + (unsigned)(4 * jm + 3) * 4096 + lane * 16 : 0xffff0000u;
    f32x2 rs[4], nm[4];
    chunk([&](int k) {
      if (k < 4) { rs[k] = bco(st[k][0]); nm[k] = bco(-st[k][1]); asm volatile("" : "+v"(rs[k]), "+v"(nm[k])); }
      if (k < 8) return;
#pragma unroll
      for (int n = 0; n < 4; ++n) ra[n] = home + r2l + ((n ^ r2x) * 32);
      asm volatile("" : "+v"(ra[0]), "+v"(ra[1]), "+v"(ra[2]), "+v"(ra[3]), "+v"(ho));
    });
    TRACE(14); barrier(); TRACE(15);
    STAMP(4);
    const f32x4 cs0 = ldsf4(csa), cs1 = ldsf4(csa + 64);
    const u32x2 b10 = *(LAS u32x2*)(size_t)(LB + L_B1 + 192 + g * 8), b11 = *(LAS u32x2*)(size_t)(LB + L_B1 + 224 + g * 8);
    Col col;
    auto take = [&](int i, int pg) {
#pragma unroll
      for (int n = 0; n < 4; ++n) we[i][n] = ldstr(ra[n] + pg * 4096, ra[n] + pg * 4096 + 2048);
    };
    chunk([&](int k) {
      if (k == 2) { mkcol(col, cs0, cs1, b10, b11); asm volatile("" : "+v"(col.bias[0]), "+v"(col.bias[1]), "+v"(col.bias[2]), "+v"(col.bias[3])); }
      if (k >= 3 && k < 6) take(k - 3, k - 3);
    });
    barrier();
    __builtin_amdgcn_s_setprio(2);
    STAMP(5);
    u32x4 oa[4];
#pragma unroll
    for (int m = 0; m < 4; ++m) {
      TRACE2(1 + 2 * m);
      oa[m] = gelu8(acc[0][m], acc[1][m], col, rs[m], nm[m]);
      TRACE2(2 + 2 * m);
      __builtin_amdgcn_raw_buffer_store_b128(oa[m], wsr, ho + m * 1024, 0, HST);
      if (m >= 1) asm volatile("" :: "v"(oa[m - 1]));
      if (m == 0) { take(3, 3); take(4, 4); }
      SCHED();
    }
    STAMP(6);
    prog_wait(PW + (6 + d) * 4, 2);
#pragma unroll
    for (int i = 5; i < 8; ++i) take(i, i - 5);
    u32x4 s2v[2];
#pragma unroll
    for (int m = 0; m < 2; ++m) s2v[m] = *(LAS u32x4*)(size_t)(LB + L_ST2 + (tok + 8 * (mtile & 1) + 4 * d + 2 * m) * 16);
    setup2(8);
    wait_lgkm0();
    asm volatile("" :: "v"(oa[3]));
    prog_set(PW + w * 4, 1);
    snap(false);
#pragma unroll
    for (int m = 0; m < 2; ++m) {
      const u32x4 s2 = s2v[m];
      const f32x2 r2 = bco(u2f(s2.y)), n2 = bco(-u2f(s2.w));
#pragma unroll
      for (int n = 0; n < 4; ++n) {
        f32x2 o[2];
#pragma unroll
        for (int h = 0; h < 2; ++h) {
          const f32x2 xx = {bflo(xv[m][n][h]), bfhi(xv[m][n][h])};
          o[h] = pfma(pfma(xx, r2, n2), (f32x2){bflo(dv[n][h]), bfhi(dv[n][h])}, xx + (f32x2){bflo(bv[n][h]), bfhi(bv[n][h])});
        }
        acc2[n][m] = __builtin_shufflevector(o[0], o[1], 0, 1, 2, 3);
        acc2[n][2 + m] = (f32x4){0, 0, 0, 0};
        asm volatile("" : "+v"(acc2[n][m]), "+v"(acc2[n][2 + m]));
        if (4 * m + n == APUB) {
          SCHED();
          wait_vm<0>();
          __builtin_amdgcn_raw_buffer_store_b64((u32x2){tagA, tagB}, wsr, mlp && lane == 0 ? 0u : pubo, WS_FLAG + (2 * r + d) * 2048 + flag_of(jm, 3), 16);
          SCHED();
        }
      }
    }
    seen();
    STAMP(8);
  };

  unsigned tw[2][2], ra[4][2];
  u32x4 sl[2];
  char* op;
  auto mktail = [&]() {
    int tl = threadIdx.x;
    asm volatile("" : "+v"(tl));
    const int ck = tl & 15, gt = (tl >> 4) & 3, hw = tl >> 6;
#pragma unroll
    for (int h = 0; h < 2; ++h) {
      tw[0][h] = home + ck * 128 + (((4 * h + gt) ^ (ck & 7)) * 16) + (w == 1 ? 4096 : 0);
      tw[1][h] = tw[0][h] + (w == 1 ? -4096 : 4096);
      asm volatile("" : "+v"(tw[0][h]), "+v"(tw[1][h]));
    }
#pragma unroll
    for (int q = 0; q < 4; ++q) {
      ra[q][0] = LB + gt * HALF + (8 * hw + 2 * q + (ck >> 3)) * 128 + (((ck & 7) ^ (2 * q + (ck >> 3))) * 16);
      ra[q][1] = ra[q][0] + 4 * HALF;
      asm volatile("" : "+v"(ra[q][0]), "+v"(ra[q][1]));
    }
#pragma unroll
    for (int e = 0; e < 8; ++e) sl[e >> 2][e & 3] = (ck >> 1) == e ? ((ck & 1) ? 0x3f800000u : 0x3f80u) : 0u;
    asm volatile("" : "+v"(sl[0]), "+v"(sl[1]));
    const int orow = 64 * (hw & 1) + 16 * (ck >> 3) + 2 * (hw >> 1) + (gt >> 1) + 8 * (mtile & 1);
    op = p.out + (128 * (mtile >> 1) + orow) * 2048 + ntile * 256 + ((ck & 4) * 8 + (gt & 1) * 16 + (ck & 3) * 4) * 4;
    asm volatile("" : "+v"(op));
  };
  auto phase2 = [&](auto nm_) __attribute__((always_inline)) {
    STAMP(9);
    constexpr int NM = decltype(nm_)::value, S0 = NM == 8 ? 0 : 5, N = NM == 8 ? 13 : 9;
    const u64 tag = ((u64)tagB << 32) | tagA;
    auto has = [&](int k) { return fl[k < NM ? k : k - NM + S0] == tag; };
    auto need = [&](int k) {
      if (__builtin_expect(!has(k), 0)) {
#pragma unroll 1
        for (int spin = 1; spin < SPIN_CAP; ++spin) {
          COUNT(20);
          snap(k >= NM);
          if (has(k)) break;
          if (!(spin & 31)) {
            const u32x2 f = __builtin_amdgcn_raw_buffer_load_b64(wsr, 0, k < NM ? fm + 8 * k : fs + 8 * (k - NM + S0), 16);
            if (__builtin_amdgcn_readfirstlane(f.x) == tagA && __builtin_amdgcn_readfirstlane(f.y) == tagB) break;
          }
          __builtin_amdgcn_s_sleep(PSLEEP);
        }
      }
      asm volatile("" ::: "memory");
    };
    auto publish = [&]() { __builtin_amdgcn_raw_buffer_store_b64((u32x2){tagA, tagB}, wsr, pubo, 0, 16); };
    auto loadH = [&](int i, int m) -> u32x4 {
      if (m == 0) asm volatile("" : "+v"(hb));
      if ((NOC & 8) && (m & 1)) { u32x4 z; asm volatile("" : "=v"(z)); return z; }
      return __builtin_amdgcn_raw_buffer_load_b128(wsr, hb + (m & 1) * 1024, __builtin_amdgcn_readlane(ksl, i) ^ ((m & 2) * 1024), HAUX);
    };
    u32x4 hf[N][4];
    auto load4 = [&](int i) {
#pragma unroll
      for (int m = 0; m < 4; ++m) hf[i][m] = loadH(i, m);
    };
    STAMP(10);
    if (NM == 8) {
      __builtin_amdgcn_s_setprio(EPRI);
      need(0); load4(0);
      need(1); load4(1);
    } else {
      unsigned vc[8];
      mkvc(vc);
      asm volatile("" : "+v"(vc[0]), "+v"(vc[1]), "+v"(vc[2]), "+v"(vc[3]), "+v"(vc[4]), "+v"(vc[5]), "+v"(vc[6]), "+v"(vc[7]));
      const unsigned o = ntile * 65536 + kg * 128, hs = fresh(HB + (w < 4 ? SLOT : 0));
      if (LPUB) { wait_vm<4>(); publish(); }
      __builtin_amdgcn_s_setprio(0);
      dma_x8(vc, p.cre + o, hs);
      if (!LPUB) { wait_vm<12>(); publish(); }
      snap(false);
      dma_x8(vc, p.cim + o, hs + 8192);
      seen();
      need(0); load4(0);
      need(1); load4(1);
    }
    STAMP(11);
    s16x8 cfr[N - NM][4];
    auto rdc = [&](int q, unsigned base) {
#pragma unroll
      for (int n = 0; n < 4; ++n) {
        unsigned a = base + n * 2048;
        asm volatile("" : "+v"(a));
        cfr[q][n] = cat(*(LAS s16x4*)(size_t)a, *(LAS s16x4*)(size_t)(a + 8192));
      }
    };
    SCHED();
#pragma unroll
    for (int i = 0; i < N; ++i) {
      const int k = i + (i < NM - 2 ? 2 : SDEP);
      if (i == 2) STAMP(12);
      if (!(i & 1) || k >= NM) TRACE4(2 * i + 1);
      if (i == NM - 2) {
        STAMP(13);
#pragma unroll
        for (int e = NM; e < NM + SDEP - 2; ++e) { need(e); load4(e); }
      }
      if (k < NM) {
        if (NM == 6 && i == 0) snap(false);
        else if (NM == 6 && i == 2) { need(2); load4(2); need(3); load4(3); }
        if (!(i & 1) && (NM == 8 || i)) { need(k); need(k + 1); TRACE4(2 * i + 2); if (NM == 6 && i == 2) absorb(5, home + 16384); if (!(ESEP && i == 0)) snap(k + 2 == NM); }
      } else if (k < N) { need(k); TRACE4(2 * i + 2); }
      if (i == NM - 2) STAMP(14);
      if (NM == 8 && i == 1) prog_set(PW + w * 4, 4);
      if (NM == 8 && i == 6) prog_wait(PW + (w | 6) * 4, 4);
      if (NM == 6 && i == 1) prog_set(PW + w * 4, 1);
      if (NM == 8 && i == EOFF) __builtin_amdgcn_s_setprio(0);
      if (NM == 8 && i == PSTEP) { prog_wait(PW + (w ^ 4) * 4, 1); rdc(4, crd - 2 * SLOT); }
      if (NM == 8 && i == PSTEP + 1) { wait_lgkm0(); prog_set(PW + w * 4, 5); }
      if (i >= NM - 1 && i < NM + (NM == 8 ? 3 : 2)) rdc(i + 1 - NM, crd ^ (32 * (i + 1 - NM + (NM == 8 ? 0 : 1))));
      if (NM == 6 && i == N - 1) prog_wait(PW + (w ^ 4) * 4, 5);
      auto put = [&](int m) {
#pragma unroll
        for (int h = 0; h < 2; ++h) {
          const f32x4 u = acc2[2 * h][m], v = acc2[2 * h + 1][m];
          *(LAS u32x4*)(size_t)(tw[m >> 1][h] + (m & 1) * 2048) = (u32x4){pk2(u[0], u[1]), pk2(u[2], u[3]), pk2(v[0], v[1]), pk2(v[2], v[3])};
        }
        SCHED();
      };
      SCHED();
#pragma unroll
      for (int m = 0; m < 4; ++m) {
#pragma unroll
        for (int n = 0; n < 4; ++n)
          if (NM == 8 && i == 0) asm volatile("v_mfma_f32_16x16x32_bf16 %0, %1, %2, %0" : "+v"(acc2[n][m]) : "v"(we[0][n]), "v"(hf[0][m]));
          else acc2[n][m] = mfma(i < NM ? we[i < NM ? i : 0][n] : cfr[i < NM ? 0 : i - NM][n], __builtin_bit_cast(s16x8, hf[i][m]), NM == 6 && i == 0 ? (f32x4){0, 0, 0, 0} : acc2[n][m]);
        SCHED();
        if (k < N && (NM == 8 || i > 1)) hf[k < N ? k : 0][m] = loadH(k, m);
        if (i == N - 2 && m == 0) mktail();
        if (i == N - 1 && m > 0) put(m - 1);
        SCHED();
      }
      if (i == N - 1) put(3);
      if (ESEP && NM == 8 && i < 2) TRACE4(3 + i);
      if (ESEP && NM == 8 && i == 1) snap(false);
      if ((i & 1) && k < NM) seen();
    }
    TRACE4(30);
  };
  if (__builtin_expect(w >= 6 || w < 2, 1)) {
    if (__builtin_expect(w >= 6, 1)) roleD(); else roleA();
    phase2(std::integral_constant<int, 8>{});
  } else {
    if (w >= 4) roleL(); else roleB();
    phase2(std::integral_constant<int, 6>{});
  }
  STAMP(15);
  const int tl = threadIdx.x;
  barrier();
  STAMP(16);
  {
    s16x8 pt[4][2];
#pragma unroll
    for (int q = 0; q < 4; ++q) { pt[q][0] = lds128(ra[q][0]); pt[q][1] = lds128(ra[q][1]); }
#pragma unroll
    for (int t = 0; t < 2; ++t) {
      f32x4 s = {0, 0, 0, 0};
#pragma unroll
      for (int k = 0; k < 4; ++k) s = mfma(__builtin_bit_cast(s16x8, sl[k >> 1]), pt[2 * t + (k >> 1)][k & 1], s);
      __builtin_nontemporal_store(s, (GAS f32x4*)(op + t * 65536));
    }
  }
  if (b == 255 && tl == 0) __builtin_amdgcn_raw_buffer_store_b64((u32x2){tagA, tagB}, wsr, WS_EPOCH, 0, 16);
}

extern "C" void kernel_launch(void* const* d_in, const int* in_sizes, int n_in, void* d_out, int out_size, void* d_ws,
                              size_t ws_size, hipStream_t stream) {
  Params p;
  p.x = (const char*)d_in[0]; p.lre = (const char*)d_in[1]; p.lim = (const char*)d_in[2];
  p.bre = (const char*)d_in[3]; p.bim = (const char*)d_in[4]; p.cre = (const char*)d_in[5]; p.cim = (const char*)d_in[6];
  p.logdt = (const char*)d_in[7]; p.dsk = (const char*)d_in[8];
  p.w1 = (const char*)d_in[10]; p.b1 = (const char*)d_in[11]; p.w2 = (const char*)d_in[12]; p.b2 = (const char*)d_in[13];
  p.out = (char*)d_out; p.ws = (char*)d_ws;
  fused_kernel<<<256, 512, 0, stream>>>(p);
}
```

```cpp
#include <hip/hip_runtime.h>
#include <stdint.h>
#include <type_traits>

typedef short s16x4 __attribute__((ext_vector_type(4)));
typedef short s16x8 __attribute__((ext_vector_type(8)));
typedef __bf16 bf16x8 __attribute__((ext_vector_type(8)));
typedef __bf16 bf16x2 __attribute__((ext_vector_type(2)));
typedef float f32x4 __attribute__((ext_vector_type(4)));
typedef float f32x2 __attribute__((ext_vector_type(2)));
typedef unsigned u32x4 __attribute__((ext_vector_type(4)));
typedef unsigned u32x2 __attribute__((ext_vector_type(2)));
typedef int i32x4 __attribute__((ext_vector_type(4)));
typedef unsigned long long u64;
typedef u64 u64x4 __attribute__((ext_vector_type(4)));
typedef u64 u64x8 __attribute__((ext_vector_type(8)));
typedef int i32x8 __attribute__((ext_vector_type(8)));
typedef int i32x16 __attribute__((ext_vector_type(16)));
__device__ i32x8 sbl8(i32x4, int, int) __asm("llvm.amdgcn.s.buffer.load.v8i32");
__device__ i32x16 sbl16(i32x4, int, int) __asm("llvm.amdgcn.s.buffer.load.v16i32");
#define LAS __attribute__((address_space(3)))
#define GAS __attribute__((address_space(1)))
#define DI __device__ __forceinline__

constexpr int NOFF = 0, PSLEEP = 4, BFLAG = 2, KROT = 0, LPRIO = 3, BSPLIT = 8, APRIO = 0, BFLAG0 = 1, HAUX = 16, SDEP = 2, KEEP = 2, PSTEP = 9, HST = 16, EPRI = 1, EOFF = 6, WOFF = 4, CPW = 5, TST = 1, TPP = 3, APUB = 3;

struct Params {
  const char *x, *lre, *lim, *bre, *bim, *cre, *cim, *logdt, *dsk, *w1, *b1, *w2, *b2;
  char *out, *ws;
};

constexpr unsigned WS_EPOCH = 0;
constexpr unsigned WS_STAT = 4096;
constexpr unsigned WS_TOT = WS_STAT + 2048 * 16;
constexpr unsigned WS_CS = WS_TOT + 16 * 2 * 16 * 256;
constexpr unsigned WS_BS = WS_CS + 16 * 16 * 512;
constexpr unsigned WS_FLAG = WS_BS + 16 * 1024;
constexpr unsigned WS_H = WS_FLAG + 32 * 2048;
constexpr unsigned H_MT = 88 * 4096;
constexpr unsigned WS_END = WS_H + 32 * H_MT;
constexpr unsigned SLOT = 40960, HALF = 20480, SL_W = 16384, SL_B = 32768;
constexpr unsigned L_ST1 = 3 * SLOT + 16384, L_ST2 = L_ST1 + 2048;
constexpr unsigned L_CS = 3 * SLOT + HALF + 16384, L_FLG = L_CS + 512, L_B1 = L_CS + 768, L_BS = L_CS + 1024, L_LS = L_CS + 2048, L_DUMMY = L_CS + 3328;
constexpr unsigned LDS_BYTES = 163840;
constexpr int SPIN_CAP = 40000;
constexpr unsigned CODE_BYTES = 45056;

DI void dma4w(unsigned voff, const char* sbase, unsigned ldsaddr) {
  asm volatile("s_mov_b32 m0, %2\n\ts_nop 0\n\tglobal_load_lds_dword %0, %1" ::"v"(voff), "s"(sbase), "s"(ldsaddr));
}
DI void dma_x4(const unsigned* v, const char* sbase, unsigned ldsaddr, bool low = false) {
  if (low) {
    asm volatile("s_mov_b32 m0, %5\n\ts_setprio 0\n\t"
               "global_load_lds_dwordx4 %0, %4\n\tglobal_load_lds_dwordx4 %1, %4 offset:1024\n\t"
               "global_load_lds_dwordx4 %2, %4 offset:2048\n\tglobal_load_lds_dwordx4 %3, %4 offset:3072\n\ts_setprio 3"
               ::"v"(v[0]), "v"(v[1]), "v"(v[2]), "v"(v[3]), "s"(sbase), "s"(ldsaddr));
    return;
  }
  asm volatile("s_mov_b32 m0, %5\n\ts_nop 0\n\t"
               "global_load_lds_dwordx4 %0, %4\n\tglobal_load_lds_dwordx4 %1, %4 offset:1024\n\t"
               "global_load_lds_dwordx4 %2, %4 offset:2048\n\tglobal_load_lds_dwordx4 %3, %4 offset:3072"
               ::"v"(v[0]), "v"(v[1]), "v"(v[2]), "v"(v[3]), "s"(sbase), "s"(ldsaddr));
}
DI void dma_x8(const unsigned* v, const char* sbase, unsigned ldsaddr) {
  asm volatile("s_mov_b32 m0, %9\n\ts_nop 0\n\t"
               "global_load_lds_dwordx4 %0, %8 offset:-4096\n\tglobal_load_lds_dwordx4 %1, %8 offset:-3072\n\t"
               "global_load_lds_dwordx4 %2, %8 offset:-2048\n\tglobal_load_lds_dwordx4 %3, %8 offset:-1024\n\t"
               "global_load_lds_dwordx4 %4, %8\n\tglobal_load_lds_dwordx4 %5, %8 offset:1024\n\t"
               "global_load_lds_dwordx4 %6, %8 offset:2048\n\tglobal_load_lds_dwordx4 %7, %8 offset:3072"
               ::"v"(v[0]), "v"(v[1]), "v"(v[2]), "v"(v[3]), "v"(v[4]), "v"(v[5]), "v"(v[6]), "v"(v[7]), "s"(sbase), "s"(ldsaddr + 4096u));
}
constexpr int PG(int i) { return i & 3; }
template <int N> DI void wait_vm() { asm volatile("s_waitcnt vmcnt(%0)" ::"i"(N) : "memory"); }
DI void wait_lgkm0() { asm volatile("" ::: "memory"); __builtin_amdgcn_s_waitcnt(0xC07F); asm volatile("" ::: "memory"); }
DI void barrier() {
  asm volatile("" ::: "memory");
  __builtin_amdgcn_s_waitcnt(0xC07F);
  __builtin_amdgcn_s_barrier();
  asm volatile("" ::: "memory");
}
#define SCHED() __builtin_amdgcn_sched_barrier(0)
DI s16x8 lds128(unsigned a) { return *(LAS s16x8*)(size_t)a; }
DI f32x4 ldsf4(unsigned a) { return *(LAS f32x4*)(size_t)a; }
DI f32x2 ldsf2(unsigned a) { return *(LAS f32x2*)(size_t)a; }
DI float ldsf1(unsigned a) { return *(LAS float*)(size_t)a; }
DI s16x4 ldstr4(unsigned a) { return __builtin_amdgcn_ds_read_tr16_b64_v4i16((LAS s16x4*)(size_t)a); }
DI s16x8 cat(s16x4 lo, s16x4 hi) { return __builtin_shufflevector(lo, hi, 0, 1, 2, 3, 4, 5, 6, 7); }
DI s16x8 ldstr(unsigned a0, unsigned a1) { return cat(ldstr4(a0), ldstr4(a1)); }
DI f32x4 mfma(s16x8 a, s16x8 b, f32x4 c) {
  return __builtin_amdgcn_mfma_f32_16x16x32_bf16(__builtin_bit_cast(bf16x8, a), __builtin_bit_cast(bf16x8, b), c, 0, 0, 0);
}
DI unsigned pk2(float a, float b) { f32x2 v = {a, b}; return __builtin_bit_cast(unsigned, __builtin_convertvector(v, bf16x2)); }
DI float u2f(unsigned u) { return __builtin_bit_cast(float, u); }
DI unsigned f2u(float u) { return __builtin_bit_cast(unsigned, u); }
DI float bflo(unsigned u) { return __builtin_bit_cast(float, u << 16); }
DI float bfhi(unsigned u) { return __builtin_bit_cast(float, u & 0xffff0000u); }
template <int CTRL> DI float dpp_add(float v) {
  return v + __builtin_bit_cast(float, __builtin_amdgcn_update_dpp(0, __builtin_bit_cast(int, v), CTRL, 0xf, 0xf, true));
}
DI float row16_sum(float v) {
  v = dpp_add<0xB1>(v);
  v = dpp_add<0x4E>(v);
  v = dpp_add<0x141>(v);
  v = dpp_add<0x140>(v);
  return v;
}
DI float smul(float a, float b) { float d; asm("v_mul_f32 %0, %1, %2" : "=v"(d) : "v"(a), "v"(b)); return d; }
DI float sadd(float a, float b) { float d; asm("v_add_f32 %0, %1, %2" : "=v"(d) : "v"(a), "v"(b)); return d; }
DI float sfma(float a, float b, float c) { float d; asm("v_fma_f32 %0, %1, %2, %3" : "=v"(d) : "v"(a), "v"(b), "v"(c)); return d; }
DI float sfms(float a, float b, float c) { float d; asm("v_fma_f32 %0, %1, %2, -%3" : "=v"(d) : "v"(a), "v"(b), "v"(c)); return d; }
DI void zero4(f32x4& v) {
  float a, b, c, d;
  asm volatile("v_mov_b32 %0, 0\n\tv_mov_b32 %1, 0\n\tv_mov_b32 %2, 0\n\tv_mov_b32 %3, 0" : "=v"(a), "=v"(b), "=v"(c), "=v"(d));
  v = (f32x4){a, b, c, d};
}
DI u32x2 swap32(float a, float b) { asm volatile("s_nop 1" : "+v"(a), "+v"(b)); return __builtin_amdgcn_permlane32_swap(f2u(a), f2u(b), false, false); }
DI u32x2 swap16(float a, float b) { asm volatile("s_nop 1" : "+v"(a), "+v"(b)); return __builtin_amdgcn_permlane16_swap(f2u(a), f2u(b), false, false); }
DI f32x2 pfma(f32x2 a, f32x2 b, f32x2 c) { return __builtin_elementwise_fma(a, b, c); }
DI f32x2 bc(float a) { return (f32x2){a, a}; }
DI f32x2 bco(float a) { f32x2 v = {a, a}; asm("" : "+v"(v)); return v; }
DI f32x2 cmul(f32x2 a, f32x2 b) { return (f32x2){sfms(a[0], b[0], smul(a[1], b[1])), sfma(a[0], b[1], smul(a[1], b[0]))}; }
DI f32x2 cfma(f32x2 a, f32x2 b, f32x2 c) { return (f32x2){sfms(a[0], b[0], sfms(a[1], b[1], c[0])), sfma(a[0], b[1], sfma(a[1], b[0], c[1]))}; }
DI f32x2 csel(bool p, f32x2 a, f32x2 b) { return (f32x2){p ? a[0] : b[0], p ? a[1] : b[1]}; }
DI void prog_set(unsigned a, unsigned v) {
  asm volatile("s_mov_b64 exec, 1\n\tds_write_b32 %0, %1\n\ts_mov_b64 exec, -1" ::"v"(a), "v"(v) : "memory");
}
DI void prog_wait(unsigned a, unsigned v) {
#pragma unroll 1
  for (int spin = 0; spin < SPIN_CAP * 16; ++spin) {
    if (__builtin_amdgcn_readfirstlane(*(volatile LAS unsigned*)(size_t)a) >= v) break;
    __builtin_amdgcn_s_sleep(1);
  }
  asm volatile("" ::: "memory");
}
DI void bar() { asm volatile("" ::: "memory"); __builtin_amdgcn_s_barrier(); asm volatile("" ::: "memory"); }
DI void dma1_dev(unsigned voff, const char* sbase, unsigned ldsaddr) {
  asm volatile("s_mov_b32 m0, %2\n\ts_nop 0\n\tglobal_load_lds_dwordx4 %0, %1 sc1" ::"v"(voff), "s"(sbase), "s"(ldsaddr) : "memory");
}
DI void dma2_dev(unsigned voff, const char* s0, const char* s1, unsigned ldsaddr) {
  asm volatile("s_mov_b32 m0, %3\n\ts_nop 0\n\tglobal_load_lds_dwordx4 %0, %1 sc1\n\tglobal_load_lds_dwordx4 %0, %2 offset:2048 sc1" ::"v"(voff), "s"(s0), "s"(s1), "s"(ldsaddr) : "memory");
}
DI void dma4_dev(unsigned voff, const char* s0, const char* s1, unsigned ldsaddr) {
  asm volatile("s_mov_b32 m0, %3\n\ts_nop 0\n\tglobal_load_lds_dwordx4 %0, %1 sc1\n\tglobal_load_lds_dwordx4 %0, %1 offset:1024 sc1\n\t"
               "global_load_lds_dwordx4 %0, %2 offset:2048 sc1\n\tglobal_load_lds_dwordx4 %0, %2 offset:3072 sc1" ::"v"(voff), "s"(s0), "s"(s1), "s"(ldsaddr) : "memory");
}
DI int kstep_of(int slot, int i) {
  const int q = slot + 4 * i + (slot >= 4 ? 28 : 0), cls = (q >= 14) + (q >= 28) + (q >= 42);
  return 4 * (q - 14 * cls) + ((0x9C >> (2 * cls)) & 3);
}
DI unsigned flag_of(int j, int c) {
  const int q = 14 * ((0x78 >> (2 * c)) & 3) + j;
  return q < 32 ? (q & 3) * 128 + (q >> 2) * 8 : 512 + (q & 3) * 128 + ((q - 32) >> 2) * 8;
}
DI void cnt_add(unsigned a) {
  asm volatile("s_mov_b64 exec, 1\n\tds_add_u32 %0, %1\n\ts_mov_b64 exec, -1" ::"v"(a), "v"(1u) : "memory");
}
DI f32x2 lo2(f32x4 v) { return __builtin_shufflevector(v, v, 0, 1); }
DI f32x2 hi2(f32x4 v) { return __builtin_shufflevector(v, v, 2, 3); }
DI void rows4(float v, float* o) {
  asm volatile("s_nop 1" : "+v"(v));
  const unsigned u = f2u(v);
  const u32x2 h = __builtin_amdgcn_permlane32_swap(u, u, false, false);
  const u32x2 a = __builtin_amdgcn_permlane16_swap(h.x, h.x, false, false), b = __builtin_amdgcn_permlane16_swap(h.y, h.y, false, false);
  o[0] = u2f(a.x); o[1] = u2f(a.y); o[2] = u2f(b.x); o[3] = u2f(b.y);
}
struct Col { f32x2 cs[4], bias[4]; };
DI void mkcol(Col& c, f32x4 cs0, f32x4 cs1, u32x2 b0, u32x2 b1) {
  c.cs[0] = lo2(cs0); c.cs[1] = hi2(cs0); c.cs[2] = lo2(cs1); c.cs[3] = hi2(cs1);
  c.bias[0] = (f32x2){bflo(b0.x), bfhi(b0.x)}; c.bias[1] = (f32x2){bflo(b0.y), bfhi(b0.y)};
  c.bias[2] = (f32x2){bflo(b1.x), bfhi(b1.x)}; c.bias[3] = (f32x2){bflo(b1.y), bfhi(b1.y)};
}
DI u32x4 gelu8(f32x4 a0, f32x4 a1, const Col& c, f32x2 rs, f32x2 nmrs) {
  f32x2 v[4], z[4];
  u32x4 o;
#define ST4(stmt) _Pragma("unroll") for (int h = 0; h < 4; ++h) { stmt; } SCHED();
  ST4(v[h] = pfma(c.cs[h], nmrs, c.bias[h]))
  ST4(v[h] = pfma(h == 0 ? lo2(a0) : h == 1 ? hi2(a0) : h == 2 ? lo2(a1) : hi2(a1), rs, v[h]))
  ST4(z[h] = v[h] * v[h])
  ST4(z[h] = pfma(z[h], bc(-0.10294324f), bc(-2.30220820f)))
  ST4(z[h] = z[h] * v[h])
  ST4((z[h] = (f32x2){__builtin_amdgcn_exp2f(z[h][0]), __builtin_amdgcn_exp2f(z[h][1])}))
  ST4(z[h] = z[h] + bc(1.f))
  ST4((z[h] = (f32x2){__builtin_amdgcn_rcpf(z[h][0]), __builtin_amdgcn_rcpf(z[h][1])}))
  ST4(v[h] = v[h] * z[h])
  ST4(o[h] = pk2(v[h][0], v[h][1]))
#undef ST4
  return o;
}

__global__ void __launch_bounds__(512, 2) fused_kernel(Params p) {
  __shared__ __attribute__((aligned(16))) char lds_mem[LDS_BYTES];
  unsigned long long pc;
  asm volatile("s_getpc_b64 %0" : "=s"(pc));
  const unsigned LB = (unsigned)(size_t)(LAS char*)lds_mem;
  const int tid = threadIdx.x, lane = tid & 63;
  const int w = (__builtin_amdgcn_readfirstlane(tid >> 6) + WOFF) & 7;
  if (w == CPW) {
    const unsigned co = ((unsigned)(lane & 15) * 32u + (blockIdx.x >> 3)) * 128u;
    dma4w(co < CODE_BYTES ? co : 0u, (const char*)(pc & ~255ull), LB + L_DUMMY + 512);
  }
  const int b = blockIdx.x, idx = b >> 3, xcd = b & 7;
  const int r = 8 * (xcd >> 2) + (idx >> 2), j = 4 * (xcd & 3) + (idx & 3);
  const int rot = (2 * (xcd & 3) + 4 * (xcd >> 2)) & 7;
  const int mtile = 4 * xcd + (idx >> 3), ntile = (idx + NOFF) & 7;
  const int i16 = lane & 15, g = lane >> 4, qq = i16 >> 2, pp = i16 & 3;
  const int d = w & 1;
  const int jm = j < 14 ? j : 13, lq = j & 1;
  const bool mlp = j < 14;
  u32x4 pre[4];
  unsigned v_lre, v_lim, v_ldt;
  if ((w >> 1) == 2) {
    const unsigned po = (unsigned)(32 * j + 16 * d + i16) * 2u;
    v_lre = *(const GAS unsigned short*)(p.lre + po); v_lim = *(const GAS unsigned short*)(p.lim + po); v_ldt = *(const GAS unsigned short*)(p.logdt + po);
    asm volatile("" ::: "memory");
  }
  u32x4 prb[2];
  if (w < 4) {
    const bool isb = w >= 2;
    const char* pb = isb ? p.w1 + (64 * (r & 7) + 32 * d) * 3584 + jm * 256 + (r >> 3) * 128 : p.x + (128 * r + 8 * j + 4 * d) * 1024;
    const unsigned po = isb ? (lane >> 3) * 3584 + (lane & 7) * 16 : g * 1024 + i16 * 16, ps = isb ? 28672 : 256;
#pragma unroll
    for (int i = 0; i < 4; ++i) pre[i] = *(const GAS u32x4*)(pb + i * ps + po);
    if (isb) {
#pragma unroll
      for (int i = 0; i < 2; ++i) prb[i] = *(const GAS u32x4*)((r & 8 ? p.bim : p.bre) + (32 * j + 4 * (r & 7) + 2 * d) * 1024 + i * 512 + (lane >> 5) * 1024 + (lane & 31) * 16);
    }
    asm volatile("" ::: "memory");
  }
  const int kgo = 2 * (idx >> 3), late = ((w + 2) >> 2) & 1, kg = 2 * (((w & 2) + d + KROT * ntile) & 3) + late;
  auto slot_of = [&](int role) { return (((role + 1) & 2) << 1) + (((((role + 1) & 2) ? 0 : kgo) + ((role & 1) ? 0 : 2) + d) & 3); };
  const int slot = slot_of(w >> 1);
  const u32x2 ep = *(const __attribute__((address_space(4))) u32x2*)(p.ws + WS_EPOCH);
  unsigned tagA, tagB;
  auto mktag = [&]() { tagA = ep.x; tagB = ep.y; asm volatile("" : "+s"(tagA), "+s"(tagB)); tagA += 1u; tagB += 1u; };
  const __amdgpu_buffer_rsrc_t wsr = __builtin_amdgcn_make_buffer_rsrc(p.ws, 0, WS_END, 0x00020000);
  const i32x4 wss = {(int)(size_t)p.ws, (int)((size_t)p.ws >> 32) & 0xffff, (int)WS_END, 0x00020000};
  auto fresh = [](int o) { asm volatile("" : "+s"(o) :: "memory"); return o; };
  const unsigned HB = LB + d * HALF;
  const unsigned PW = LB + L_FLG;
  s16x8 we[8][4];
  f32x4 acc2[4][4];
  const unsigned home = HB + ((2 - (w >> 1)) & 3) * SLOT;
  const unsigned r2l = (4 * g + qq) * 128 + pp * 8, r2x = (2 * g + (qq >> 1)) & 3;
  auto absorb = [&](int i, unsigned page) {
#pragma unroll
    for (int n = 0; n < 4; ++n) { const unsigned a = page + r2l + ((n ^ r2x) * 32); we[i][n] = ldstr(a, a + 2048); }
  };
  auto mkvc = [&](unsigned* vc) {
#pragma unroll
    for (int i = 0; i < 8; ++i) vc[i] = (lane >> 3) * 1024 + (((lane & 7) ^ (4 * (i & 1) + g)) * 16) + 4096 + i * 7168;
  };
  auto mkv2 = [&](unsigned* v2) {
#pragma unroll
    for (int i = 0; i < 4; ++i) v2[i] = (lane >> 3) * 1024 + (((((lane & 7) >> 1) ^ g) << 1) | (lane & 1)) * 16 + i * 7168;
  };
  auto loadC = [&](const unsigned* vc, int kgc, unsigned dst) {
    const unsigned o = ntile * 65536 + kgc * 128;
    dma_x8(vc, p.cre + o, dst);
    dma_x8(vc, p.cim + o, dst + 8192);
  };
  const char* w2b = p.w2 + ntile * 128;
  auto adv = [&](unsigned& a, int dl) { a += dl; asm volatile("" : "+v"(a)); };
  const int tok = 32 * qq + 16 * (i16 & 1) + ((i16 >> 1) & 1);
  const unsigned kx = (g ^ (i16 >> 1)) * 16;
  const unsigned awb = LB + SL_W + (8 * g + qq) * 256 + pp * 8, awx = qq | ((g & 1) << 2);
  auto sum32 = [&](const u32x4* v) {
    float s0 = 0.f, s1 = 0.f;
#pragma unroll
    for (int i = 0; i < 4; ++i)
#pragma unroll
      for (int e = 0; e < 4; ++e) { s0 += bflo(v[i][e]); s1 += bfhi(v[i][e]); }
    return row16_sum(s0 + s1);
  };
  auto zero2 = [&]() {
#pragma unroll
    for (int n = 0; n < 4; ++n) {
#pragma unroll
      for (int m = 0; m < 4; ++m) acc2[n][m] = (f32x4){0, 0, 0, 0};
      asm volatile("" : "+v"(acc2[n][0]), "+v"(acc2[n][1]), "+v"(acc2[n][2]), "+v"(acc2[n][3]));
    }
  };
  int ksl;
  unsigned hb, crd, pubo = 0xffff0000u;
  const int fm = WS_FLAG + mtile * 2048 + slot * 128, fs = WS_FLAG + mtile * 2048 + 1024 + (kg >> 1) * 64;
  u64x8 fl;
  auto snap = [&](bool ssm) { fl = __builtin_bit_cast(u64x8, sbl16(wss, fresh(ssm ? fs : fm), 1)); SCHED(); };
  auto seen = [&]() { SCHED(); asm volatile("" : "+s"(fl)); };
  auto setup2 = [&](int nm) {
    ksl = (lane < nm ? kstep_of(slot, lane) : 56 + 4 * kg + late + ((lane - nm) & 7)) * 4096;
    ksl += w == 1 ? 2048 : 0;
    hb = WS_H + (unsigned)mtile * H_MT + lane * 16;
    crd = home + i16 * 128 + (g & 1) * 8 + (((g >> 1) ^ (i16 >> 1)) * 16);
    asm volatile("" : "+v"(ksl), "+v"(hb), "+v"(crd));
  };

  auto roleD = [&]() __attribute__((always_inline)) {
    __builtin_amdgcn_s_waitcnt(0x0F70);
    __builtin_amdgcn_s_setprio(3);
    unsigned vx[8], vw[8], vb[4], v2[4];
#pragma unroll
    for (int i = 0; i < 8; ++i) vw[i] = g * 3584 + ((((i16 >> 1) ^ (g | (((i >> 1) & 1) << 2))) << 1) | (i16 & 1)) * 16 + 4096 + i * (14336 - 1024);
    const char* wb = p.w1 + jm * 256 + d * (32 * 3584);
    auto part1 = [&](int c, unsigned slot) { if (mlp) dma_x8(vw, wb + ((c + rot) & 7) * (64 * 3584), LB + slot + SL_W + d * 8192); };
    asm volatile("" :: "s"(wb));
    part1(0, 0);
#pragma unroll
    for (int i = 0; i < 8; ++i)
      vx[i] = (32 * (g >> 1) + 16 * ((lane >> 3) & 1) + (g & 1)) * 1024 + (((lane & 7) ^ (4 * (i & 1) + g)) * 16) + 4096 + (i & 1) * 65536 + (i >> 1) * 2048 - i * 1024;
#pragma unroll
    for (int i = 0; i < 4; ++i) vb[i] = (lane >> 3) * 1024 + (((lane & 7) ^ (4 * (i & 1) + g)) * 16) + i * 7168;
    const char* xb = p.x + (r * 128 + d * 8) * 1024;
    const char* bb = (d ? p.bim : p.bre) + j * 32768;
    auto partx = [&](int c, unsigned slot) { dma_x8(vx, xb + ((c + rot) & 7) * 128, LB + slot + d * 8192); };
    auto partb = [&](int c, unsigned slot) { dma_x4(vb, bb + ((c + rot) & 7) * 128, LB + slot + SL_B + d * 4096); };
    auto part0 = [&](int c, unsigned slot) { partx(c, slot); partb(c, slot); };
    mkv2(v2);
    unsigned ra[4];
#pragma unroll
    for (int n = 0; n < 4; ++n) ra[n] = home + r2l + ((n ^ r2x) * 32);
    int kt = kstep_of(slot_of((lane >> 3) & 3), lane & 7) * 32768;
    asm volatile("" : "+v"(kt), "+v"(ra[0]), "+v"(ra[1]), "+v"(ra[2]), "+v"(ra[3]), "+v"(v2[0]), "+v"(v2[1]), "+v"(v2[2]), "+v"(v2[3]));
    bool low = false;
    auto page = [&](int role, int i, int grp, int pg) { dma_x4(v2, w2b + (unsigned)__builtin_amdgcn_readlane(kt, 8 * role + i), HB + grp * SLOT + pg * 4096, low); };
    auto take = [&](int i) {
      SCHED();
#pragma unroll
      for (int n = 0; n < 4; ++n) we[i][n] = ldstr(ra[n] + PG(i) * 4096, ra[n] + PG(i) * 4096 + 2048);
      SCHED();
    };
    auto own = [&](int i) { page(3, i, 3, PG(i)); };
    unsigned l16 = lane * 16, la = home + 12288 + lane * 16, lb = LB + L_ST1 + d * 1024 + lane * 16, lc = LB + L_BS + d * 512 + (lane & 31) * 16;
    unsigned wa = LB + L_CS + d * 256 + (lane & 31) * 8 + (lane >> 5) * 4, wb2 = LB + L_LS + (2 * d + (lane >> 5)) * 288 + i16 * 16 + (g & 1) * 4;
    asm volatile("" : "+v"(l16), "+v"(la), "+v"(lb), "+v"(lc), "+v"(wa), "+v"(wb2));
    auto gather = [&](bool bs) {
      const char* cp = p.ws + WS_CS + jm * 8192 + d * 4096;
      dma4_dev(l16, cp, cp, home + 12288);
      dma2_dev(l16, p.ws + WS_STAT + r * 2048 + d * 1024, p.ws + WS_STAT + (mtile >> 1) * 2048 + d * 1024 - 2048, LB + L_ST1 + d * 1024);
      if (bs) dma1_dev(l16, p.ws + WS_BS + j * 1024, LB + L_BS);
    };
    auto check = [&](auto between) -> bool {
      auto ld16 = [](unsigned a) { return *(volatile LAS u32x4*)(size_t)a; };
      auto bad = [&](unsigned a, u32x4 v) { return __builtin_amdgcn_bitop3_b32(v.z, tagB, __builtin_amdgcn_bitop3_b32(v.x, tagA, a, 0xBE), 0xBE); };
      const u32x4 c0 = ld16(la), c1 = ld16(la + 1024), c2 = ld16(la + 2048), c3 = ld16(la + 3072), s1 = ld16(lb), s2 = ld16(lb + 2048), s3 = ld16(lc);
      SCHED(); between(); SCHED();
      const u32x2 h = swap32(sadd(sadd(u2f(c0.y), u2f(c1.y)), sadd(u2f(c2.y), u2f(c3.y))), sadd(sadd(u2f(c0.w), u2f(c1.w)), sadd(u2f(c2.w), u2f(c3.w))));
      *(LAS float*)(size_t)wa = sadd(u2f(h.x), u2f(h.y));
      *(LAS unsigned*)(size_t)wb2 = s1.y;
      *(LAS unsigned*)(size_t)(wb2 + 8) = s1.w;
      const unsigned e = bad(bad(bad(bad(bad(bad(bad(0u, c0), c1), c2), c3) & (mlp ? ~0u : 0u), s1), s2), s3);
      return __builtin_amdgcn_ballot_w64(e != 0) == 0;
    };
    const unsigned me = PW + w * 4;
    zero2();
    setup2(8);
    auto landed = [&]() { if (mlp) wait_vm<20>(); else wait_vm<12>(); };
    SCHED();
    part0(0, 0);
    part1(1, SLOT); part0(1, SLOT);
    mktag();
    if (mlp) wait_vm<20>(); else wait_vm<12>();
    bar();
    part1(2, 2 * SLOT); part0(2, 2 * SLOT); landed();
    bar();
    if (d) dma4w(lane * 4, p.b1 + jm * 256, LB + L_B1);
    part1(3, 0); part0(3, 0); landed();
    unsigned vt = (lane < 56 ? lane : 0) * 128;
    asm volatile("" : "+v"(vt));
    auto s3 = [&]() { dma4w((lane < 28 ? lane : 0) * 1024, p.w2 + (2 * idx + d) * 28672 + ((3u - ((unsigned)(size_t)p.w2 >> 7)) & 7u) * 128, LB + L_DUMMY + d * 256); };
    bar();
    dma_x4(vx, p.x + (128 * (mtile >> 1) + 8 * (mtile & 1) + 4 * d) * 1024 + ntile * 128 - 4096, home + 12288);
    own(0); own(1); part1(4, SLOT); part0(4, SLOT); landed();
    bar();
    own(2); part1(5, 2 * SLOT); take(0); take(1); part0(5, 2 * SLOT); landed(); wait_lgkm0(); s3();
    bar();
    gather(d); own(4); part1(6, 0); take(2); part0(6, 0); landed(); wait_lgkm0();
    bar();
    own(5); part1(7, SLOT); take(4);
    const bool ok = check([&]() { partx(7, SLOT); });
    SCHED();
    partb(7, SLOT);
    if (__builtin_expect(!ok, 0)) {
#pragma unroll 1
      for (int spin = 0; spin < SPIN_CAP; ++spin) {
        __builtin_amdgcn_s_sleep(10);
        gather(true);
        wait_vm<0>();
        if (check([]() {})) break;
      }
    }
    landed();
    barrier();
    auto c8 = [&](const char* src, unsigned dst) { dma_x4(vb, src, dst, low); dma_x4(vb, src + 32768, dst + 4096, low); };
    const unsigned co = ntile * 65536 + kg * 128;
    page(0, 0, 2, 0); take(5); page(0, 1, 2, 1); page(0, 2, 2, 2); page(0, 3, 2, 3); page(0, 4, 2, 4); own(3);
    wait_vm<12>();
    bar();
    own(6); page(2, 0, 0, 0); page(2, 1, 0, 1); page(2, 2, 0, 2); page(2, 3, 0, 3); page(2, 4, 0, 4);
    wait_vm<20>();
    bar();
    low = true;
    take(3); take(6);
    page(0, 5, 2, 0); page(0, 6, 2, 1); page(0, 7, 2, 2);
    wait_lgkm0();
    own(7); page(1, 0, 1, 0);
    wait_vm<20>(); prog_set(me, 1);
    page(1, 1, 1, 1); page(1, 2, 1, 2); page(1, 3, 1, 3); page(1, 4, 1, 4);
    wait_vm<20>(); prog_set(me, 2);
    take(7);
    c8(p.cre + co, home);
    wait_lgkm0();
    c8(p.cim + co, home + 8192);
    wait_vm<16>();
    prog_set(me, 3);
    prog_wait(PW + d * 4, 1);
    snap(false);
    c8(p.cre + (co ^ 512), HB + 2 * SLOT); c8(p.cim + (co ^ 512), HB + 2 * SLOT + 8192);
    seen();
  };
  auto roleL = [&]() __attribute__((always_inline)) {
    f32x4 acr[8], aci[8];
#pragma unroll
    for (int m = 0; m < 8; ++m) { acr[m] = (f32x4){0, 0, 0, 0}; aci[m] = (f32x4){0, 0, 0, 0}; asm volatile("" : "+v"(acr[m]), "+v"(aci[m])); }
    unsigned ax[2], ab[2];
    ax[0] = LB + i16 * 128 + kx; ax[1] = ax[0] ^ 64;
    ab[0] = LB + SL_B + (16 * d + i16) * 128 + kx; ab[1] = ab[0] ^ 64;
    unsigned sa = LB + L_LS + g * 288, csa = LB + L_BS + (16 * d + i16) * 16 + 4;
    asm volatile("" : "+v"(sa), "+v"(csa), "+v"(ax[0]), "+v"(ax[1]), "+v"(ab[0]), "+v"(ab[1]));
    s16x8 xf[2][8], bf[2][2];
    auto rd = [&](int set, int i) {
      if (i < 2) bf[set][i] = lds128(ab[set] + i * 4096);
      else xf[set][i - 2] = lds128(ax[set] + (i - 2) * 2048);
    };
    auto mm = [&](int set, int i) {
      if (i & 1) aci[i >> 1] = mfma(xf[set][i >> 1], bf[set][1], aci[i >> 1]);
      else acr[i >> 1] = mfma(xf[set][i >> 1], bf[set][0], acr[i >> 1]);
    };
    asm volatile("" : "+v"(v_lre), "+v"(v_lim), "+v"(v_ldt));
    const float lr = bflo(v_lre), li = bflo(v_lim);
    const float dt = bflo(pk2(expf(bflo(v_ldt)), 0.f));
    const float ang = smul(dt, li);
    const float rv = smul(ang, 0.15915494f);
    const float rev = (rv - __builtin_rintf(rv)) + sfma(ang, 6.4206382e-9f, sfms(ang, 0.15915494f, rv));
    float sn = __builtin_amdgcn_sinf(rev), cn = __builtin_amdgcn_cosf(rev);
    asm volatile("s_nop 0" : "+v"(sn), "+v"(cn));
    const float ex = expf(smul(dt, lr));
    f32x2 a1 = {smul(ex, cn), smul(ex, sn)};
    const float nr = a1[0] - 1.f, ni = a1[1], iden = 2.f / sfma(lr, lr, smul(li, li));
    f32x2 kk = {smul(sfma(nr, lr, smul(ni, li)), iden), smul(sfms(ni, lr, smul(nr, li)), iden)};
    f32x2 t = cmul(a1, a1); t = cmul(t, t); t = cmul(t, t);
    f32x2 a16 = cmul(t, t);
    f32x2 a32 = cmul(a16, a16);
    const f32x2 a64 = cmul(a32, a32), a128 = cmul(a64, a64), a256 = cmul(a128, a128);
    f32x2 a512 = cmul(a256, a256);
    const f32x2 one = {1.f, 0.f};
    f32x2 aseg = cmul(kk, cmul(csel(g & 1, a32, one), csel(g & 2, a64, one)));
    const int e4 = (r - 1 - g) & 3;
    f32x2 acar = cmul(csel(e4 & 1, a128, one), csel(e4 & 2, a256, one));
    const f32x2 ar = bco(a1[0]), ai = bco(a1[1]);
    asm volatile("" :: "v"(ar), "v"(ai));
    asm volatile("" : "+v"(kk), "+v"(a16), "+v"(a32), "+v"(a512), "+v"(aseg), "+v"(acar));
    const unsigned on = (qq == g) ? ((pp & 1) ? 0xffff0000u : 0xffffu) : 0u;
    unsigned m0 = (pp & 2) ? 0u : on, m1 = (pp & 2) ? on : 0u;
    auto scan = [&](f32x2 h0, f32x2 h1, const f32x2* ur, const f32x2* ui, auto out) {
      const u32x4 z0 = {m0 & 0x3f803f80u, m1 & 0x3f803f80u, 0u, 0u}, z1 = {0u, 0u, z0.x, z0.y};
      const f32x4 zz = {0, 0, 0, 0};
      f32x2 hr = {h0[0], h1[0]}, hi = {-h0[1], -h1[1]};
      u32x4 fr[4], fi[4];
      f32x4 tr[2], ti[2];
      SCHED();
#pragma unroll
      for (int mp = 0; mp < 5; ++mp) {
        if (mp > 0) {
#pragma unroll
          for (int k = 0; k < 2; ++k) {
            tr[k] = mfma(__builtin_bit_cast(s16x8, fr[mp - 1]), __builtin_bit_cast(s16x8, k ? z1 : z0), zz);
            ti[k] = mfma(__builtin_bit_cast(s16x8, fi[mp - 1]), __builtin_bit_cast(s16x8, k ? z1 : z0), zz);
          }
          SCHED();
        }
        if (mp < 4) {
#pragma unroll
          for (int e = 0; e < 4; ++e) {
            const int s = 4 * mp + e;
            const f32x2 t1 = ur ? pfma(hr, -ai, ui[s]) : hr * -ai; SCHED();
            const f32x2 t0 = ur ? pfma(hi, ai, ur[s]) : hi * ai; SCHED();
            hi = pfma(hi, ar, t1); SCHED();
            hr = pfma(hr, ar, t0); SCHED();
            fi[mp][e] = pk2(hi[0], hi[1]); SCHED();
            fr[mp][e] = pk2(hr[0], hr[1]); SCHED();
          }
        }
        if (mp > 0) { out(2 * mp - 2, tr[0], ti[0]); out(2 * mp - 1, tr[1], ti[1]); SCHED(); }
      }
    };
    auto pack = [](f32x4 a, f32x4 b) { return (u32x4){pk2(a[0], a[1]), pk2(a[2], a[3]), pk2(b[0], b[1]), pk2(b[2], b[3])}; };
    u32x4 yb[8];
    auto resp = [&]() { scan(aseg, cmul(a16, aseg), nullptr, nullptr, [&](int q, f32x4 a, f32x4 b) { yb[q] = pack(a, b); asm volatile("" : "+v"(yb[q])); }); };
    resp();
    mktag();
    __builtin_amdgcn_s_waitcnt(0x0F70);
    __builtin_amdgcn_s_setprio(1);
    bar();
#pragma unroll
    for (int i = 0; i < 10; ++i) rd(0, i);
    wait_lgkm0();
    auto stage1 = [&](int dl) {
      SCHED();
#pragma unroll
      for (int i = 0; i < 16; ++i) {
        mm(0, i); SCHED();
        if (i < 10) rd(1, i);
        if (i == 11) { adv(ax[0], dl); adv(ab[0], dl); }
        SCHED();
      }
    };
    stage1(SLOT);
    barrier();
    int ph = 0;
#pragma unroll 1
    for (int c = 2; c <= 8; ++c) {
      const int dl = ph == 2 ? -2 * (int)SLOT : (int)SLOT;
      ph = ph == 2 ? 0 : ph + 1;
      SCHED();
#pragma unroll
      for (int i = 0; i < 16; ++i) {
        mm(1, i); SCHED();
        if (i < 10) rd(0, i);
        if (i == 11) { adv(ax[1], dl); adv(ab[1], dl); }
        SCHED();
      }
      wait_lgkm0();
      stage1(ph == 2 ? -2 * (int)SLOT : (int)SLOT);
      barrier();
    }
    __builtin_amdgcn_s_setprio(LPRIO);
    f32x2 rs[16], mrs[16];
    float csr, csi;
#pragma unroll
    for (int i = 0; i < 16; ++i) {
      mm(1, i); SCHED();
      if (i == 0) { csr = ldsf1(csa); csi = ldsf1(csa + 512); }
      if (i < 8) { const f32x4 t = ldsf4(sa + i * 16); rs[i] = lo2(t); mrs[i] = hi2(t); }
      SCHED();
    }
    const f32x2 ncr = bco(-csr), nci = bco(-csi);
    f32x2 ur[16], ui[16], Er, Ei;
    SCHED();
#pragma unroll
    for (int s = 0; s < 16; ++s) {
      if (s < 8) { const f32x4 t = ldsf4(sa + (s + 8) * 16); rs[s + 8] = lo2(t); mrs[s + 8] = hi2(t); SCHED(); }
      ur[s] = mrs[s] * ncr; SCHED();
      ui[s] = mrs[s] * nci; SCHED();
      ur[s] = pfma((s & 1) ? hi2(acr[s >> 1]) : lo2(acr[s >> 1]), rs[s], ur[s]); SCHED();
      ui[s] = pfma((s & 1) ? hi2(aci[s >> 1]) : lo2(aci[s >> 1]), rs[s], ui[s]); SCHED();
      if (s == 0) { Er = ur[0]; Ei = ui[0]; }
      else {
        const f32x2 t0 = pfma(Ei, -ai, ur[s]); SCHED();
        const f32x2 t1 = pfma(Er, ai, ui[s]); SCHED();
        Er = pfma(Er, ar, t0); SCHED();
        Ei = pfma(Ei, ar, t1); SCHED();
      }
    }
    const f32x2 E0 = {Er[0], Ei[0]};
    const f32x2 E = cfma(a16, E0, (f32x2){Er[1], Ei[1]});
    f32x2 P = {0.f, 0.f}, Tt = {0.f, 0.f};
    float er4[4], ei4[4];
    rows4(E[0], er4); rows4(E[1], ei4);
#pragma unroll
    for (int s = 0; s < 4; ++s) {
      const f32x2 e = {er4[s], ei4[s]};
      P = csel(s == g, Tt, P);
      Tt = s ? cfma(a32, Tt, e) : e;
    }
    const unsigned totb = WS_TOT + (unsigned)(j * 2 + d) * 4096;
    if (g == 0) __builtin_amdgcn_raw_buffer_store_b128((u32x4){tagA, f2u(Tt[0]), tagB, f2u(Tt[1])}, wsr, totb + r * 256 + i16 * 16, 0, 16);
    {
      const f32x2 kr = bco(kk[0]), nki = bco(-kk[1]);
#pragma unroll
      for (int s = 0; s < 16; ++s) {
        const f32x2 t0 = ur[s] * kr; SCHED();
        const f32x2 t1 = ur[s] * nki; SCHED();
        ur[s] = pfma(ui[s], nki, t0); SCHED();
        ui[s] = pfma(ui[s], -kr, t1); SCHED();
      }
    }
#pragma unroll
    for (int s = 0; s < 16; s += 4) asm volatile("" : "+v"(ur[s]), "+v"(ui[s]), "+v"(ur[s + 1]), "+v"(ui[s + 1]), "+v"(ur[s + 2]), "+v"(ui[s + 2]), "+v"(ur[s + 3]), "+v"(ui[s + 3]));
    f32x4 hr4[8], hi4[8];
    {
      const f32x2 kP = cmul(kk, P);
      scan(kP, cfma(a16, kP, cmul(kk, E0)), ur, ui, [&](int q, f32x4 a, f32x4 b) { hr4[q] = a; hi4[q] = b; asm volatile("" : "+v"(hr4[q]), "+v"(hi4[q])); });
    }
    prog_wait(PW + (6 + d) * 4, 1);
#pragma unroll
    for (int i = 0; i < 5; ++i) absorb(i, home + i * 4096);
    unsigned v2[4], vt[4];
    mkv2(v2);
#pragma unroll
    for (int i = 0; i < 4; ++i) { vt[i] = (4 * i + g < r ? g * 256 : -i * 1024) + i16 * 16 + 4096; asm volatile("" : "+v"(vt[i]), "+v"(v2[i])); }
    {
      const i32x4 tr = {wss[0], wss[1], (int)(totb + r * 256), wss[3]};
#pragma unroll 1
      for (int spin = 0; spin < 64; ++spin) {
        int n, t[15];
        asm volatile("s_buffer_load_dword %1, %16, %17 offset:0 glc\n\ts_buffer_load_dword %2, %16, %17 offset:256 glc\n\ts_buffer_load_dword %3, %16, %17 offset:512 glc\n\ts_buffer_load_dword %4, %16, %17 offset:768 glc\n\ts_buffer_load_dword %5, %16, %17 offset:1024 glc\n\ts_buffer_load_dword %6, %16, %17 offset:1280 glc\n\ts_buffer_load_dword %7, %16, %17 offset:1536 glc\n\ts_buffer_load_dword %8, %16, %17 offset:1792 glc\n\ts_buffer_load_dword %9, %16, %17 offset:2048 glc\n\ts_buffer_load_dword %10, %16, %17 offset:2304 glc\n\ts_buffer_load_dword %11, %16, %17 offset:2560 glc\n\ts_buffer_load_dword %12, %16, %17 offset:2816 glc\n\ts_buffer_load_dword %13, %16, %17 offset:3072 glc\n\ts_buffer_load_dword %14, %16, %17 offset:3328 glc\n\ts_buffer_load_dword %15, %16, %17 offset:3584 glc\n\t"
                     "s_mov_b32 %0, 0\n\ts_waitcnt lgkmcnt(0)\n\t"
                     "s_cmp_eq_u32 %1, %18\n\ts_addc_u32 %0, %0, 0\n\ts_cmp_eq_u32 %2, %18\n\ts_addc_u32 %0, %0, 0\n\ts_cmp_eq_u32 %3, %18\n\ts_addc_u32 %0, %0, 0\n\ts_cmp_eq_u32 %4, %18\n\ts_addc_u32 %0, %0, 0\n\ts_cmp_eq_u32 %5, %18\n\ts_addc_u32 %0, %0, 0\n\ts_cmp_eq_u32 %6, %18\n\ts_addc_u32 %0, %0, 0\n\ts_cmp_eq_u32 %7, %18\n\ts_addc_u32 %0, %0, 0\n\ts_cmp_eq_u32 %8, %18\n\ts_addc_u32 %0, %0, 0\n\ts_cmp_eq_u32 %9, %18\n\ts_addc_u32 %0, %0, 0\n\ts_cmp_eq_u32 %10, %18\n\ts_addc_u32 %0, %0, 0\n\ts_cmp_eq_u32 %11, %18\n\ts_addc_u32 %0, %0, 0\n\ts_cmp_eq_u32 %12, %18\n\ts_addc_u32 %0, %0, 0\n\ts_cmp_eq_u32 %13, %18\n\ts_addc_u32 %0, %0, 0\n\ts_cmp_eq_u32 %14, %18\n\ts_addc_u32 %0, %0, 0\n\ts_cmp_eq_u32 %15, %18\n\ts_addc_u32 %0, %0, 0"
                     : "=&s"(n), "=&s"(t[0]), "=&s"(t[1]), "=&s"(t[2]), "=&s"(t[3]), "=&s"(t[4]), "=&s"(t[5]), "=&s"(t[6]), "=&s"(t[7]), "=&s"(t[8]), "=&s"(t[9]), "=&s"(t[10]), "=&s"(t[11]), "=&s"(t[12]), "=&s"(t[13]), "=&s"(t[14]) : "s"(tr), "s"(totb), "s"(tagA) : "scc", "memory");
        if (n >= r) break;
      }
    }
    auto fetch = [&]() {
      asm volatile("s_mov_b32 m0, %5\n\ts_nop 0\n\tglobal_load_lds_dwordx4 %0, %4 sc1\n\tglobal_load_lds_dwordx4 %1, %4 offset:1024 sc1\n\t"
                   "global_load_lds_dwordx4 %2, %4 offset:2048 sc1\n\tglobal_load_lds_dwordx4 %3, %4 offset:3072 sc1"
                   ::"v"(vt[0]), "v"(vt[1]), "v"(vt[2]), "v"(vt[3]), "s"(p.ws + totb - 4096), "s"(home + 16384) : "memory");
    };
    wait_lgkm0();
    if (r > 0) fetch();
    f32x2 wt[4];
#pragma unroll
    for (int i = 3; i >= 0; --i) {
      const bool v = 4 * i + g < r;
      wt[i] = csel(v, acar, (f32x2){0.f, 0.f});
      if (i) acar = csel(v, cmul(acar, a512), acar);
      asm volatile("" : "+v"(wt[i]));
    }
    setup2(6);
    unsigned ho = WS_H + (unsigned)(2 * r) * H_MT + (unsigned)(56 + 2 * j + d) * 4096 + lane * 16, ta = home + 16384 + lane * 16;
    asm volatile("" : "+v"(ho), "+v"(ta), "+v"(m0), "+v"(m1));
    wait_vm<0>();
    float cr = 0.f, ci = 0.f;
    if (r > 0) {
#pragma unroll 1
      for (int spin = 0; spin < SPIN_CAP; ++spin) {
        u32x4 tv[4];
        unsigned e = 0;
#pragma unroll
        for (int i = 0; i < 4; ++i) tv[i] = *(volatile LAS u32x4*)(size_t)(ta + i * 1024);
        f32x2 c[2];
#pragma unroll
        for (int i = 0; i < 4; ++i) {
          const f32x2 t = {u2f(tv[i].y), u2f(tv[i].w)};
          c[i & 1] = i < 2 ? cmul(wt[i], t) : cfma(wt[i], t, c[i & 1]);
          e = __builtin_amdgcn_bitop3_b32(tv[i].z, tagB, __builtin_amdgcn_bitop3_b32(tv[i].x, tagA, e, 0xBE), 0xBE);
        }
        const u32x2 h = swap32(sadd(c[0][0], c[1][0]), sadd(c[0][1], c[1][1]));
        const float x = sadd(u2f(h.x), u2f(h.y));
        const u32x2 h2 = swap16(x, x);
        const float y = sadd(u2f(h2.x), u2f(h2.y));
        const u32x2 h3 = swap32(y, y);
        cr = u2f(h3.x); ci = u2f(h3.y);
        if (__builtin_expect(__builtin_amdgcn_ballot_w64(e != 0) == 0, 1)) break;
        __builtin_amdgcn_s_sleep(8);
        fetch();
        wait_vm<0>();
      }
    }
    {
      const unsigned c0 = pk2(cr, cr), c1 = pk2(ci, ci), c2 = pk2(-ci, -ci);
      const s16x8 are = __builtin_bit_cast(s16x8, (u32x4){m0 & c0, m1 & c0, m0 & c1, m1 & c1}), aim = __builtin_bit_cast(s16x8, (u32x4){m0 & c2, m1 & c2, m0 & c0, m1 & c0});
      u32x4 o[8];
      SCHED();
#pragma unroll
      for (int q = 0; q < 10; ++q) {
        if (q < 8) {
          hr4[q] = mfma(are, __builtin_bit_cast(s16x8, yb[q]), hr4[q]);
          hi4[q] = mfma(aim, __builtin_bit_cast(s16x8, yb[q]), hi4[q]);
          SCHED();
        }
        if (q >= 2) { o[q - 2] = pack(hr4[q - 2], hi4[q - 2]); __builtin_amdgcn_raw_buffer_store_b128(o[q - 2], wsr, ho + ((q - 2) & 3) * 1024, ((q - 2) >> 2) * H_MT, HST); }
        SCHED();
      }
      dma_x4(v2, w2b + kstep_of(slot, 5) * 32768, home + 16384);
      asm volatile("" :: "v"(o[0]), "v"(o[1]), "v"(o[2]), "v"(o[3]), "v"(o[4]), "v"(o[5]), "v"(o[6]), "v"(o[7]), "v"(ho));
      pubo = lane < 2 ? WS_FLAG + (2 * r + lane) * 2048 + 1024 + (2 * j + d) * 8 : pubo;
    }
  };
  auto roleB = [&]() __attribute__((always_inline)) {
    mktag();
    {
      float s0 = 0.f, s1 = 0.f;
#pragma unroll
      for (int e = 0; e < 8; ++e) { s0 = sadd(s0, bflo(prb[e >> 2][e & 3])); s1 = sadd(s1, bfhi(prb[e >> 2][e & 3])); }
      *(LAS float*)(size_t)(LB + L_LS + 512 + (4 * d + g) * 4) = row16_sum(s0 + s1);
      __builtin_amdgcn_s_waitcnt(0x0F70);
    }
    if (mlp) {
      f32x4 acc[6][4];
#pragma unroll
      for (int s = 0; s < 6; ++s) {
#pragma unroll
        for (int m = 0; m < 4; ++m) acc[s][m] = (f32x4){0, 0, 0, 0};
        asm volatile("" : "+v"(acc[s][0]), "+v"(acc[s][1]), "+v"(acc[s][2]), "+v"(acc[s][3]));
      }
      unsigned ax[2], aw[6];
      ax[0] = LB + (64 * d + i16) * 128 + kx; ax[1] = ax[0] ^ 64;
#pragma unroll
      for (int s = 0; s < 6; ++s) aw[s] = awb + ((s ^ awx) * 32);
      unsigned sta = LB + L_LS + qq * 288 + (8 * d + ((i16 >> 1) & 1)) * 16 + (i16 & 1) * 4, csa = LB + L_CS + g * 16;
      asm volatile("" : "+v"(ax[0]), "+v"(ax[1]), "+v"(aw[0]), "+v"(aw[1]), "+v"(aw[2]), "+v"(aw[3]), "+v"(aw[4]), "+v"(aw[5]), "+v"(sta), "+v"(csa));
      s16x8 xf[2][4];
      s16x4 wf[2][12];
      auto rd = [&](int set, int i) {
        if (i < 4) xf[set][i] = lds128(ax[set] + i * 2048);
        else wf[set][i - 4] = ldstr4(aw[(i - 4) >> 1] + set * 8192 + (i & 1) * 1024);
      };
      auto mm = [&](int set, int i) { const int s = i >> 2, m = i & 3; acc[s][m] = mfma(cat(wf[set][2 * s], wf[set][2 * s + 1]), xf[set][m], acc[s][m]); };
      {
        float t[8];
#pragma unroll
        for (int e = 0; e < 8; ++e)
          t[e] = (e & 1) ? sadd(sadd(bfhi(pre[0][e >> 1]), bfhi(pre[1][e >> 1])), sadd(bfhi(pre[2][e >> 1]), bfhi(pre[3][e >> 1])))
                         : sadd(sadd(bflo(pre[0][e >> 1]), bflo(pre[1][e >> 1])), sadd(bflo(pre[2][e >> 1]), bflo(pre[3][e >> 1])));
#pragma unroll
        for (int e = 0; e < 4; ++e) { const u32x2 h = swap32(t[e], t[e + 4]); t[e] = sadd(u2f(h.x), u2f(h.y)); }
#pragma unroll
        for (int e = 0; e < 2; ++e) { const u32x2 h = swap16(t[e], t[e + 2]); t[e] = dpp_add<0x128>(sadd(u2f(h.x), u2f(h.y))); }
        *(LAS f32x2*)(size_t)(LB + L_LS + d * 256 + (4 * (lane & 7) + g) * 8) = (f32x2){t[0], t[1]};
      }
      __builtin_amdgcn_s_setprio(1);
      barrier();
#pragma unroll
      for (int i = 0; i < 16; ++i) rd(0, i);
      wait_lgkm0();
      auto stage1 = [&](int c, int dl) {
        SCHED();
#pragma unroll
        for (int i = 0; i < 24; ++i) {
          mm(0, i); SCHED();
          if (i < 16) rd(1, i);
          else if (i < 22) adv(aw[i - 16], dl);
          else if (i == 22) adv(ax[0], dl);
          SCHED();
        }
        barrier();
        SCHED();
      };
      int ph = 0;
#pragma unroll 1
      for (int c = 1; c < 8; ++c) {
        const int dl = ph == 2 ? -2 * (int)SLOT : (int)SLOT;
        ph = ph == 2 ? 0 : ph + 1;
        stage1(c, dl);
#pragma unroll
        for (int i = 0; i < 24; ++i) {
          mm(1, i); SCHED();
          if (i < 16) rd(0, i);
          else if (i == 16) adv(ax[1], dl);
          else if (i == 23) wait_lgkm0();
          SCHED();
        }
      }
      stage1(8, 0);
      unsigned b1a = LB + L_B1 + g * 8;
      asm volatile("" : "+v"(b1a));
      f32x2 rs[4], nm[4];
      f32x4 cs[6];
      u32x2 b1v[6];
      f32x2 st[4];
      auto colrd = [&](int s) { cs[s] = ldsf4(csa + 64 * s); b1v[s] = *(LAS u32x2*)(size_t)(b1a + 32 * s); };
#pragma unroll
      for (int i = 0; i < 4; ++i) st[i] = (f32x2){ldsf1(sta + i * 32), ldsf1(sta + i * 32 + 8)};
      colrd(0); colrd(1);
      SCHED();
#pragma unroll
      for (int i = 0; i < BSPLIT; ++i) mm(1, i);
      SCHED();
#pragma unroll
      for (int i = 0; i < 4; ++i) { rs[i] = bco(st[i][0]); nm[i] = bco(-st[i][1]); }
      const unsigned fo = WS_FLAG + (2 * r + d) * 2048;
      pubo = lane ? pubo : 0u;
      u32x4 ob[12];
      const unsigned ho = WS_H + (unsigned)(2 * r + d) * H_MT + (unsigned)(4 * jm) * 4096 + lane * 16;
#pragma unroll
      for (int pr = 0; pr < 3; ++pr) {
        Col col;
        mkcol(col, cs[2 * pr], cs[2 * pr + 1], b1v[2 * pr], b1v[2 * pr + 1]);
        if (pr < 2) { colrd(2 * pr + 2); colrd(2 * pr + 3); }
        SCHED();
        if (pr == 1) {
#pragma unroll
          for (int i = BSPLIT; i < 24; ++i) mm(1, i);
          SCHED();
        }
#pragma unroll
        for (int m = 0; m < 4; ++m) {
          ob[4 * pr + m] = gelu8(acc[2 * pr][m], acc[2 * pr + 1][m], col, rs[m], nm[m]);
          __builtin_amdgcn_raw_buffer_store_b128(ob[4 * pr + m], wsr, ho + m * 1024, pr * 4096, HST);
          if (4 * pr + m >= KEEP) asm volatile("" :: "v"(ob[4 * pr + m - KEEP]));
          if (pr > 0 && m == (pr == 1 ? BFLAG0 : BFLAG)) {
            if (pr == 1) wait_vm<BFLAG0 + 1>(); else wait_vm<BFLAG + 1>();
            __builtin_amdgcn_raw_buffer_store_b64((u32x2){tagA, tagB}, wsr, pubo, fo + flag_of(jm, pr - 1), 16);
          }
          SCHED();
        }
      }
      pubo |= fo + flag_of(jm, 2);
    } else {
      const int q = 4 * r + 2 * lq + d;
      const unsigned vt = (lane < 56 ? lane : 0) * 128, dm = LB + L_DUMMY + d * 256;
#pragma unroll 1
      for (int c = 0; c < 9; ++c) {
        {
          const int t = (c - TST) * TPP;
#pragma unroll
          for (int e = 0; e < TPP; ++e) {
            if (t + e >= 0 && t + e < 4) dma4w(vt, p.w2 + ((t + e) * 64 + q) * 7168, dm);
            else if (t + e == 4 || t + e == 5) dma4w(lane * 128, (t + e == 4 ? p.cre : p.cim) + q * 8192, dm);
          }
        }
        barrier();
      }
    }
    prog_wait(PW + (6 + d) * 4, 3);
#pragma unroll
    for (int i = 0; i < 5; ++i) absorb(i, home + i * 4096);
    unsigned v2[4];
    mkv2(v2);
    setup2(6);
#pragma unroll
    for (int i = 0; i < 4; ++i) asm volatile("" : "+v"(v2[i]));
    wait_lgkm0();
    dma_x4(v2, w2b + kstep_of(slot, 5) * 32768, home + 16384);
  };
  auto roleA = [&]() __attribute__((always_inline)) {
    const int srow = 128 * r + 8 * j + 4 * d + g;
    u32x2 dv[4], bv[4];
#pragma unroll
    for (int n = 0; n < 4; ++n) { dv[n] = *(const GAS u32x2*)(p.dsk + ntile * 128 + n * 32 + g * 8); bv[n] = *(const GAS u32x2*)(p.b2 + ntile * 128 + n * 32 + g * 8); }
    if (lane < 8) *(LAS unsigned*)(size_t)(PW + lane * 4) = 0u;
    f32x4 acc[2][4];
#pragma unroll
    for (int m = 0; m < 4; ++m) { acc[0][m] = (f32x4){0, 0, 0, 0}; acc[1][m] = (f32x4){0, 0, 0, 0}; asm volatile("" : "+v"(acc[0][m]), "+v"(acc[1][m])); }
    unsigned ax0 = LB + (64 * d + i16) * 128 + kx, ax1 = ax0 ^ 64, aw0 = awb + ((6 ^ awx) * 32), aw1 = awb + ((7 ^ awx) * 32);
    unsigned sta = LB + L_LS + qq * 288 + (8 * d + ((i16 >> 1) & 1)) * 16 + (i16 & 1) * 4, csa = LB + L_CS + 384 + g * 16;
    asm volatile("" : "+v"(ax0), "+v"(ax1), "+v"(aw0), "+v"(aw1), "+v"(sta), "+v"(csa));
    int ph = 0;
    auto chunk = [&](auto side) {
      s16x8 xf[2][4], wf[2][2];
#pragma unroll
      for (int k = 0; k < 2; ++k) {
        wf[k][0] = ldstr(aw0 + k * 8192, aw0 + k * 8192 + 1024);
        wf[k][1] = ldstr(aw1 + k * 8192, aw1 + k * 8192 + 1024);
#pragma unroll
        for (int m = 0; m < 4; ++m) xf[k][m] = lds128((k ? ax1 : ax0) + m * 2048);
      }
      SCHED();
      const int dl = ph == 2 ? -2 * (int)SLOT : (int)SLOT;
      ph = ph == 2 ? 0 : ph + 1;
      adv(ax0, dl); adv(ax1, dl); adv(aw0, dl); adv(aw1, dl);
      side(8);
      SCHED();
#pragma unroll
      for (int k = 0; k < 2; ++k)
#pragma unroll
        for (int m = 0; m < 4; ++m) {
          acc[0][m] = mfma(wf[k][0], xf[k][m], acc[0][m]); acc[1][m] = mfma(wf[k][1], xf[k][m], acc[1][m]);
          SCHED();
          side(4 * k + m);
          SCHED();
        }
    };
    auto none = [](int) {};
    mktag();
    asm volatile("" : "+v"(pre[3]));
    {
      const float mean = sum32(pre) * (1.f / 512.f);
      float v0 = 0.f, v1 = 0.f;
#pragma unroll
      for (int i = 0; i < 4; ++i)
#pragma unroll
        for (int e = 0; e < 4; ++e) {
          const float d0 = bflo(pre[i][e]) - mean, d1 = bfhi(pre[i][e]) - mean;
          v0 = sfma(d0, d0, v0); v1 = sfma(d1, d1, v1);
        }
      const float rsd = __builtin_amdgcn_rsqf(row16_sum(v0 + v1) * (1.f / 512.f) + 1e-5f);
      if (i16 == 0) __builtin_amdgcn_raw_buffer_store_b128((u32x4){tagA, f2u(rsd), tagB, f2u(mean * rsd)}, wsr, WS_STAT + srow * 16, 0, 16);
    }
    __builtin_amdgcn_s_setprio(APRIO);
    barrier();
    chunk(none);
    if (d) {
      const f32x2 c0 = ldsf2(LB + L_LS + 512 + (lane & 3) * 8);
      if (lane < 4) __builtin_amdgcn_raw_buffer_store_b128((u32x4){tagA, f2u(sadd(c0[0], c0[1])), tagB, 0u}, wsr, WS_BS + j * 1024 + (4 * r + lane) * 16, 0, 16);
    } else if (mlp) {
      const f32x2 c0 = ldsf2(LB + L_LS + (lane & 31) * 8), c1 = ldsf2(LB + L_LS + 256 + (lane & 31) * 8);
      if (lane < 32) __builtin_amdgcn_raw_buffer_store_b128((u32x4){tagA, f2u(sadd(c0[0], c1[0])), tagB, f2u(sadd(c0[1], c1[1]))}, wsr, WS_CS + (jm * 16 + r) * 512 + lane * 16, 0, 16);
    }
    barrier();
#pragma unroll 1
    for (int c = 1; c < 3; ++c) { if (mlp) chunk(none); barrier(); }
    chunk(none);
    asm volatile("" : "+v"(dv[0]), "+v"(dv[1]), "+v"(dv[2]), "+v"(dv[3]), "+v"(bv[0]), "+v"(bv[1]), "+v"(bv[2]), "+v"(bv[3]));
    __builtin_amdgcn_s_waitcnt(0x0F70);
    u32x2 xv[2][4];
#pragma unroll
    for (int m = 0; m < 2; ++m)
#pragma unroll
      for (int n = 0; n < 4; ++n) xv[m][n] = *(LAS u32x2*)(size_t)(HB + 3 * SLOT + 12288 + (16 * m + i16) * 128 + (g & 1) * 8 + (((2 * n + (g >> 1)) ^ (i16 >> 1)) * 16));
    barrier();
#pragma unroll 1
    for (int c = 4; c < 6; ++c) { if (mlp) chunk(none); barrier(); }
    f32x2 st[4];
#pragma unroll
    for (int m = 0; m < 4; ++m) st[m] = (f32x2){ldsf1(sta + m * 32), ldsf1(sta + m * 32 + 8)};
    unsigned ra[4], ho = mlp ? WS_H + (unsigned)(2 * r + d) * H_MT + (unsigned)(4 * jm + 3) * 4096 + lane * 16 : 0xffff0000u;
    f32x2 rs[4], nm[4];
    chunk([&](int k) {
      if (k < 4) { rs[k] = bco(st[k][0]); nm[k] = bco(-st[k][1]); asm volatile("" : "+v"(rs[k]), "+v"(nm[k])); }
      if (k < 8) return;
#pragma unroll
      for (int n = 0; n < 4; ++n) ra[n] = home + r2l + ((n ^ r2x) * 32);
      asm volatile("" : "+v"(ra[0]), "+v"(ra[1]), "+v"(ra[2]), "+v"(ra[3]), "+v"(ho));
    });
    barrier();
    const f32x4 cs0 = ldsf4(csa), cs1 = ldsf4(csa + 64);
    const u32x2 b10 = *(LAS u32x2*)(size_t)(LB + L_B1 + 192 + g * 8), b11 = *(LAS u32x2*)(size_t)(LB + L_B1 + 224 + g * 8);
    Col col;
    auto take = [&](int i, int pg) {
#pragma unroll
      for (int n = 0; n < 4; ++n) we[i][n] = ldstr(ra[n] + pg * 4096, ra[n] + pg * 4096 + 2048);
    };
    chunk([&](int k) {
      if (k == 2) { mkcol(col, cs0, cs1, b10, b11); asm volatile("" : "+v"(col.bias[0]), "+v"(col.bias[1]), "+v"(col.bias[2]), "+v"(col.bias[3])); }
      if (k >= 3 && k < 6) take(k - 3, k - 3);
    });
    barrier();
    __builtin_amdgcn_s_setprio(2);
    u32x4 oa[4];
#pragma unroll
    for (int m = 0; m < 4; ++m) {
      oa[m] = gelu8(acc[0][m], acc[1][m], col, rs[m], nm[m]);
      __builtin_amdgcn_raw_buffer_store_b128(oa[m], wsr, ho + m * 1024, 0, HST);
      if (m >= 1) asm volatile("" :: "v"(oa[m - 1]));
      if (m == 0) { take(3, 3); take(4, 4); }
      SCHED();
    }
    prog_wait(PW + (6 + d) * 4, 2);
#pragma unroll
    for (int i = 5; i < 8; ++i) take(i, i - 5);
    u32x4 s2v[2];
#pragma unroll
    for (int m = 0; m < 2; ++m) s2v[m] = *(LAS u32x4*)(size_t)(LB + L_ST2 + (tok + 8 * (mtile & 1) + 4 * d + 2 * m) * 16);
    setup2(8);
    wait_lgkm0();
    asm volatile("" :: "v"(oa[3]));
    prog_set(PW + w * 4, 1);
    snap(false);
#pragma unroll
    for (int m = 0; m < 2; ++m) {
      const u32x4 s2 = s2v[m];
      const f32x2 r2 = bco(u2f(s2.y)), n2 = bco(-u2f(s2.w));
#pragma unroll
      for (int n = 0; n < 4; ++n) {
        f32x2 o[2];
#pragma unroll
        for (int h = 0; h < 2; ++h) {
          const f32x2 xx = {bflo(xv[m][n][h]), bfhi(xv[m][n][h])};
          o[h] = pfma(pfma(xx, r2, n2), (f32x2){bflo(dv[n][h]), bfhi(dv[n][h])}, xx + (f32x2){bflo(bv[n][h]), bfhi(bv[n][h])});
        }
        acc2[n][m] = __builtin_shufflevector(o[0], o[1], 0, 1, 2, 3);
        acc2[n][2 + m] = (f32x4){0, 0, 0, 0};
        asm volatile("" : "+v"(acc2[n][m]), "+v"(acc2[n][2 + m]));
        if (4 * m + n == APUB) {
          SCHED();
          wait_vm<0>();
          __builtin_amdgcn_raw_buffer_store_b64((u32x2){tagA, tagB}, wsr, mlp && lane == 0 ? 0u : pubo, WS_FLAG + (2 * r + d) * 2048 + flag_of(jm, 3), 16);
          SCHED();
        }
      }
    }
    seen();
  };

  unsigned tw[2][2], ra[4][2];
  u32x4 sl[2];
  char* op;
  auto mktail = [&]() {
    int tl = threadIdx.x;
    asm volatile("" : "+v"(tl));
    const int ck = tl & 15, gt = (tl >> 4) & 3, hw = tl >> 6;
#pragma unroll
    for (int h = 0; h < 2; ++h) {
      tw[0][h] = home + ck * 128 + (((4 * h + gt) ^ (ck & 7)) * 16) + (w == 1 ? 4096 : 0);
      tw[1][h] = tw[0][h] + (w == 1 ? -4096 : 4096);
      asm volatile("" : "+v"(tw[0][h]), "+v"(tw[1][h]));
    }
#pragma unroll
    for (int q = 0; q < 4; ++q) {
      ra[q][0] = LB + gt * HALF + (8 * hw + 2 * q + (ck >> 3)) * 128 + (((ck & 7) ^ (2 * q + (ck >> 3))) * 16);
      ra[q][1] = ra[q][0] + 4 * HALF;
      asm volatile("" : "+v"(ra[q][0]), "+v"(ra[q][1]));
    }
#pragma unroll
    for (int e = 0; e < 8; ++e) sl[e >> 2][e & 3] = (ck >> 1) == e ? ((ck & 1) ? 0x3f800000u : 0x3f80u) : 0u;
    asm volatile("" : "+v"(sl[0]), "+v"(sl[1]));
    const int orow = 64 * (hw & 1) + 16 * (ck >> 3) + 2 * (hw >> 1) + (gt >> 1) + 8 * (mtile & 1);
    op = p.out + (128 * (mtile >> 1) + orow) * 2048 + ntile * 256 + ((ck & 4) * 8 + (gt & 1) * 16 + (ck & 3) * 4) * 4;
    asm volatile("" : "+v"(op));
  };
  auto phase2 = [&](auto nm_) __attribute__((always_inline)) {
    constexpr int NM = decltype(nm_)::value, S0 = NM == 8 ? 0 : 5, N = NM == 8 ? 13 : 9;
    const u64 tag = ((u64)tagB << 32) | tagA;
    auto has = [&](int k) { return fl[k < NM ? k : k - NM + S0] == tag; };
    auto need = [&](int k) {
      if (__builtin_expect(!has(k), 0)) {
#pragma unroll 1
        for (int spin = 1; spin < SPIN_CAP; ++spin) {
          snap(k >= NM);
          if (has(k)) break;
          if (!(spin & 31)) {
            const u32x2 f = __builtin_amdgcn_raw_buffer_load_b64(wsr, 0, k < NM ? fm + 8 * k : fs + 8 * (k - NM + S0), 16);
            if (__builtin_amdgcn_readfirstlane(f.x) == tagA && __builtin_amdgcn_readfirstlane(f.y) == tagB) break;
          }
          __builtin_amdgcn_s_sleep(PSLEEP);
        }
      }
      asm volatile("" ::: "memory");
    };
    auto publish = [&]() { __builtin_amdgcn_raw_buffer_store_b64((u32x2){tagA, tagB}, wsr, pubo, 0, 16); };
    auto loadH = [&](int i, int m) -> u32x4 {
      if (m == 0) asm volatile("" : "+v"(hb));
      return __builtin_amdgcn_raw_buffer_load_b128(wsr, hb + (m & 1) * 1024, __builtin_amdgcn_readlane(ksl, i) ^ ((m & 2) * 1024), HAUX);
    };
    u32x4 hf[N][4];
    auto load4 = [&](int i) {
#pragma unroll
      for (int m = 0; m < 4; ++m) hf[i][m] = loadH(i, m);
    };
    if (NM == 8) {
      __builtin_amdgcn_s_setprio(EPRI);
      need(0); load4(0);
      need(1); load4(1);
    } else {
      unsigned vc[8];
      mkvc(vc);
      asm volatile("" : "+v"(vc[0]), "+v"(vc[1]), "+v"(vc[2]), "+v"(vc[3]), "+v"(vc[4]), "+v"(vc[5]), "+v"(vc[6]), "+v"(vc[7]));
      const unsigned o = ntile * 65536 + kg * 128, hs = fresh(HB + (w < 4 ? SLOT : 0));
      wait_vm<4>(); publish();
      __builtin_amdgcn_s_setprio(0);
      dma_x8(vc, p.cre + o, hs);
      snap(false);
      dma_x8(vc, p.cim + o, hs + 8192);
      seen();
      need(0); load4(0);
      need(1); load4(1);
    }
    s16x8 cfr[N - NM][4];
    auto rdc = [&](int q, unsigned base) {
#pragma unroll
      for (int n = 0; n < 4; ++n) {
        unsigned a = base + n * 2048;
        asm volatile("" : "+v"(a));
        cfr[q][n] = cat(*(LAS s16x4*)(size_t)a, *(LAS s16x4*)(size_t)(a + 8192));
      }
    };
    SCHED();
#pragma unroll
    for (int i = 0; i < N; ++i) {
      const int k = i + (i < NM - 2 ? 2 : SDEP);
      if (i == NM - 2) {
#pragma unroll
        for (int e = NM; e < NM + SDEP - 2; ++e) { need(e); load4(e); }
      }
      if (k < NM) {
        if (NM == 6 && i == 0) snap(false);
        else if (NM == 6 && i == 2) { need(2); load4(2); need(3); load4(3); }
        if (!(i & 1) && (NM == 8 || i)) { need(k); need(k + 1); if (NM == 6 && i == 2) absorb(5, home + 16384); snap(k + 2 == NM); }
      } else if (k < N) { need(k); }
      if (NM == 8 && i == 1) prog_set(PW + w * 4, 4);
      if (NM == 8 && i == 6) prog_wait(PW + (w | 6) * 4, 4);
      if (NM == 6 && i == 1) prog_set(PW + w * 4, 1);
      if (NM == 8 && i == EOFF) __builtin_amdgcn_s_setprio(0);
      if (NM == 8 && i == PSTEP) { prog_wait(PW + (w ^ 4) * 4, 1); rdc(4, crd - 2 * SLOT); }
      if (NM == 8 && i == PSTEP + 1) { wait_lgkm0(); prog_set(PW + w * 4, 5); }
      if (i >= NM - 1 && i < NM + (NM == 8 ? 3 : 2)) rdc(i + 1 - NM, crd ^ (32 * (i + 1 - NM + (NM == 8 ? 0 : 1))));
      if (NM == 6 && i == N - 1) prog_wait(PW + (w ^ 4) * 4, 5);
      auto put = [&](int m) {
#pragma unroll
        for (int h = 0; h < 2; ++h) {
          const f32x4 u = acc2[2 * h][m], v = acc2[2 * h + 1][m];
          *(LAS u32x4*)(size_t)(tw[m >> 1][h] + (m & 1) * 2048) = (u32x4){pk2(u[0], u[1]), pk2(u[2], u[3]), pk2(v[0], v[1]), pk2(v[2], v[3])};
        }
        SCHED();
      };
      SCHED();
#pragma unroll
      for (int m = 0; m < 4; ++m) {
#pragma unroll
        for (int n = 0; n < 4; ++n)
          if (NM == 8 && i == 0) asm volatile("v_mfma_f32_16x16x32_bf16 %0, %1, %2, %0" : "+v"(acc2[n][m]) : "v"(we[0][n]), "v"(hf[0][m]));
          else acc2[n][m] = mfma(i < NM ? we[i < NM ? i : 0][n] : cfr[i < NM ? 0 : i - NM][n], __builtin_bit_cast(s16x8, hf[i][m]), NM == 6 && i == 0 ? (f32x4){0, 0, 0, 0} : acc2[n][m]);
        SCHED();
        if (k < N && (NM == 8 || i > 1)) hf[k < N ? k : 0][m] = loadH(k, m);
        if (i == N - 2 && m == 0) mktail();
        if (i == N - 1 && m > 0) put(m - 1);
        SCHED();
      }
      if (i == N - 1) put(3);
      if ((i & 1) && k < NM) seen();
    }
  };
  if (__builtin_expect(w >= 6 || w < 2, 1)) {
    if (__builtin_expect(w >= 6, 1)) roleD(); else roleA();
    phase2(std::integral_constant<int, 8>{});
  } else {
    if (w >= 4) roleL(); else roleB();
    phase2(std::integral_constant<int, 6>{});
  }
  const int tl = threadIdx.x;
  barrier();
  {
    s16x8 pt[4][2];
#pragma unroll
    for (int q = 0; q < 4; ++q) { pt[q][0] = lds128(ra[q][0]); pt[q][1] = lds128(ra[q][1]); }
#pragma unroll
    for (int t = 0; t < 2; ++t) {
      f32x4 s = {0, 0, 0, 0};
#pragma unroll
      for (int k = 0; k < 4; ++k) s = mfma(__builtin_bit_cast(s16x8, sl[k >> 1]), pt[2 * t + (k >> 1)][k & 1], s);
      __builtin_nontemporal_store(s, (GAS f32x4*)(op + t * 65536));
    }
  }
  if (b == 255 && tl == 0) __builtin_amdgcn_raw_buffer_store_b64((u32x2){tagA, tagB}, wsr, WS_EPOCH, 0, 16);
}

extern "C" void kernel_launch(void* const* d_in, const int* in_sizes, int n_in, void* d_out, int out_size, void* d_ws,
                              size_t ws_size, hipStream_t stream) {
  Params p;
  p.x = (const char*)d_in[0]; p.lre = (const char*)d_in[1]; p.lim = (const char*)d_in[2];
  p.bre = (const char*)d_in[3]; p.bim = (const char*)d_in[4]; p.cre = (const char*)d_in[5]; p.cim = (const char*)d_in[6];
  p.logdt = (const char*)d_in[7]; p.dsk = (const char*)d_in[8];
  p.w1 = (const char*)d_in[10]; p.b1 = (const char*)d_in[11]; p.w2 = (const char*)d_in[12]; p.b2 = (const char*)d_in[13];
  p.out = (char*)d_out; p.ws = (char*)d_ws;
  fused_kernel<<<256, 512, 0, stream>>>(p);
}
```
